```python
import torch

def _softmax(x, axis=-1):
    return torch.softmax(x, dim=axis)
def _gelu(x):
    return torch.nn.functional.gelu(x, approximate="tanh")
def _silu(x):
    return torch.nn.functional.silu(x)
def _sigmoid(x):
    return torch.sigmoid(x)
def _layernorm(x, axis=-1, eps=1e-5):
    m = x.mean(dim=axis, keepdim=True)
    v = x.var(dim=axis, keepdim=True, unbiased=False)
    return (x - m) / torch.sqrt(v + eps)
def _scatter_add(tgt, idx, src, axis=0):
    return tgt.clone().index_add_(axis, idx, src)
def _segment_sum(src, seg, n):
    out = torch.zeros((n,) + src.shape[1:], dtype=src.dtype)
    return out.index_add_(0, seg, src)
def _pad(x, pw):
    flat = tuple(v for pair in reversed(pw) for v in pair)
    return torch.nn.functional.pad(x, flat)

kernel_name = 'concat_heads__compressed_kv_cla__linear_kernel_hedgehog__prompt'

def setup_inputs():

    _w = (lambda a: (lambda t: t.to(torch.bfloat16) if t.is_floating_point() else t)(torch.as_tensor(a)))
    def _A_setup():
        B, H, Sq, Sk, D, M = 1, 32, 2048, 2048, 128, 512
        Dc = 4 * D
        return {
            "x": _w((lambda *s: torch.randn(*s, dtype=torch.bfloat16) * 0.8)(B, Sq, M)),
            "c_kv": _w((lambda *s: torch.randn(*s, dtype=torch.bfloat16) * 0.4)(B, Sk, Dc)),
            "w_q": _w((lambda *s: torch.randn(*s, dtype=torch.bfloat16) * 6.4)(M, H, D) * (M ** -0.5)),
            "w_uk": _w((lambda *s: torch.randn(*s, dtype=torch.bfloat16) * 6.4)(Dc, H, D) * (Dc ** -0.5)),
            "w_uv": _w((lambda *s: torch.randn(*s, dtype=torch.bfloat16) * 0.4)(Dc, H, D) * (Dc ** -0.5)),
            "w_o": _w((lambda *s: torch.randn(*s, dtype=torch.bfloat16) * 0.4)(H, D, M) * ((H * D) ** -0.5)),
        }
    _A_in = _A_setup()

    def _B_setup():
        B, S, D, H = 1, 2048, 512, 32
        Dh = D // H
        return {
            "x": _w((lambda *s: torch.randn(*s, dtype=torch.bfloat16) * 0.4)(B, S, D)),
            "wqkv": _w((lambda *s: torch.randn(*s, dtype=torch.bfloat16) * 0.4)(D, 3 * D)),
            "w_phi": _w((lambda *s: torch.randn(*s, dtype=torch.bfloat16) * 0.4)(H, Dh, Dh) * (Dh ** -0.5)),
            "wo": _w((lambda *s: torch.randn(*s, dtype=torch.bfloat16) * 0.05)(D, D)),
        }
    _B_in = _B_setup()

    return {
        "A_x": _A_in["x"],
        "A_c_kv": _A_in["c_kv"],
        "A_w_q": _A_in["w_q"],
        "A_w_uk": _A_in["w_uk"],
        "A_w_uv": _A_in["w_uv"],
        "A_w_o": _A_in["w_o"],
        "B_x": _B_in["x"],
        "B_wqkv": _B_in["wqkv"],
        "B_w_phi": _B_in["w_phi"],
        "B_wo": _B_in["wo"],
    }

def reference(A_x, A_c_kv, A_w_q, A_w_uk, A_w_uv, A_w_o, B_x, B_wqkv, B_w_phi, B_wo):
    def _A_ref(x, c_kv, w_q, w_uk, w_uv, w_o):
        B, Sq, M = x.shape[0], x.shape[1], x.shape[2]
        Sk = c_kv.shape[1]
        H, D = w_q.shape[1], w_q.shape[2]
        q = torch.einsum("bqm,mhd->bhqd", x, w_q)
        k = torch.einsum("bkc,chd->bhkd", c_kv, w_uk)
        v = torch.einsum("bkc,chd->bhkd", c_kv, w_uv)
        scores = torch.einsum("bhqd,bhkd->bhqk", q, k) * (D ** -0.5)
        off = Sk - Sq
        qi = torch.arange(Sq)[:, None]
        kj = torch.arange(Sk)[None, :]
        cmask = kj <= (qi + off)
        scores = torch.where(cmask[None, None], scores, -1e9)
        p = _softmax(scores, axis=-1)
        ctx = torch.einsum("bhqk,bhkd->bhqd", p, v)
        return torch.einsum("bhqd,hdm->bqm", ctx, w_o)

    def _B_ref(x, wqkv, w_phi, wo):
        B, S, D = x.shape
        H = 32; Dh = D // H
        qkv = (lambda x, *d: x.permute(*d))((x @ wqkv).reshape(B, S, 3, H, Dh), 2, 0, 3, 1, 4)
        q, k, v = qkv[0], qkv[1], qkv[2]
        pq = torch.einsum("bhsd,hdf->bhsf", q, w_phi)
        pk = torch.einsum("bhsd,hdf->bhsf", k, w_phi)
        phi_q = torch.concatenate([_softmax(pq, axis=-1), _softmax(-pq, axis=-1)], axis=-1)
        phi_k = torch.concatenate([_softmax(pk, axis=-1), _softmax(-pk, axis=-1)], axis=-1)
        kv = torch.einsum("bhsf,bhsd->bhfd", phi_k, v)
        z  = torch.einsum("bhsf->bhf", phi_k)
        num = torch.einsum("bhqf,bhfd->bhqd", phi_q, kv)
        den = torch.einsum("bhqf,bhf->bhq", phi_q, z)[..., None] + 1e-6
        o = num / den
        return (lambda x, *d: x.permute(*d))(o, 0, 2, 1, 3).reshape(B, S, D) @ wo

    a = _A_ref(A_x, A_c_kv, A_w_q, A_w_uk, A_w_uv, A_w_o)
    b = _B_ref(B_x, B_wqkv, B_w_phi, B_wo)
    return torch.concatenate([a, b], axis=-1)
```

```cpp
#include <hip/hip_runtime.h>
#include <hip/hip_bf16.h>
#include <stdint.h>

typedef unsigned short u16;
__device__ __forceinline__ float bf2f(u16 b) { return __uint_as_float(((unsigned)b) << 16); }
__device__ __forceinline__ u16 f2bf(float f) { unsigned u = __float_as_uint(f); u += 0x7FFFu + ((u >> 16) & 1u); return (u16)(u >> 16); }
__device__ __forceinline__ float rbf(float f) { return bf2f(f2bf(f)); }

__global__ __launch_bounds__(256) void gemm_naive(const u16* __restrict__ A, int lda, const u16* __restrict__ B, int ldb, u16* __restrict__ C, int ldc, int K) {
    __shared__ float As[16][65];
    __shared__ float Bs[16][65];
    const int tx = threadIdx.x & 15, ty = threadIdx.x >> 4;
    const int row0 = blockIdx.y * 64, col0 = blockIdx.x * 64;
    float acc[4][4] = {};
    for (int k0 = 0; k0 < K; k0 += 16) {
        for (int i = threadIdx.x; i < 64 * 16; i += 256) { int r = i >> 4, k = i & 15; As[k][r] = bf2f(A[(size_t)(row0 + r) * lda + k0 + k]); }
        for (int i = threadIdx.x; i < 64 * 16; i += 256) { int k = i >> 6, c = i & 63; Bs[k][c] = bf2f(B[(size_t)(k0 + k) * ldb + col0 + c]); }
        __syncthreads();
        for (int k = 0; k < 16; ++k) {
            float a[4], b[4];
            for (int i = 0; i < 4; ++i) a[i] = As[k][ty * 4 + i];
            for (int j = 0; j < 4; ++j) b[j] = Bs[k][tx * 4 + j];
            for (int i = 0; i < 4; ++i) for (int j = 0; j < 4; ++j) acc[i][j] = fmaf(a[i], b[j], acc[i][j]);
        }
        __syncthreads();
    }
    for (int i = 0; i < 4; ++i) for (int j = 0; j < 4; ++j) C[(size_t)(row0 + ty * 4 + i) * ldc + col0 + tx * 4 + j] = f2bf(acc[i][j]);
}

__global__ __launch_bounds__(64) void attn_naive(const u16* __restrict__ Q, const u16* __restrict__ K, const u16* __restrict__ V, u16* __restrict__ ctx) {
    __shared__ float p[2048];
    __shared__ float qs[128];
    const int q = blockIdx.x, h = blockIdx.y, lane = threadIdx.x;
    qs[lane] = bf2f(Q[(size_t)q * 4096 + h * 128 + lane]); qs[lane + 64] = bf2f(Q[(size_t)q * 4096 + h * 128 + 64 + lane]);
    __syncthreads();
    float m = -INFINITY;
    for (int k = lane; k <= q; k += 64) {
        const u16* kr = K + (size_t)k * 4096 + h * 128; float s = 0.f;
        for (int d = 0; d < 128; d += 8) { uint4 w = *(const uint4*)(kr + d); unsigned ww[4] = {w.x, w.y, w.z, w.w};
            for (int j = 0; j < 4; ++j) { s = fmaf(qs[d + 2 * j], __uint_as_float(ww[j] << 16), s); s = fmaf(qs[d + 2 * j + 1], __uint_as_float(ww[j] & 0xffff0000u), s); } }
        s *= 0.08838834764831845f; p[k] = s; m = fmaxf(m, s);
    }
    for (int o = 32; o >= 1; o >>= 1) m = fmaxf(m, __shfl_xor(m, o));
    float l = 0.f;
    for (int k = lane; k <= q; k += 64) { float e = __expf(p[k] - m); p[k] = e; l += e; }
    for (int o = 32; o >= 1; o >>= 1) l += __shfl_xor(l, o);
    __syncthreads();
    float a0 = 0.f, a1 = 0.f;
    for (int k = 0; k <= q; ++k) { unsigned w = *(const unsigned*)(V + (size_t)k * 4096 + h * 128 + 2 * lane); float e = rbf(p[k]); a0 = fmaf(e, __uint_as_float(w << 16), a0); a1 = fmaf(e, __uint_as_float(w & 0xffff0000u), a1); }
    const float il = 1.f / l;
    ctx[(size_t)q * 4096 + h * 128 + 2 * lane] = f2bf(a0 * il); ctx[(size_t)q * 4096 + h * 128 + 2 * lane + 1] = f2bf(a1 * il);
}

__device__ __forceinline__ void phi_map(const u16* src, const u16* wphi, float* phi) {
    float x[16], p[16];
    for (int d = 0; d < 16; ++d) x[d] = bf2f(src[d]);
    float mx = -INFINITY, mn = INFINITY;
    for (int f = 0; f < 16; ++f) { float s = 0.f; for (int d = 0; d < 16; ++d) s = fmaf(x[d], bf2f(wphi[d * 16 + f]), s); p[f] = rbf(s); mx = fmaxf(mx, p[f]); mn = fminf(mn, p[f]); }
    float s1 = 0.f, s2 = 0.f;
    for (int f = 0; f < 16; ++f) { phi[f] = __expf(p[f] - mx); s1 += phi[f]; phi[16 + f] = __expf(mn - p[f]); s2 += phi[16 + f]; }
    for (int f = 0; f < 16; ++f) { phi[f] = rbf(phi[f] / s1); phi[16 + f] = rbf(phi[16 + f] / s2); }
}
__global__ __launch_bounds__(256) void b_state(const u16* __restrict__ qkv, const u16* __restrict__ wphi, float* __restrict__ st) {
    __shared__ float ph[256][33];
    __shared__ float vs[256][17];
    const int h = blockIdx.x, t = threadIdx.x;
    float acc[2] = {0.f, 0.f}; float zacc = 0.f;
    for (int s0 = 0; s0 < 2048; s0 += 256) {
        float phi[32]; phi_map(qkv + (size_t)(s0 + t) * 1536 + 512 + h * 16, wphi + h * 256, phi);
        for (int f = 0; f < 32; ++f) ph[t][f] = phi[f];
        for (int d = 0; d < 16; ++d) vs[t][d] = bf2f(qkv[(size_t)(s0 + t) * 1536 + 1024 + h * 16 + d]);
        __syncthreads();
        for (int i = 0; i < 2; ++i) { int e = t + 256 * i, f = e >> 4, d = e & 15; float a = acc[i]; for (int s = 0; s < 256; ++s) a = fmaf(ph[s][f], vs[s][d], a); acc[i] = a; }
        if (t < 32) { float a = zacc; for (int s = 0; s < 256; ++s) a += ph[s][t]; zacc = a; }
        __syncthreads();
    }
    for (int i = 0; i < 2; ++i) st[h * 544 + t + 256 * i] = rbf(acc[i]);
    if (t < 32) st[h * 544 + 512 + t] = rbf(zacc);
}
__global__ __launch_bounds__(256) void b_out(const u16* __restrict__ qkv, const u16* __restrict__ wphi, const float* __restrict__ st, u16* __restrict__ o) {
    const int idx = blockIdx.x * 256 + threadIdx.x, s = idx >> 5, h = idx & 31;
    float phi[32]; phi_map(qkv + (size_t)s * 1536 + h * 16, wphi + h * 256, phi);
    const float* kv = st + h * 544; float den = 0.f;
    for (int f = 0; f < 32; ++f) den = fmaf(phi[f], kv[512 + f], den);
    den = rbf(rbf(den) + 1e-6f);
    for (int d = 0; d < 16; ++d) { float n = 0.f; for (int f = 0; f < 32; ++f) n = fmaf(phi[f], kv[f * 16 + d], n); o[(size_t)s * 512 + h * 16 + d] = f2bf(rbf(n) / den); }
}

extern "C" void kernel_launch(void* const* d_in, const int* in_sizes, int n_in, void* d_out, int out_size, void* d_ws, size_t ws_size, hipStream_t stream) {
    const u16* Ax = (const u16*)d_in[0]; const u16* Ac = (const u16*)d_in[1]; const u16* Wq = (const u16*)d_in[2]; const u16* Wuk = (const u16*)d_in[3];
    const u16* Wuv = (const u16*)d_in[4]; const u16* Wo = (const u16*)d_in[5]; const u16* Bx = (const u16*)d_in[6]; const u16* Wqkv = (const u16*)d_in[7];
    const u16* Wphi = (const u16*)d_in[8]; const u16* Bwo = (const u16*)d_in[9];
    u16* out = (u16*)d_out; char* ws = (char*)d_ws;
    u16* Q = (u16*)ws; u16* K = (u16*)(ws + (16 << 20)); u16* V = (u16*)(ws + (32 << 20)); u16* ctx = (u16*)(ws + (48 << 20));
    u16* qkv = (u16*)(ws + (64 << 20)); u16* o = (u16*)(ws + (72 << 20)); float* st = (float*)(ws + (76 << 20));
    gemm_naive<<<dim3(64, 32), 256, 0, stream>>>(Ax, 512, Wq, 4096, Q, 4096, 512);
    gemm_naive<<<dim3(64, 32), 256, 0, stream>>>(Ac, 512, Wuk, 4096, K, 4096, 512);
    gemm_naive<<<dim3(64, 32), 256, 0, stream>>>(Ac, 512, Wuv, 4096, V, 4096, 512);
    attn_naive<<<dim3(2048, 32), 64, 0, stream>>>(Q, K, V, ctx);
    gemm_naive<<<dim3(8, 32), 256, 0, stream>>>(ctx, 4096, Wo, 512, out, 1024, 4096);
    gemm_naive<<<dim3(24, 32), 256, 0, stream>>>(Bx, 512, Wqkv, 1536, qkv, 1536, 512);
    b_state<<<32, 256, 0, stream>>>(qkv, Wphi, st);
    b_out<<<256, 256, 0, stream>>>(qkv, Wphi, st, o);
    gemm_naive<<<dim3(8, 32), 256, 0, stream>>>(o, 512, Bwo, 512, out + 512, 1024, 512);
}
```

```cpp
#include <hip/hip_runtime.h>
#include <hip/hip_bf16.h>
#include <stdint.h>

typedef unsigned short u16;
__device__ __forceinline__ float bf2f(u16 b) { return __uint_as_float(((unsigned)b) << 16); }
__device__ __forceinline__ u16 f2bf(float f) { unsigned u = __float_as_uint(f); u += 0x7FFFu + ((u >> 16) & 1u); return (u16)(u >> 16); }
__device__ __forceinline__ float rbf(float f) { return bf2f(f2bf(f)); }

typedef __bf16 bf16x8 __attribute__((ext_vector_type(8)));
typedef __bf16 bf16x2 __attribute__((ext_vector_type(2)));
typedef float f32x16 __attribute__((ext_vector_type(16)));
typedef float f32x4 __attribute__((ext_vector_type(4)));
typedef float f32x2 __attribute__((ext_vector_type(2)));
typedef unsigned u32x4 __attribute__((ext_vector_type(4)));
#define LAS __attribute__((address_space(3)))
#define SBAR() __builtin_amdgcn_sched_barrier(0)
#define PIN(x) asm volatile("" : "+v"(x))
#define PINS(x) asm volatile("" : "+s"(x))
#define MFMA32(a, b, c) __builtin_amdgcn_mfma_f32_32x32x16_bf16((a), (b), (c), 0, 0, 0)
#define DI __device__ __forceinline__
typedef __amdgpu_buffer_rsrc_t srd_t;

DI unsigned cvtpk(float lo, float hi) { f32x2 v = {lo, hi}; bf16x2 b = __builtin_convertvector(v, bf16x2); return __builtin_bit_cast(unsigned, b); }
DI unsigned rfl(unsigned x) { return __builtin_amdgcn_readfirstlane(x); }
DI float swap_max(float x) { auto rr = __builtin_amdgcn_permlane32_swap(__float_as_uint(x), __float_as_uint(x), false, false); return __builtin_fmaxf(__uint_as_float(rr[0]), __uint_as_float(rr[1])); }
DI float swap_add(float x) { auto rr = __builtin_amdgcn_permlane32_swap(__float_as_uint(x), __float_as_uint(x), false, false); return __uint_as_float(rr[0]) + __uint_as_float(rr[1]); }
DI srd_t mk_srd(const void* p, int bytes) { return __builtin_amdgcn_make_buffer_rsrc((void*)p, (short)0, bytes, 0x00020000); }

template <int OFF, bool SETM0, bool INLOOP = false> DI void dma_piece(unsigned m0v, unsigned voff, srd_t srd, unsigned soff) {
    if constexpr (SETM0) asm volatile("s_mov_b32 m0, %0\n\ts_nop 0\n\tbuffer_load_dwordx4 %1, %2, %3 offen offset:%4 lds" :: "s"(m0v), "v"(voff), "s"(srd), "s"(soff), "i"(OFF) : "memory");
    else asm volatile("buffer_load_dwordx4 %0, %1, %2 offen offset:%3 lds" :: "v"(voff), "s"(srd), "s"(soff), "i"(OFF) : "memory");
}

#define ABL 0
#define ATT_SUM 0
#define ATT_THRV 64.f
constexpr float ATT_THR = ATT_THRV;
#define ATT_WIN 6
constexpr int ATT_W = ATT_WIN;
constexpr int ATT_LDS = 163840;

#define STAMP() do {} while (0)
struct AttnState {
    f32x16 o[4]; f32x16 s0, s1, negm; bf16x8 q[8]; u32x4 p[4]; float l, mhat;
};
struct AttnCtx {
    srd_t srdK, srdV, srdQ, srdC; unsigned voff, lane16, kslot0, vslot0, qoffH, coffL;
    int g, nl  , thrg, r32, hi, relH, jump; const LAS char* qstash;
    unsigned soA, soB; int rel;
};

DI bf16x8 ldsfrag(const LAS char* p) { return *(const LAS bf16x8*)p; }

template <bool DO_QK, bool DO_PV, int KP, int VP>
DI void m_phase(AttnState& st, const LAS char* kbase, const LAS char* vbase, bf16x8 (&pre)[ATT_W]) {
    constexpr int N0 = DO_QK ? 0 : 16, N1 = DO_PV ? 32 : 16;
    bf16x8 fr[32];
#pragma unroll
    for (int i = 0; i < ATT_W; ++i) fr[N0 + i] = pre[i];
#pragma unroll
    for (int i = N0; i < N1; ++i) {
        const int n = i + ATT_W;
        if (n < N1) { if (n < 16) fr[n] = ldsfrag(kbase + KP * 16384 + (n >> 1) * 2048 + (n & 1) * 512); else fr[n] = ldsfrag(vbase + VP * 16384 + ((n - 16) >> 2) * 4096 + ((n - 16) & 3) * 512); }
        SBAR();
        if (i < 16) { const int s = i >> 1;
            if (i == 0) asm volatile("v_mfma_f32_32x32x16_bf16 %0, %2, %3, %1" : "=&v"(st.s0), "+v"(st.negm) : "v"(fr[i]), "v"(st.q[0]));
            else if (i == 1) asm volatile("v_mfma_f32_32x32x16_bf16 %0, %2, %3, %1" : "=&v"(st.s1), "+v"(st.negm) : "v"(fr[i]), "v"(st.q[0]));
            else if ((i & 1) == 0) st.s0 = MFMA32(fr[i], st.q[s], st.s0);
            else              st.s1 = MFMA32(fr[i], st.q[s], st.s1);
        } else { const int ks = (i - 16) >> 2, db = (i - 16) & 3;
            st.o[db] = MFMA32(fr[i], __builtin_bit_cast(bf16x8, st.p[ks]), st.o[db]); }
        SBAR();
    }
    PIN(st.negm);
}

DI void store_ctx(const f32x16 (&o)[4], float inv, srd_t srd, unsigned voff) {
#pragma unroll
    for (int db = 0; db < 4; ++db)
#pragma unroll
        for (int s = 0; s < 2; ++s) {
            u32x4 w;
#pragma unroll
            for (int e = 0; e < 4; ++e) w[e] = cvtpk(o[db][8 * s + 2 * e] * inv, o[db][8 * s + 2 * e + 1] * inv);
            __builtin_amdgcn_raw_buffer_store_b128(w, srd, voff + (4 * db + 2 * s) * 512, 0, 0);
        }
}

template <bool LAST, int PAR>
DI void v_phase(AttnState& st, AttnCtx& c, const int t) {
    const bool sw = (t == c.nl);
    const unsigned vm0 = c.vslot0 + (PAR ^ 1) * 16384, km0 = c.kslot0 + PAR * 16384;
    const unsigned vso = c.soA, kso = c.soB;
    SBAR();
    if (__builtin_expect(sw, 0)) { const float lt = swap_add(st.l); store_ctx(st.o, __builtin_amdgcn_rcpf(lt), c.srdC, c.lane16 + c.coffL);
#pragma unroll
        for (int d = 0; d < 4; ++d) st.o[d] = f32x16{};
        st.l = 0.f; c.rel = c.relH; }
    if (__builtin_expect(c.rel > c.thrg, 0)) { const int dq = 32 * c.g + c.r32 - c.rel - 4 * c.hi; const float NEG = -__builtin_inff();
#pragma unroll
        for (int r = 0; r < 16; ++r) { const int cc = (r & 3) + 8 * (r >> 2); if (cc > dq) st.s0[r] = NEG; if (cc + 32 > dq) st.s1[r] = NEG; } }
    SBAR();
    if (!LAST) dma_piece<0, true, true>(vm0, c.voff, c.srdV, vso);
#define MX3(a, b, c) __builtin_fmaxf(__builtin_fmaxf((a), (b)), (c))
    float a = MX3(st.s0[0], st.s0[1], st.s1[0]), b = MX3(st.s0[2], st.s0[3], st.s1[1]);
    a = MX3(a, st.s1[2], st.s1[3]);
#pragma unroll
    for (int r = 4; r < 16; r += 4) {
        a = MX3(a, st.s0[r], st.s0[r + 1]); b = MX3(b, st.s0[r + 2], st.s0[r + 3]);
        a = MX3(a, st.s1[r], st.s1[r + 1]); b = MX3(b, st.s1[r + 2], st.s1[r + 3]); }
    const float rmax = __builtin_fmaxf(a, b);
    const unsigned long long firstm = (t == 0 || sw) ? ~0ull : 0ull;
    if (__builtin_expect((__builtin_amdgcn_ballot_w64(rmax > ATT_THR) | firstm) != 0ull, 0)) {
        const bool first = firstm != 0ull;
        const float m = swap_max(rmax);
        const float delta = __builtin_fmaxf(m, first ? -1e30f : 0.f);
        st.mhat = (first ? 0.f : st.mhat) + delta;
        const float f = first ? 1.f : __builtin_amdgcn_exp2f(-delta); st.l *= f;
#pragma unroll
        for (int r = 0; r < 16; ++r) { st.s0[r] -= delta; st.s1[r] -= delta; st.negm[r] = -st.mhat; }
#pragma unroll
        for (int d = 0; d < 4; ++d)
#pragma unroll
            for (int r = 0; r < 16; ++r) st.o[d][r] *= f;
    }
    SBAR();
    float e[32];
#define EXPG(k) do { _Pragma("unroll") for (int i_ = 0; i_ < 4; ++i_) e[4 * (k) + i_] = __builtin_amdgcn_exp2f((k) < 4 ? st.s0[4 * (k) + i_] : st.s1[4 * ((k) - 4) + i_]); } while (0)
    float l0 = st.l, l1 = 0.f, l2 = 0.f, l3 = 0.f;
#define FING(k) do { l0 += e[4 * (k)]; if ((k) == 0) { l1 = e[1]; l2 = e[2]; l3 = e[3]; } else { l1 += e[4 * (k) + 1]; l2 += e[4 * (k) + 2]; l3 += e[4 * (k) + 3]; } \
        unsigned w0_ = cvtpk(e[4 * (k)], e[4 * (k) + 1]), w1_ = cvtpk(e[4 * (k) + 2], e[4 * (k) + 3]); PIN(w0_); PIN(w1_); PIN(l0); PIN(l1); PIN(l2); PIN(l3); \
        st.p[(k) >> 1][2 * ((k) & 1)] = w0_; st.p[(k) >> 1][2 * ((k) & 1) + 1] = w1_; } while (0)
#define SUMFIN() st.l = (l0 + l1) + (l2 + l3)
    EXPG(0); SBAR();
    if (!LAST) dma_piece<1024, false, true>(vm0, c.voff, c.srdV, vso);
    EXPG(1); FING(0); SBAR();
    if (!LAST) dma_piece<2048, false, true>(vm0, c.voff, c.srdV, vso);
    EXPG(2); FING(1); SBAR();
    if (!LAST) dma_piece<3072, false, true>(vm0, c.voff, c.srdV, vso);
    EXPG(3); FING(2); SBAR();
    if (!LAST) dma_piece<0, true, true>(km0, c.voff, c.srdK, kso);
    EXPG(4); FING(3); SBAR();
    if (!LAST) dma_piece<1024, false, true>(km0, c.voff, c.srdK, kso);
    EXPG(5); FING(4); SBAR();
    if (!LAST) dma_piece<2048, false, true>(km0, c.voff, c.srdK, kso);
    EXPG(6); FING(5); SBAR();
    if (!LAST) dma_piece<3072, false, true>(km0, c.voff, c.srdK, kso);
    EXPG(7); FING(6); SBAR();
    FING(7);
    SUMFIN();
#undef EXPG
#undef FING
#undef SUMFIN
    if (!LAST) {
        if (__builtin_expect(t == c.nl - 1, 0)) {
#pragma unroll
            for (int s = 0; s < 8; ++s) st.q[s] = ldsfrag(c.qstash + s * 1024);
            st.negm = f32x16{};
        }
        c.soA = c.soB; c.soB += (t + 3 == c.nl) ? c.jump : 16384; c.rel += 64;
    }
    SBAR();
}

__global__ __launch_bounds__(512, 2) void attn_kernel(const u16* __restrict__ Qf, const u16* __restrict__ Kf, const u16* __restrict__ Vf, u16* __restrict__ Cf, unsigned* __restrict__ dbg) {
    extern __shared__ __attribute__((aligned(16))) char lds_[];
    LAS char* lds = (LAS char*)lds_;
    const int tid = threadIdx.x, lane = tid & 63;
    const int wid = rfl(tid >> 6);
    unsigned stampv = 0; int nst = 0; (void)stampv; (void)nst;
    STAMP();
    AttnCtx c;
    const int grp = wid >> 2; c.g = wid & 3; c.r32 = lane & 31; c.hi = lane >> 5;
    const int bid = blockIdx.x, h = (bid & 7) * 4 + (bid >> 6), j = (bid >> 3) & 7;
    const int qbH = 15 - j, qbL = j, nl = 2 * j + 2;
    c.nl = grp ? nl : 1000; c.thrg = 32 * c.g - 63; c.jump = (18 - nl) * 16384; c.relH = 64 * (17 - 2 * qbH);
    c.rel = grp ? -128 * qbL : -128 * qbH;
    c.soA = 16384; c.soB = (c.nl == 2) ? 17 * 16384 : 2 * 16384;
    const unsigned lds0 = (unsigned)(uintptr_t)lds_;
    c.srdK = mk_srd((const char*)Kf + (size_t)h * 524288, 524288);
    c.srdV = mk_srd((const char*)Vf + (size_t)h * 524288, 524288);
    c.srdQ = mk_srd((const char*)Qf + (size_t)h * 524288, 524288);
    c.srdC = mk_srd((const char*)Cf, 16777216);
    c.lane16 = lane * 16; c.voff = lane * 16 + c.g * 4096;
    c.kslot0 = lds0 + grp * 65536 + c.g * 4096; c.vslot0 = c.kslot0 + 32768;
    c.qoffH = qbH * 32768 + c.g * 8192;
    c.coffL = ((qbL * 4 + c.g) * 32 + h) * 8192;
    const unsigned coffH = ((qbH * 4 + c.g) * 32 + h) * 8192;
    const LAS char* kbase = lds + grp * 65536 + c.hi * 1024 + c.r32 * 16;
    const LAS char* vbase = lds + grp * 65536 + 32768 + c.hi * 2048 + c.r32 * 16;
#define DMA4(m_, vo_, srd, so_) do { dma_piece<0, true>(m_, vo_, srd, so_); dma_piece<1024, false>(m_, vo_, srd, so_); dma_piece<2048, false>(m_, vo_, srd, so_); dma_piece<3072, false>(m_, vo_, srd, so_); } while (0)
#define BARRIER() asm volatile("s_waitcnt lgkmcnt(0)\n\ts_barrier" ::: "memory")
#define BARRIER_VM() asm volatile("s_waitcnt vmcnt(0) lgkmcnt(0)\n\ts_barrier" ::: "memory")
    AttnState st;
    c.qstash = lds + 131072 + c.g * 8192 + lane * 16;
    DMA4(c.kslot0, c.voff, c.srdK, 0u);
    { const unsigned qo = (grp == 0 ? qbH : qbL) * 32768 + c.g * 8192;
#pragma unroll
      for (int s = 0; s < 8; ++s) st.q[s] = __builtin_bit_cast(bf16x8, __builtin_amdgcn_raw_buffer_load_b128(c.srdQ, c.lane16 + s * 1024, qo, 0)); }
    SBAR();
    DMA4(c.vslot0, c.voff, c.srdV, 0u); DMA4(c.kslot0 + 16384, c.voff, c.srdK, 16384u);
    if (grp == 1) { const unsigned m_ = lds0 + 131072 + c.g * 8192;
        DMA4(m_, c.lane16, c.srdQ, c.qoffH); DMA4(m_ + 4096, c.lane16, c.srdQ, c.qoffH + 4096); }
#pragma unroll
    for (int d = 0; d < 4; ++d) st.o[d] = f32x16{};
    st.negm = f32x16{}; st.l = 0.f; st.mhat = 0.f;
    PIN(st.negm);
    BARRIER_VM();
    STAMP();
    if (grp == 1) BARRIER();
    bf16x8 pre[ATT_W];
#define PRE_K(P) do { _Pragma("unroll") for (int i = 0; i < ATT_W; ++i) pre[i] = ldsfrag(kbase + (P) * 16384 + (i >> 1) * 2048 + (i & 1) * 512); } while (0)
    PRE_K(0);
    __builtin_amdgcn_s_setprio(1);
    m_phase<true, false, 0, 0>(st, kbase, vbase, pre);
    __builtin_amdgcn_s_setprio(0);
    STAMP();
    BARRIER();
#define STEP(PAR, t) do { \
        STAMP(); \
        v_phase<false, PAR>(st, c, t); \
        PRE_K(PAR ^ 1); \
        STAMP(); \
        asm volatile("s_barrier" ::: "memory"); \
        STAMP(); \
        __builtin_amdgcn_s_setprio(1); \
        m_phase<true, true, PAR ^ 1, PAR>(st, kbase, vbase, pre); \
        __builtin_amdgcn_s_setprio(0); \
        if ((t) < 6) STAMP(); \
        BARRIER_VM(); } while (0)
    for (int t = 0; t < 16; t += 2) { STEP(0, t); STEP(1, t + 1); }
    STAMP();
    v_phase<true, 0>(st, c, 16);
#pragma unroll
    for (int i = 0; i < ATT_W; ++i) pre[i] = ldsfrag(vbase + (i >> 2) * 4096 + (i & 3) * 512);
    STAMP();
    asm volatile("s_barrier" ::: "memory");
    __builtin_amdgcn_s_setprio(1);
    m_phase<false, true, 0, 0>(st, kbase, vbase, pre);
    __builtin_amdgcn_s_setprio(0);
    STAMP();
    BARRIER_VM();
    STAMP();
    const float lt = swap_add(st.l);
    LAS char* xo = lds + c.g * 16384 + lane * 16;
    LAS f32x2* xs = (LAS f32x2*)(lds + 65536 + c.g * 512) + lane;
    if (grp == 0) {
#pragma unroll
        for (int d = 0; d < 4; ++d)
#pragma unroll
            for (int rq = 0; rq < 4; ++rq) *(LAS f32x4*)(xo + (d * 4 + rq) * 1024) = (f32x4){st.o[d][4 * rq], st.o[d][4 * rq + 1], st.o[d][4 * rq + 2], st.o[d][4 * rq + 3]};
        *xs = (f32x2){st.mhat, lt};
        BARRIER();
    } else {
        const f32x2 o0 = *xs;
        const float m = __builtin_fmaxf(o0[0], st.mhat);
        const float f0 = __builtin_amdgcn_exp2f(o0[0] - m), f1 = __builtin_amdgcn_exp2f(st.mhat - m);
        const float inv = __builtin_amdgcn_rcpf(o0[1] * f0 + lt * f1), a0 = f0 * inv, a1 = f1 * inv;
#pragma unroll
        for (int d = 0; d < 4; ++d)
#pragma unroll
            for (int rq = 0; rq < 4; ++rq) { const f32x4 x = *(const LAS f32x4*)(xo + (d * 4 + rq) * 1024);
#pragma unroll
                for (int e = 0; e < 4; ++e) st.o[d][4 * rq + e] = st.o[d][4 * rq + e] * a1 + x[e] * a0; }
        store_ctx(st.o, 1.0f, c.srdC, c.lane16 + coffH);
    }
    STAMP();
#undef DMA4
}

#define PABL 0
#define PPRIO 0
typedef __bf16 bf16x4 __attribute__((ext_vector_type(4)));
DI bf16x4 ldstr(unsigned a) { return __builtin_amdgcn_ds_read_tr16_b64_v4bf16((LAS bf16x4*)a); }
DI bf16x8 ldsf(unsigned a) { return *(const LAS bf16x8*)a; }
DI bf16x8 cat4(bf16x4 a, bf16x4 b) { return __builtin_shufflevector(a, b, 0, 1, 2, 3, 4, 5, 6, 7); }
DI void dma1(unsigned m0v, unsigned voff, srd_t srd, unsigned soff) {
    asm volatile("s_mov_b32 m0, %0\n\ts_nop 0\n\tbuffer_load_dwordx4 %1, %2, %3 offen lds" :: "s"(m0v), "v"(voff), "s"(srd), "s"(soff) : "memory");
}
constexpr int PRJ_LDS = 131072;

template <int MB, int NB> struct Frags { bf16x8 a[MB]; bf16x4 w0[NB], w1[NB]; };
struct GAddr { unsigned a[4]; unsigned w[2]; };

template <int MB, int NB, int S> DI void frag_read(Frags<MB, NB>& f, const GAddr& g, int i) {
    if (i == 2) { f.a[0] = ldsf(g.a[S]); return; }
    const int k = i < 2 ? i : i - 1;
    if (k < 2 * NB) { const int nb = k >> 1; if (k & 1) f.w1[nb] = ldstr(g.w[1] + S * 4096 + nb * 512); else f.w0[nb] = ldstr(g.w[0] + S * 4096 + nb * 512); }
    else { const int mb = k - 2 * NB + 1; if (mb < MB) f.a[mb] = ldsf(g.a[S] + mb * 4096); }
}
template <int MB, int NB, bool ACT_A, int SN, class Hook>
DI void kstep(f32x16 (&acc)[MB][NB], const Frags<MB, NB>& cur, Frags<MB, NB>& nxt, const GAddr& g, Hook hook) {
    constexpr int NM = MB * NB, NR = MB + 2 * NB, PER = (NR + NM - 1) / NM;
#pragma unroll
    for (int i = 0; i < NM; ++i) {
        const int mb = i / NB, nb = i % NB;
#if !(PABL & 1)
        const bf16x8 w = cat4(cur.w0[nb], cur.w1[nb]);
        acc[mb][nb] = ACT_A ? MFMA32(cur.a[mb], w, acc[mb][nb]) : MFMA32(w, cur.a[mb], acc[mb][nb]);
#endif
        SBAR();
#if !(PABL & 4)
#pragma unroll
        for (int r = 0; r < PER; ++r) if (i * PER + r < NR) frag_read<MB, NB, SN>(nxt, g, i * PER + r);
#endif
        hook(i);
        SBAR();
    }
}
struct Stamper { unsigned v; int n; int lane;
    DI void operator()() {
    } };
struct NoHook { DI void operator()(int) const {} };

template <int MB, int NB, bool ACT_A, int PPT, int NT, class Issue>
DI void gemm_loop(f32x16 (&acc)[MB][NB], GAddr g, unsigned stg_xor, Issue issue, Stamper& stamp) {
    Frags<MB, NB> f0, f1;
    constexpr int NM = MB * NB, NR = MB + 2 * NB;
    stamp();
    asm volatile("s_waitcnt vmcnt(%0)\n\ts_barrier" :: "i"(PPT) : "memory");
    stamp();
#pragma unroll
    for (int i = 0; i < NR; ++i) frag_read<MB, NB, 0>(f0, g, i);
    for (int kt = 0; kt < NT; ++kt) {
        kstep<MB, NB, ACT_A, 1>(acc, f0, f1, g, NoHook{});
        kstep<MB, NB, ACT_A, 2>(acc, f1, f0, g, NoHook{});
        kstep<MB, NB, ACT_A, 3>(acc, f0, f1, g, NoHook{});
        stamp();
        asm volatile("s_waitcnt vmcnt(0) lgkmcnt(0)\n\ts_barrier" ::: "memory");
        stamp();
#pragma unroll
        for (int i = 0; i < 4; ++i) g.a[i] ^= stg_xor;
        g.w[0] ^= stg_xor; g.w[1] ^= stg_xor;
        const bool more = kt + 2 < NT;
        auto hook = [&](int i) { constexpr int PER = (PPT + NM - 1) / NM;
#pragma unroll
            for (int r = 0; r < PER; ++r) if (i * PER + r < PPT) { if (more && !(PABL & 2)) issue(kt + 2, i * PER + r); } };
        kstep<MB, NB, ACT_A, 0>(acc, f1, f0, g, hook);
    }
}

__global__ __launch_bounds__(512, 2) void proj_kernel(const u16* __restrict__ X, const u16* __restrict__ Ckv, const u16* __restrict__ Wq, const u16* __restrict__ Wuk, const u16* __restrict__ Wuv,
                                                      u16* __restrict__ Qf, u16* __restrict__ Kf, u16* __restrict__ Vf, unsigned* __restrict__ dbg) {
    extern __shared__ __attribute__((aligned(16))) char lds_[];
    const int tid = threadIdx.x, lane = tid & 63, r32 = lane & 31, hi = lane >> 5;
    const int wid = rfl(tid >> 6), wr = wid >> 2, wc = wid & 3;
    const int bid = blockIdx.x, h = (bid & 7) * 4 + (bid >> 6), j = (bid >> 3) & 7;
    const unsigned lds0 = (unsigned)(uintptr_t)lds_;
    Stamper stamp{0u, 0, lane};
    stamp();
    const srd_t srdC = mk_srd(Ckv, 2097152), srdX = mk_srd(X, 2097152);
    const srd_t srdW = mk_srd((wid < 4 ? Wuk : Wuv), 4194304), srdWq = mk_srd(Wq, 4194304);
    unsigned va[2], vw[2];
#pragma unroll
    for (int p = 0; p < 2; ++p) {
        va[p] = (lane >> 3) * 1024 + (((lane & 7) ^ (4 * p + (lane >> 4))) * 16);
        vw[p] = ((lane >> 2) & 7) * 8192 + (4 * (lane >> 5) + ((lane & 3) ^ ((2 * p + ((lane >> 4) & 1)) & 3))) * 16;
    }
    auto issueKV = [&](int kt, int i) {
        const unsigned st = lds0 + (kt & 1) * 65536;
        if (i < 4) dma1(st + (4 * wid + i) * 1024, va[i & 1], srdC, (unsigned)((256 * j + 8 * (4 * wid + i)) * 1024 + kt * 128));
        else { const int pw = 4 * (wid & 3) + (i - 4), rg = pw >> 1;
            dma1(st + 32768 + (wid >> 2) * 16384 + pw * 1024, vw[(i - 4) >> 1], srdW, (unsigned)((kt * 64 + 8 * rg) * 8192 + h * 256 + (pw & 1) * 128)); }
    };
#pragma unroll
    for (int i = 0; i < 8; ++i) issueKV(0, i);
#pragma unroll
    for (int i = 0; i < 8; ++i) issueKV(1, i);
    const int q4 = (lane & 15) >> 2, p4 = lane & 3, blk = (lane >> 4) & 1;
    {
        GAddr g;
#pragma unroll
        for (int s = 0; s < 4; ++s) g.a[s] = lds0 + (128 * wr + r32) * 128 + (((2 * s + hi) ^ ((r32 >> 1) & 7)) << 4);
#pragma unroll
        for (int t = 0; t < 2; ++t) g.w[t] = lds0 + 32768 + (wc >> 1) * 16384 + 2048 * hi + 1024 * (wc & 1) + 64 * (4 * t + q4) + 16 * ((2 * blk + (p4 >> 1)) ^ ((2 * hi + t) & 3)) + 8 * (p4 & 1);
        f32x16 acc[4][2];
#pragma unroll
        for (int a = 0; a < 4; ++a) { acc[a][0] = f32x16{}; acc[a][1] = f32x16{}; }
        if (wc < 2) {
            gemm_loop<4, 2, false, 8, 8>(acc, g, 65536u, issueKV, stamp);
            const srd_t srd = mk_srd((const char*)Kf + (size_t)h * 524288, 524288);
            const unsigned vo = hi * 1024 + r32 * 16 + (4 * j + 2 * wr) * 16384 + (wc & 1) * 8192;
#pragma unroll
            for (int mb = 0; mb < 4; ++mb)
#pragma unroll
                for (int nb = 0; nb < 2; ++nb)
#pragma unroll
                    for (int s = 0; s < 2; ++s) { u32x4 w;
#pragma unroll
                        for (int e = 0; e < 4; ++e) w[e] = cvtpk(acc[mb][nb][8 * s + 2 * e], acc[mb][nb][8 * s + 2 * e + 1]);
                        __builtin_amdgcn_raw_buffer_store_b128(w, srd, vo + (mb >> 1) * 16384 + (mb & 1) * 512 + (4 * nb + 2 * s) * 1024, 0, 0); }
        } else {
            gemm_loop<4, 2, true, 8, 8>(acc, g, 65536u, issueKV, stamp);
            const srd_t srd = mk_srd((const char*)Vf + (size_t)h * 524288, 524288);
            const unsigned vo = hi * 2048 + r32 * 16 + (4 * j + 2 * wr) * 16384 + (wc & 1) * 1024;
#pragma unroll
            for (int mb = 0; mb < 4; ++mb)
#pragma unroll
                for (int nb = 0; nb < 2; ++nb)
#pragma unroll
                    for (int s = 0; s < 2; ++s) { u32x4 w;
#pragma unroll
                        for (int e = 0; e < 4; ++e) w[e] = cvtpk(acc[mb][nb][8 * s + 2 * e], acc[mb][nb][8 * s + 2 * e + 1]);
                        __builtin_amdgcn_raw_buffer_store_b128(w, srd, vo + (mb >> 1) * 16384 + (2 * (mb & 1) + s) * 4096 + nb * 512, 0, 0); }
        }
    }
    stamp();
    asm volatile("s_waitcnt lgkmcnt(0)\n\ts_barrier" ::: "memory");
    auto issueQ = [&](int kt, int i) {
        const unsigned st = lds0 + (kt & 1) * 65536;
        if (i < 4) dma1(st + (4 * wid + i) * 1024, va[i & 1], srdX, (unsigned)((256 * j + 8 * (4 * wid + i)) * 1024 + kt * 128));
        else { const int pw = 2 * wid + (i - 4), rg = pw >> 1;
            dma1(st + 32768 + pw * 1024, vw[wid & 1], srdWq, (unsigned)((kt * 64 + 8 * rg) * 8192 + h * 256 + (pw & 1) * 128)); }
    };
#pragma unroll
    for (int i = 0; i < 6; ++i) issueQ(0, i);
#pragma unroll
    for (int i = 0; i < 6; ++i) issueQ(1, i);
    {
        GAddr g;
#pragma unroll
        for (int s = 0; s < 4; ++s) g.a[s] = lds0 + (32 * wid + r32) * 128 + (((2 * s + hi) ^ ((r32 >> 1) & 7)) << 4);
#pragma unroll
        for (int t = 0; t < 2; ++t) g.w[t] = lds0 + 32768 + 2048 * hi + 64 * (4 * t + q4) + 16 * ((2 * blk + (p4 >> 1)) ^ ((2 * hi + t) & 3)) + 8 * (p4 & 1);
        f32x16 acc[1][4];
#pragma unroll
        for (int a = 0; a < 4; ++a) acc[0][a] = f32x16{};
        gemm_loop<1, 4, false, 6, 8>(acc, g, 65536u, issueQ, stamp);
        const srd_t srd = mk_srd((const char*)Qf + (size_t)h * 524288, 524288);
        const unsigned vo = lane * 16 + (8 * j + wid) * 8192;
        constexpr float QS = 0.08838834764831845f * 1.4426950408889634f;
#pragma unroll
        for (int nb = 0; nb < 4; ++nb)
#pragma unroll
            for (int s = 0; s < 2; ++s) { u32x4 w;
#pragma unroll
                for (int e = 0; e < 4; ++e) w[e] = cvtpk(acc[0][nb][8 * s + 2 * e] * QS, acc[0][nb][8 * s + 2 * e + 1] * QS);
                __builtin_amdgcn_raw_buffer_store_b128(w, srd, vo + (2 * nb + s) * 1024, 0, 0); }
    }
    stamp();
}

constexpr int OUT_LDS = 131072;
constexpr int OUT_D = 4;
template <int N> DI void wait_vm() { asm volatile("s_waitcnt vmcnt(%0)" :: "i"(N) : "memory"); }

__global__ __launch_bounds__(512, 2) void outp_kernel(const u16* __restrict__ Cf, const u16* __restrict__ Of, const u16* __restrict__ Wo, const u16* __restrict__ Bwo, u16* __restrict__ out) {
    extern __shared__ __attribute__((aligned(16))) char lds_[];
    const int tid = threadIdx.x, lane = tid & 63, r32 = lane & 31, hi = lane >> 5;
    const int wid = rfl(tid >> 6);
    const int bid = blockIdx.x, rb = (bid & 7) * 4 + (bid >> 6), cb = (bid >> 3) & 7;
    const unsigned lds0 = (unsigned)(uintptr_t)lds_;
    const srd_t srdC = mk_srd((const char*)Cf + (size_t)rb * 2 * 262144, 2 * 262144), srdO = mk_srd((const char*)Of + (size_t)rb * 2 * 32768, 2 * 32768);
    const srd_t srdW = mk_srd(Wo, 4194304), srdB = mk_srd(Bwo, 524288);
    unsigned vw[2];
#pragma unroll
    for (int p = 0; p < 2; ++p) vw[p] = ((lane >> 2) & 7) * 1024 + (4 * (lane >> 5) + ((lane & 3) ^ ((2 * p + ((lane >> 4) & 1)) & 3))) * 16;
    const unsigned lane16 = lane * 16;
    const unsigned ring = lds0 + wid * (OUT_D * 4096);
    auto issue = [&](int n) {
        const unsigned sl = ring + (n % OUT_D) * 4096;
        if (n < 32) { const unsigned ks = 32 * wid + n;
            dma1(sl, lane16, srdC, ks * 1024); dma1(sl + 1024, lane16, srdC, 262144 + ks * 1024);
            dma1(sl + 2048, vw[0], srdW, ks * 16384 + cb * 128); dma1(sl + 3072, vw[1], srdW, ks * 16384 + 8192 + cb * 128);
        } else { const unsigned ks = 4 * wid + (n - 32);
            dma1(sl, lane16, srdO, ks * 1024); dma1(sl + 1024, lane16, srdO, 32768 + ks * 1024);
            dma1(sl + 2048, vw[0], srdB, ks * 16384 + cb * 128); dma1(sl + 3072, vw[1], srdB, ks * 16384 + 8192 + cb * 128); }
    };
#pragma unroll
    for (int n = 0; n < OUT_D; ++n) issue(n);
    const int q4 = (lane & 15) >> 2, p4 = lane & 3, blk = (lane >> 4) & 1;
    const unsigned aaddr = ring + lane16;
    unsigned waddr[2];
#pragma unroll
    for (int t = 0; t < 2; ++t) waddr[t] = ring + 2048 + 1024 * t + 64 * (4 * hi + q4) + 16 * ((2 * blk + (p4 >> 1)) ^ ((2 * t + hi) & 3)) + 8 * (p4 & 1);
    f32x16 accA[2][2], accB[2][2];
#pragma unroll
    for (int a = 0; a < 2; ++a) { accA[a][0] = f32x16{}; accA[a][1] = f32x16{}; accB[a][0] = f32x16{}; accB[a][1] = f32x16{}; }
#pragma unroll
    for (int n = 0; n < 36; ++n) {
        constexpr int NT = 36;
        const int rem = NT - 1 - n;
        if (rem >= OUT_D - 1) wait_vm<4 * (OUT_D - 1)>(); else if (rem == 2) wait_vm<8>(); else if (rem == 1) wait_vm<4>(); else wait_vm<0>();
        const int so = (n % OUT_D) * 4096;
        const bf16x8 a0 = ldsf(aaddr + so), a1 = ldsf(aaddr + so + 1024);
        const bf16x8 w0 = cat4(ldstr(waddr[0] + so), ldstr(waddr[1] + so)), w1 = cat4(ldstr(waddr[0] + so + 512), ldstr(waddr[1] + so + 512));
        asm volatile("s_waitcnt lgkmcnt(0)" ::: "memory");
        bf16x8 a0v = a0, a1v = a1, w0v = w0, w1v = w1;
        asm volatile("" : "+v"(a0v), "+v"(a1v), "+v"(w0v), "+v"(w1v));
        if (n + OUT_D < NT) issue(n + OUT_D);
        if (n < 32) { accA[0][0] = MFMA32(w0v, a0v, accA[0][0]); accA[0][1] = MFMA32(w1v, a0v, accA[0][1]); accA[1][0] = MFMA32(w0v, a1v, accA[1][0]); accA[1][1] = MFMA32(w1v, a1v, accA[1][1]); }
        else        { accB[0][0] = MFMA32(w0v, a0v, accB[0][0]); accB[0][1] = MFMA32(w1v, a0v, accB[0][1]); accB[1][0] = MFMA32(w0v, a1v, accB[1][0]); accB[1][1] = MFMA32(w1v, a1v, accB[1][1]); }
        SBAR();
    }
#pragma unroll
    for (int part = 0; part < 2; ++part) {
        asm volatile("s_waitcnt lgkmcnt(0)\n\ts_barrier" ::: "memory");
        const unsigned wb = lds0 + wid * 16384 + lane16;
#pragma unroll
        for (int mb = 0; mb < 2; ++mb)
#pragma unroll
            for (int nb = 0; nb < 2; ++nb)
#pragma unroll
                for (int rq = 0; rq < 4; ++rq) { const f32x16& x = part ? accB[mb][nb] : accA[mb][nb];
                    *(LAS f32x4*)(wb + ((mb * 2 + nb) * 4 + rq) * 1024) = (f32x4){x[4 * rq], x[4 * rq + 1], x[4 * rq + 2], x[4 * rq + 3]}; }
        asm volatile("s_waitcnt lgkmcnt(0)\n\ts_barrier" ::: "memory");
        const int mb = wid >> 2, nb = (wid >> 1) & 1, rq0 = 2 * (wid & 1);
        f32x4 s0 = {0.f, 0.f, 0.f, 0.f}, s1 = {0.f, 0.f, 0.f, 0.f};
#pragma unroll
        for (int w = 0; w < 8; ++w) { const unsigned rbse = lds0 + w * 16384 + ((mb * 2 + nb) * 4 + rq0) * 1024 + lane16;
            s0 += *(const LAS f32x4*)rbse; s1 += *(const LAS f32x4*)(rbse + 1024); }
        unsigned a0 = cvtpk(s0[0], s0[1]), a1 = cvtpk(s0[2], s0[3]), b0 = cvtpk(s1[0], s1[1]), b1 = cvtpk(s1[2], s1[3]);
        auto x0 = __builtin_amdgcn_permlane32_swap(a0, b0, false, false); auto x1 = __builtin_amdgcn_permlane32_swap(a1, b1, false, false);
        const u32x4 w = {x0[0], x1[0], x0[1], x1[1]};
        const int row = rb * 64 + 32 * mb + r32, col = part * 512 + cb * 64 + 32 * nb + 8 * (rq0 + hi);
        *(u32x4*)(out + (size_t)row * 1024 + col) = w;
    }
}

typedef __bf16 bf16x4v __attribute__((ext_vector_type(4)));
typedef unsigned u32x2 __attribute__((ext_vector_type(2)));
#define MFMA16_32(a, b, c) __builtin_amdgcn_mfma_f32_16x16x32_bf16((a), (b), (c), 0, 0, 0)
typedef short s16x4 __attribute__((ext_vector_type(4)));
DI f32x4 mfma16_16(u32x2 a, u32x2 b, f32x4 c) { return __builtin_amdgcn_mfma_f32_16x16x16bf16_1k(__builtin_bit_cast(s16x4, a), __builtin_bit_cast(s16x4, b), c, 0, 0, 0); }
DI float rbf16(float x) { return __uint_as_float(cvtpk(x, 0.f) << 16); }
template <int CTRL> DI float dpp(float x) { return __builtin_bit_cast(float, __builtin_amdgcn_update_dpp(0, __builtin_bit_cast(int, x), CTRL, 0xf, 0xf, true)); }
DI float row_max(float x) { x = fmaxf(x, dpp<0x128>(x)); x = fmaxf(x, dpp<0x124>(x)); x = fmaxf(x, dpp<0x122>(x)); x = fmaxf(x, dpp<0x121>(x)); return x; }
DI float row_min(float x) { x = fminf(x, dpp<0x128>(x)); x = fminf(x, dpp<0x124>(x)); x = fminf(x, dpp<0x122>(x)); x = fminf(x, dpp<0x121>(x)); return x; }
DI float row_sum(float x) { x += dpp<0x128>(x); x += dpp<0x124>(x); x += dpp<0x122>(x); x += dpp<0x121>(x); return x; }
DI float grp_max(float x) { x = fmaxf(x, __shfl_xor(x, 16)); return fmaxf(x, __shfl_xor(x, 32)); }
DI float grp_min(float x) { x = fminf(x, __shfl_xor(x, 16)); return fminf(x, __shfl_xor(x, 32)); }
DI float grp_sum(float x) { x += __shfl_xor(x, 16); return x + __shfl_xor(x, 32); }
constexpr float LOG2E = 1.4426950408889634f;
constexpr int B1_LDS = 49152 + 8 * 2304;

DI void b1_body(const u16* __restrict__ Bx, const u16* __restrict__ Wqkv, const u16* __restrict__ Wphi, u16* __restrict__ PhiQ, float* __restrict__ Bst, char* lds_, int h, int j) {
    const int tid = threadIdx.x, lane = tid & 63, l16 = lane & 15, g = lane >> 4;
    const int wid = rfl(tid >> 6);
    const unsigned lds0 = (unsigned)(uintptr_t)lds_;
    const srd_t srdW = mk_srd(Wqkv, 512 * 1536 * 2), srdX = mk_srd(Bx, 2097152);
    const unsigned vw = (lane >> 1) * 3072 + (lane & 1) * 16;
#pragma unroll
    for (int i = 0; i < 6; ++i) { const int p = wid * 6 + i, m = p >> 4, rg = p & 15; dma1(lds0 + p * 1024, vw, srdW, (unsigned)(rg * 32 * 3072 + m * 1024 + h * 32)); }
    bf16x8 xf[2][16];
    const unsigned vx = l16 * 1024 + g * 16;
#pragma unroll
    for (int rbk = 0; rbk < 2; ++rbk)
#pragma unroll
        for (int ks = 0; ks < 16; ++ks) xf[rbk][ks] = __builtin_bit_cast(bf16x8, __builtin_amdgcn_raw_buffer_load_b128(srdX, vx + ks * 64, (256 * j + 32 * wid + 16 * rbk) * 1024, 0));
    u32x2 wp; { const u16* w = Wphi + h * 256; wp[0] = (unsigned)w[(4 * g) * 16 + l16] | ((unsigned)w[(4 * g + 1) * 16 + l16] << 16); wp[1] = (unsigned)w[(4 * g + 2) * 16 + l16] | ((unsigned)w[(4 * g + 3) * 16 + l16] << 16); }
    asm volatile("s_waitcnt vmcnt(0)\n\ts_barrier" ::: "memory");
    const int q4 = l16 >> 2, p4 = lane & 3;
    const unsigned wa = lds0 + (8 * g + q4) * 32 + p4 * 8;
    f32x4 aq[2], ak[2], av[2];
#pragma unroll
    for (int r = 0; r < 2; ++r) { aq[r] = f32x4{}; ak[r] = f32x4{}; av[r] = f32x4{}; }
#pragma unroll
    for (int ks = 0; ks < 16; ++ks) {
        const bf16x8 wq = cat4(ldstr(wa + ks * 1024), ldstr(wa + ks * 1024 + 128));
        const bf16x8 wk = cat4(ldstr(wa + 16384 + ks * 1024), ldstr(wa + 16384 + ks * 1024 + 128));
        const bf16x8 wv = cat4(ldstr(wa + 32768 + ks * 1024), ldstr(wa + 32768 + ks * 1024 + 128));
#pragma unroll
        for (int r = 0; r < 2; ++r) { aq[r] = MFMA16_32(wq, xf[r][ks], aq[r]); ak[r] = MFMA16_32(wk, xf[r][ks], ak[r]); av[r] = MFMA16_32(xf[r][ks], wv, av[r]); }
    }
    f32x4 sp = f32x4{}, sn = f32x4{}; float zp = 0.f, zn = 0.f;
#pragma unroll
    for (int r = 0; r < 2; ++r) {
        { const u32x2 qb = {cvtpk(aq[r][0], aq[r][1]), cvtpk(aq[r][2], aq[r][3])};
          f32x4 pq = mfma16_16(wp, qb, f32x4{});
          float x[4];
#pragma unroll
          for (int e = 0; e < 4; ++e) x[e] = rbf16(pq[e]);
          const float mx = grp_max(fmaxf(fmaxf(x[0], x[1]), fmaxf(x[2], x[3]))), mn = grp_min(fminf(fminf(x[0], x[1]), fminf(x[2], x[3])));
          float ep[4], en[4];
#pragma unroll
          for (int e = 0; e < 4; ++e) { ep[e] = __builtin_amdgcn_exp2f((x[e] - mx) * LOG2E); en[e] = __builtin_amdgcn_exp2f((mn - x[e]) * LOG2E); }
          const float ip = __builtin_amdgcn_rcpf(grp_sum((ep[0] + ep[1]) + (ep[2] + ep[3]))), in = __builtin_amdgcn_rcpf(grp_sum((en[0] + en[1]) + (en[2] + en[3])));
          const u32x4 w = {cvtpk(ep[0] * ip, ep[1] * ip), cvtpk(ep[2] * ip, ep[3] * ip), cvtpk(en[0] * in, en[1] * in), cvtpk(en[2] * in, en[3] * in)};
          *(u32x4*)((char*)PhiQ + ((size_t)(h * 128 + 16 * j + 2 * wid + r) * 64 + lane) * 16) = w; }
        { const u32x2 kb = {cvtpk(ak[r][0], ak[r][1]), cvtpk(ak[r][2], ak[r][3])};
          f32x4 pk = mfma16_16(kb, wp, f32x4{});
          float ep[4], en[4];
#pragma unroll
          for (int e = 0; e < 4; ++e) { const float x = rbf16(pk[e]); const float mx = row_max(x), mn = row_min(x);
              const float a = __builtin_amdgcn_exp2f((x - mx) * LOG2E), b = __builtin_amdgcn_exp2f((mn - x) * LOG2E);
              ep[e] = a * __builtin_amdgcn_rcpf(row_sum(a)); en[e] = b * __builtin_amdgcn_rcpf(row_sum(b)); }
          const u32x2 pp = {cvtpk(ep[0], ep[1]), cvtpk(ep[2], ep[3])}, pn = {cvtpk(en[0], en[1]), cvtpk(en[2], en[3])};
          const u32x2 vb = {cvtpk(av[r][0], av[r][1]), cvtpk(av[r][2], av[r][3])};
          sp = mfma16_16(pp, vb, sp); sn = mfma16_16(pn, vb, sn);
          zp += (__uint_as_float(pp[0] << 16) + __uint_as_float(pp[0] & 0xffff0000u)) + (__uint_as_float(pp[1] << 16) + __uint_as_float(pp[1] & 0xffff0000u));
          zn += (__uint_as_float(pn[0] << 16) + __uint_as_float(pn[0] & 0xffff0000u)) + (__uint_as_float(pn[1] << 16) + __uint_as_float(pn[1] & 0xffff0000u)); }
    }
    zp = grp_sum(zp); zn = grp_sum(zn);
    LAS char* red = (LAS char*)(lds0 + 49152 + wid * 2304);
    *(LAS f32x4*)(red + lane * 32) = sp; *(LAS f32x4*)(red + lane * 32 + 16) = sn;
    if (lane < 16) *(LAS f32x2*)(red + 2048 + lane * 8) = (f32x2){zp, zn};
    asm volatile("s_waitcnt lgkmcnt(0)\n\ts_barrier" ::: "memory");
    float* dst = Bst + (size_t)(h * 8 + j) * 640;
    for (int i = tid; i < 576; i += 512) { float s = 0.f;
#pragma unroll
        for (int w = 0; w < 8; ++w) s += *(const LAS float*)(lds0 + 49152 + w * 2304 + i * 4);
        dst[i] = s; }
}
DI void b2_body(const u16* __restrict__ PhiQ, const float* __restrict__ Bst, u16* __restrict__ Of, int h, int j) {
    const int tid = threadIdx.x, lane = tid & 63, l16 = lane & 15, g = lane >> 4;
    const int wid = rfl(tid >> 6);
    f32x4 sp = f32x4{}, sn = f32x4{}, zp4 = f32x4{}, zn4 = f32x4{};
#pragma unroll
    for (int jj = 0; jj < 8; ++jj) { const float* p = Bst + (size_t)(h * 8 + jj) * 640;
        sp += *(const f32x4*)(p + lane * 8); sn += *(const f32x4*)(p + lane * 8 + 4);
#pragma unroll
        for (int e = 0; e < 4; ++e) { zp4[e] += p[512 + (4 * g + e) * 2]; zn4[e] += p[512 + (4 * g + e) * 2 + 1]; } }
    const u32x4 kvw = {cvtpk(sp[0], sp[1]), cvtpk(sp[2], sp[3]), cvtpk(sn[0], sn[1]), cvtpk(sn[2], sn[3])};
    const u32x4 zw = {cvtpk(zp4[0], zp4[1]), cvtpk(zp4[2], zp4[3]), cvtpk(zn4[0], zn4[1]), cvtpk(zn4[2], zn4[3])};
#pragma unroll
    for (int r = 0; r < 2; ++r) { const int rb16 = 16 * j + 2 * wid + r;
        const bf16x8 ph = *(const bf16x8*)((const char*)PhiQ + ((size_t)(h * 128 + rb16) * 64 + lane) * 16);
        const f32x4 num = MFMA16_32(__builtin_bit_cast(bf16x8, kvw), ph, f32x4{});
        const f32x4 den = MFMA16_32(__builtin_bit_cast(bf16x8, zw), ph, f32x4{});
        const float dn = rbf16(rbf16(den[0]) + 1e-6f);
        const u32x2 w = {cvtpk(rbf16(num[0]) / dn, rbf16(num[1]) / dn), cvtpk(rbf16(num[2]) / dn, rbf16(num[3]) / dn)};
        *(u32x2*)((char*)Of + ((size_t)((rb16 >> 1) * 32 + h) * 2 + (g & 1)) * 512 + ((rb16 & 1) * 16 + l16) * 16 + (g >> 1) * 8) = w; }
}
__global__ __launch_bounds__(512, 2) void b1_kernel(const u16* __restrict__ Bx, const u16* __restrict__ Wqkv, const u16* __restrict__ Wphi, u16* __restrict__ PhiQ, float* __restrict__ Bst) {
    extern __shared__ __attribute__((aligned(16))) char lds_[];
    const int bid = blockIdx.x; b1_body(Bx, Wqkv, Wphi, PhiQ, Bst, lds_, (bid & 7) * 4 + (bid >> 6), (bid >> 3) & 7);
}
__global__ __launch_bounds__(512, 2) void b2_kernel(const u16* __restrict__ PhiQ, const float* __restrict__ Bst, u16* __restrict__ Of) {
    const int bid = blockIdx.x; b2_body(PhiQ, Bst, Of, (bid & 7) * 4 + (bid >> 6), (bid >> 3) & 7);
}

extern "C" void kernel_launch(void* const* d_in, const int* in_sizes, int n_in, void* d_out, int out_size, void* d_ws, size_t ws_size, hipStream_t stream) {
    const u16* Ax = (const u16*)d_in[0]; const u16* Ac = (const u16*)d_in[1]; const u16* Wq = (const u16*)d_in[2]; const u16* Wuk = (const u16*)d_in[3];
    const u16* Wuv = (const u16*)d_in[4]; const u16* Wo = (const u16*)d_in[5]; const u16* Bx = (const u16*)d_in[6]; const u16* Wqkv = (const u16*)d_in[7];
    const u16* Wphi = (const u16*)d_in[8]; const u16* Bwo = (const u16*)d_in[9];
    u16* out = (u16*)d_out; char* ws = (char*)d_ws;
    u16* PhiQ = (u16*)(ws + (64 << 20)); u16* Of = (u16*)(ws + (72 << 20)); float* Bst = (float*)(ws + (76 << 20));
    u16* Qf = (u16*)(ws + (80 << 20)); u16* Kf = (u16*)(ws + (96 << 20)); u16* Vf = (u16*)(ws + (112 << 20)); u16* Cf = (u16*)(ws + (128 << 20));
    static bool attr = false;
    if (!attr) { (void)hipFuncSetAttribute((const void*)attn_kernel, hipFuncAttributeMaxDynamicSharedMemorySize, ATT_LDS);
                 (void)hipFuncSetAttribute((const void*)proj_kernel, hipFuncAttributeMaxDynamicSharedMemorySize, PRJ_LDS);
                 (void)hipFuncSetAttribute((const void*)outp_kernel, hipFuncAttributeMaxDynamicSharedMemorySize, OUT_LDS);
                 (void)hipFuncSetAttribute((const void*)b1_kernel, hipFuncAttributeMaxDynamicSharedMemorySize, B1_LDS); attr = true; }
    proj_kernel<<<256, 512, PRJ_LDS, stream>>>(Ax, Ac, Wq, Wuk, Wuv, Qf, Kf, Vf, (unsigned*)(ws + (201 << 20)));
    b1_kernel<<<256, 512, B1_LDS, stream>>>(Bx, Wqkv, Wphi, PhiQ, Bst);
    attn_kernel<<<256, 512, ATT_LDS, stream>>>(Qf, Kf, Vf, Cf, (unsigned*)(ws + (200 << 20)));
    b2_kernel<<<256, 512, 0, stream>>>(PhiQ, Bst, Of);
    outp_kernel<<<256, 512, OUT_LDS, stream>>>(Cf, Of, Wo, Bwo, out);
}
```

```cpp
#include <hip/hip_runtime.h>
#include <stdint.h>

typedef unsigned short u16;
typedef unsigned long long u64;
typedef __bf16 bf16x8 __attribute__((ext_vector_type(8)));
typedef __bf16 bf16x4 __attribute__((ext_vector_type(4)));
typedef __bf16 bf16x2 __attribute__((ext_vector_type(2)));
typedef short s16x4 __attribute__((ext_vector_type(4)));
typedef float f32x16 __attribute__((ext_vector_type(16)));
typedef float f32x4 __attribute__((ext_vector_type(4)));
typedef float f32x2 __attribute__((ext_vector_type(2)));
typedef unsigned u32x4 __attribute__((ext_vector_type(4)));
typedef unsigned u32x2 __attribute__((ext_vector_type(2)));
typedef __amdgpu_buffer_rsrc_t srd_t;
#define LAS __attribute__((address_space(3)))
#define GAS __attribute__((address_space(1)))
#define SBAR() __builtin_amdgcn_sched_barrier(0)
#define PIN(x) asm volatile("" : "+v"(x))
#define MFMA32(a, b, c) __builtin_amdgcn_mfma_f32_32x32x16_bf16((a), (b), (c), 0, 0, 0)
#define MFMA16(a, b, c) __builtin_amdgcn_mfma_f32_16x16x32_bf16((a), (b), (c), 0, 0, 0)
#define DI __device__ __forceinline__
#pragma clang diagnostic ignored "-Wint-to-pointer-cast"

DI unsigned cvtpk(float lo, float hi) { f32x2 v = {lo, hi}; bf16x2 b = __builtin_convertvector(v, bf16x2); return __builtin_bit_cast(unsigned, b); }
DI unsigned rfl(unsigned x) { return __builtin_amdgcn_readfirstlane(x); }
DI float swap_max(float x) { auto rr = __builtin_amdgcn_permlane32_swap(__float_as_uint(x), __float_as_uint(x), false, false); return __builtin_fmaxf(__uint_as_float(rr[0]), __uint_as_float(rr[1])); }
DI float swap_add(float x) { auto rr = __builtin_amdgcn_permlane32_swap(__float_as_uint(x), __float_as_uint(x), false, false); return __uint_as_float(rr[0]) + __uint_as_float(rr[1]); }
DI srd_t mk_srd(const void* p, int bytes) { return __builtin_amdgcn_make_buffer_rsrc((void*)p, (short)0, bytes, 0x00020000); }
DI bf16x4 ldstr(unsigned a) { return __builtin_amdgcn_ds_read_tr16_b64_v4bf16((LAS bf16x4*)a); }
DI bf16x8 ldsf(unsigned a) { return *(const LAS bf16x8*)a; }
DI bf16x8 cat4(bf16x4 a, bf16x4 b) { return __builtin_shufflevector(a, b, 0, 1, 2, 3, 4, 5, 6, 7); }
#define SCALARIZE2(a, b) do {} while (0)
DI void dma1(unsigned m0v, unsigned voff, srd_t srd, unsigned soff) {
    SCALARIZE2(m0v, soff);
    asm volatile("s_mov_b32 m0, %0\n\ts_nop 0\n\tbuffer_load_dwordx4 %1, %2, %3 offen lds" :: "s"(m0v), "v"(voff), "s"(srd), "s"(soff) : "memory");
}
DI void touch(unsigned m0v, unsigned voff, srd_t srd, unsigned soff) {
    SCALARIZE2(m0v, soff);
    asm volatile("s_mov_b32 m0, %0\n\ts_nop 0\n\tbuffer_load_dword %1, %2, %3 offen lds" :: "s"(m0v), "v"(voff), "s"(srd), "s"(soff) : "memory");
}
DI void store16_sc1(u32x4 w, unsigned voff, srd_t srd, unsigned soff) {
    unsigned z = soff; SCALARIZE2(soff, z);
    asm volatile("buffer_store_dwordx4 %0, %1, %2, %3 offen sc1\n\ts_nop 1" :: "v"(w), "v"(voff), "s"(srd), "s"(soff) : "memory");
}
template <int N> DI void wait_vm() { asm volatile("s_waitcnt vmcnt(%0)" :: "i"(N) : "memory"); }
template <int N> DI void wait_bar() { asm volatile("s_waitcnt vmcnt(%0) lgkmcnt(0)\n\ts_barrier" :: "i"(N) : "memory"); }
#define BARRIER() asm volatile("s_waitcnt lgkmcnt(0)\n\ts_barrier" ::: "memory")

struct Stamper { unsigned v; int n; int lane;
    DI void operator()() {
    } };

constexpr size_t WS_FLAG = 0;
constexpr size_t WS_KF = 1 << 20, WS_VF = WS_KF + (16 << 20), WS_CF = WS_VF + (16 << 20), WS_PHIQ = WS_CF + (16 << 20), WS_OF = WS_PHIQ + (4 << 20), WS_BST = WS_OF + (2 << 20), WS_BRED = WS_BST + (3 << 20), WS_DBG = 200 << 20;
constexpr int FL_STRIDE = 16;
constexpr int FL_EPOCH = 0, FL_KV = 32, FL_BS = 32 + 256, FL_BR = 32 + 512;
typedef GAS u64 gu64;
DI u64 ld_agent(const u64* p) { return __hip_atomic_load((gu64*)p, __ATOMIC_RELAXED, __HIP_MEMORY_SCOPE_AGENT); }
DI void st_agent(u64* p, u64 v) { __hip_atomic_store((gu64*)p, v, __ATOMIC_RELAXED, __HIP_MEMORY_SCOPE_AGENT); }
DI u64 mix64(u64 z) { z += 0x9E3779B97F4A7C15ull; z = (z ^ (z >> 30)) * 0xBF58476D1CE4E5B9ull; z = (z ^ (z >> 27)) * 0x94D049BB133111EBull; return z ^ (z >> 31); }
DI f32x4 mfma16_16(u32x2 a, u32x2 b, f32x4 c) { return __builtin_amdgcn_mfma_f32_16x16x16bf16_1k(__builtin_bit_cast(s16x4, a), __builtin_bit_cast(s16x4, b), c, 0, 0, 0); }
DI float rbf16(float x) { return __uint_as_float(cvtpk(x, 0.f) << 16); }
DI float x16_max(float x) { auto rr = __builtin_amdgcn_permlane16_swap(__float_as_uint(x), __float_as_uint(x), false, false); return __builtin_fmaxf(__uint_as_float(rr[0]), __uint_as_float(rr[1])); }
DI float x16_add(float x) { auto rr = __builtin_amdgcn_permlane16_swap(__float_as_uint(x), __float_as_uint(x), false, false); return __uint_as_float(rr[0]) + __uint_as_float(rr[1]); }
DI float grp_max(float x) { return swap_max(x16_max(x)); }
DI float grp_sum(float x) { return swap_add(x16_add(x)); }
constexpr float LOG2E = 1.4426950408889634f;
constexpr unsigned B1_RED = 131072;
struct B1Ctx { srd_t srdX, srdW; unsigned vx, vw[2], sx, sw; int wid; unsigned lds0; };
DI void b1_setup(B1Ctx& c, const u16* Bx, const u16* Wqkv, unsigned lds0, int rc, int hg, int wid, int lane) {
    c.srdX = mk_srd(Bx, 2097152); c.srdW = mk_srd(Wqkv, 512 * 1536 * 2); c.wid = wid; c.lds0 = lds0;
    c.vx = (lane >> 3) * 1024 + (((lane & 7) ^ ((4 * wid + (lane >> 4)) & 7)) * 16);
#pragma unroll
    for (int p = 0; p < 2; ++p) c.vw[p] = (lane >> 3) * 3072 + (((lane & 7) ^ (2 * (((lane >> 4) & 1) | (p << 1)))) * 16);
    c.sx = (64 * rc + 8 * wid) * 1024; c.sw = hg * 128;
}
DI void b1_issue(const B1Ctx& c, int kt) {
    const unsigned st = c.lds0 + (kt & 3) * 32768;
    dma1(st + c.wid * 1024, c.vx, c.srdX, c.sx + kt * 128);
#pragma unroll
    for (int i = 0; i < 3; ++i) { const int pw = 3 * c.wid + i, m = pw >> 3, pr = pw & 7;
        dma1(st + 8192 + pw * 1024, (pr & 1) ? c.vw[1] : c.vw[0], c.srdW, c.sw + (kt * 64 + 8 * pr) * 3072 + m * 1024); }
}
struct B1Acc { f32x4 aq[2], ak[2], av[2]; };
struct B1Fr { bf16x8 x[2][2]; bf16x4 w[2][3][2]; };
template <int KT> DI void b1_read(B1Fr& f, unsigned xa0, unsigned xa1, unsigned wa) {
    constexpr unsigned st = (KT & 3) * 32768;
#pragma unroll
    for (int ks = 0; ks < 2; ++ks) { const unsigned xa = ks ? xa1 : xa0;
        f.x[ks][0] = ldsf(xa + st); f.x[ks][1] = ldsf(xa + st + 2048);
#pragma unroll
        for (int m = 0; m < 3; ++m) { f.w[ks][m][0] = ldstr(wa + st + m * 8192 + ks * 4096); f.w[ks][m][1] = ldstr(wa + st + m * 8192 + ks * 4096 + 512); } }
}
DI void b1_mma(B1Acc& a, const B1Fr& f) {
#pragma unroll
    for (int ks = 0; ks < 2; ++ks) {
        const bf16x8 wq = cat4(f.w[ks][0][0], f.w[ks][0][1]), wk = cat4(f.w[ks][1][0], f.w[ks][1][1]), wv = cat4(f.w[ks][2][0], f.w[ks][2][1]);
        a.aq[0] = MFMA16(wq, f.x[ks][0], a.aq[0]); a.aq[1] = MFMA16(wq, f.x[ks][1], a.aq[1]); a.ak[0] = MFMA16(wk, f.x[ks][0], a.ak[0]); a.ak[1] = MFMA16(wk, f.x[ks][1], a.ak[1]);
        a.av[0] = MFMA16(f.x[ks][0], wv, a.av[0]); a.av[1] = MFMA16(f.x[ks][1], wv, a.av[1]);
    }
}
template <class St, class H6, class HE>
DI void b1_gemm(const B1Ctx& c, B1Acc& a, int lane, St store, H6 afterB6, HE afterEnd, Stamper& stamp) {
    const int l16 = lane & 15, g = lane >> 4, q4 = l16 >> 2, p4 = lane & 3, wid = c.wid;
    const int row = 32 * (wid >> 2) + l16;
    const unsigned xa0 = c.lds0 + row * 128 + (((0 + g) ^ ((row >> 1) & 7)) << 4), xa1 = c.lds0 + row * 128 + (((4 + g) ^ ((row >> 1) & 7)) << 4);
    const unsigned wa = c.lds0 + 8192 + (8 * g + q4) * 128 + (((wid & 3) ^ (((q4 >> 1) & 1) | ((g & 1) << 1))) * 32) + p4 * 8;
#pragma unroll
    for (int r = 0; r < 2; ++r) { a.aq[r] = f32x4{}; a.ak[r] = f32x4{}; a.av[r] = f32x4{}; }
    B1Fr f;
#define B1T(KT, PRE) do { stamp(); b1_read<KT>(f, xa0, xa1, wa); SBAR(); PRE; store(KT); SBAR(); b1_mma(a, f); SBAR(); stamp(); } while (0)
    B1T(0, (void)0);
    wait_bar<9>();  B1T(1, b1_issue(c, 4));
    wait_bar<10>(); B1T(2, b1_issue(c, 5));
    wait_bar<11>(); B1T(3, b1_issue(c, 6));
    wait_bar<11>(); B1T(4, b1_issue(c, 7));
    wait_bar<11>(); B1T(5, (void)0);
    wait_bar<7>();  B1T(6, afterB6());
    wait_bar<9>();  B1T(7, (void)0);
#undef B1T
    BARRIER(); afterEnd();
}
template <int N> DI void phi_map(const f32x4 (&p)[N], u32x4 (&out)[N]) {
    float x[N][4], mx[N], nm[N], ep[N][4], en[N][4], sp[N], sn[N];
#pragma unroll
    for (int i = 0; i < N; ++i) {
#pragma unroll
        for (int e = 0; e < 4; ++e) x[i][e] = rbf16(p[i][e]) * LOG2E;
        mx[i] = __builtin_fmaxf(__builtin_fmaxf(x[i][0], x[i][1]), __builtin_fmaxf(x[i][2], x[i][3])); nm[i] = -__builtin_fminf(__builtin_fminf(x[i][0], x[i][1]), __builtin_fminf(x[i][2], x[i][3])); }
#pragma unroll
    for (int i = 0; i < N; ++i) { mx[i] = x16_max(mx[i]); nm[i] = x16_max(nm[i]); }
#pragma unroll
    for (int i = 0; i < N; ++i) { mx[i] = swap_max(mx[i]); nm[i] = swap_max(nm[i]); }
#pragma unroll
    for (int i = 0; i < N; ++i) {
#pragma unroll
        for (int e = 0; e < 4; ++e) { ep[i][e] = __builtin_amdgcn_exp2f(x[i][e] - mx[i]); en[i][e] = __builtin_amdgcn_exp2f(-x[i][e] - nm[i]); }
        sp[i] = (ep[i][0] + ep[i][1]) + (ep[i][2] + ep[i][3]); sn[i] = (en[i][0] + en[i][1]) + (en[i][2] + en[i][3]); }
#pragma unroll
    for (int i = 0; i < N; ++i) { sp[i] = x16_add(sp[i]); sn[i] = x16_add(sn[i]); }
#pragma unroll
    for (int i = 0; i < N; ++i) { sp[i] = __builtin_amdgcn_rcpf(swap_add(sp[i])); sn[i] = __builtin_amdgcn_rcpf(swap_add(sn[i])); }
#pragma unroll
    for (int i = 0; i < N; ++i) out[i] = (u32x4){cvtpk(ep[i][0] * sp[i], ep[i][1] * sp[i]), cvtpk(ep[i][2] * sp[i], ep[i][3] * sp[i]), cvtpk(en[i][0] * sn[i], en[i][1] * sn[i]), cvtpk(en[i][2] * sn[i], en[i][3] * sn[i])};
}
DI void b1_phi(const B1Acc& a, u32x2 wp, u16* PhiQ, float* Bst, unsigned lds0, int rc, int hg, int wid, int lane, Stamper& stamp) {
    const int h = 4 * hg + (wid & 3), row0 = 64 * rc + 32 * (wid >> 2);
    const int l16 = lane & 15, g = lane >> 4;
    f32x4 sp = f32x4{}, sn = f32x4{}; float zp = 0.f, zn = 0.f;
    f32x4 pin[4]; u32x4 pout[4];
#pragma unroll
    for (int r = 0; r < 2; ++r) {
        const u32x2 qb = {cvtpk(a.aq[r][0], a.aq[r][1]), cvtpk(a.aq[r][2], a.aq[r][3])}, kb = {cvtpk(a.ak[r][0], a.ak[r][1]), cvtpk(a.ak[r][2], a.ak[r][3])};
        pin[r] = mfma16_16(wp, qb, f32x4{}); pin[2 + r] = mfma16_16(wp, kb, f32x4{});
    }
    phi_map<4>(pin, pout);
    const u32x4 wq[2] = {pout[0], pout[1]}, wk[2] = {pout[2], pout[3]};
    const unsigned tb = lds0 + B1_RED + wid * 2304;
#pragma unroll
    for (int r = 0; r < 2; ++r) {
        *(LAS u32x2*)(tb + r * 1024 + l16 * 32 + g * 8) = (u32x2){wk[r][0], wk[r][1]};
        *(LAS u32x2*)(tb + r * 1024 + 512 + l16 * 32 + g * 8) = (u32x2){wk[r][2], wk[r][3]};
        __builtin_amdgcn_raw_buffer_store_b128(wq[r], mk_srd(PhiQ, 4194304), ((h * 128 + (row0 >> 4) + r) * 64 + lane) * 16, 0, 0);
    }
    stamp();
    asm volatile("s_waitcnt lgkmcnt(0)" ::: "memory");
#pragma unroll
    for (int r = 0; r < 2; ++r) {
        const unsigned ta = tb + r * 1024 + (4 * g + (l16 >> 2)) * 32 + (lane & 3) * 8;
        const u32x2 pp = __builtin_bit_cast(u32x2, ldstr(ta)), pn = __builtin_bit_cast(u32x2, ldstr(ta + 512));
        const u32x2 vb = {cvtpk(a.av[r][0], a.av[r][1]), cvtpk(a.av[r][2], a.av[r][3])};
        sp = mfma16_16(pp, vb, sp); sn = mfma16_16(pn, vb, sn);
        zp += (__uint_as_float(pp[0] << 16) + __uint_as_float(pp[0] & 0xffff0000u)) + (__uint_as_float(pp[1] << 16) + __uint_as_float(pp[1] & 0xffff0000u));
        zn += (__uint_as_float(pn[0] << 16) + __uint_as_float(pn[0] & 0xffff0000u)) + (__uint_as_float(pn[1] << 16) + __uint_as_float(pn[1] & 0xffff0000u));
    }
    asm volatile("s_waitcnt lgkmcnt(0)" ::: "memory");
    zp = grp_sum(zp); zn = grp_sum(zn);
    LAS char* red = (LAS char*)(lds0 + B1_RED + wid * 2304);
    *(LAS f32x4*)(red + lane * 32) = sp; *(LAS f32x4*)(red + lane * 32 + 16) = sn;
    if (lane < 16) *(LAS f32x2*)(red + 2048 + lane * 8) = (f32x2){zp, zn};
    stamp();
    BARRIER();
    stamp();
    const int tid = wid * 64 + lane;
    const srd_t srd = mk_srd(Bst, 32 * 32 * 2304);
#pragma unroll
    for (int rep = 0; rep < 2; ++rep) { const int i = tid + rep * 512, ic = i < 576 ? i : 0, hh = ic / 144, ch = ic - hh * 144;
        const f32x4 s = *(const LAS f32x4*)(lds0 + B1_RED + hh * 2304 + ch * 16) + *(const LAS f32x4*)(lds0 + B1_RED + (hh + 4) * 2304 + ch * 16);
        store16_sc1(__builtin_bit_cast(u32x4, s), i < 576 ? ((4 * hg + hh) * 32 + rc) * 2304 + ch * 16 : 0x7fffffffu, srd, 0u); }
}
template <int N> DI void waitN(const u64* flags, u64 tok, int lane) {
    const u64* p = flags + (lane & (N - 1)) * FL_STRIDE;
    for (unsigned spins = 0; spins < (1u << 15); ++spins) { const u64 v = ld_agent(p); if (__all(v == tok)) break; __builtin_amdgcn_s_sleep(1); }
    asm volatile("" ::: "memory");
}
DI void b2a_body(const float* Bst, float* Bred, int rc, int hg, int lane) {
    const int h = 4 * hg + (rc >> 3), c = lane & 31, half = lane >> 5;
    const srd_t srd = mk_srd(Bst + (size_t)h * 32 * 576, 32 * 2304);
    const unsigned vo = (c < 18 ? (18 * (rc & 7) + c) * 16 : 0x7fffffff) + half * 16 * 2304;
    f32x4 v[16];
#pragma unroll
    for (int i = 0; i < 16; ++i) v[i] = __builtin_bit_cast(f32x4, __builtin_amdgcn_raw_buffer_load_b128(srd, vo, i * 2304, 16));
    f32x4 s = v[0];
#pragma unroll
    for (int i = 1; i < 16; ++i) s += v[i];
#pragma unroll
    for (int e = 0; e < 4; ++e) s[e] = swap_add(s[e]);
    const srd_t srdR = mk_srd(Bred + (size_t)h * 576, 2304);
    if (half == 0 && c < 18) __builtin_amdgcn_raw_buffer_store_b128(__builtin_bit_cast(u32x4, s), srdR, (18 * (rc & 7) + c) * 16, 0, 16);
}
template <int NRB>
DI void b2b_body(const u16* PhiQ, const float* Bred, u16* Of, const u64* flagsR, u64 tok, int h, int rb0, int lane) {
    const int l16 = lane & 15, g = lane >> 4;
    bf16x8 ph[NRB];
#pragma unroll
    for (int r = 0; r < NRB; ++r) ph[r] = *(const bf16x8*)((const char*)PhiQ + ((size_t)(h * 128 + rb0 + r) * 64 + lane) * 16);
    waitN<8>(flagsR, tok, lane);
    const srd_t srd = mk_srd(Bred + (size_t)h * 576, 2304);
    const f32x4 sp = __builtin_bit_cast(f32x4, __builtin_amdgcn_raw_buffer_load_b128(srd, lane * 32, 0, 16)), sn = __builtin_bit_cast(f32x4, __builtin_amdgcn_raw_buffer_load_b128(srd, lane * 32 + 16, 0, 16));
    const f32x4 za = __builtin_bit_cast(f32x4, __builtin_amdgcn_raw_buffer_load_b128(srd, 2048 + g * 32, 0, 16)), zb = __builtin_bit_cast(f32x4, __builtin_amdgcn_raw_buffer_load_b128(srd, 2048 + g * 32 + 16, 0, 16));
    const u32x4 kvw = {cvtpk(sp[0], sp[1]), cvtpk(sp[2], sp[3]), cvtpk(sn[0], sn[1]), cvtpk(sn[2], sn[3])};
    const u32x4 zw = {cvtpk(za[0], za[2]), cvtpk(zb[0], zb[2]), cvtpk(za[1], za[3]), cvtpk(zb[1], zb[3])};
    const srd_t srdO = mk_srd(Of, 2097152);
#pragma unroll
    for (int r = 0; r < NRB; ++r) { const int rb16 = rb0 + r;
        const f32x4 num = MFMA16(__builtin_bit_cast(bf16x8, kvw), ph[r], f32x4{});
        const f32x4 den = MFMA16(__builtin_bit_cast(bf16x8, zw), ph[r], f32x4{});
        const float dn = rbf16(rbf16(den[0]) + 1e-6f);
        const u32x2 w = {cvtpk(rbf16(num[0]) / dn, rbf16(num[1]) / dn), cvtpk(rbf16(num[2]) / dn, rbf16(num[3]) / dn)};
        __builtin_amdgcn_raw_buffer_store_b64(w, srdO, (((rb16 >> 1) * 32 + h) * 2 + (g & 1)) * 512 + ((rb16 & 1) * 16 + l16) * 16 + (g >> 1) * 8, 0, 0); }
}
template <int MB, int NB> struct Frags { bf16x8 a[MB]; bf16x4 w0[NB], w1[NB]; };
struct GAddr { unsigned a[4]; unsigned w[2]; };

template <int MB, int NB, int S> DI void frag_read(Frags<MB, NB>& f, const GAddr& g, int i) {
    if (i == 2) { f.a[0] = ldsf(g.a[S]); return; }
    const int k = i < 2 ? i : i - 1;
    if (k < 2 * NB) { const int nb = k >> 1; if (k & 1) f.w1[nb] = ldstr(g.w[1] + S * 4096 + nb * 512); else f.w0[nb] = ldstr(g.w[0] + S * 4096 + nb * 512); }
    else { const int mb = k - 2 * NB + 1; if (mb < MB) f.a[mb] = ldsf(g.a[S] + mb * 4096); }
}
template <int MB, int NB, bool ACT_A, int SN, int RPG, class Hook>
DI void kstep(f32x16 (&acc)[MB][NB], const Frags<MB, NB>& cur, Frags<MB, NB>& nxt, const GAddr& g, Hook hook) {
    constexpr int NM = MB * NB, NR = MB + 2 * NB;
#pragma unroll
    for (int i = 0; i < NM; ++i) {
        const int mb = i / NB, nb = i % NB;
        const bf16x8 w = cat4(cur.w0[nb], cur.w1[nb]);
        acc[mb][nb] = ACT_A ? MFMA32(cur.a[mb], w, acc[mb][nb]) : MFMA32(w, cur.a[mb], acc[mb][nb]);
        SBAR();
        if constexpr (SN >= 0) {
#pragma unroll
            for (int r = 0; r < RPG; ++r) if (i * RPG + r < NR) frag_read<MB, NB, SN < 0 ? 0 : SN>(nxt, g, i * RPG + r);
        }
        hook(i);
        SBAR();
    }
}
struct NoHook { DI void operator()(int) const {} };

struct ProjCtx {
    srd_t srdC, srdX, srdW, srdWq, srdO;
    unsigned va[2], vw[2], vwq, vo, lds0, l128;
    unsigned sA, sW, sWq, sXa;
    int wid, isV;
};
constexpr unsigned TOUCH_LDS = 163840 - 256;
DI void issueKV(const ProjCtx& c, int kt, int i) {
    const unsigned st = c.lds0 + (kt & 1) * 65536;
    if (i < 4) dma1(st + (4 * c.wid + i) * 1024, c.va[i & 1], c.srdC, c.sA + i * 8192 + kt * 128);
    else dma1(st + 32768 + (c.wid >> 2) * 16384 + (4 * (c.wid & 3) + (i - 4)) * 1024, c.vw[(i - 4) >> 1], c.srdW, c.sW + ((i - 4) >> 1) * 65536 + ((i - 4) & 1) * 128 + kt * 524288);
}
DI void issueQ(const ProjCtx& c, int kt, int i) {
    const unsigned st = c.lds0 + (kt % 3) * 49152;
    if (i < 4) dma1(st + (4 * c.wid + i) * 1024, c.va[i & 1], c.srdX, c.sXa + i * 8192 + kt * 128);
    else dma1(st + 32768 + (2 * c.wid + (i - 4)) * 1024, c.vwq, c.srdWq, c.sWq + (i - 4) * 128 + kt * 524288);
}

#define TOUCHMODE 3
template <int WHICH> DI void touch_g(unsigned m0v, unsigned voff, const void* base) {
    if (!(TOUCHMODE & WHICH)) voff = 0;
    const u64 b = (u64)base; const u64 sb = ((u64)rfl((unsigned)(b >> 32)) << 32) | rfl((unsigned)b);
    asm volatile("s_nop 4\n\ts_mov_b32 m0, %0\n\ts_nop 0\n\tglobal_load_lds_dword %1, %2" :: "s"(rfl(m0v)), "v"(voff), "s"(sb) : "memory");
}
template <bool ISV, class Touch, class IssueB1>
DI void kv_part(const ProjCtx& c, GAddr g, u32x4 (&pk)[16], Touch touchk, IssueB1 issueB1, Stamper& stamp) {
    f32x16 acc[4][2];
#pragma unroll
    for (int a = 0; a < 4; ++a) { acc[a][0] = f32x16{}; acc[a][1] = f32x16{}; }
    Frags<4, 2> f0, f1;
    wait_bar<9>();
    stamp();
#pragma unroll
    for (int i = 0; i < 8; ++i) frag_read<4, 2, 0>(f0, g, i);
    for (int kt = 0; kt < 6; ++kt) {
        kstep<4, 2, ISV, 1, 2>(acc, f0, f1, g, [&](int i) { if (kt > 0 && (i & 1)) issueKV(c, kt + 1, 4 + (i >> 1)); });
        kstep<4, 2, ISV, 2, 2>(acc, f1, f0, g, [&](int i) { if (i == 1) touchk(kt); });
        kstep<4, 2, ISV, 3, 2>(acc, f0, f1, g, NoHook{});
        wait_bar<1>();
#pragma unroll
        for (int i = 0; i < 4; ++i) g.a[i] ^= 65536u;
        g.w[0] ^= 65536u; g.w[1] ^= 65536u;
        kstep<4, 2, ISV, 0, 2>(acc, f1, f0, g, [&](int i) { if (i & 1) issueKV(c, kt + 2, i >> 1); });
    }
    kstep<4, 2, ISV, 1, 2>(acc, f0, f1, g, [&](int i) { if (i & 1) issueKV(c, 7, 4 + (i >> 1)); });
    kstep<4, 2, ISV, 2, 2>(acc, f1, f0, g, NoHook{});
    kstep<4, 2, ISV, 3, 2>(acc, f0, f1, g, NoHook{});
    stamp();
    wait_bar<0>();
    stamp();
#pragma unroll
    for (int i = 0; i < 4; ++i) g.a[i] ^= 65536u;
    g.w[0] ^= 65536u; g.w[1] ^= 65536u;
    kstep<4, 2, ISV, 0, 2>(acc, f1, f0, g, [&](int i) { if (i == 1) issueB1(0); if (i == 5) issueB1(1); });
    kstep<4, 2, ISV, 1, 2>(acc, f0, f1, g, NoHook{});
    kstep<4, 2, ISV, 2, 2>(acc, f1, f0, g, NoHook{});
    kstep<4, 2, ISV, 3, 2>(acc, f0, f1, g, NoHook{});
    stamp();
    wait_bar<4>();
    stamp();
    kstep<4, 2, ISV, -1, 2>(acc, f1, f0, g, [&](int i) { if (i == 1) issueB1(2); if (i == 5) issueB1(3); });
#pragma unroll
    for (int mb = 0; mb < 4; ++mb)
#pragma unroll
        for (int nb = 0; nb < 2; ++nb)
#pragma unroll
            for (int s = 0; s < 2; ++s)
#pragma unroll
                for (int e = 0; e < 4; ++e) pk[(mb * 2 + nb) * 2 + s][e] = cvtpk(acc[mb][nb][8 * s + 2 * e], acc[mb][nb][8 * s + 2 * e + 1]);
}
DI void kv_store(const ProjCtx& c, const u32x4 (&pk)[16], int n) {
    const int mb = n >> 2, nb = (n >> 1) & 1, s = n & 1;
    const unsigned offK = (mb >> 1) * 16384 + (mb & 1) * 512 + (4 * nb + 2 * s) * 1024, offV = (mb >> 1) * 16384 + (2 * (mb & 1) + s) * 4096 + nb * 512;
    store16_sc1(pk[n], c.vo, c.srdO, c.isV ? offV : offK);
}
template <int KT, class Pub>
DI void q_tile(const ProjCtx& c, GAddr& g, f32x16 (&acc)[1][4], Frags<1, 4>& f0, Frags<1, 4>& f1, const u32x4 (&pk)[16], Pub publish, Stamper& stamp) {
    kstep<1, 4, false, 1, 3>(acc, f0, f1, g, [&](int i) { if (KT >= 1 && KT + 2 < 8 && i < 3) issueQ(c, KT + 2, 3 + i); });
    kstep<1, 4, false, 2, 3>(acc, f1, f0, g, [&](int i) { if (KT < 4 && i == 1) kv_store(c, pk, 8 + 2 * KT); });
    kstep<1, 4, false, 3, 3>(acc, f0, f1, g, [&](int i) { if (KT < 4 && i == 1) kv_store(c, pk, 8 + 2 * KT + 1); });
    if constexpr (KT < 7) {
        constexpr int W = KT == 0 ? 12 : (KT <= 3 ? 10 : (KT == 4 ? 8 : (KT == 5 ? 6 : 0)));
        wait_bar<W>();
        stamp();
        publish(KT);
        constexpr int d = ((KT + 1) % 3 - KT % 3) * 49152;
#pragma unroll
        for (int i = 0; i < 4; ++i) g.a[i] += d;
        g.w[0] += d; g.w[1] += d;
        kstep<1, 4, false, 0, 3>(acc, f1, f0, g, [&](int i) { if (KT + 3 < 8 && i < 3) issueQ(c, KT + 3, i); });
    } else kstep<1, 4, false, -1, 3>(acc, f1, f0, g, NoHook{});
}

DI void proj_setup(ProjCtx& c, const u16* X, const u16* Ckv, const u16* Wq, const u16* Wuk, const u16* Wuv, u16* Kf, u16* Vf, unsigned lds0, int h, int j, int wid, int lane) {
    const int r32 = lane & 31, hi = lane >> 5, wr = wid >> 2, wc = wid & 3;
    c.wid = wid; c.isV = wc >> 1; c.lds0 = lds0;
    c.srdC = mk_srd(Ckv, 2097152); c.srdX = mk_srd(X, 2097152);
    c.srdW = mk_srd((wid < 4 ? Wuk : Wuv), 4194304); c.srdWq = mk_srd(Wq, 4194304);
    c.srdO = mk_srd((const char*)(c.isV ? Vf : Kf) + (size_t)h * 524288, 524288);
#pragma unroll
    for (int p = 0; p < 2; ++p) {
        c.va[p] = (lane >> 3) * 1024 + (((lane & 7) ^ (4 * p + (lane >> 4))) * 16);
        c.vw[p] = ((lane >> 2) & 7) * 8192 + (4 * (lane >> 5) + ((lane & 3) ^ ((2 * p + ((lane >> 4) & 1)) & 3))) * 16;
    }
    c.vwq = (wid & 1) ? c.vw[1] : c.vw[0];
    c.l128 = lane * 128;
    c.sA = (256 * j + 32 * wid) * 1024;
    c.sW = (16 * (wid & 3)) * 8192 + h * 256;
    c.sXa = (wid < 4 ? 128 * (15 - j) + 32 * wid : 128 * j + 32 * (wid - 4)) * 1024;
    c.sWq = (8 * wid) * 8192 + h * 256;
    c.vo = c.isV ? hi * 2048 + r32 * 16 + (4 * j + 2 * wr) * 16384 + (wc & 1) * 1024 : hi * 1024 + r32 * 16 + (4 * j + 2 * wr) * 16384 + (wc & 1) * 8192;
}
template <class Touch, class IssueB1>
DI void kv_run(const ProjCtx& c, int lane, u32x4 (&pk)[16], Touch touchk, IssueB1 issueB1, Stamper& stamp) {
    const int r32 = lane & 31, hi = lane >> 5, wid = c.wid, wr = wid >> 2, wc = wid & 3;
    const int q4 = (lane & 15) >> 2, p4 = lane & 3, blk = (lane >> 4) & 1;
    GAddr g;
#pragma unroll
    for (int s = 0; s < 4; ++s) g.a[s] = c.lds0 + (128 * wr + r32) * 128 + (((2 * s + hi) ^ ((r32 >> 1) & 7)) << 4);
#pragma unroll
    for (int t = 0; t < 2; ++t) g.w[t] = c.lds0 + 32768 + (wc >> 1) * 16384 + 2048 * hi + 1024 * (wc & 1) + 64 * (4 * t + q4) + 16 * ((2 * blk + (p4 >> 1)) ^ ((2 * hi + t) & 3)) + 8 * (p4 & 1);
    if (c.isV) kv_part<true>(c, g, pk, touchk, issueB1, stamp); else kv_part<false>(c, g, pk, touchk, issueB1, stamp);
}
template <class Pub>
DI void q_run(const ProjCtx& c, int lane, const u32x4 (&pk)[16], bf16x8 (&q)[8], Pub publish, Stamper& stamp) {
    const int r32 = lane & 31, hi = lane >> 5, wid = c.wid;
    const int q4 = (lane & 15) >> 2, p4 = lane & 3, blk = (lane >> 4) & 1;
    GAddr g;
#pragma unroll
    for (int s = 0; s < 4; ++s) g.a[s] = c.lds0 + (32 * wid + r32) * 128 + (((2 * s + hi) ^ ((r32 >> 1) & 7)) << 4);
#pragma unroll
    for (int t = 0; t < 2; ++t) g.w[t] = c.lds0 + 32768 + 2048 * hi + 64 * (4 * t + q4) + 16 * ((2 * blk + (p4 >> 1)) ^ ((2 * hi + t) & 3)) + 8 * (p4 & 1);
    f32x16 acc[1][4];
#pragma unroll
    for (int a = 0; a < 4; ++a) acc[0][a] = f32x16{};
    Frags<1, 4> f0, f1;
#pragma unroll
    for (int i = 0; i < 9; ++i) frag_read<1, 4, 0>(f0, g, i);
    q_tile<0>(c, g, acc, f0, f1, pk, publish, stamp); q_tile<1>(c, g, acc, f0, f1, pk, publish, stamp); q_tile<2>(c, g, acc, f0, f1, pk, publish, stamp); q_tile<3>(c, g, acc, f0, f1, pk, publish, stamp);
    q_tile<4>(c, g, acc, f0, f1, pk, publish, stamp); q_tile<5>(c, g, acc, f0, f1, pk, publish, stamp); q_tile<6>(c, g, acc, f0, f1, pk, publish, stamp); q_tile<7>(c, g, acc, f0, f1, pk, publish, stamp);
    constexpr float QSC = 0.08838834764831845f * 1.4426950408889634f;
#pragma unroll
    for (int nb = 0; nb < 4; ++nb)
#pragma unroll
        for (int s = 0; s < 2; ++s) { u32x4 w;
#pragma unroll
            for (int e = 0; e < 4; ++e) w[e] = cvtpk(acc[0][nb][8 * s + 2 * e] * QSC, acc[0][nb][8 * s + 2 * e + 1] * QSC);
            q[2 * nb + s] = __builtin_bit_cast(bf16x8, w); }
}
template <int OFF, bool SETM0> DI void dma_piece(unsigned m0v, unsigned voff, srd_t srd, unsigned soff) {
    if constexpr (SETM0) asm volatile("s_mov_b32 m0, %0\n\ts_nop 0\n\tbuffer_load_dwordx4 %1, %2, %3 offen offset:%4 sc1 lds" :: "s"(m0v), "v"(voff), "s"(srd), "s"(soff), "i"(OFF) : "memory");
    else asm volatile("buffer_load_dwordx4 %0, %1, %2 offen offset:%3 sc1 lds" :: "v"(voff), "s"(srd), "s"(soff), "i"(OFF) : "memory");
}
#define ATT_THRV 64.f
constexpr float ATT_THR = ATT_THRV;
#define ATT_WIN 6
constexpr int ATT_W = ATT_WIN;

struct AttnState { f32x16 o[4]; f32x16 s0, s1, negm; bf16x8 q[8]; u32x4 p[4]; float l, mhat; };
struct AttnCtx {
    srd_t srdK, srdV, srdC; unsigned voff, lane16, kslot0, vslot0, coffL;
    int g, nl  , thrg, dq0, relH, jump; unsigned qstash;
    unsigned soA, soB; int rel;
};

template <bool DO_QK, bool DO_PV, int KP, int VP>
DI void m_phase(AttnState& st, unsigned kbase, unsigned vbase, bf16x8 (&pre)[ATT_W]) {
    constexpr int N0 = DO_QK ? 0 : 16, N1 = DO_PV ? 32 : 16;
    bf16x8 fr[32];
#pragma unroll
    for (int i = 0; i < ATT_W; ++i) fr[N0 + i] = pre[i];
#pragma unroll
    for (int i = N0; i < N1; ++i) {
        const int n = i + ATT_W;
        if (n < N1) { if (n < 16) fr[n] = ldsf(kbase + KP * 16384 + (n >> 1) * 2048 + (n & 1) * 512); else fr[n] = ldsf(vbase + VP * 16384 + ((n - 16) >> 2) * 4096 + ((n - 16) & 3) * 512); }
        SBAR();
        if (i < 16) { const int s = i >> 1;
            if (i == 0) asm volatile("v_mfma_f32_32x32x16_bf16 %0, %2, %3, %1" : "=&v"(st.s0), "+v"(st.negm) : "v"(fr[i]), "v"(st.q[0]));
            else if (i == 1) asm volatile("v_mfma_f32_32x32x16_bf16 %0, %2, %3, %1" : "=&v"(st.s1), "+v"(st.negm) : "v"(fr[i]), "v"(st.q[0]));
            else if ((i & 1) == 0) st.s0 = MFMA32(fr[i], st.q[s], st.s0);
            else              st.s1 = MFMA32(fr[i], st.q[s], st.s1);
        } else { const int ks = (i - 16) >> 2, db = (i - 16) & 3;
            st.o[db] = MFMA32(fr[i], __builtin_bit_cast(bf16x8, st.p[ks]), st.o[db]); }
        SBAR();
    }
    PIN(st.negm);
}

DI void store_ctx(const f32x16 (&o)[4], float inv, srd_t srd, unsigned voff) {
#pragma unroll
    for (int db = 0; db < 4; ++db)
#pragma unroll
        for (int s = 0; s < 2; ++s) {
            u32x4 w;
#pragma unroll
            for (int e = 0; e < 4; ++e) w[e] = cvtpk(o[db][8 * s + 2 * e] * inv, o[db][8 * s + 2 * e + 1] * inv);
            __builtin_amdgcn_raw_buffer_store_b128(w, srd, voff + (4 * db + 2 * s) * 512, 0, 0);
        }
}

template <bool LAST, int PAR>
DI void v_phase(AttnState& st, AttnCtx& c, const int t) {
    const bool sw = (t == c.nl);
    const unsigned vm0 = c.vslot0 + (PAR ^ 1) * 16384, km0 = c.kslot0 + PAR * 16384;
    const unsigned vso = c.soA, kso = c.soB;
    SBAR();
    if (__builtin_expect(sw, 0)) { const float lt = swap_add(st.l); store_ctx(st.o, __builtin_amdgcn_rcpf(lt), c.srdC, c.lane16 + c.coffL);
#pragma unroll
        for (int d = 0; d < 4; ++d) st.o[d] = f32x16{};
        st.l = 0.f; c.rel = c.relH; }
    if (__builtin_expect(c.rel > c.thrg, 0)) {
        const int u = 32 * c.g - c.rel; const float NEG = -__builtin_inff();
        if (u == 32) {
#pragma unroll
            for (int r = 0; r < 16; ++r) if ((r & 3) + 8 * (r >> 2) > c.dq0) st.s1[r] = NEG;
        } else {
            if (u == 0) {
#pragma unroll
                for (int r = 0; r < 16; ++r) if ((r & 3) + 8 * (r >> 2) > c.dq0) st.s0[r] = NEG;
            } else {
#pragma unroll
                for (int r = 0; r < 16; ++r) st.s0[r] = NEG; }
#pragma unroll
            for (int r = 0; r < 16; ++r) st.s1[r] = NEG;
        } }
    SBAR();
    if (!LAST) dma_piece<0, true>(vm0, c.voff, c.srdV, vso);
#define MX3(a, b, c) __builtin_fmaxf(__builtin_fmaxf((a), (b)), (c))
    float a = MX3(st.s0[0], st.s0[1], st.s1[0]), b = MX3(st.s0[2], st.s0[3], st.s1[1]);
    a = MX3(a, st.s1[2], st.s1[3]);
#pragma unroll
    for (int r = 4; r < 16; r += 4) {
        a = MX3(a, st.s0[r], st.s0[r + 1]); b = MX3(b, st.s0[r + 2], st.s0[r + 3]);
        a = MX3(a, st.s1[r], st.s1[r + 1]); b = MX3(b, st.s1[r + 2], st.s1[r + 3]); }
    const float rmax = __builtin_fmaxf(a, b);
    const unsigned long long firstm = (t == 0 || sw) ? ~0ull : 0ull;
    if (__builtin_expect((__builtin_amdgcn_ballot_w64(rmax > ATT_THR) | firstm) != 0ull, 0)) {
        const bool first = firstm != 0ull;
        const float m = swap_max(rmax);
        const float delta = __builtin_fmaxf(m, first ? -1e30f : 0.f);
        st.mhat = (first ? 0.f : st.mhat) + delta;
#pragma unroll
        for (int r = 0; r < 16; ++r) { st.s0[r] -= delta; st.s1[r] -= delta; st.negm[r] = -st.mhat; }
        if (!first) { const float f = __builtin_amdgcn_exp2f(-delta); st.l *= f;
#pragma unroll
            for (int d = 0; d < 4; ++d)
#pragma unroll
                for (int r = 0; r < 16; ++r) st.o[d][r] *= f; }
    }
    SBAR();
    float e[32];
#define EXPG(k) do { _Pragma("unroll") for (int i_ = 0; i_ < 4; ++i_) e[4 * (k) + i_] = __builtin_amdgcn_exp2f((k) < 4 ? st.s0[4 * (k) + i_] : st.s1[4 * ((k) - 4) + i_]); } while (0)
    float l0 = st.l, l1 = 0.f, l2 = 0.f, l3 = 0.f;
#define FING(k) do { l0 += e[4 * (k)]; if ((k) == 0) { l1 = e[1]; l2 = e[2]; l3 = e[3]; } else { l1 += e[4 * (k) + 1]; l2 += e[4 * (k) + 2]; l3 += e[4 * (k) + 3]; } \
        unsigned w0_ = cvtpk(e[4 * (k)], e[4 * (k) + 1]), w1_ = cvtpk(e[4 * (k) + 2], e[4 * (k) + 3]); PIN(w0_); PIN(w1_); PIN(l0); PIN(l1); PIN(l2); PIN(l3); \
        st.p[(k) >> 1][2 * ((k) & 1)] = w0_; st.p[(k) >> 1][2 * ((k) & 1) + 1] = w1_; } while (0)
    EXPG(0); SBAR();
    if (!LAST) dma_piece<1024, false>(vm0, c.voff, c.srdV, vso);
    EXPG(1); FING(0); SBAR();
    if (!LAST) dma_piece<2048, false>(vm0, c.voff, c.srdV, vso);
    EXPG(2); FING(1); SBAR();
    if (!LAST) dma_piece<3072, false>(vm0, c.voff, c.srdV, vso);
    EXPG(3); FING(2); SBAR();
    if (!LAST) dma_piece<0, true>(km0, c.voff, c.srdK, kso);
    EXPG(4); FING(3); SBAR();
    if (!LAST) dma_piece<1024, false>(km0, c.voff, c.srdK, kso);
    EXPG(5); FING(4); SBAR();
    if (!LAST) dma_piece<2048, false>(km0, c.voff, c.srdK, kso);
    EXPG(6); FING(5); SBAR();
    if (!LAST) dma_piece<3072, false>(km0, c.voff, c.srdK, kso);
    EXPG(7); FING(6); SBAR();
    FING(7);
    st.l = (l0 + l1) + (l2 + l3);
#undef EXPG
#undef FING
    if (!LAST) {
        if (__builtin_expect(t == c.nl - 1, 0)) {
#pragma unroll
            for (int s = 0; s < 8; ++s) st.q[s] = ldsf(c.qstash + s * 1024);
            st.negm = f32x16{};
        }
        c.soA = c.soB; c.soB += (t + 3 == c.nl) ? c.jump : 16384; c.rel += 64;
    }
    SBAR();
}

DI void attn_setup(AttnCtx& c, const u16* Kf, const u16* Vf, u16* Cf, unsigned lds0, int h, int j, int wid, int lane) {
    const int grp = wid >> 2; c.g = wid & 3; c.dq0 = (lane & 31) - 4 * (lane >> 5);
    const int qbH = 15 - j, qbL = j, nl = 2 * j + 2;
    c.nl = grp ? nl : 1000; c.thrg = 32 * c.g - 63; c.jump = (18 - nl) * 16384; c.relH = 64 * (17 - 2 * qbH);
    c.rel = grp ? -128 * qbL : -128 * qbH;
    c.soA = 16384; c.soB = (c.nl == 2) ? 17 * 16384 : 2 * 16384;
    c.srdK = mk_srd((const char*)Kf + (size_t)h * 524288, 524288);
    c.srdV = mk_srd((const char*)Vf + (size_t)h * 524288, 524288);
    c.srdC = mk_srd((const char*)Cf, 16777216);
    c.lane16 = lane * 16; c.voff = lane * 16 + c.g * 4096;
    c.kslot0 = lds0 + grp * 65536 + c.g * 4096; c.vslot0 = c.kslot0 + 32768;
    c.coffL = ((qbL * 4 + c.g) * 32 + h) * 8192;
    c.qstash = lds0 + 131072 + c.g * 8192 + lane * 16;
}
#define DMA4(m_, vo_, srd, so_) do { dma_piece<0, true>(m_, vo_, srd, so_); dma_piece<1024, false>(m_, vo_, srd, so_); dma_piece<2048, false>(m_, vo_, srd, so_); dma_piece<3072, false>(m_, vo_, srd, so_); } while (0)
DI void attn_prime(const AttnCtx& c) { DMA4(c.kslot0, c.voff, c.srdK, 0u); DMA4(c.vslot0, c.voff, c.srdV, 0u); DMA4(c.kslot0 + 16384, c.voff, c.srdK, 16384u); }

template <class First, class G0Tail>
DI void attn_run(AttnState& st, AttnCtx& c, unsigned lds0, int h, int j, int wid, int lane, First afterFirst, G0Tail g0tail, Stamper& stamp) {
    const int grp = wid >> 2;
    const unsigned kbase = lds0 + grp * 65536 + (lane >> 5) * 1024 + (lane & 31) * 16;
    const unsigned vbase = lds0 + grp * 65536 + 32768 + (lane >> 5) * 2048 + (lane & 31) * 16;
#pragma unroll
    for (int d = 0; d < 4; ++d) st.o[d] = f32x16{};
    st.negm = f32x16{}; st.l = 0.f; st.mhat = 0.f;
    PIN(st.negm);
    wait_bar<0>();
    afterFirst();
    stamp();
    if (grp == 1) BARRIER();
    bf16x8 pre[ATT_W];
#define PRE_K(P) do { _Pragma("unroll") for (int i = 0; i < ATT_W; ++i) pre[i] = ldsf(kbase + (P) * 16384 + (i >> 1) * 2048 + (i & 1) * 512); } while (0)
    PRE_K(0);
    __builtin_amdgcn_s_setprio(1);
    m_phase<true, false, 0, 0>(st, kbase, vbase, pre);
    __builtin_amdgcn_s_setprio(0);
    BARRIER();
#define STEP(PAR, t) do { \
        v_phase<false, PAR>(st, c, t); \
        PRE_K(PAR ^ 1); \
        asm volatile("s_barrier" ::: "memory"); \
        __builtin_amdgcn_s_setprio(1); \
        m_phase<true, true, PAR ^ 1, PAR>(st, kbase, vbase, pre); \
        __builtin_amdgcn_s_setprio(0); \
        wait_bar<0>(); } while (0)
    for (int t = 0; t < 16; t += 2) { stamp(); STEP(0, t); STEP(1, t + 1); }
    stamp();
    v_phase<true, 0>(st, c, 16);
#pragma unroll
    for (int i = 0; i < ATT_W; ++i) pre[i] = ldsf(vbase + (i >> 2) * 4096 + (i & 3) * 512);
    asm volatile("s_barrier" ::: "memory");
    __builtin_amdgcn_s_setprio(1);
    m_phase<false, true, 0, 0>(st, kbase, vbase, pre);
    __builtin_amdgcn_s_setprio(0);
    wait_bar<0>();
    stamp();
    const float lt = swap_add(st.l);
    const unsigned xo = lds0 + c.g * 16384 + lane * 16;
    LAS f32x2* xs = (LAS f32x2*)(lds0 + 65536 + c.g * 512 + lane * 8);
    if (grp == 0) {
#pragma unroll
        for (int d = 0; d < 4; ++d)
#pragma unroll
            for (int rq = 0; rq < 4; ++rq) *(LAS f32x4*)(xo + (d * 4 + rq) * 1024) = (f32x4){st.o[d][4 * rq], st.o[d][4 * rq + 1], st.o[d][4 * rq + 2], st.o[d][4 * rq + 3]};
        *xs = (f32x2){st.mhat, lt};
        BARRIER();
        g0tail();
    } else {
        const f32x2 o0 = *xs;
        const float m = __builtin_fmaxf(o0[0], st.mhat);
        const float f0 = __builtin_amdgcn_exp2f(o0[0] - m), f1 = __builtin_amdgcn_exp2f(st.mhat - m);
        const float inv = __builtin_amdgcn_rcpf(o0[1] * f0 + lt * f1), a0 = f0 * inv, a1 = f1 * inv;
#pragma unroll
        for (int d = 0; d < 4; ++d)
#pragma unroll
            for (int rq = 0; rq < 4; ++rq) { const f32x4 x = *(const LAS f32x4*)(xo + (d * 4 + rq) * 1024);
#pragma unroll
                for (int e = 0; e < 4; ++e) st.o[d][4 * rq + e] = st.o[d][4 * rq + e] * a1 + x[e] * a0; }
        store_ctx(st.o, 1.0f, c.srdC, c.lane16 + (((15 - j) * 4 + c.g) * 32 + h) * 8192);
    }
    stamp();
#undef PRE_K
#undef STEP
}
constexpr int OUT_LDS = 131072;
constexpr int OUT_D = 4;

__global__ __launch_bounds__(512, 2) void outp_kernel(const u16* __restrict__ Cf, const u16* __restrict__ Of, const u16* __restrict__ Wo, const u16* __restrict__ Bwo, u16* __restrict__ out) {
    extern __shared__ __attribute__((aligned(16))) char lds_[];
    const int tid = threadIdx.x, lane = tid & 63, r32 = lane & 31, hi = lane >> 5;
    const int wid = rfl(tid >> 6);
    const int bid = blockIdx.x, rb = (bid & 7) * 4 + (bid >> 6), cb = (bid >> 3) & 7;
    const unsigned lds0 = (unsigned)(uintptr_t)lds_;
    const srd_t srdC = mk_srd((const char*)Cf + (size_t)rb * 2 * 262144, 2 * 262144), srdO = mk_srd((const char*)Of + (size_t)rb * 2 * 32768, 2 * 32768);
    const srd_t srdW = mk_srd(Wo, 4194304), srdB = mk_srd(Bwo, 524288);
    unsigned vw[2];
#pragma unroll
    for (int p = 0; p < 2; ++p) vw[p] = ((lane >> 2) & 7) * 1024 + (4 * (lane >> 5) + ((lane & 3) ^ ((2 * p + ((lane >> 4) & 1)) & 3))) * 16;
    const unsigned lane16 = lane * 16;
    const unsigned ring = lds0 + wid * (OUT_D * 4096);
    auto issue = [&](int n) {
        const unsigned sl = ring + (n % OUT_D) * 4096;
        if (n < 32) { const unsigned ks = 32 * wid + n;
            dma1(sl, lane16, srdC, ks * 1024); dma1(sl + 1024, lane16, srdC, 262144 + ks * 1024);
            dma1(sl + 2048, vw[0], srdW, ks * 16384 + cb * 128); dma1(sl + 3072, vw[1], srdW, ks * 16384 + 8192 + cb * 128);
        } else { const unsigned ks = 4 * wid + (n - 32);
            dma1(sl, lane16, srdO, ks * 1024); dma1(sl + 1024, lane16, srdO, 32768 + ks * 1024);
            dma1(sl + 2048, vw[0], srdB, ks * 16384 + cb * 128); dma1(sl + 3072, vw[1], srdB, ks * 16384 + 8192 + cb * 128); }
    };
#pragma unroll
    for (int n = 0; n < OUT_D; ++n) issue(n);
    const int q4 = (lane & 15) >> 2, p4 = lane & 3, blk = (lane >> 4) & 1;
    const unsigned aaddr = ring + lane16;
    unsigned waddr[2];
#pragma unroll
    for (int t = 0; t < 2; ++t) waddr[t] = ring + 2048 + 1024 * t + 64 * (4 * hi + q4) + 16 * ((2 * blk + (p4 >> 1)) ^ ((2 * t + hi) & 3)) + 8 * (p4 & 1);
    f32x16 accA[2][2], accB[2][2];
#pragma unroll
    for (int a = 0; a < 2; ++a) { accA[a][0] = f32x16{}; accA[a][1] = f32x16{}; accB[a][0] = f32x16{}; accB[a][1] = f32x16{}; }
#pragma unroll
    for (int n = 0; n < 36; ++n) {
        constexpr int NT = 36;
        const int rem = NT - 1 - n;
        if (rem >= OUT_D - 1) wait_vm<4 * (OUT_D - 1)>(); else if (rem == 2) wait_vm<8>(); else if (rem == 1) wait_vm<4>(); else wait_vm<0>();
        const int so = (n % OUT_D) * 4096;
        const bf16x8 a0 = ldsf(aaddr + so), a1 = ldsf(aaddr + so + 1024);
        const bf16x8 w0 = cat4(ldstr(waddr[0] + so), ldstr(waddr[1] + so)), w1 = cat4(ldstr(waddr[0] + so + 512), ldstr(waddr[1] + so + 512));
        asm volatile("s_waitcnt lgkmcnt(0)" ::: "memory");
        bf16x8 a0v = a0, a1v = a1, w0v = w0, w1v = w1;
        asm volatile("" : "+v"(a0v), "+v"(a1v), "+v"(w0v), "+v"(w1v));
        if (n + OUT_D < NT) issue(n + OUT_D);
        if (n < 32) { accA[0][0] = MFMA32(w0v, a0v, accA[0][0]); accA[0][1] = MFMA32(w1v, a0v, accA[0][1]); accA[1][0] = MFMA32(w0v, a1v, accA[1][0]); accA[1][1] = MFMA32(w1v, a1v, accA[1][1]); }
        else        { accB[0][0] = MFMA32(w0v, a0v, accB[0][0]); accB[0][1] = MFMA32(w1v, a0v, accB[0][1]); accB[1][0] = MFMA32(w0v, a1v, accB[1][0]); accB[1][1] = MFMA32(w1v, a1v, accB[1][1]); }
        SBAR();
    }
#pragma unroll
    for (int part = 0; part < 2; ++part) {
        asm volatile("s_waitcnt lgkmcnt(0)\n\ts_barrier" ::: "memory");
        const unsigned wb = lds0 + wid * 16384 + lane16;
#pragma unroll
        for (int mb = 0; mb < 2; ++mb)
#pragma unroll
            for (int nb = 0; nb < 2; ++nb)
#pragma unroll
                for (int rq = 0; rq < 4; ++rq) { const f32x16& x = part ? accB[mb][nb] : accA[mb][nb];
                    *(LAS f32x4*)(wb + ((mb * 2 + nb) * 4 + rq) * 1024) = (f32x4){x[4 * rq], x[4 * rq + 1], x[4 * rq + 2], x[4 * rq + 3]}; }
        asm volatile("s_waitcnt lgkmcnt(0)\n\ts_barrier" ::: "memory");
        const int mb = wid >> 2, nb = (wid >> 1) & 1, rq0 = 2 * (wid & 1);
        f32x4 s0 = {0.f, 0.f, 0.f, 0.f}, s1 = {0.f, 0.f, 0.f, 0.f};
#pragma unroll
        for (int w = 0; w < 8; ++w) { const unsigned rbse = lds0 + w * 16384 + ((mb * 2 + nb) * 4 + rq0) * 1024 + lane16;
            s0 += *(const LAS f32x4*)rbse; s1 += *(const LAS f32x4*)(rbse + 1024); }
        unsigned a0 = cvtpk(s0[0], s0[1]), a1 = cvtpk(s0[2], s0[3]), b0 = cvtpk(s1[0], s1[1]), b1 = cvtpk(s1[2], s1[3]);
        auto x0 = __builtin_amdgcn_permlane32_swap(a0, b0, false, false); auto x1 = __builtin_amdgcn_permlane32_swap(a1, b1, false, false);
        const u32x4 w = {x0[0], x1[0], x0[1], x1[1]};
        const int row = rb * 64 + 32 * mb + r32, col = part * 512 + cb * 64 + 32 * nb + 8 * (rq0 + hi);
        *(u32x4*)(out + (size_t)row * 1024 + col) = w;
    }
}
constexpr int FUSED_LDS = 163840;
constexpr unsigned WPHI_LDS = 151552;
struct FusedArgs { const u16 *X, *Ckv, *Wq, *Wuk, *Wuv, *Wo, *Bx, *Wqkv, *Wphi, *Bwo; char* ws; };

__global__ __launch_bounds__(512, 2) void fused_kernel(FusedArgs a) {
    extern __shared__ __attribute__((aligned(16))) char lds_[];
    const int tid = threadIdx.x, lane = tid & 63;
    const int wid = rfl(tid >> 6);
    const int bid = blockIdx.x, h = (bid & 7) * 4 + (bid >> 6), j = (bid >> 3) & 7;
    const unsigned lds0 = (unsigned)(uintptr_t)lds_;
    Stamper stamp{0u, 0, lane};
    stamp();
    u16* Kf = (u16*)(a.ws + WS_KF); u16* Vf = (u16*)(a.ws + WS_VF); u16* Cf = (u16*)(a.ws + WS_CF);
    u16* PhiQ = (u16*)(a.ws + WS_PHIQ); u16* Of = (u16*)(a.ws + WS_OF); float* Bst = (float*)(a.ws + WS_BST);
    u64* flags = (u64*)(a.ws + WS_FLAG);
    float* Bred = (float*)(a.ws + WS_BRED);
    const int hg = bid & 7, rc = bid >> 3;
    if (wid < 2) dma1(lds0 + WPHI_LDS + wid * 1024, lane * 16, mk_srd(a.Wphi, 16384), (unsigned)(hg * 2048 + wid * 1024));
    ProjCtx pc; proj_setup(pc, a.X, a.Ckv, a.Wq, a.Wuk, a.Wuv, Kf, Vf, lds0, h, j, wid, lane);
#pragma unroll
    for (int i = 0; i < 8; ++i) issueKV(pc, 0, i);
#pragma unroll
    for (int i = 0; i < 8; ++i) issueKV(pc, 1, i);
    const unsigned tsc = lds0 + TOUCH_LDS;
    { const unsigned vW = (lane >> 4) * 524288 + (lane & 15) * 128;
      const char* base; unsigned vo = pc.l128;
      if (wid < 4) { base = (const char*)((wid & 1) ? a.Wuv : a.Wuk) + ((wid & 2) ? 4 : 2) * 524288 + bid * 2048 - ((wid & 2) ? 0 : 0); vo = vW; }
      else if (wid == 4) base = (const char*)a.Ckv + bid * 8192;
      else { base = (const char*)a.Ckv; vo = 0; }
      touch_g<1>(tsc, vo, base); }
    const u64 epoch = *(const u64 __attribute__((address_space(4)))*)(flags + (FL_EPOCH + hg) * FL_STRIDE);
    u64 tok = mix64(epoch); if (tok == epoch) tok ^= 1ull;
    B1Ctx bc; b1_setup(bc, a.Bx, a.Wqkv, lds0, rc, hg, wid, lane);
    stamp();
    auto touchk = [&](int kt) {
        const char* base = (const char*)a.Ckv; unsigned vo = 0;
        if (kt == 0) {
            if (wid == 0) { base = (const char*)a.Bx + bid * 8192; vo = pc.l128; }
            else if (wid == 1) { base = (const char*)a.X + bid * 8192; vo = pc.l128; }
            else if (wid == 2) { base = (const char*)a.Wqkv + (bid < 192 ? bid : 0) * 8192; vo = pc.l128; }
        } else if (kt == 1) {
            if (wid < 2) { base = (const char*)a.Wq + (bid * 2 + wid) * 8192; vo = pc.l128; }
        } else if (kt == 2) {
            if (wid < 2) { base = (const char*)a.Wo + (bid * 2 + wid) * 8192; vo = pc.l128; }
            else if (wid == 2) { base = (const char*)a.Bwo + (bid & 63) * 8192; vo = pc.l128; }
        }
        touch_g<2>(tsc, vo, base); };
    u32x4 pk[16];
    kv_run(pc, lane, pk, touchk, [&](int kt) { b1_issue(bc, kt); }, stamp);
    stamp();
    B1Acc ba;
    b1_gemm(bc, ba, lane, [&](int n) { kv_store(pc, pk, n); },
            [&]() {
#pragma unroll
                for (int i = 0; i < 6; ++i) issueQ(pc, 0, i); },
            [&]() {
#pragma unroll
                for (int i = 0; i < 6; ++i) issueQ(pc, 1, i); }, stamp);
    stamp();
    u32x2 wp; { const int l16 = lane & 15, g = lane >> 4; const LAS u16* w = (const LAS u16*)(lds0 + WPHI_LDS + (wid & 3) * 512);
        wp[0] = (unsigned)w[(4 * g) * 16 + l16] | ((unsigned)w[(4 * g + 1) * 16 + l16] << 16); wp[1] = (unsigned)w[(4 * g + 2) * 16 + l16] | ((unsigned)w[(4 * g + 3) * 16 + l16] << 16); }
    b1_phi(ba, wp, PhiQ, Bst, lds0, rc, hg, wid, lane, stamp);
    stamp();
    wait_bar<10>();
    stamp();
#pragma unroll
    for (int i = 0; i < 6; ++i) issueQ(pc, 2, i);
    stamp();
    AttnState st;
    q_run(pc, lane, pk, st.q, [&](int kt) { if (tid == 0) { if (kt == 1) st_agent(flags + (FL_BS + 32 * hg + rc) * FL_STRIDE, tok); if (kt == 5) st_agent(flags + (FL_KV + 8 * h + j) * FL_STRIDE, tok); } }, stamp);
    stamp();
    AttnCtx c; attn_setup(c, Kf, Vf, Cf, lds0, h, j, wid, lane);
    BARRIER();
    if (wid < 4) {
#pragma unroll
        for (int s = 0; s < 8; ++s) *(LAS bf16x8*)(c.qstash + s * 1024) = st.q[s]; }
    { const u64* fp = flags + ((wid == 7 && lane >= 32) ? (FL_BS + 32 * hg + (lane - 32)) : (FL_KV + 8 * h + (lane & 7))) * FL_STRIDE;
      for (unsigned spins = 0; spins < (1u << 15); ++spins) { const u64 v = ld_agent(fp); if (__all(v == tok)) break; __builtin_amdgcn_s_sleep(1); }
      asm volatile("" ::: "memory"); }
    stamp();
    attn_prime(c);
    if (wid == 7) {
        if (rc == 0 && lane == 0) st_agent(flags + (FL_EPOCH + hg) * FL_STRIDE, tok);
        b2a_body(Bst, Bred, rc, hg, lane);
    }
    stamp();
    attn_run(st, c, lds0, h, j, wid, lane,
             [&]() { if (tid == 448) st_agent(flags + (FL_BR + 32 * hg + rc) * FL_STRIDE, tok); },
             [&]() { b2b_body<4>(PhiQ, Bred, Of, flags + (FL_BR + 32 * hg + 8 * wid) * FL_STRIDE, tok, 4 * hg + wid, 4 * rc, lane); }, stamp);
}

extern "C" void kernel_launch(void* const* d_in, const int* in_sizes, int n_in, void* d_out, int out_size, void* d_ws, size_t ws_size, hipStream_t stream) {
    FusedArgs a;
    a.X = (const u16*)d_in[0]; a.Ckv = (const u16*)d_in[1]; a.Wq = (const u16*)d_in[2]; a.Wuk = (const u16*)d_in[3]; a.Wuv = (const u16*)d_in[4]; a.Wo = (const u16*)d_in[5];
    a.Bx = (const u16*)d_in[6]; a.Wqkv = (const u16*)d_in[7]; a.Wphi = (const u16*)d_in[8]; a.Bwo = (const u16*)d_in[9]; a.ws = (char*)d_ws;
    (void)hipFuncSetAttribute((const void*)fused_kernel, hipFuncAttributeMaxDynamicSharedMemorySize, FUSED_LDS);
    (void)hipFuncSetAttribute((const void*)outp_kernel, hipFuncAttributeMaxDynamicSharedMemorySize, OUT_LDS);
    fused_kernel<<<256, 512, FUSED_LDS, stream>>>(a);
    outp_kernel<<<256, 512, OUT_LDS, stream>>>((const u16*)(a.ws + WS_CF), (const u16*)(a.ws + WS_OF), a.Wo, a.Bwo, (u16*)d_out);
}
```

```cpp
#include <hip/hip_runtime.h>
#include <stdint.h>

typedef unsigned short u16;
typedef unsigned long long u64;
typedef __bf16 bf16x8 __attribute__((ext_vector_type(8)));
typedef __bf16 bf16x4 __attribute__((ext_vector_type(4)));
typedef __bf16 bf16x2 __attribute__((ext_vector_type(2)));
typedef short s16x4 __attribute__((ext_vector_type(4)));
typedef float f32x16 __attribute__((ext_vector_type(16)));
typedef float f32x4 __attribute__((ext_vector_type(4)));
typedef float f32x2 __attribute__((ext_vector_type(2)));
typedef unsigned u32x4 __attribute__((ext_vector_type(4)));
typedef unsigned u32x2 __attribute__((ext_vector_type(2)));
typedef __amdgpu_buffer_rsrc_t srd_t;
#define LAS __attribute__((address_space(3)))
#define GAS __attribute__((address_space(1)))
#define SBAR() __builtin_amdgcn_sched_barrier(0)
#define PIN(x) asm volatile("" : "+v"(x))
#define MFMA32(a, b, c) __builtin_amdgcn_mfma_f32_32x32x16_bf16((a), (b), (c), 0, 0, 0)
#define MFMA16(a, b, c) __builtin_amdgcn_mfma_f32_16x16x32_bf16((a), (b), (c), 0, 0, 0)
#define DI __device__ __forceinline__
#pragma clang diagnostic ignored "-Wint-to-pointer-cast"

DI unsigned cvtpk(float lo, float hi) { f32x2 v = {lo, hi}; bf16x2 b = __builtin_convertvector(v, bf16x2); return __builtin_bit_cast(unsigned, b); }
DI unsigned rfl(unsigned x) { return __builtin_amdgcn_readfirstlane(x); }
DI float swap_max(float x) { auto rr = __builtin_amdgcn_permlane32_swap(__float_as_uint(x), __float_as_uint(x), false, false); return __builtin_fmaxf(__uint_as_float(rr[0]), __uint_as_float(rr[1])); }
DI float swap_add(float x) { auto rr = __builtin_amdgcn_permlane32_swap(__float_as_uint(x), __float_as_uint(x), false, false); return __uint_as_float(rr[0]) + __uint_as_float(rr[1]); }
DI srd_t mk_srd(const void* p, int bytes) { return __builtin_amdgcn_make_buffer_rsrc((void*)p, (short)0, bytes, 0x00020000); }
DI bf16x4 ldstr(unsigned a) { return __builtin_amdgcn_ds_read_tr16_b64_v4bf16((LAS bf16x4*)a); }
DI bf16x8 ldsf(unsigned a) { return *(const LAS bf16x8*)a; }
DI bf16x8 cat4(bf16x4 a, bf16x4 b) { return __builtin_shufflevector(a, b, 0, 1, 2, 3, 4, 5, 6, 7); }
#define SCALARIZE2(a, b) do {} while (0)
DI void dma1(unsigned m0v, unsigned voff, srd_t srd, unsigned soff) {
    SCALARIZE2(m0v, soff);
    asm volatile("s_mov_b32 m0, %0\n\ts_nop 0\n\tbuffer_load_dwordx4 %1, %2, %3 offen lds" :: "s"(m0v), "v"(voff), "s"(srd), "s"(soff) : "memory");
}
DI void touch(unsigned m0v, unsigned voff, srd_t srd, unsigned soff) {
    SCALARIZE2(m0v, soff);
    asm volatile("s_mov_b32 m0, %0\n\ts_nop 0\n\tbuffer_load_dword %1, %2, %3 offen lds" :: "s"(m0v), "v"(voff), "s"(srd), "s"(soff) : "memory");
}
DI void store16_sc1(u32x4 w, unsigned voff, srd_t srd, unsigned soff) {
    unsigned z = soff; SCALARIZE2(soff, z);
    asm volatile("buffer_store_dwordx4 %0, %1, %2, %3 offen sc1\n\ts_nop 1" :: "v"(w), "v"(voff), "s"(srd), "s"(soff) : "memory");
}
template <int N> DI void wait_vm() { asm volatile("s_waitcnt vmcnt(%0)" :: "i"(N) : "memory"); }
template <int N> DI void wait_bar() { asm volatile("s_waitcnt vmcnt(%0) lgkmcnt(0)\n\ts_barrier" :: "i"(N) : "memory"); }
#define BARRIER() asm volatile("s_waitcnt lgkmcnt(0)\n\ts_barrier" ::: "memory")

struct Stamper { unsigned v; int n; int lane;
    DI void operator()() {
    } };

constexpr size_t WS_FLAG = 0;
constexpr size_t WS_KF = 1 << 20, WS_VF = WS_KF + (16 << 20), WS_CF = WS_VF + (16 << 20), WS_PHIQ = WS_CF + (16 << 20), WS_OF = WS_PHIQ + (4 << 20), WS_BST = WS_OF + (2 << 20), WS_BRED = WS_BST + (3 << 20), WS_XCH = WS_BRED + (1 << 20), WS_DBG = 200 << 20;
constexpr int FL_STRIDE = 16;
constexpr int FL_EPOCH = 0, FL_KV = 32, FL_BS = 32 + 256, FL_BR = 32 + 512, FL_XEP = 32 + 768, FL_XF = FL_XEP + 64;
typedef GAS u64 gu64;
DI u64 ld_agent(const u64* p) { return __hip_atomic_load((gu64*)p, __ATOMIC_RELAXED, __HIP_MEMORY_SCOPE_AGENT); }
DI void st_agent(u64* p, u64 v) { __hip_atomic_store((gu64*)p, v, __ATOMIC_RELAXED, __HIP_MEMORY_SCOPE_AGENT); }
DI u64 mix64(u64 z) { z += 0x9E3779B97F4A7C15ull; z = (z ^ (z >> 30)) * 0xBF58476D1CE4E5B9ull; z = (z ^ (z >> 27)) * 0x94D049BB133111EBull; return z ^ (z >> 31); }
DI f32x4 mfma16_16(u32x2 a, u32x2 b, f32x4 c) { return __builtin_amdgcn_mfma_f32_16x16x16bf16_1k(__builtin_bit_cast(s16x4, a), __builtin_bit_cast(s16x4, b), c, 0, 0, 0); }
DI float rbf16(float x) { return __uint_as_float(cvtpk(x, 0.f) << 16); }
DI float x16_max(float x) { auto rr = __builtin_amdgcn_permlane16_swap(__float_as_uint(x), __float_as_uint(x), false, false); return __builtin_fmaxf(__uint_as_float(rr[0]), __uint_as_float(rr[1])); }
DI float x16_add(float x) { auto rr = __builtin_amdgcn_permlane16_swap(__float_as_uint(x), __float_as_uint(x), false, false); return __uint_as_float(rr[0]) + __uint_as_float(rr[1]); }
DI float grp_max(float x) { return swap_max(x16_max(x)); }
DI float grp_sum(float x) { return swap_add(x16_add(x)); }
constexpr float LOG2E = 1.4426950408889634f;
constexpr unsigned B1_RED = 131072;
struct B1Ctx { srd_t srdX, srdW; unsigned vx, vw[2], sx, sw; int wid; unsigned lds0; };
DI void b1_setup(B1Ctx& c, const u16* Bx, const u16* Wqkv, unsigned lds0, int rc, int hg, int wid, int lane) {
    c.srdX = mk_srd(Bx, 2097152); c.srdW = mk_srd(Wqkv, 512 * 1536 * 2); c.wid = wid; c.lds0 = lds0;
    c.vx = (lane >> 3) * 1024 + (((lane & 7) ^ ((4 * wid + (lane >> 4)) & 7)) * 16);
#pragma unroll
    for (int p = 0; p < 2; ++p) c.vw[p] = (lane >> 3) * 3072 + (((lane & 7) ^ (2 * (((lane >> 4) & 1) | (p << 1)))) * 16);
    c.sx = (64 * rc + 8 * wid) * 1024; c.sw = hg * 128;
}
DI void b1_issue(const B1Ctx& c, int kt) {
    const unsigned st = c.lds0 + (kt & 3) * 32768;
    dma1(st + c.wid * 1024, c.vx, c.srdX, c.sx + kt * 128);
#pragma unroll
    for (int i = 0; i < 3; ++i) { const int pw = 3 * c.wid + i, m = pw >> 3, pr = pw & 7;
        dma1(st + 8192 + pw * 1024, (pr & 1) ? c.vw[1] : c.vw[0], c.srdW, c.sw + (kt * 64 + 8 * pr) * 3072 + m * 1024); }
}
struct B1Acc { f32x4 aq[2], ak[2], av[2]; };
struct B1Fr { bf16x8 x[2][2]; bf16x4 w[2][3][2]; };
template <int KT> DI void b1_read(B1Fr& f, unsigned xa0, unsigned xa1, unsigned wa) {
    constexpr unsigned st = (KT & 3) * 32768;
#pragma unroll
    for (int ks = 0; ks < 2; ++ks) { const unsigned xa = ks ? xa1 : xa0;
        f.x[ks][0] = ldsf(xa + st); f.x[ks][1] = ldsf(xa + st + 2048);
#pragma unroll
        for (int m = 0; m < 3; ++m) { f.w[ks][m][0] = ldstr(wa + st + m * 8192 + ks * 4096); f.w[ks][m][1] = ldstr(wa + st + m * 8192 + ks * 4096 + 512); } }
}
DI void b1_mma(B1Acc& a, const B1Fr& f) {
#pragma unroll
    for (int ks = 0; ks < 2; ++ks) {
        const bf16x8 wq = cat4(f.w[ks][0][0], f.w[ks][0][1]), wk = cat4(f.w[ks][1][0], f.w[ks][1][1]), wv = cat4(f.w[ks][2][0], f.w[ks][2][1]);
        a.aq[0] = MFMA16(wq, f.x[ks][0], a.aq[0]); a.aq[1] = MFMA16(wq, f.x[ks][1], a.aq[1]); a.ak[0] = MFMA16(wk, f.x[ks][0], a.ak[0]); a.ak[1] = MFMA16(wk, f.x[ks][1], a.ak[1]);
        a.av[0] = MFMA16(f.x[ks][0], wv, a.av[0]); a.av[1] = MFMA16(f.x[ks][1], wv, a.av[1]);
    }
}
template <class St, class H6, class HE>
DI void b1_gemm(const B1Ctx& c, B1Acc& a, int lane, St store, H6 afterB6, HE afterEnd, Stamper& stamp) {
    const int l16 = lane & 15, g = lane >> 4, q4 = l16 >> 2, p4 = lane & 3, wid = c.wid;
    const int row = 32 * (wid >> 2) + l16;
    const unsigned xa0 = c.lds0 + row * 128 + (((0 + g) ^ ((row >> 1) & 7)) << 4), xa1 = c.lds0 + row * 128 + (((4 + g) ^ ((row >> 1) & 7)) << 4);
    const unsigned wa = c.lds0 + 8192 + (8 * g + q4) * 128 + (((wid & 3) ^ (((q4 >> 1) & 1) | ((g & 1) << 1))) * 32) + p4 * 8;
#pragma unroll
    for (int r = 0; r < 2; ++r) { a.aq[r] = f32x4{}; a.ak[r] = f32x4{}; a.av[r] = f32x4{}; }
    B1Fr f;
#define B1T(KT, PRE) do { stamp(); b1_read<KT>(f, xa0, xa1, wa); SBAR(); PRE; store(KT); SBAR(); b1_mma(a, f); SBAR(); stamp(); } while (0)
    B1T(0, (void)0);
    wait_bar<9>();  B1T(1, b1_issue(c, 4));
    wait_bar<10>(); B1T(2, b1_issue(c, 5));
    wait_bar<11>(); B1T(3, b1_issue(c, 6));
    wait_bar<11>(); B1T(4, b1_issue(c, 7));
    wait_bar<11>(); B1T(5, (void)0);
    wait_bar<7>();  B1T(6, afterB6());
    wait_bar<9>();  B1T(7, (void)0);
#undef B1T
    BARRIER(); afterEnd();
}
template <int N> DI void phi_map(const f32x4 (&p)[N], u32x4 (&out)[N]) {
    float x[N][4], mx[N], nm[N], ep[N][4], en[N][4], sp[N], sn[N];
#pragma unroll
    for (int i = 0; i < N; ++i) {
#pragma unroll
        for (int e = 0; e < 4; ++e) x[i][e] = rbf16(p[i][e]) * LOG2E;
        mx[i] = __builtin_fmaxf(__builtin_fmaxf(x[i][0], x[i][1]), __builtin_fmaxf(x[i][2], x[i][3])); nm[i] = -__builtin_fminf(__builtin_fminf(x[i][0], x[i][1]), __builtin_fminf(x[i][2], x[i][3])); }
#pragma unroll
    for (int i = 0; i < N; ++i) { mx[i] = x16_max(mx[i]); nm[i] = x16_max(nm[i]); }
#pragma unroll
    for (int i = 0; i < N; ++i) { mx[i] = swap_max(mx[i]); nm[i] = swap_max(nm[i]); }
#pragma unroll
    for (int i = 0; i < N; ++i) {
#pragma unroll
        for (int e = 0; e < 4; ++e) { ep[i][e] = __builtin_amdgcn_exp2f(x[i][e] - mx[i]); en[i][e] = __builtin_amdgcn_exp2f(-x[i][e] - nm[i]); }
        sp[i] = (ep[i][0] + ep[i][1]) + (ep[i][2] + ep[i][3]); sn[i] = (en[i][0] + en[i][1]) + (en[i][2] + en[i][3]); }
#pragma unroll
    for (int i = 0; i < N; ++i) { sp[i] = x16_add(sp[i]); sn[i] = x16_add(sn[i]); }
#pragma unroll
    for (int i = 0; i < N; ++i) { sp[i] = __builtin_amdgcn_rcpf(swap_add(sp[i])); sn[i] = __builtin_amdgcn_rcpf(swap_add(sn[i])); }
#pragma unroll
    for (int i = 0; i < N; ++i) out[i] = (u32x4){cvtpk(ep[i][0] * sp[i], ep[i][1] * sp[i]), cvtpk(ep[i][2] * sp[i], ep[i][3] * sp[i]), cvtpk(en[i][0] * sn[i], en[i][1] * sn[i]), cvtpk(en[i][2] * sn[i], en[i][3] * sn[i])};
}
DI void b1_phi(const B1Acc& a, u32x2 wp, u16* PhiQ, float* Bst, unsigned lds0, int rc, int hg, int wid, int lane, Stamper& stamp) {
    const int h = 4 * hg + (wid & 3), row0 = 64 * rc + 32 * (wid >> 2);
    const int l16 = lane & 15, g = lane >> 4;
    f32x4 sp = f32x4{}, sn = f32x4{}; float zp = 0.f, zn = 0.f;
    f32x4 pin[4]; u32x4 pout[4];
#pragma unroll
    for (int r = 0; r < 2; ++r) {
        const u32x2 qb = {cvtpk(a.aq[r][0], a.aq[r][1]), cvtpk(a.aq[r][2], a.aq[r][3])}, kb = {cvtpk(a.ak[r][0], a.ak[r][1]), cvtpk(a.ak[r][2], a.ak[r][3])};
        pin[r] = mfma16_16(wp, qb, f32x4{}); pin[2 + r] = mfma16_16(wp, kb, f32x4{});
    }
    phi_map<4>(pin, pout);
    const u32x4 wq[2] = {pout[0], pout[1]}, wk[2] = {pout[2], pout[3]};
    const unsigned tb = lds0 + B1_RED + wid * 2304;
#pragma unroll
    for (int r = 0; r < 2; ++r) {
        *(LAS u32x2*)(tb + r * 1024 + l16 * 32 + g * 8) = (u32x2){wk[r][0], wk[r][1]};
        *(LAS u32x2*)(tb + r * 1024 + 512 + l16 * 32 + g * 8) = (u32x2){wk[r][2], wk[r][3]};
        __builtin_amdgcn_raw_buffer_store_b128(wq[r], mk_srd(PhiQ, 4194304), ((h * 128 + (row0 >> 4) + r) * 64 + lane) * 16, 0, 0);
    }
    stamp();
    asm volatile("s_waitcnt lgkmcnt(0)" ::: "memory");
#pragma unroll
    for (int r = 0; r < 2; ++r) {
        const unsigned ta = tb + r * 1024 + (4 * g + (l16 >> 2)) * 32 + (lane & 3) * 8;
        const u32x2 pp = __builtin_bit_cast(u32x2, ldstr(ta)), pn = __builtin_bit_cast(u32x2, ldstr(ta + 512));
        const u32x2 vb = {cvtpk(a.av[r][0], a.av[r][1]), cvtpk(a.av[r][2], a.av[r][3])};
        sp = mfma16_16(pp, vb, sp); sn = mfma16_16(pn, vb, sn);
        zp += (__uint_as_float(pp[0] << 16) + __uint_as_float(pp[0] & 0xffff0000u)) + (__uint_as_float(pp[1] << 16) + __uint_as_float(pp[1] & 0xffff0000u));
        zn += (__uint_as_float(pn[0] << 16) + __uint_as_float(pn[0] & 0xffff0000u)) + (__uint_as_float(pn[1] << 16) + __uint_as_float(pn[1] & 0xffff0000u));
    }
    asm volatile("s_waitcnt lgkmcnt(0)" ::: "memory");
    zp = grp_sum(zp); zn = grp_sum(zn);
    LAS char* red = (LAS char*)(lds0 + B1_RED + wid * 2304);
    *(LAS f32x4*)(red + lane * 32) = sp; *(LAS f32x4*)(red + lane * 32 + 16) = sn;
    if (lane < 16) *(LAS f32x2*)(red + 2048 + lane * 8) = (f32x2){zp, zn};
    stamp();
    BARRIER();
    stamp();
    const int tid = wid * 64 + lane;
    const srd_t srd = mk_srd(Bst, 32 * 32 * 2304);
#pragma unroll
    for (int rep = 0; rep < 2; ++rep) { const int i = tid + rep * 512, ic = i < 576 ? i : 0, hh = ic / 144, ch = ic - hh * 144;
        const f32x4 s = *(const LAS f32x4*)(lds0 + B1_RED + hh * 2304 + ch * 16) + *(const LAS f32x4*)(lds0 + B1_RED + (hh + 4) * 2304 + ch * 16);
        store16_sc1(__builtin_bit_cast(u32x4, s), i < 576 ? ((4 * hg + hh) * 32 + rc) * 2304 + ch * 16 : 0x7fffffffu, srd, 0u); }
}
template <int N> DI void waitN(const u64* flags, u64 tok, int lane) {
    const u64* p = flags + (lane & (N - 1)) * FL_STRIDE;
    for (unsigned spins = 0; spins < (1u << 15); ++spins) { const u64 v = ld_agent(p); if (__all(v == tok)) break; __builtin_amdgcn_s_sleep(1); }
    asm volatile("" ::: "memory");
}
struct B2a { f32x4 v[16]; };
DI void b2a_load(B2a& b, const float* Bst, int rc, int hg, int lane) {
    const int h = 4 * hg + (rc >> 3), c = lane & 31, half = lane >> 5;
    const srd_t srd = mk_srd(Bst + (size_t)h * 32 * 576, 32 * 2304);
    const unsigned vo = (c < 18 ? (18 * (rc & 7) + c) * 16 : 0x7fffffff) + half * 16 * 2304;
#pragma unroll
    for (int i = 0; i < 16; ++i) b.v[i] = __builtin_bit_cast(f32x4, __builtin_amdgcn_raw_buffer_load_b128(srd, vo, i * 2304, 16));
}
DI void b2a_finish(const B2a& b, float* Bred, int rc, int hg, int lane) {
    const int h = 4 * hg + (rc >> 3), c = lane & 31, half = lane >> 5;
    f32x4 s = b.v[0];
#pragma unroll
    for (int i = 1; i < 16; ++i) s += b.v[i];
#pragma unroll
    for (int e = 0; e < 4; ++e) s[e] = swap_add(s[e]);
    const srd_t srdR = mk_srd(Bred + (size_t)h * 576, 2304);
    store16_sc1(__builtin_bit_cast(u32x4, s), (half == 0 && c < 18) ? (18 * (rc & 7) + c) * 16 : 0x7fffffffu, srdR, 0u);
}
template <int NRB>
DI void b2b_body(const u16* PhiQ, const float* Bred, u16* Of, const u64* flagsR, u64 tok, int h, int rb0, int lane) {
    const int l16 = lane & 15, g = lane >> 4;
    bf16x8 ph[NRB];
#pragma unroll
    for (int r = 0; r < NRB; ++r) ph[r] = *(const bf16x8*)((const char*)PhiQ + ((size_t)(h * 128 + rb0 + r) * 64 + lane) * 16);
    waitN<8>(flagsR, tok, lane);
    const srd_t srd = mk_srd(Bred + (size_t)h * 576, 2304);
    const f32x4 sp = __builtin_bit_cast(f32x4, __builtin_amdgcn_raw_buffer_load_b128(srd, lane * 32, 0, 16)), sn = __builtin_bit_cast(f32x4, __builtin_amdgcn_raw_buffer_load_b128(srd, lane * 32 + 16, 0, 16));
    const f32x4 za = __builtin_bit_cast(f32x4, __builtin_amdgcn_raw_buffer_load_b128(srd, 2048 + g * 32, 0, 16)), zb = __builtin_bit_cast(f32x4, __builtin_amdgcn_raw_buffer_load_b128(srd, 2048 + g * 32 + 16, 0, 16));
    const u32x4 kvw = {cvtpk(sp[0], sp[1]), cvtpk(sp[2], sp[3]), cvtpk(sn[0], sn[1]), cvtpk(sn[2], sn[3])};
    const u32x4 zw = {cvtpk(za[0], za[2]), cvtpk(zb[0], zb[2]), cvtpk(za[1], za[3]), cvtpk(zb[1], zb[3])};
    const srd_t srdO = mk_srd(Of, 2097152);
#pragma unroll
    for (int r = 0; r < NRB; ++r) { const int rb16 = rb0 + r;
        const f32x4 num = MFMA16(__builtin_bit_cast(bf16x8, kvw), ph[r], f32x4{});
        const f32x4 den = MFMA16(__builtin_bit_cast(bf16x8, zw), ph[r], f32x4{});
        const float dn = rbf16(rbf16(den[0]) + 1e-6f);
        const u32x2 w = {cvtpk(rbf16(num[0]) / dn, rbf16(num[1]) / dn), cvtpk(rbf16(num[2]) / dn, rbf16(num[3]) / dn)};
        __builtin_amdgcn_raw_buffer_store_b64(w, srdO, (((rb16 >> 1) * 32 + h) * 2 + (g & 1)) * 512 + ((rb16 & 1) * 16 + l16) * 16 + (g >> 1) * 8, 0, 0); }
}
template <int MB, int NB> struct Frags { bf16x8 a[MB]; bf16x4 w0[NB], w1[NB]; };
struct GAddr { unsigned a[4]; unsigned w[2]; };

template <int MB, int NB, int S> DI void frag_read(Frags<MB, NB>& f, const GAddr& g, int i) {
    if (i == 2) { f.a[0] = ldsf(g.a[S]); return; }
    const int k = i < 2 ? i : i - 1;
    if (k < 2 * NB) { const int nb = k >> 1; if (k & 1) f.w1[nb] = ldstr(g.w[1] + S * 4096 + nb * 512); else f.w0[nb] = ldstr(g.w[0] + S * 4096 + nb * 512); }
    else { const int mb = k - 2 * NB + 1; if (mb < MB) f.a[mb] = ldsf(g.a[S] + mb * 4096); }
}
template <int MB, int NB, bool ACT_A, int SN, int RPG, class Hook>
DI void kstep(f32x16 (&acc)[MB][NB], const Frags<MB, NB>& cur, Frags<MB, NB>& nxt, const GAddr& g, Hook hook) {
    constexpr int NM = MB * NB, NR = MB + 2 * NB;
#pragma unroll
    for (int i = 0; i < NM; ++i) {
        const int mb = i / NB, nb = i % NB;
        const bf16x8 w = cat4(cur.w0[nb], cur.w1[nb]);
        acc[mb][nb] = ACT_A ? MFMA32(cur.a[mb], w, acc[mb][nb]) : MFMA32(w, cur.a[mb], acc[mb][nb]);
        SBAR();
        if constexpr (SN >= 0) {
#pragma unroll
            for (int r = 0; r < RPG; ++r) if (i * RPG + r < NR) frag_read<MB, NB, SN < 0 ? 0 : SN>(nxt, g, i * RPG + r);
        }
        hook(i);
        SBAR();
    }
}
struct NoHook { DI void operator()(int) const {} };

struct ProjCtx {
    srd_t srdC, srdX, srdW, srdWq, srdO;
    unsigned va[2], vw[2], vwq, vo, lds0, l128;
    unsigned sA, sW, sWq, sXa;
    int wid, isV;
};
constexpr unsigned TOUCH_LDS = 163840 - 256;
DI void issueKV(const ProjCtx& c, int kt, int i) {
    const unsigned st = c.lds0 + (kt & 1) * 65536;
    if (i < 4) dma1(st + (4 * c.wid + i) * 1024, c.va[i & 1], c.srdC, c.sA + i * 8192 + kt * 128);
    else dma1(st + 32768 + (c.wid >> 2) * 16384 + (4 * (c.wid & 3) + (i - 4)) * 1024, c.vw[(i - 4) >> 1], c.srdW, c.sW + ((i - 4) >> 1) * 65536 + ((i - 4) & 1) * 128 + kt * 524288);
}
DI void issueQ(const ProjCtx& c, int kt, int i) {
    const unsigned st = c.lds0 + (kt % 3) * 49152;
    if (i < 4) dma1(st + (4 * c.wid + i) * 1024, c.va[i & 1], c.srdX, c.sXa + i * 8192 + kt * 128);
    else dma1(st + 32768 + (2 * c.wid + (i - 4)) * 1024, c.vwq, c.srdWq, c.sWq + (i - 4) * 128 + kt * 524288);
}

#define TOUCHMODE 3
template <int WHICH> DI void touch_g(unsigned m0v, unsigned voff, const void* base) {
    if (!(TOUCHMODE & WHICH)) voff = 0;
    const u64 b = (u64)base; const u64 sb = ((u64)rfl((unsigned)(b >> 32)) << 32) | rfl((unsigned)b);
    asm volatile("s_nop 4\n\ts_mov_b32 m0, %0\n\ts_nop 0\n\tglobal_load_lds_dword %1, %2" :: "s"(rfl(m0v)), "v"(voff), "s"(sb) : "memory");
}
template <bool ISV, class Touch, class IssueB1>
DI void kv_part(const ProjCtx& c, GAddr g, u32x4 (&pk)[16], Touch touchk, IssueB1 issueB1, Stamper& stamp) {
    f32x16 acc[4][2];
#pragma unroll
    for (int a = 0; a < 4; ++a) { acc[a][0] = f32x16{}; acc[a][1] = f32x16{}; }
    Frags<4, 2> f0, f1;
    wait_bar<9>();
    stamp();
#pragma unroll
    for (int i = 0; i < 8; ++i) frag_read<4, 2, 0>(f0, g, i);
    for (int kt = 0; kt < 6; ++kt) {
        kstep<4, 2, ISV, 1, 2>(acc, f0, f1, g, [&](int i) { if (kt > 0 && (i & 1)) issueKV(c, kt + 1, 4 + (i >> 1)); });
        kstep<4, 2, ISV, 2, 2>(acc, f1, f0, g, [&](int i) { if (i == 1) touchk(kt); });
        kstep<4, 2, ISV, 3, 2>(acc, f0, f1, g, NoHook{});
        wait_bar<1>();
#pragma unroll
        for (int i = 0; i < 4; ++i) g.a[i] ^= 65536u;
        g.w[0] ^= 65536u; g.w[1] ^= 65536u;
        kstep<4, 2, ISV, 0, 2>(acc, f1, f0, g, [&](int i) { if (i & 1) issueKV(c, kt + 2, i >> 1); });
    }
    kstep<4, 2, ISV, 1, 2>(acc, f0, f1, g, [&](int i) { if (i & 1) issueKV(c, 7, 4 + (i >> 1)); });
    kstep<4, 2, ISV, 2, 2>(acc, f1, f0, g, NoHook{});
    kstep<4, 2, ISV, 3, 2>(acc, f0, f1, g, NoHook{});
    stamp();
    wait_bar<0>();
    stamp();
#pragma unroll
    for (int i = 0; i < 4; ++i) g.a[i] ^= 65536u;
    g.w[0] ^= 65536u; g.w[1] ^= 65536u;
    kstep<4, 2, ISV, 0, 2>(acc, f1, f0, g, [&](int i) { if (i == 1) issueB1(0); if (i == 5) issueB1(1); });
    kstep<4, 2, ISV, 1, 2>(acc, f0, f1, g, NoHook{});
    kstep<4, 2, ISV, 2, 2>(acc, f1, f0, g, NoHook{});
    kstep<4, 2, ISV, 3, 2>(acc, f0, f1, g, NoHook{});
    stamp();
    wait_bar<4>();
    stamp();
    kstep<4, 2, ISV, -1, 2>(acc, f1, f0, g, [&](int i) { if (i == 1) issueB1(2); if (i == 5) issueB1(3); });
#pragma unroll
    for (int mb = 0; mb < 4; ++mb)
#pragma unroll
        for (int nb = 0; nb < 2; ++nb)
#pragma unroll
            for (int s = 0; s < 2; ++s)
#pragma unroll
                for (int e = 0; e < 4; ++e) pk[(mb * 2 + nb) * 2 + s][e] = cvtpk(acc[mb][nb][8 * s + 2 * e], acc[mb][nb][8 * s + 2 * e + 1]);
}
DI void kv_store(const ProjCtx& c, const u32x4 (&pk)[16], int n) {
    const int mb = n >> 2, nb = (n >> 1) & 1, s = n & 1;
    const unsigned offK = (mb >> 1) * 16384 + (mb & 1) * 512 + (4 * nb + 2 * s) * 1024, offV = (mb >> 1) * 16384 + (2 * (mb & 1) + s) * 4096 + nb * 512;
    store16_sc1(pk[n], c.vo, c.srdO, c.isV ? offV : offK);
}
template <int KT, class Pub>
DI void q_tile(const ProjCtx& c, GAddr& g, f32x16 (&acc)[1][4], Frags<1, 4>& f0, Frags<1, 4>& f1, const u32x4 (&pk)[16], Pub after, Stamper& stamp) {
    kstep<1, 4, false, 1, 3>(acc, f0, f1, g, [&](int i) { if (KT >= 1 && KT + 2 < 8 && i < 3) issueQ(c, KT + 2, 3 + i); });
    kstep<1, 4, false, 2, 3>(acc, f1, f0, g, [&](int i) { if (KT < 3 && i == 0) kv_store(c, pk, 8 + 3 * KT); if (KT < 2 && i == 3) kv_store(c, pk, 8 + 3 * KT + 2); });
    kstep<1, 4, false, 3, 3>(acc, f0, f1, g, [&](int i) { if (KT < 3 && i == 2) kv_store(c, pk, 8 + 3 * KT + 1); });
    if constexpr (KT < 7) {
        constexpr int W = KT == 0 ? 13 : (KT == 1 ? 12 : (KT == 2 ? 11 : (KT == 3 ? 8 : (KT <= 5 ? 6 : 0))));
        if (KT == 4 && c.wid == 7) wait_bar<W + 16>(); else wait_bar<W>();
        stamp();
        after(KT);
        constexpr int d = ((KT + 1) % 3 - KT % 3) * 49152;
#pragma unroll
        for (int i = 0; i < 4; ++i) g.a[i] += d;
        g.w[0] += d; g.w[1] += d;
        kstep<1, 4, false, 0, 3>(acc, f1, f0, g, [&](int i) { if (KT + 3 < 8 && i < 3) issueQ(c, KT + 3, i); });
    } else kstep<1, 4, false, -1, 3>(acc, f1, f0, g, NoHook{});
}

DI void proj_setup(ProjCtx& c, const u16* X, const u16* Ckv, const u16* Wq, const u16* Wuk, const u16* Wuv, u16* Kf, u16* Vf, unsigned lds0, int h, int j, int wid, int lane) {
    const int r32 = lane & 31, hi = lane >> 5, wr = wid >> 2, wc = wid & 3;
    c.wid = wid; c.isV = wc >> 1; c.lds0 = lds0;
    c.srdC = mk_srd(Ckv, 2097152); c.srdX = mk_srd(X, 2097152);
    c.srdW = mk_srd((wid < 4 ? Wuk : Wuv), 4194304); c.srdWq = mk_srd(Wq, 4194304);
    c.srdO = mk_srd((const char*)(c.isV ? Vf : Kf) + (size_t)h * 524288, 524288);
#pragma unroll
    for (int p = 0; p < 2; ++p) {
        c.va[p] = (lane >> 3) * 1024 + (((lane & 7) ^ (4 * p + (lane >> 4))) * 16);
        c.vw[p] = ((lane >> 2) & 7) * 8192 + (4 * (lane >> 5) + ((lane & 3) ^ ((2 * p + ((lane >> 4) & 1)) & 3))) * 16;
    }
    c.vwq = (wid & 1) ? c.vw[1] : c.vw[0];
    c.l128 = lane * 128;
    c.sA = (256 * j + 32 * wid) * 1024;
    c.sW = (16 * (wid & 3)) * 8192 + h * 256;
    c.sXa = (wid < 4 ? 128 * (15 - j) + 32 * wid : 128 * j + 32 * (wid - 4)) * 1024;
    c.sWq = (8 * wid) * 8192 + h * 256;
    c.vo = c.isV ? hi * 2048 + r32 * 16 + (4 * j + 2 * wr) * 16384 + (wc & 1) * 1024 : hi * 1024 + r32 * 16 + (4 * j + 2 * wr) * 16384 + (wc & 1) * 8192;
}
template <class Touch, class IssueB1>
DI void kv_run(const ProjCtx& c, int lane, u32x4 (&pk)[16], Touch touchk, IssueB1 issueB1, Stamper& stamp) {
    const int r32 = lane & 31, hi = lane >> 5, wid = c.wid, wr = wid >> 2, wc = wid & 3;
    const int q4 = (lane & 15) >> 2, p4 = lane & 3, blk = (lane >> 4) & 1;
    GAddr g;
#pragma unroll
    for (int s = 0; s < 4; ++s) g.a[s] = c.lds0 + (128 * wr + r32) * 128 + (((2 * s + hi) ^ ((r32 >> 1) & 7)) << 4);
#pragma unroll
    for (int t = 0; t < 2; ++t) g.w[t] = c.lds0 + 32768 + (wc >> 1) * 16384 + 2048 * hi + 1024 * (wc & 1) + 64 * (4 * t + q4) + 16 * ((2 * blk + (p4 >> 1)) ^ ((2 * hi + t) & 3)) + 8 * (p4 & 1);
    if (c.isV) kv_part<true>(c, g, pk, touchk, issueB1, stamp); else kv_part<false>(c, g, pk, touchk, issueB1, stamp);
}
template <class Pub>
DI void q_run(const ProjCtx& c, int lane, const u32x4 (&pk)[16], bf16x8 (&q)[8], Pub publish, Stamper& stamp) {
    const int r32 = lane & 31, hi = lane >> 5, wid = c.wid;
    const int q4 = (lane & 15) >> 2, p4 = lane & 3, blk = (lane >> 4) & 1;
    GAddr g;
#pragma unroll
    for (int s = 0; s < 4; ++s) g.a[s] = c.lds0 + (32 * wid + r32) * 128 + (((2 * s + hi) ^ ((r32 >> 1) & 7)) << 4);
#pragma unroll
    for (int t = 0; t < 2; ++t) g.w[t] = c.lds0 + 32768 + 2048 * hi + 64 * (4 * t + q4) + 16 * ((2 * blk + (p4 >> 1)) ^ ((2 * hi + t) & 3)) + 8 * (p4 & 1);
    f32x16 acc[1][4];
#pragma unroll
    for (int a = 0; a < 4; ++a) acc[0][a] = f32x16{};
    Frags<1, 4> f0, f1;
#pragma unroll
    for (int i = 0; i < 9; ++i) frag_read<1, 4, 0>(f0, g, i);
    q_tile<0>(c, g, acc, f0, f1, pk, publish, stamp); q_tile<1>(c, g, acc, f0, f1, pk, publish, stamp); q_tile<2>(c, g, acc, f0, f1, pk, publish, stamp); q_tile<3>(c, g, acc, f0, f1, pk, publish, stamp);
    q_tile<4>(c, g, acc, f0, f1, pk, publish, stamp); q_tile<5>(c, g, acc, f0, f1, pk, publish, stamp); q_tile<6>(c, g, acc, f0, f1, pk, publish, stamp); q_tile<7>(c, g, acc, f0, f1, pk, publish, stamp);
    constexpr float QSC = 0.08838834764831845f * 1.4426950408889634f;
#pragma unroll
    for (int nb = 0; nb < 4; ++nb)
#pragma unroll
        for (int s = 0; s < 2; ++s) { u32x4 w;
#pragma unroll
            for (int e = 0; e < 4; ++e) w[e] = cvtpk(acc[0][nb][8 * s + 2 * e] * QSC, acc[0][nb][8 * s + 2 * e + 1] * QSC);
            q[2 * nb + s] = __builtin_bit_cast(bf16x8, w); }
}
template <int OFF, bool SETM0> DI void dma_piece(unsigned m0v, unsigned voff, srd_t srd, unsigned soff) {
    if constexpr (SETM0) asm volatile("s_mov_b32 m0, %0\n\ts_nop 0\n\tbuffer_load_dwordx4 %1, %2, %3 offen offset:%4 sc1 lds" :: "s"(m0v), "v"(voff), "s"(srd), "s"(soff), "i"(OFF) : "memory");
    else asm volatile("buffer_load_dwordx4 %0, %1, %2 offen offset:%3 sc1 lds" :: "v"(voff), "s"(srd), "s"(soff), "i"(OFF) : "memory");
}
#define ATT_THRV 64.f
constexpr float ATT_THR = ATT_THRV;
#define ATT_WIN 6
constexpr int ATT_W = ATT_WIN;

struct AttnState { f32x16 o[4]; f32x16 s0, s1, negm; bf16x8 q[8]; u32x4 p[4]; float l, mhat; };
struct AttnCtx {
    srd_t srdK, srdV, srdC; unsigned voff, lane16, kslot0, vslot0, coffL;
    int g, nl  , thrg, dq0, relH, jump; unsigned qstash;
    unsigned soA, soB; int rel;
};

template <bool DO_QK, bool DO_PV, int KP, int VP>
DI void m_phase(AttnState& st, unsigned kbase, unsigned vbase, bf16x8 (&pre)[ATT_W]) {
    constexpr int N0 = DO_QK ? 0 : 16, N1 = DO_PV ? 32 : 16;
    bf16x8 fr[32];
#pragma unroll
    for (int i = 0; i < ATT_W; ++i) fr[N0 + i] = pre[i];
#pragma unroll
    for (int i = N0; i < N1; ++i) {
        const int n = i + ATT_W;
        if (n < N1) { if (n < 16) fr[n] = ldsf(kbase + KP * 16384 + (n >> 1) * 2048 + (n & 1) * 512); else fr[n] = ldsf(vbase + VP * 16384 + ((n - 16) >> 2) * 4096 + ((n - 16) & 3) * 512); }
        SBAR();
        if (i < 16) { const int s = i >> 1;
            if (i == 0) asm volatile("v_mfma_f32_32x32x16_bf16 %0, %2, %3, %1" : "=&v"(st.s0), "+v"(st.negm) : "v"(fr[i]), "v"(st.q[0]));
            else if (i == 1) asm volatile("v_mfma_f32_32x32x16_bf16 %0, %2, %3, %1" : "=&v"(st.s1), "+v"(st.negm) : "v"(fr[i]), "v"(st.q[0]));
            else if ((i & 1) == 0) st.s0 = MFMA32(fr[i], st.q[s], st.s0);
            else              st.s1 = MFMA32(fr[i], st.q[s], st.s1);
        } else { const int ks = (i - 16) >> 2, db = (i - 16) & 3;
            st.o[db] = MFMA32(fr[i], __builtin_bit_cast(bf16x8, st.p[ks]), st.o[db]); }
        SBAR();
    }
    PIN(st.negm);
}

DI void store_ctx(const f32x16 (&o)[4], float inv, srd_t srd, unsigned voff) {
#pragma unroll
    for (int db = 0; db < 4; ++db)
#pragma unroll
        for (int s = 0; s < 2; ++s) {
            u32x4 w;
#pragma unroll
            for (int e = 0; e < 4; ++e) w[e] = cvtpk(o[db][8 * s + 2 * e] * inv, o[db][8 * s + 2 * e + 1] * inv);
            __builtin_amdgcn_raw_buffer_store_b128(w, srd, voff + (4 * db + 2 * s) * 512, 0, 0);
        }
}

template <bool LAST, int PAR>
DI void v_phase(AttnState& st, AttnCtx& c, const int t) {
    const bool sw = (t == c.nl);
    const unsigned vm0 = c.vslot0 + (PAR ^ 1) * 16384, km0 = c.kslot0 + PAR * 16384;
    const unsigned vso = c.soA, kso = c.soB;
    SBAR();
    if (__builtin_expect(sw, 0)) { const float lt = swap_add(st.l); store_ctx(st.o, __builtin_amdgcn_rcpf(lt), c.srdC, c.lane16 + c.coffL);
#pragma unroll
        for (int d = 0; d < 4; ++d) st.o[d] = f32x16{};
        st.l = 0.f; c.rel = c.relH; }
    if (__builtin_expect(c.rel > c.thrg, 0)) {
        const int u = 32 * c.g - c.rel; const float NEG = -__builtin_inff();
        if (u == 32) {
#pragma unroll
            for (int r = 0; r < 16; ++r) if ((r & 3) + 8 * (r >> 2) > c.dq0) st.s1[r] = NEG;
        } else {
            if (u == 0) {
#pragma unroll
                for (int r = 0; r < 16; ++r) if ((r & 3) + 8 * (r >> 2) > c.dq0) st.s0[r] = NEG;
            } else {
#pragma unroll
                for (int r = 0; r < 16; ++r) st.s0[r] = NEG; }
#pragma unroll
            for (int r = 0; r < 16; ++r) st.s1[r] = NEG;
        } }
    SBAR();
    if (!LAST) dma_piece<0, true>(vm0, c.voff, c.srdV, vso);
#define MX3(a, b, c) __builtin_fmaxf(__builtin_fmaxf((a), (b)), (c))
    float a = MX3(st.s0[0], st.s0[1], st.s1[0]), b = MX3(st.s0[2], st.s0[3], st.s1[1]);
    a = MX3(a, st.s1[2], st.s1[3]);
#pragma unroll
    for (int r = 4; r < 16; r += 4) {
        a = MX3(a, st.s0[r], st.s0[r + 1]); b = MX3(b, st.s0[r + 2], st.s0[r + 3]);
        a = MX3(a, st.s1[r], st.s1[r + 1]); b = MX3(b, st.s1[r + 2], st.s1[r + 3]); }
    const float rmax = __builtin_fmaxf(a, b);
    const unsigned long long firstm = (t == 0 || sw) ? ~0ull : 0ull;
    if (__builtin_expect((__builtin_amdgcn_ballot_w64(rmax > ATT_THR) | firstm) != 0ull, 0)) {
        const bool first = firstm != 0ull;
        const float m = swap_max(rmax);
        const float delta = __builtin_fmaxf(m, first ? -1e30f : 0.f);
        st.mhat = (first ? 0.f : st.mhat) + delta;
#pragma unroll
        for (int r = 0; r < 16; ++r) { st.s0[r] -= delta; st.s1[r] -= delta; st.negm[r] = -st.mhat; }
        if (!first) { const float f = __builtin_amdgcn_exp2f(-delta); st.l *= f;
#pragma unroll
            for (int d = 0; d < 4; ++d)
#pragma unroll
                for (int r = 0; r < 16; ++r) st.o[d][r] *= f; }
    }
    SBAR();
    float e[32];
#define EXPG(k) do { _Pragma("unroll") for (int i_ = 0; i_ < 4; ++i_) e[4 * (k) + i_] = __builtin_amdgcn_exp2f((k) < 4 ? st.s0[4 * (k) + i_] : st.s1[4 * ((k) - 4) + i_]); } while (0)
    float l0 = st.l, l1 = 0.f, l2 = 0.f, l3 = 0.f;
#define FING(k) do { l0 += e[4 * (k)]; if ((k) == 0) { l1 = e[1]; l2 = e[2]; l3 = e[3]; } else { l1 += e[4 * (k) + 1]; l2 += e[4 * (k) + 2]; l3 += e[4 * (k) + 3]; } \
        unsigned w0_ = cvtpk(e[4 * (k)], e[4 * (k) + 1]), w1_ = cvtpk(e[4 * (k) + 2], e[4 * (k) + 3]); PIN(w0_); PIN(w1_); PIN(l0); PIN(l1); PIN(l2); PIN(l3); \
        st.p[(k) >> 1][2 * ((k) & 1)] = w0_; st.p[(k) >> 1][2 * ((k) & 1) + 1] = w1_; } while (0)
    EXPG(0); SBAR();
    if (!LAST) dma_piece<1024, false>(vm0, c.voff, c.srdV, vso);
    EXPG(1); FING(0); SBAR();
    if (!LAST) dma_piece<2048, false>(vm0, c.voff, c.srdV, vso);
    EXPG(2); FING(1); SBAR();
    if (!LAST) dma_piece<3072, false>(vm0, c.voff, c.srdV, vso);
    EXPG(3); FING(2); SBAR();
    if (!LAST) dma_piece<0, true>(km0, c.voff, c.srdK, kso);
    EXPG(4); FING(3); SBAR();
    if (!LAST) dma_piece<1024, false>(km0, c.voff, c.srdK, kso);
    EXPG(5); FING(4); SBAR();
    if (!LAST) dma_piece<2048, false>(km0, c.voff, c.srdK, kso);
    EXPG(6); FING(5); SBAR();
    if (!LAST) dma_piece<3072, false>(km0, c.voff, c.srdK, kso);
    EXPG(7); FING(6); SBAR();
    FING(7);
    st.l = (l0 + l1) + (l2 + l3);
#undef EXPG
#undef FING
    if (!LAST) {
        if (__builtin_expect(t == c.nl - 1, 0)) {
#pragma unroll
            for (int s = 0; s < 8; ++s) st.q[s] = ldsf(c.qstash + s * 1024);
            st.negm = f32x16{};
        }
        c.soA = c.soB; c.soB += (t + 3 == c.nl) ? c.jump : 16384; c.rel += 64;
    }
    SBAR();
}

DI void attn_setup(AttnCtx& c, const u16* Kf, const u16* Vf, u16* Cf, unsigned lds0, int h, int j, int wid, int lane) {
    const int grp = wid >> 2; c.g = wid & 3; c.dq0 = (lane & 31) - 4 * (lane >> 5);
    const int qbH = 15 - j, qbL = j, nl = 2 * j + 2;
    c.nl = grp ? nl : 1000; c.thrg = 32 * c.g - 63; c.jump = (18 - nl) * 16384; c.relH = 64 * (17 - 2 * qbH);
    c.rel = grp ? -128 * qbL : -128 * qbH;
    c.soA = 16384; c.soB = (c.nl == 2) ? 17 * 16384 : 2 * 16384;
    c.srdK = mk_srd((const char*)Kf + (size_t)h * 524288, 524288);
    c.srdV = mk_srd((const char*)Vf + (size_t)h * 524288, 524288);
    c.srdC = mk_srd((const char*)Cf, 16777216);
    c.lane16 = lane * 16; c.voff = lane * 16 + c.g * 4096;
    c.kslot0 = lds0 + grp * 65536 + c.g * 4096; c.vslot0 = c.kslot0 + 32768;
    c.coffL = ((qbL * 4 + c.g) * 32 + h) * 8192;
    c.qstash = lds0 + 131072 + c.g * 8192 + lane * 16;
}
#define DMA4(m_, vo_, srd, so_) do { dma_piece<0, true>(m_, vo_, srd, so_); dma_piece<1024, false>(m_, vo_, srd, so_); dma_piece<2048, false>(m_, vo_, srd, so_); dma_piece<3072, false>(m_, vo_, srd, so_); } while (0)
DI void attn_prime(const AttnCtx& c, int grp) { DMA4(grp ? c.vslot0 + 16384 : c.kslot0, c.voff, c.srdK, 0u); DMA4(c.vslot0, c.voff, c.srdV, 0u); if (grp == 0) DMA4(c.kslot0 + 16384, c.voff, c.srdK, 16384u); }

template <class First, class G0Tail>
DI void attn_run(AttnState& st, AttnCtx& c, unsigned lds0, int h, int j, int wid, int lane, First afterFirst, G0Tail g0tail, Stamper& stamp) {
    const int grp = wid >> 2;
    const unsigned kbase = lds0 + grp * 65536 + (lane >> 5) * 1024 + (lane & 31) * 16;
    const unsigned vbase = lds0 + grp * 65536 + 32768 + (lane >> 5) * 2048 + (lane & 31) * 16;
#pragma unroll
    for (int d = 0; d < 4; ++d) st.o[d] = f32x16{};
    st.negm = f32x16{}; st.l = 0.f; st.mhat = 0.f;
    PIN(st.negm);
    if (grp == 1) { DMA4(c.kslot0 + 16384, c.voff, c.srdK, 16384u); BARRIER(); }
    wait_bar<8>();
    stamp();
    bf16x8 pre[ATT_W];
#define PRE_K(P) do { _Pragma("unroll") for (int i = 0; i < ATT_W; ++i) pre[i] = ldsf(kbase + (P) * 16384 + (i >> 1) * 2048 + (i & 1) * 512); } while (0)
    const unsigned kbase0 = grp ? kbase + 49152 : kbase;
#pragma unroll
    for (int i = 0; i < ATT_W; ++i) pre[i] = ldsf(kbase0 + (i >> 1) * 2048 + (i & 1) * 512);
    __builtin_amdgcn_s_setprio(1);
    m_phase<true, false, 0, 0>(st, kbase0, vbase, pre);
    __builtin_amdgcn_s_setprio(0);
    wait_bar<0>();
    afterFirst();
#define STEP(PAR, t) do { \
        v_phase<false, PAR>(st, c, t); \
        PRE_K(PAR ^ 1); \
        asm volatile("s_barrier" ::: "memory"); \
        __builtin_amdgcn_s_setprio(1); \
        m_phase<true, true, PAR ^ 1, PAR>(st, kbase, vbase, pre); \
        __builtin_amdgcn_s_setprio(0); \
        wait_bar<0>(); } while (0)
    for (int t = 0; t < 16; t += 2) { stamp(); STEP(0, t); STEP(1, t + 1); }
    stamp();
    v_phase<true, 0>(st, c, 16);
#pragma unroll
    for (int i = 0; i < ATT_W; ++i) pre[i] = ldsf(vbase + (i >> 2) * 4096 + (i & 3) * 512);
    asm volatile("s_barrier" ::: "memory");
    __builtin_amdgcn_s_setprio(1);
    m_phase<false, true, 0, 0>(st, kbase, vbase, pre);
    __builtin_amdgcn_s_setprio(0);
    wait_bar<0>();
    stamp();
    const float lt = swap_add(st.l);
    const unsigned xo = lds0 + c.g * 16384 + lane * 16;
    LAS f32x2* xs = (LAS f32x2*)(lds0 + 65536 + c.g * 512 + lane * 8);
    if (grp == 0) {
#pragma unroll
        for (int d = 0; d < 4; ++d)
#pragma unroll
            for (int rq = 0; rq < 4; ++rq) *(LAS f32x4*)(xo + (d * 4 + rq) * 1024) = (f32x4){st.o[d][4 * rq], st.o[d][4 * rq + 1], st.o[d][4 * rq + 2], st.o[d][4 * rq + 3]};
        *xs = (f32x2){st.mhat, lt};
        BARRIER();
        g0tail();
    } else {
        const f32x2 o0 = *xs;
        const float m = __builtin_fmaxf(o0[0], st.mhat);
        const float f0 = __builtin_amdgcn_exp2f(o0[0] - m), f1 = __builtin_amdgcn_exp2f(st.mhat - m);
        const float inv = __builtin_amdgcn_rcpf(o0[1] * f0 + lt * f1), a0 = f0 * inv, a1 = f1 * inv;
#pragma unroll
        for (int d = 0; d < 4; ++d)
#pragma unroll
            for (int rq = 0; rq < 4; ++rq) { const f32x4 x = *(const LAS f32x4*)(xo + (d * 4 + rq) * 1024);
#pragma unroll
                for (int e = 0; e < 4; ++e) st.o[d][4 * rq + e] = st.o[d][4 * rq + e] * a1 + x[e] * a0; }
        store_ctx(st.o, 1.0f, c.srdC, c.lane16 + (((15 - j) * 4 + c.g) * 32 + h) * 8192);
    }
    stamp();
#undef PRE_K
#undef STEP
}
constexpr int OUT_LDS = 163840;
constexpr unsigned OUT_O = 131072;
struct OutCtx { srd_t srdC, srdW, srdB; unsigned lane16, vw0, vwA, sC, sW, sB, lds0; int wid, rot; };
DI void out_issueA(const OutCtx& c, int kt, int i) {
    const unsigned st = c.lds0 + (kt & 3) * 32768;
    const int kr = (kt + c.rot) & 15;
    if (i < 2) { const int p = 2 * c.wid + i;
        dma1(st + p * 1024, c.lane16, c.srdC, c.sC + (p >> 2) * 262144 + (p & 3) * 1024 + kr * 4096); }
    else dma1(st + 16384 + (2 * c.wid + (i - 2)) * 1024, c.vwA, c.srdW, c.sW + (8 * c.wid) * 1024 + (i - 2) * 128 + kr * 65536);
}
DI void out_issueB(const OutCtx& c, int ch, int i) {
    const int rg = 2 * c.wid + (i >> 1);
    dma1(c.lds0 + ch * 32768 + rg * 2048 + (i & 1) * 1024, (i >> 1) ? (c.vw0 ^ 32u) : c.vw0, c.srdB, c.sB + (ch * 128 + 8 * rg) * 1024 + (i & 1) * 128);
}
DI u32x4 out_pack(f32x4 s0, f32x4 s1) {
    unsigned a0 = cvtpk(s0[0], s0[1]), a1 = cvtpk(s0[2], s0[3]), b0 = cvtpk(s1[0], s1[1]), b1 = cvtpk(s1[2], s1[3]);
    auto x0 = __builtin_amdgcn_permlane32_swap(a0, b0, false, false); auto x1 = __builtin_amdgcn_permlane32_swap(a1, b1, false, false);
    return (u32x4){x0[0], x1[0], x0[1], x1[1]};
}
DI f32x4 quad(const f32x16& x, int rq) { return (f32x4){x[4 * rq], x[4 * rq + 1], x[4 * rq + 2], x[4 * rq + 3]}; }

__global__ __launch_bounds__(512, 2) void outp_kernel(const u16* __restrict__ Wo, const u16* __restrict__ Bwo, u16* __restrict__ out, char* __restrict__ ws) {
    extern __shared__ __attribute__((aligned(16))) char lds_[];
    const int tid = threadIdx.x, lane = tid & 63, r32 = lane & 31, hi = lane >> 5;
    const int wid = rfl(tid >> 6), wr = wid >> 2, wc = wid & 3;
    const int bid = blockIdx.x, x = bid & 7, u = bid >> 3, s = x >> 1, rb = 8 * (x & 1) + (u >> 2), cb = u & 3, tile = rb * 4 + cb;
    const unsigned lds0 = (unsigned)(uintptr_t)lds_;
    Stamper stamp{0u, 0, lane};
    stamp();
    OutCtx c; c.wid = wid; c.lds0 = lds0; c.lane16 = lane * 16;
    c.srdC = mk_srd(ws + WS_CF + (size_t)rb * 4 * 262144, 4 * 262144); c.srdW = mk_srd(Wo, 4194304); c.srdB = mk_srd(Bwo, 524288);
    c.vw0 = ((lane >> 2) & 7) * 1024 + (4 * (lane >> 5) + ((lane & 3) ^ ((lane >> 4) & 1))) * 16; c.vwA = (wid & 1) ? (c.vw0 ^ 32u) : c.vw0;
    c.rot = 4 * cb + 2 * (rb & 7);
    c.sC = s * 8 * 8192; c.sW = s * 1024 * 1024 + cb * 256; c.sB = cb * 256;
#pragma unroll
    for (int kt = 0; kt < 4; ++kt)
#pragma unroll
        for (int i = 0; i < 4; ++i) out_issueA(c, kt, i);
    { const srd_t srdO = mk_srd(ws + WS_OF + (size_t)(rb * 4 + s) * 32768, 32768);
#pragma unroll
      for (int i = 0; i < 4; ++i) dma1(lds0 + OUT_O + (4 * wid + i) * 1024, c.lane16, srdO, (unsigned)((4 * wid + i) * 1024)); }
    u64* flags = (u64*)(ws + WS_FLAG);
    const u64 epoch = *(const u64 __attribute__((address_space(4)))*)(flags + (FL_XEP + tile) * FL_STRIDE);
    u64 tok = mix64(epoch); if (tok == epoch) tok ^= 1ull;
    const int q4 = (lane & 15) >> 2, p4 = lane & 3, blk = (lane >> 4) & 1;
    const unsigned aaddr = lds0 + (2 * wr) * 4096 + c.lane16;
    unsigned waddr[2];
#pragma unroll
    for (int t = 0; t < 2; ++t) waddr[t] = lds0 + 2048 * t + 512 * wc + 64 * (4 * hi + q4) + 16 * ((2 * blk + (p4 >> 1)) ^ ((2 * t + hi) & 3)) + 8 * (p4 & 1);
    f32x16 acc[2] = {f32x16{}, f32x16{}};
    stamp();
    struct Fr { bf16x8 a[2][2]; bf16x4 w[2][2]; };
    auto rd = [&](Fr& f, unsigned st, int half) {
#pragma unroll
        for (int k = 0; k < 2; ++k) { const int ks = 2 * half + k;
            f.w[k][0] = ldstr(waddr[0] + st + 16384 + ks * 4096); f.w[k][1] = ldstr(waddr[1] + st + 16384 + ks * 4096);
            f.a[k][0] = ldsf(aaddr + st + ks * 1024); f.a[k][1] = ldsf(aaddr + st + 4096 + ks * 1024); } };
    Fr f0, f1;
    wait_bar<16>();
    rd(f0, 0u, 0);
    auto step = [&](int kt, auto late, auto early) {
        const unsigned st = (kt & 3) * 32768, stn = ((kt + 1) & 3) * 32768;
        rd(f1, st, 1);
        SBAR();
#pragma unroll
        for (int k = 0; k < 2; ++k) { const bf16x8 wf = cat4(f0.w[k][0], f0.w[k][1]);
            acc[0] = MFMA32(wf, f0.a[k][0], acc[0]); acc[1] = MFMA32(wf, f0.a[k][1], acc[1]); SBAR(); late(2 + k); SBAR(); }
        wait_bar<8>();
        rd(f0, stn, 0);
        SBAR();
#pragma unroll
        for (int k = 0; k < 2; ++k) { const bf16x8 wf = cat4(f1.w[k][0], f1.w[k][1]);
            acc[0] = MFMA32(wf, f1.a[k][0], acc[0]); acc[1] = MFMA32(wf, f1.a[k][1], acc[1]); SBAR(); early(k); SBAR(); }
    };
    step(0, [&](int) {}, [&](int i) { out_issueA(c, 4, i); });
    for (int kt = 1; kt < 12; ++kt) step(kt, [&](int i) { out_issueA(c, kt + 3, i); }, [&](int i) { out_issueA(c, kt + 4, i); });
    step(12, [&](int i) { out_issueA(c, 15, i); }, [&](int i) { out_issueB(c, 0, i); });
    step(13, [&](int i) { out_issueB(c, 0, i); }, [&](int i) { out_issueB(c, 1, i); });
    step(14, [&](int i) { out_issueB(c, 1, i); }, [&](int i) { out_issueB(c, 2, i); });
    step(15, [&](int i) { out_issueB(c, 2, i); }, [&](int i) { out_issueB(c, 3, i); });
    out_issueB(c, 3, 2); out_issueB(c, 3, 3);
    stamp();
    const srd_t srdX = mk_srd(ws + WS_XCH + (size_t)tile * 262144, 262144);
#pragma unroll
    for (int mb = 0; mb < 2; ++mb) { const int q = 2 * wr + mb;
        if (q != s) {
#pragma unroll
            for (int rq = 0; rq < 4; ++rq) store16_sc1(__builtin_bit_cast(u32x4, quad(acc[mb], rq)), c.lane16, srdX, (unsigned)(((q * 4 + s) * 4 + wc) * 4096 + rq * 1024)); } }
    const int kh = (wr == (s >> 1)) ? 1 : 0;
    f32x16 accB = f32x16{};
#pragma unroll
    for (int ch = 0; ch < 4; ++ch) {
        if (ch == 0) { wait_bar<0>(); if (tid == 0) st_agent(flags + (FL_XF + tile * 4 + s) * FL_STRIDE, tok); }
        const unsigned st = ((16 + ch) & 3) * 32768;
        bf16x8 a[4]; bf16x4 w[4][2];
#pragma unroll
        for (int ks = 0; ks < 4; ++ks) { const int k = 4 * kh + ks;
            w[ks][0] = ldstr(waddr[0] + st + k * 4096); w[ks][1] = ldstr(waddr[1] + st + k * 4096); a[ks] = ldsf(lds0 + OUT_O + (8 * ch + k) * 1024 + c.lane16); }
#pragma unroll
        for (int ks = 0; ks < 4; ++ks) accB = MFMA32(cat4(w[ks][0], w[ks][1]), a[ks], accB);
    }
    stamp();
    BARRIER();
    const int row = rb * 128 + 32 * s + r32;
    if (kh == 1) {
#pragma unroll
        for (int rq = 0; rq < 4; ++rq) *(LAS f32x4*)(lds0 + (wc * 4 + rq) * 1024 + c.lane16) = quad(accB, rq);
        BARRIER();
        const u64* fp = flags + (FL_XF + tile * 4 + (lane & 3)) * FL_STRIDE;
        for (unsigned spins = 0; spins < (1u << 15); ++spins) { const u64 v = ld_agent(fp); if (__all(v == tok)) break; __builtin_amdgcn_s_sleep(1); }
        asm volatile("" ::: "memory");
        if (s == 0 && wc == 0 && lane == 0) st_agent(flags + (FL_XEP + tile) * FL_STRIDE, tok);
        f32x4 p[4][4];
#pragma unroll
        for (int q = 0; q < 4; ++q)
#pragma unroll
            for (int rq = 0; rq < 4; ++rq) p[q][rq] = __builtin_bit_cast(f32x4, __builtin_amdgcn_raw_buffer_load_b128(srdX, (q == s ? 0x7ffffff0u : c.lane16) + rq * 1024, ((s * 4 + q) * 4 + wc) * 4096, 16));
        f32x4 r[4];
#pragma unroll
        for (int rq = 0; rq < 4; ++rq) r[rq] = ((p[0][rq] + p[1][rq]) + (p[2][rq] + p[3][rq])) + ((s & 1) ? quad(acc[1], rq) : quad(acc[0], rq));
        u16* o = out + (size_t)row * 1024 + cb * 128 + 32 * wc + 8 * hi;
        *(u32x4*)o = out_pack(r[0], r[1]); *(u32x4*)(o + 16) = out_pack(r[2], r[3]);
    } else {
        BARRIER();
        f32x4 r[4];
#pragma unroll
        for (int rq = 0; rq < 4; ++rq) r[rq] = quad(accB, rq) + *(const LAS f32x4*)(lds0 + (wc * 4 + rq) * 1024 + c.lane16);
        u16* o = out + (size_t)row * 1024 + 512 + cb * 128 + 32 * wc + 8 * hi;
        *(u32x4*)o = out_pack(r[0], r[1]); *(u32x4*)(o + 16) = out_pack(r[2], r[3]);
    }
    stamp();
}
constexpr int FUSED_LDS = 163840;
constexpr unsigned WPHI_LDS = 151552;
struct FusedArgs { const u16 *X, *Ckv, *Wq, *Wuk, *Wuv, *Wo, *Bx, *Wqkv, *Wphi, *Bwo; char* ws; };

__global__ __launch_bounds__(512, 2) void fused_kernel(FusedArgs a) {
    extern __shared__ __attribute__((aligned(16))) char lds_[];
    const int tid = threadIdx.x, lane = tid & 63;
    const int wid = rfl(tid >> 6);
    const int bid = blockIdx.x, h = (bid & 7) * 4 + (bid >> 6), j = (bid >> 3) & 7;
    const unsigned lds0 = (unsigned)(uintptr_t)lds_;
    Stamper stamp{0u, 0, lane};
    stamp();
    u16* Kf = (u16*)(a.ws + WS_KF); u16* Vf = (u16*)(a.ws + WS_VF); u16* Cf = (u16*)(a.ws + WS_CF);
    u16* PhiQ = (u16*)(a.ws + WS_PHIQ); u16* Of = (u16*)(a.ws + WS_OF); float* Bst = (float*)(a.ws + WS_BST);
    u64* flags = (u64*)(a.ws + WS_FLAG);
    float* Bred = (float*)(a.ws + WS_BRED);
    const int hg = bid & 7, rc = bid >> 3;
    if (wid < 2) dma1(lds0 + WPHI_LDS + wid * 1024, lane * 16, mk_srd(a.Wphi, 16384), (unsigned)(hg * 2048 + wid * 1024));
    ProjCtx pc; proj_setup(pc, a.X, a.Ckv, a.Wq, a.Wuk, a.Wuv, Kf, Vf, lds0, h, j, wid, lane);
#pragma unroll
    for (int i = 0; i < 8; ++i) issueKV(pc, 0, i);
#pragma unroll
    for (int i = 0; i < 8; ++i) issueKV(pc, 1, i);
    const unsigned tsc = lds0 + TOUCH_LDS;
    { const unsigned vW = (lane >> 4) * 524288 + (lane & 15) * 128;
      const char* base; unsigned vo = pc.l128;
      if (wid < 4) { base = (const char*)((wid & 1) ? a.Wuv : a.Wuk) + ((wid & 2) ? 4 : 2) * 524288 + bid * 2048 - ((wid & 2) ? 0 : 0); vo = vW; }
      else if (wid == 4) base = (const char*)a.Ckv + bid * 8192;
      else { base = (const char*)a.Ckv; vo = 0; }
      touch_g<1>(tsc, vo, base); }
    const u64 epoch = *(const u64 __attribute__((address_space(4)))*)(flags + (FL_EPOCH + hg) * FL_STRIDE);
    u64 tok = mix64(epoch); if (tok == epoch) tok ^= 1ull;
    B1Ctx bc; b1_setup(bc, a.Bx, a.Wqkv, lds0, rc, hg, wid, lane);
    stamp();
    auto touchk = [&](int kt) {
        const char* base = (const char*)a.Ckv; unsigned vo = 0;
        if (kt == 0) {
            if (wid == 0) { base = (const char*)a.Bx + bid * 8192; vo = pc.l128; }
            else if (wid == 1) { base = (const char*)a.X + bid * 8192; vo = pc.l128; }
            else if (wid == 2) { base = (const char*)a.Wqkv + (bid < 192 ? bid : 0) * 8192; vo = pc.l128; }
        } else if (kt == 1) {
            if (wid < 2) { base = (const char*)a.Wq + (bid * 2 + wid) * 8192; vo = pc.l128; }
        } else if (kt == 2) {
            if (wid < 2) { base = (const char*)a.Wo + (bid * 2 + wid) * 8192; vo = pc.l128; }
            else if (wid == 2) { base = (const char*)a.Bwo + (bid & 63) * 8192; vo = pc.l128; }
        }
        touch_g<2>(tsc, vo, base); };
    u32x4 pk[16];
    kv_run(pc, lane, pk, touchk, [&](int kt) { b1_issue(bc, kt); }, stamp);
    stamp();
    B1Acc ba;
    b1_gemm(bc, ba, lane, [&](int n) { kv_store(pc, pk, n); },
            [&]() {
#pragma unroll
                for (int i = 0; i < 6; ++i) issueQ(pc, 0, i); },
            [&]() {
#pragma unroll
                for (int i = 0; i < 6; ++i) issueQ(pc, 1, i); }, stamp);
    stamp();
    u32x2 wp; { const int l16 = lane & 15, g = lane >> 4; const LAS u16* w = (const LAS u16*)(lds0 + WPHI_LDS + (wid & 3) * 512);
        wp[0] = (unsigned)w[(4 * g) * 16 + l16] | ((unsigned)w[(4 * g + 1) * 16 + l16] << 16); wp[1] = (unsigned)w[(4 * g + 2) * 16 + l16] | ((unsigned)w[(4 * g + 3) * 16 + l16] << 16); }
    b1_phi(ba, wp, PhiQ, Bst, lds0, rc, hg, wid, lane, stamp);
    stamp();
    wait_bar<10>();
    stamp();
#pragma unroll
    for (int i = 0; i < 6; ++i) issueQ(pc, 2, i);
    stamp();
    AttnState st;
    AttnCtx c; attn_setup(c, Kf, Vf, Cf, lds0, h, j, wid, lane);
    B2a b2;
    auto spin = [&](const u64* fp) {
        for (unsigned spins = 0; spins < (1u << 15); ++spins) { const u64 v = ld_agent(fp); if (__all(v == tok)) break; __builtin_amdgcn_s_sleep(1); }
        asm volatile("" ::: "memory"); };
    const u64* fkv = flags + (FL_KV + 8 * h + (lane & 7)) * FL_STRIDE;
    const u64* fbs = flags + (FL_BS + 32 * hg + (lane & 31)) * FL_STRIDE;
    u64 early = 0;
    q_run(pc, lane, pk, st.q, [&](int kt) {
        if (kt == 1) { if (tid == 0) st_agent(flags + (FL_BS + 32 * hg + rc) * FL_STRIDE, tok); }
        if (kt == 2) { if (wid == 7) early = ld_agent(fbs); }
        if (kt == 4) { if (tid == 0) st_agent(flags + (FL_KV + 8 * h + j) * FL_STRIDE, tok); }
        if (kt == 3) {
            if (wid == 7) {
                if (!__all(early == tok)) spin(fbs);
                asm volatile("" ::: "memory");
                if (rc == 0 && lane == 0) st_agent(flags + (FL_EPOCH + hg) * FL_STRIDE, tok);
                b2a_load(b2, Bst, rc, hg, lane); } }
        if (kt == 5) { if (wid == 7) b2a_finish(b2, Bred, rc, hg, lane); early = ld_agent(fkv); }
        if (kt == 6) {
            if (tid == 448) st_agent(flags + (FL_BR + 32 * hg + rc) * FL_STRIDE, tok);
            if (!__all(early == tok)) spin(fkv);
            asm volatile("" ::: "memory");
            attn_prime(c, wid >> 2);
        } }, stamp);
    stamp();
    BARRIER();
    if (wid < 4) {
#pragma unroll
        for (int s = 0; s < 8; ++s) *(LAS bf16x8*)(c.qstash + s * 1024) = st.q[s]; }
    stamp();
    attn_run(st, c, lds0, h, j, wid, lane,
             [&]() {},
             [&]() { b2b_body<4>(PhiQ, Bred, Of, flags + (FL_BR + 32 * hg + 8 * wid) * FL_STRIDE, tok, 4 * hg + wid, 4 * rc, lane); }, stamp);
}

extern "C" void kernel_launch(void* const* d_in, const int* in_sizes, int n_in, void* d_out, int out_size, void* d_ws, size_t ws_size, hipStream_t stream) {
    FusedArgs a;
    a.X = (const u16*)d_in[0]; a.Ckv = (const u16*)d_in[1]; a.Wq = (const u16*)d_in[2]; a.Wuk = (const u16*)d_in[3]; a.Wuv = (const u16*)d_in[4]; a.Wo = (const u16*)d_in[5];
    a.Bx = (const u16*)d_in[6]; a.Wqkv = (const u16*)d_in[7]; a.Wphi = (const u16*)d_in[8]; a.Bwo = (const u16*)d_in[9]; a.ws = (char*)d_ws;
    (void)hipFuncSetAttribute((const void*)fused_kernel, hipFuncAttributeMaxDynamicSharedMemorySize, FUSED_LDS);
    (void)hipFuncSetAttribute((const void*)outp_kernel, hipFuncAttributeMaxDynamicSharedMemorySize, OUT_LDS);
    fused_kernel<<<256, 512, FUSED_LDS, stream>>>(a);
outp_kernel<<<256, 512, OUT_LDS, stream>>>(a.Wo, a.Bwo, (u16*)d_out, a.ws);
}
```

```cpp
#include <hip/hip_runtime.h>
#include <stdint.h>

typedef unsigned short u16;
typedef unsigned long long u64;
typedef __bf16 bf16x8 __attribute__((ext_vector_type(8)));
typedef __bf16 bf16x4 __attribute__((ext_vector_type(4)));
typedef __bf16 bf16x2 __attribute__((ext_vector_type(2)));
typedef short s16x4 __attribute__((ext_vector_type(4)));
typedef float f32x16 __attribute__((ext_vector_type(16)));
typedef float f32x4 __attribute__((ext_vector_type(4)));
typedef float f32x2 __attribute__((ext_vector_type(2)));
typedef unsigned u32x4 __attribute__((ext_vector_type(4)));
typedef unsigned u32x2 __attribute__((ext_vector_type(2)));
typedef __amdgpu_buffer_rsrc_t srd_t;
#define LAS __attribute__((address_space(3)))
#define GAS __attribute__((address_space(1)))
#define SBAR() __builtin_amdgcn_sched_barrier(0)
#define PIN(x) asm volatile("" : "+v"(x))
#define MFMA32(a, b, c) __builtin_amdgcn_mfma_f32_32x32x16_bf16((a), (b), (c), 0, 0, 0)
#define MFMA16(a, b, c) __builtin_amdgcn_mfma_f32_16x16x32_bf16((a), (b), (c), 0, 0, 0)
#define DI __device__ __forceinline__
#pragma clang diagnostic ignored "-Wint-to-pointer-cast"

DI unsigned cvtpk(float lo, float hi) { f32x2 v = {lo, hi}; bf16x2 b = __builtin_convertvector(v, bf16x2); return __builtin_bit_cast(unsigned, b); }
DI unsigned rfl(unsigned x) { return __builtin_amdgcn_readfirstlane(x); }
DI float swap_max(float x) { auto rr = __builtin_amdgcn_permlane32_swap(__float_as_uint(x), __float_as_uint(x), false, false); return __builtin_fmaxf(__uint_as_float(rr[0]), __uint_as_float(rr[1])); }
DI float swap_add(float x) { auto rr = __builtin_amdgcn_permlane32_swap(__float_as_uint(x), __float_as_uint(x), false, false); return __uint_as_float(rr[0]) + __uint_as_float(rr[1]); }
DI srd_t mk_srd(const void* p, int bytes) { return __builtin_amdgcn_make_buffer_rsrc((void*)p, (short)0, bytes, 0x00020000); }
DI bf16x4 ldstr(unsigned a) { return __builtin_amdgcn_ds_read_tr16_b64_v4bf16((LAS bf16x4*)a); }
DI bf16x8 ldsf(unsigned a) { return *(const LAS bf16x8*)a; }
DI bf16x8 cat4(bf16x4 a, bf16x4 b) { return __builtin_shufflevector(a, b, 0, 1, 2, 3, 4, 5, 6, 7); }
#define SCALARIZE2(a, b) do {} while (0)
DI void dma1(unsigned m0v, unsigned voff, srd_t srd, unsigned soff) {
    SCALARIZE2(m0v, soff);
    asm volatile("s_mov_b32 m0, %0\n\ts_nop 0\n\tbuffer_load_dwordx4 %1, %2, %3 offen lds" :: "s"(m0v), "v"(voff), "s"(srd), "s"(soff) : "memory");
}
DI void dma1s(unsigned m0v, unsigned voff, srd_t srd, unsigned soff) {
    SCALARIZE2(m0v, soff);
    asm volatile("s_mov_b32 m0, %0\n\ts_nop 0\n\tbuffer_load_dwordx4 %1, %2, %3 offen sc1 lds" :: "s"(m0v), "v"(voff), "s"(srd), "s"(soff) : "memory");
}
DI void touch(unsigned m0v, unsigned voff, srd_t srd, unsigned soff) {
    SCALARIZE2(m0v, soff);
    asm volatile("s_mov_b32 m0, %0\n\ts_nop 0\n\tbuffer_load_dword %1, %2, %3 offen lds" :: "s"(m0v), "v"(voff), "s"(srd), "s"(soff) : "memory");
}
DI void store16_sc1(u32x4 w, unsigned voff, srd_t srd, unsigned soff) {
    unsigned z = soff; SCALARIZE2(soff, z);
    asm volatile("buffer_store_dwordx4 %0, %1, %2, %3 offen sc1\n\ts_nop 1" :: "v"(w), "v"(voff), "s"(srd), "s"(soff) : "memory");
}
template <int N> DI void wait_vm() { asm volatile("s_waitcnt vmcnt(%0)" :: "i"(N) : "memory"); }
template <int N> DI void wait_bar() { asm volatile("s_waitcnt vmcnt(%0) lgkmcnt(0)\n\ts_barrier" :: "i"(N) : "memory"); }
#define BARRIER() asm volatile("s_waitcnt lgkmcnt(0)\n\ts_barrier" ::: "memory")

struct Stamper { unsigned v; int n; int lane;
    DI void operator()() {
    } };

constexpr size_t WS_FLAG = 0;
constexpr size_t WS_KF = 1 << 20, WS_VF = WS_KF + (16 << 20), WS_CF = WS_VF + (16 << 20), WS_PHIQ = WS_CF + (16 << 20), WS_OF = WS_PHIQ + (4 << 20), WS_BST = WS_OF + (2 << 20), WS_BRED = WS_BST + (3 << 20), WS_XCH = WS_BRED + (1 << 20), WS_DBG = 200 << 20;
constexpr int FL_STRIDE = 16;
constexpr int FL_EPOCH = 0, FL_KV = 32, FL_BS = 32 + 256, FL_BR = 32 + 512, FL_XF = 32 + 768, FL_C = FL_XF + 256;
typedef GAS u64 gu64;
DI u64 ld_agent(const u64* p) { return __hip_atomic_load((gu64*)p, __ATOMIC_RELAXED, __HIP_MEMORY_SCOPE_AGENT); }
DI void st_agent(u64* p, u64 v) { __hip_atomic_store((gu64*)p, v, __ATOMIC_RELAXED, __HIP_MEMORY_SCOPE_AGENT); }
DI u64 mix64(u64 z) { z += 0x9E3779B97F4A7C15ull; z = (z ^ (z >> 30)) * 0xBF58476D1CE4E5B9ull; z = (z ^ (z >> 27)) * 0x94D049BB133111EBull; return z ^ (z >> 31); }
DI f32x4 mfma16_16(u32x2 a, u32x2 b, f32x4 c) { return __builtin_amdgcn_mfma_f32_16x16x16bf16_1k(__builtin_bit_cast(s16x4, a), __builtin_bit_cast(s16x4, b), c, 0, 0, 0); }
DI float rbf16(float x) { return __uint_as_float(cvtpk(x, 0.f) << 16); }
DI float x16_max(float x) { auto rr = __builtin_amdgcn_permlane16_swap(__float_as_uint(x), __float_as_uint(x), false, false); return __builtin_fmaxf(__uint_as_float(rr[0]), __uint_as_float(rr[1])); }
DI float x16_add(float x) { auto rr = __builtin_amdgcn_permlane16_swap(__float_as_uint(x), __float_as_uint(x), false, false); return __uint_as_float(rr[0]) + __uint_as_float(rr[1]); }
DI float grp_max(float x) { return swap_max(x16_max(x)); }
DI float grp_sum(float x) { return swap_add(x16_add(x)); }
constexpr float LOG2E = 1.4426950408889634f;
constexpr unsigned B1_RED = 131072;
struct B1Ctx { srd_t srdX, srdW; unsigned vx, vw[2], sx, sw; int wid; unsigned lds0; };
DI void b1_setup(B1Ctx& c, const u16* Bx, const u16* Wqkv, unsigned lds0, int rc, int hg, int wid, int lane) {
    c.srdX = mk_srd(Bx, 2097152); c.srdW = mk_srd(Wqkv, 512 * 1536 * 2); c.wid = wid; c.lds0 = lds0;
    c.vx = (lane >> 3) * 1024 + (((lane & 7) ^ ((4 * wid + (lane >> 4)) & 7)) * 16);
#pragma unroll
    for (int p = 0; p < 2; ++p) c.vw[p] = (lane >> 3) * 3072 + (((lane & 7) ^ (2 * (((lane >> 4) & 1) | (p << 1)))) * 16);
    c.sx = (64 * rc + 8 * wid) * 1024; c.sw = hg * 128;
}
DI void b1_issue(const B1Ctx& c, int kt) {
    const unsigned st = c.lds0 + (kt & 3) * 32768;
    dma1(st + c.wid * 1024, c.vx, c.srdX, c.sx + kt * 128);
#pragma unroll
    for (int i = 0; i < 3; ++i) { const int pw = 3 * c.wid + i, m = pw >> 3, pr = pw & 7;
        dma1(st + 8192 + pw * 1024, (pr & 1) ? c.vw[1] : c.vw[0], c.srdW, c.sw + (kt * 64 + 8 * pr) * 3072 + m * 1024); }
}
struct B1Acc { f32x4 aq[2], ak[2], av[2]; };
struct B1Fr { bf16x8 x[2][2]; bf16x4 w[2][3][2]; };
template <int KT> DI void b1_read(B1Fr& f, unsigned xa0, unsigned xa1, unsigned wa) {
    constexpr unsigned st = (KT & 3) * 32768;
#pragma unroll
    for (int ks = 0; ks < 2; ++ks) { const unsigned xa = ks ? xa1 : xa0;
        f.x[ks][0] = ldsf(xa + st); f.x[ks][1] = ldsf(xa + st + 2048);
#pragma unroll
        for (int m = 0; m < 3; ++m) { f.w[ks][m][0] = ldstr(wa + st + m * 8192 + ks * 4096); f.w[ks][m][1] = ldstr(wa + st + m * 8192 + ks * 4096 + 512); } }
}
DI void b1_mma(B1Acc& a, const B1Fr& f) {
#pragma unroll
    for (int ks = 0; ks < 2; ++ks) {
        const bf16x8 wq = cat4(f.w[ks][0][0], f.w[ks][0][1]), wk = cat4(f.w[ks][1][0], f.w[ks][1][1]), wv = cat4(f.w[ks][2][0], f.w[ks][2][1]);
        a.aq[0] = MFMA16(wq, f.x[ks][0], a.aq[0]); a.aq[1] = MFMA16(wq, f.x[ks][1], a.aq[1]); a.ak[0] = MFMA16(wk, f.x[ks][0], a.ak[0]); a.ak[1] = MFMA16(wk, f.x[ks][1], a.ak[1]);
        a.av[0] = MFMA16(f.x[ks][0], wv, a.av[0]); a.av[1] = MFMA16(f.x[ks][1], wv, a.av[1]);
    }
}
template <class St, class H6, class HE>
DI void b1_gemm(const B1Ctx& c, B1Acc& a, int lane, St store, H6 afterB6, HE afterEnd, Stamper& stamp) {
    const int l16 = lane & 15, g = lane >> 4, q4 = l16 >> 2, p4 = lane & 3, wid = c.wid;
    const int row = 32 * (wid >> 2) + l16;
    const unsigned xa0 = c.lds0 + row * 128 + (((0 + g) ^ ((row >> 1) & 7)) << 4), xa1 = c.lds0 + row * 128 + (((4 + g) ^ ((row >> 1) & 7)) << 4);
    const unsigned wa = c.lds0 + 8192 + (8 * g + q4) * 128 + (((wid & 3) ^ (((q4 >> 1) & 1) | ((g & 1) << 1))) * 32) + p4 * 8;
#pragma unroll
    for (int r = 0; r < 2; ++r) { a.aq[r] = f32x4{}; a.ak[r] = f32x4{}; a.av[r] = f32x4{}; }
    B1Fr f;
#define B1T(KT, PRE) do { b1_read<KT>(f, xa0, xa1, wa); SBAR(); PRE; store(KT); SBAR(); b1_mma(a, f); SBAR(); } while (0)
    B1T(0, (void)0);
    wait_bar<9>();  B1T(1, b1_issue(c, 4));
    wait_bar<10>(); B1T(2, b1_issue(c, 5));
    wait_bar<11>(); B1T(3, b1_issue(c, 6));
    wait_bar<11>(); B1T(4, b1_issue(c, 7));
    wait_bar<11>(); B1T(5, (void)0);
    wait_bar<7>();  B1T(6, afterB6());
    wait_bar<9>();  B1T(7, (void)0);
#undef B1T
    BARRIER(); afterEnd();
}
template <int N> DI void phi_map(const f32x4 (&p)[N], u32x4 (&out)[N]) {
    float x[N][4], mx[N], nm[N], ep[N][4], en[N][4], sp[N], sn[N];
#pragma unroll
    for (int i = 0; i < N; ++i) {
#pragma unroll
        for (int e = 0; e < 4; ++e) x[i][e] = rbf16(p[i][e]) * LOG2E;
        mx[i] = __builtin_fmaxf(__builtin_fmaxf(x[i][0], x[i][1]), __builtin_fmaxf(x[i][2], x[i][3])); nm[i] = -__builtin_fminf(__builtin_fminf(x[i][0], x[i][1]), __builtin_fminf(x[i][2], x[i][3])); }
#pragma unroll
    for (int i = 0; i < N; ++i) { mx[i] = x16_max(mx[i]); nm[i] = x16_max(nm[i]); }
#pragma unroll
    for (int i = 0; i < N; ++i) { mx[i] = swap_max(mx[i]); nm[i] = swap_max(nm[i]); }
#pragma unroll
    for (int i = 0; i < N; ++i) {
#pragma unroll
        for (int e = 0; e < 4; ++e) { ep[i][e] = __builtin_amdgcn_exp2f(x[i][e] - mx[i]); en[i][e] = __builtin_amdgcn_exp2f(-x[i][e] - nm[i]); }
        sp[i] = (ep[i][0] + ep[i][1]) + (ep[i][2] + ep[i][3]); sn[i] = (en[i][0] + en[i][1]) + (en[i][2] + en[i][3]); }
#pragma unroll
    for (int i = 0; i < N; ++i) { sp[i] = x16_add(sp[i]); sn[i] = x16_add(sn[i]); }
#pragma unroll
    for (int i = 0; i < N; ++i) { sp[i] = __builtin_amdgcn_rcpf(swap_add(sp[i])); sn[i] = __builtin_amdgcn_rcpf(swap_add(sn[i])); }
#pragma unroll
    for (int i = 0; i < N; ++i) out[i] = (u32x4){cvtpk(ep[i][0] * sp[i], ep[i][1] * sp[i]), cvtpk(ep[i][2] * sp[i], ep[i][3] * sp[i]), cvtpk(en[i][0] * sn[i], en[i][1] * sn[i]), cvtpk(en[i][2] * sn[i], en[i][3] * sn[i])};
}
DI void b1_phi(const B1Acc& a, u32x2 wp, u16* PhiQ, float* Bst, unsigned lds0, int rc, int hg, int wid, int lane, Stamper& stamp) {
    const int h = 4 * hg + (wid & 3), row0 = 64 * rc + 32 * (wid >> 2);
    const int l16 = lane & 15, g = lane >> 4;
    f32x4 sp = f32x4{}, sn = f32x4{}; float zp = 0.f, zn = 0.f;
    f32x4 pin[4]; u32x4 pout[4];
#pragma unroll
    for (int r = 0; r < 2; ++r) {
        const u32x2 qb = {cvtpk(a.aq[r][0], a.aq[r][1]), cvtpk(a.aq[r][2], a.aq[r][3])}, kb = {cvtpk(a.ak[r][0], a.ak[r][1]), cvtpk(a.ak[r][2], a.ak[r][3])};
        pin[r] = mfma16_16(wp, qb, f32x4{}); pin[2 + r] = mfma16_16(wp, kb, f32x4{});
    }
    phi_map<4>(pin, pout);
    const u32x4 wq[2] = {pout[0], pout[1]}, wk[2] = {pout[2], pout[3]};
    const unsigned tb = lds0 + B1_RED + wid * 2304;
#pragma unroll
    for (int r = 0; r < 2; ++r) {
        *(LAS u32x2*)(tb + r * 1024 + l16 * 32 + g * 8) = (u32x2){wk[r][0], wk[r][1]};
        *(LAS u32x2*)(tb + r * 1024 + 512 + l16 * 32 + g * 8) = (u32x2){wk[r][2], wk[r][3]};
        __builtin_amdgcn_raw_buffer_store_b128(wq[r], mk_srd(PhiQ, 4194304), ((h * 128 + (row0 >> 4) + r) * 64 + lane) * 16, 0, 0);
    }
    asm volatile("s_waitcnt lgkmcnt(0)" ::: "memory");
#pragma unroll
    for (int r = 0; r < 2; ++r) {
        const unsigned ta = tb + r * 1024 + (4 * g + (l16 >> 2)) * 32 + (lane & 3) * 8;
        const u32x2 pp = __builtin_bit_cast(u32x2, ldstr(ta)), pn = __builtin_bit_cast(u32x2, ldstr(ta + 512));
        const u32x2 vb = {cvtpk(a.av[r][0], a.av[r][1]), cvtpk(a.av[r][2], a.av[r][3])};
        sp = mfma16_16(pp, vb, sp); sn = mfma16_16(pn, vb, sn);
        zp += (__uint_as_float(pp[0] << 16) + __uint_as_float(pp[0] & 0xffff0000u)) + (__uint_as_float(pp[1] << 16) + __uint_as_float(pp[1] & 0xffff0000u));
        zn += (__uint_as_float(pn[0] << 16) + __uint_as_float(pn[0] & 0xffff0000u)) + (__uint_as_float(pn[1] << 16) + __uint_as_float(pn[1] & 0xffff0000u));
    }
    asm volatile("s_waitcnt lgkmcnt(0)" ::: "memory");
    zp = grp_sum(zp); zn = grp_sum(zn);
    LAS char* red = (LAS char*)(lds0 + B1_RED + wid * 2304);
    *(LAS f32x4*)(red + lane * 32) = sp; *(LAS f32x4*)(red + lane * 32 + 16) = sn;
    if (lane < 16) *(LAS f32x2*)(red + 2048 + lane * 8) = (f32x2){zp, zn};
    BARRIER();
    const int tid = wid * 64 + lane;
    const srd_t srd = mk_srd(Bst, 32 * 32 * 2304);
#pragma unroll
    for (int rep = 0; rep < 2; ++rep) { const int i = tid + rep * 512, ic = i < 576 ? i : 0, hh = ic / 144, ch = ic - hh * 144;
        const f32x4 s = *(const LAS f32x4*)(lds0 + B1_RED + hh * 2304 + ch * 16) + *(const LAS f32x4*)(lds0 + B1_RED + (hh + 4) * 2304 + ch * 16);
        store16_sc1(__builtin_bit_cast(u32x4, s), i < 576 ? ((4 * hg + hh) * 32 + rc) * 2304 + ch * 16 : 0x7fffffffu, srd, 0u); }
}
template <int N> DI void waitN(const u64* flags, u64 tok, int lane) {
    const u64* p = flags + (lane & (N - 1)) * FL_STRIDE;
    for (unsigned spins = 0; spins < (1u << 15); ++spins) { const u64 v = ld_agent(p); if (__all(v == tok)) break; __builtin_amdgcn_s_sleep(1); }
    asm volatile("" ::: "memory");
}
struct B2a { f32x4 v[16]; };
DI void b2a_load(B2a& b, const float* Bst, int rc, int hg, int lane) {
    const int h = 4 * hg + (rc >> 3), c = lane & 31, half = lane >> 5;
    const srd_t srd = mk_srd(Bst + (size_t)h * 32 * 576, 32 * 2304);
    const unsigned vo = (c < 18 ? (18 * (rc & 7) + c) * 16 : 0x7fffffff) + half * 16 * 2304;
#pragma unroll
    for (int i = 0; i < 16; ++i) b.v[i] = __builtin_bit_cast(f32x4, __builtin_amdgcn_raw_buffer_load_b128(srd, vo, i * 2304, 16));
}
DI void b2a_finish(const B2a& b, float* Bred, int rc, int hg, int lane) {
    const int h = 4 * hg + (rc >> 3), c = lane & 31, half = lane >> 5;
    f32x4 s = b.v[0];
#pragma unroll
    for (int i = 1; i < 16; ++i) s += b.v[i];
#pragma unroll
    for (int e = 0; e < 4; ++e) s[e] = swap_add(s[e]);
    const srd_t srdR = mk_srd(Bred + (size_t)h * 576, 2304);
    store16_sc1(__builtin_bit_cast(u32x4, s), (half == 0 && c < 18) ? (18 * (rc & 7) + c) * 16 : 0x7fffffffu, srdR, 0u);
}
template <int NRB>
DI void b2b_body(const u16* PhiQ, const float* Bred, u16* Of, const u64* flagsR, u64 tok, int h, int rb0, int lane) {
    const int l16 = lane & 15, g = lane >> 4;
    bf16x8 ph[NRB];
#pragma unroll
    for (int r = 0; r < NRB; ++r) ph[r] = *(const bf16x8*)((const char*)PhiQ + ((size_t)(h * 128 + rb0 + r) * 64 + lane) * 16);
    waitN<8>(flagsR, tok, lane);
    const srd_t srd = mk_srd(Bred + (size_t)h * 576, 2304);
    const f32x4 sp = __builtin_bit_cast(f32x4, __builtin_amdgcn_raw_buffer_load_b128(srd, lane * 32, 0, 16)), sn = __builtin_bit_cast(f32x4, __builtin_amdgcn_raw_buffer_load_b128(srd, lane * 32 + 16, 0, 16));
    const f32x4 za = __builtin_bit_cast(f32x4, __builtin_amdgcn_raw_buffer_load_b128(srd, 2048 + g * 32, 0, 16)), zb = __builtin_bit_cast(f32x4, __builtin_amdgcn_raw_buffer_load_b128(srd, 2048 + g * 32 + 16, 0, 16));
    const u32x4 kvw = {cvtpk(sp[0], sp[1]), cvtpk(sp[2], sp[3]), cvtpk(sn[0], sn[1]), cvtpk(sn[2], sn[3])};
    const u32x4 zw = {cvtpk(za[0], za[2]), cvtpk(zb[0], zb[2]), cvtpk(za[1], za[3]), cvtpk(zb[1], zb[3])};
    const srd_t srdO = mk_srd(Of, 2097152);
#pragma unroll
    for (int r = 0; r < NRB; ++r) { const int rb16 = rb0 + r;
        const f32x4 num = MFMA16(__builtin_bit_cast(bf16x8, kvw), ph[r], f32x4{});
        const f32x4 den = MFMA16(__builtin_bit_cast(bf16x8, zw), ph[r], f32x4{});
        const float dn = rbf16(rbf16(den[0]) + 1e-6f);
        const u32x2 w = {cvtpk(rbf16(num[0]) / dn, rbf16(num[1]) / dn), cvtpk(rbf16(num[2]) / dn, rbf16(num[3]) / dn)};
        __builtin_amdgcn_raw_buffer_store_b64(w, srdO, (((rb16 >> 1) * 32 + h) * 2 + (g & 1)) * 512 + ((rb16 & 1) * 16 + l16) * 16 + (g >> 1) * 8, 0, 16); }
}
template <int MB, int NB> struct Frags { bf16x8 a[MB]; bf16x4 w0[NB], w1[NB]; };
struct GAddr { unsigned a[4]; unsigned w[2]; };

template <int MB, int NB, int S> DI void frag_read(Frags<MB, NB>& f, const GAddr& g, int i) {
    if (i == 2) { f.a[0] = ldsf(g.a[S]); return; }
    const int k = i < 2 ? i : i - 1;
    if (k < 2 * NB) { const int nb = k >> 1; if (k & 1) f.w1[nb] = ldstr(g.w[1] + S * 4096 + nb * 512); else f.w0[nb] = ldstr(g.w[0] + S * 4096 + nb * 512); }
    else { const int mb = k - 2 * NB + 1; if (mb < MB) f.a[mb] = ldsf(g.a[S] + mb * 4096); }
}
template <int MB, int NB, bool ACT_A, int SN, int RPG, class Hook>
DI void kstep(f32x16 (&acc)[MB][NB], const Frags<MB, NB>& cur, Frags<MB, NB>& nxt, const GAddr& g, Hook hook) {
    constexpr int NM = MB * NB, NR = MB + 2 * NB;
#pragma unroll
    for (int i = 0; i < NM; ++i) {
        const int mb = i / NB, nb = i % NB;
        const bf16x8 w = cat4(cur.w0[nb], cur.w1[nb]);
        acc[mb][nb] = ACT_A ? MFMA32(cur.a[mb], w, acc[mb][nb]) : MFMA32(w, cur.a[mb], acc[mb][nb]);
        SBAR();
        if constexpr (SN >= 0) {
#pragma unroll
            for (int r = 0; r < RPG; ++r) if (i * RPG + r < NR) frag_read<MB, NB, SN < 0 ? 0 : SN>(nxt, g, i * RPG + r);
        }
        hook(i);
        SBAR();
    }
}
struct NoHook { DI void operator()(int) const {} };

struct ProjCtx {
    srd_t srdC, srdX, srdW, srdWq, srdO;
    unsigned va[2], vw[2], vwq, vo, lds0, l128;
    unsigned sA, sW, sWq, sXa;
    int wid, isV;
};
constexpr unsigned TOUCH_LDS = 163840 - 256;
DI void issueKV(const ProjCtx& c, int kt, int i) {
    const unsigned st = c.lds0 + (kt & 1) * 65536;
    if (i < 4) dma1(st + (4 * c.wid + i) * 1024, c.va[i & 1], c.srdC, c.sA + i * 8192 + kt * 128);
    else dma1(st + 32768 + (c.wid >> 2) * 16384 + (4 * (c.wid & 3) + (i - 4)) * 1024, c.vw[(i - 4) >> 1], c.srdW, c.sW + ((i - 4) >> 1) * 65536 + ((i - 4) & 1) * 128 + kt * 524288);
}
DI void issueQ(const ProjCtx& c, int kt, int i) {
    const unsigned st = c.lds0 + (kt % 3) * 49152;
    if (i < 4) dma1(st + (4 * c.wid + i) * 1024, c.va[i & 1], c.srdX, c.sXa + i * 8192 + kt * 128);
    else dma1(st + 32768 + (2 * c.wid + (i - 4)) * 1024, c.vwq, c.srdWq, c.sWq + (i - 4) * 128 + kt * 524288);
}

#define TOUCHMODE 3
template <int WHICH> DI void touch_g(unsigned m0v, unsigned voff, const void* base) {
    if (!(TOUCHMODE & WHICH)) voff = 0;
    const u64 b = (u64)base; const u64 sb = ((u64)rfl((unsigned)(b >> 32)) << 32) | rfl((unsigned)b);
    asm volatile("s_nop 4\n\ts_mov_b32 m0, %0\n\ts_nop 0\n\tglobal_load_lds_dword %1, %2" :: "s"(rfl(m0v)), "v"(voff), "s"(sb) : "memory");
}
template <bool ISV, class Touch, class IssueB1>
DI void kv_part(const ProjCtx& c, GAddr g, u32x4 (&pk)[16], Touch touchk, IssueB1 issueB1, Stamper& stamp) {
    f32x16 acc[4][2];
#pragma unroll
    for (int a = 0; a < 4; ++a) { acc[a][0] = f32x16{}; acc[a][1] = f32x16{}; }
    Frags<4, 2> f0, f1;
    wait_bar<9>();
    stamp();
#pragma unroll
    for (int i = 0; i < 8; ++i) frag_read<4, 2, 0>(f0, g, i);
    for (int kt = 0; kt < 6; ++kt) {
        kstep<4, 2, ISV, 1, 2>(acc, f0, f1, g, [&](int i) { if (kt > 0 && (i & 1)) issueKV(c, kt + 1, 4 + (i >> 1)); });
        kstep<4, 2, ISV, 2, 2>(acc, f1, f0, g, [&](int i) { if (i == 1) touchk(kt); });
        kstep<4, 2, ISV, 3, 2>(acc, f0, f1, g, NoHook{});
        wait_bar<1>();
#pragma unroll
        for (int i = 0; i < 4; ++i) g.a[i] ^= 65536u;
        g.w[0] ^= 65536u; g.w[1] ^= 65536u;
        kstep<4, 2, ISV, 0, 2>(acc, f1, f0, g, [&](int i) { if (i & 1) issueKV(c, kt + 2, i >> 1); });
    }
    kstep<4, 2, ISV, 1, 2>(acc, f0, f1, g, [&](int i) { if (i & 1) issueKV(c, 7, 4 + (i >> 1)); });
    kstep<4, 2, ISV, 2, 2>(acc, f1, f0, g, NoHook{});
    kstep<4, 2, ISV, 3, 2>(acc, f0, f1, g, NoHook{});
    stamp();
    wait_bar<0>();
    stamp();
#pragma unroll
    for (int i = 0; i < 4; ++i) g.a[i] ^= 65536u;
    g.w[0] ^= 65536u; g.w[1] ^= 65536u;
    kstep<4, 2, ISV, 0, 2>(acc, f1, f0, g, [&](int i) { if (i == 1) issueB1(0); if (i == 5) issueB1(1); });
    kstep<4, 2, ISV, 1, 2>(acc, f0, f1, g, NoHook{});
    kstep<4, 2, ISV, 2, 2>(acc, f1, f0, g, NoHook{});
    kstep<4, 2, ISV, 3, 2>(acc, f0, f1, g, NoHook{});
    stamp();
    wait_bar<4>();
    stamp();
    kstep<4, 2, ISV, -1, 2>(acc, f1, f0, g, [&](int i) { if (i == 1) issueB1(2); if (i == 5) issueB1(3); });
#pragma unroll
    for (int mb = 0; mb < 4; ++mb)
#pragma unroll
        for (int nb = 0; nb < 2; ++nb)
#pragma unroll
            for (int s = 0; s < 2; ++s)
#pragma unroll
                for (int e = 0; e < 4; ++e) pk[(mb * 2 + nb) * 2 + s][e] = cvtpk(acc[mb][nb][8 * s + 2 * e], acc[mb][nb][8 * s + 2 * e + 1]);
}
DI void kv_store(const ProjCtx& c, const u32x4 (&pk)[16], int n) {
    const int mb = n >> 2, nb = (n >> 1) & 1, s = n & 1;
    const unsigned offK = (mb >> 1) * 16384 + (mb & 1) * 512 + (4 * nb + 2 * s) * 1024, offV = (mb >> 1) * 16384 + (2 * (mb & 1) + s) * 4096 + nb * 512;
    store16_sc1(pk[n], c.vo, c.srdO, c.isV ? offV : offK);
}
template <int KT, class Pub>
DI void q_tile(const ProjCtx& c, GAddr& g, f32x16 (&acc)[1][4], Frags<1, 4>& f0, Frags<1, 4>& f1, const u32x4 (&pk)[16], Pub after, Stamper& stamp) {
    kstep<1, 4, false, 1, 3>(acc, f0, f1, g, [&](int i) { if (KT >= 1 && KT + 2 < 8 && i < 3) issueQ(c, KT + 2, 3 + i); });
    kstep<1, 4, false, 2, 3>(acc, f1, f0, g, [&](int i) { if (KT < 3 && i == 0) kv_store(c, pk, 8 + 3 * KT); if (KT < 2 && i == 3) kv_store(c, pk, 8 + 3 * KT + 2); });
    kstep<1, 4, false, 3, 3>(acc, f0, f1, g, [&](int i) { if (KT < 3 && i == 2) kv_store(c, pk, 8 + 3 * KT + 1); });
    if constexpr (KT < 7) {
        constexpr int W = KT == 0 ? 13 : (KT == 1 ? 12 : (KT == 2 ? 11 : (KT == 3 ? 8 : (KT <= 5 ? 6 : 0))));
        if (KT == 4 && c.wid == 7) wait_bar<W + 16>(); else wait_bar<W>();
        stamp();
        after(KT);
        constexpr int d = ((KT + 1) % 3 - KT % 3) * 49152;
#pragma unroll
        for (int i = 0; i < 4; ++i) g.a[i] += d;
        g.w[0] += d; g.w[1] += d;
        kstep<1, 4, false, 0, 3>(acc, f1, f0, g, [&](int i) { if (KT + 3 < 8 && i < 3) issueQ(c, KT + 3, i); });
    } else kstep<1, 4, false, -1, 3>(acc, f1, f0, g, NoHook{});
}

DI void proj_setup(ProjCtx& c, const u16* X, const u16* Ckv, const u16* Wq, const u16* Wuk, const u16* Wuv, u16* Kf, u16* Vf, unsigned lds0, int h, int j, int wid, int lane) {
    const int r32 = lane & 31, hi = lane >> 5, wr = wid >> 2, wc = wid & 3;
    c.wid = wid; c.isV = wc >> 1; c.lds0 = lds0;
    c.srdC = mk_srd(Ckv, 2097152); c.srdX = mk_srd(X, 2097152);
    c.srdW = mk_srd((wid < 4 ? Wuk : Wuv), 4194304); c.srdWq = mk_srd(Wq, 4194304);
    c.srdO = mk_srd((const char*)(c.isV ? Vf : Kf) + (size_t)h * 524288, 524288);
#pragma unroll
    for (int p = 0; p < 2; ++p) {
        c.va[p] = (lane >> 3) * 1024 + (((lane & 7) ^ (4 * p + (lane >> 4))) * 16);
        c.vw[p] = ((lane >> 2) & 7) * 8192 + (4 * (lane >> 5) + ((lane & 3) ^ ((2 * p + ((lane >> 4) & 1)) & 3))) * 16;
    }
    c.vwq = (wid & 1) ? c.vw[1] : c.vw[0];
    c.l128 = lane * 128;
    c.sA = (256 * j + 32 * wid) * 1024;
    c.sW = (16 * (wid & 3)) * 8192 + h * 256;
    c.sXa = (wid < 4 ? 128 * (15 - j) + 32 * wid : 128 * j + 32 * (wid - 4)) * 1024;
    c.sWq = (8 * wid) * 8192 + h * 256;
    c.vo = c.isV ? hi * 2048 + r32 * 16 + (4 * j + 2 * wr) * 16384 + (wc & 1) * 1024 : hi * 1024 + r32 * 16 + (4 * j + 2 * wr) * 16384 + (wc & 1) * 8192;
}
template <class Touch, class IssueB1>
DI void kv_run(const ProjCtx& c, int lane, u32x4 (&pk)[16], Touch touchk, IssueB1 issueB1, Stamper& stamp) {
    const int r32 = lane & 31, hi = lane >> 5, wid = c.wid, wr = wid >> 2, wc = wid & 3;
    const int q4 = (lane & 15) >> 2, p4 = lane & 3, blk = (lane >> 4) & 1;
    GAddr g;
#pragma unroll
    for (int s = 0; s < 4; ++s) g.a[s] = c.lds0 + (128 * wr + r32) * 128 + (((2 * s + hi) ^ ((r32 >> 1) & 7)) << 4);
#pragma unroll
    for (int t = 0; t < 2; ++t) g.w[t] = c.lds0 + 32768 + (wc >> 1) * 16384 + 2048 * hi + 1024 * (wc & 1) + 64 * (4 * t + q4) + 16 * ((2 * blk + (p4 >> 1)) ^ ((2 * hi + t) & 3)) + 8 * (p4 & 1);
    if (c.isV) kv_part<true>(c, g, pk, touchk, issueB1, stamp); else kv_part<false>(c, g, pk, touchk, issueB1, stamp);
}
template <class Pub>
DI void q_run(const ProjCtx& c, int lane, const u32x4 (&pk)[16], bf16x8 (&q)[8], Pub publish, Stamper& stamp) {
    const int r32 = lane & 31, hi = lane >> 5, wid = c.wid;
    const int q4 = (lane & 15) >> 2, p4 = lane & 3, blk = (lane >> 4) & 1;
    GAddr g;
#pragma unroll
    for (int s = 0; s < 4; ++s) g.a[s] = c.lds0 + (32 * wid + r32) * 128 + (((2 * s + hi) ^ ((r32 >> 1) & 7)) << 4);
#pragma unroll
    for (int t = 0; t < 2; ++t) g.w[t] = c.lds0 + 32768 + 2048 * hi + 64 * (4 * t + q4) + 16 * ((2 * blk + (p4 >> 1)) ^ ((2 * hi + t) & 3)) + 8 * (p4 & 1);
    f32x16 acc[1][4];
#pragma unroll
    for (int a = 0; a < 4; ++a) acc[0][a] = f32x16{};
    Frags<1, 4> f0, f1;
#pragma unroll
    for (int i = 0; i < 9; ++i) frag_read<1, 4, 0>(f0, g, i);
    q_tile<0>(c, g, acc, f0, f1, pk, publish, stamp); q_tile<1>(c, g, acc, f0, f1, pk, publish, stamp); q_tile<2>(c, g, acc, f0, f1, pk, publish, stamp); q_tile<3>(c, g, acc, f0, f1, pk, publish, stamp);
    q_tile<4>(c, g, acc, f0, f1, pk, publish, stamp); q_tile<5>(c, g, acc, f0, f1, pk, publish, stamp); q_tile<6>(c, g, acc, f0, f1, pk, publish, stamp); q_tile<7>(c, g, acc, f0, f1, pk, publish, stamp);
    constexpr float QSC = 0.08838834764831845f * 1.4426950408889634f;
#pragma unroll
    for (int nb = 0; nb < 4; ++nb)
#pragma unroll
        for (int s = 0; s < 2; ++s) { u32x4 w;
#pragma unroll
            for (int e = 0; e < 4; ++e) w[e] = cvtpk(acc[0][nb][8 * s + 2 * e] * QSC, acc[0][nb][8 * s + 2 * e + 1] * QSC);
            q[2 * nb + s] = __builtin_bit_cast(bf16x8, w); }
}
template <int OFF, bool SETM0> DI void dma_piece(unsigned m0v, unsigned voff, srd_t srd, unsigned soff) {
    if constexpr (SETM0) asm volatile("s_mov_b32 m0, %0\n\ts_nop 0\n\tbuffer_load_dwordx4 %1, %2, %3 offen offset:%4 sc1 lds" :: "s"(m0v), "v"(voff), "s"(srd), "s"(soff), "i"(OFF) : "memory");
    else asm volatile("buffer_load_dwordx4 %0, %1, %2 offen offset:%3 sc1 lds" :: "v"(voff), "s"(srd), "s"(soff), "i"(OFF) : "memory");
}
#define ATT_THRV 64.f
constexpr float ATT_THR = ATT_THRV;
#define ATT_WIN 6
constexpr int ATT_W = ATT_WIN;

struct AttnState { f32x16 o[4]; f32x16 s0, s1, negm; bf16x8 q[8]; u32x4 p[4]; float l, mhat; };
struct AttnCtx {
    srd_t srdK, srdV, srdC; unsigned voff, lane16, kslot0, vslot0, coffL;
    int g, nl  , thrg, dq0, relH, jump; unsigned qstash;
    unsigned soA, soB; int rel;
};

template <bool DO_QK, bool DO_PV, int KP, int VP>
DI void m_phase(AttnState& st, unsigned kbase, unsigned vbase, bf16x8 (&pre)[ATT_W]) {
    constexpr int N0 = DO_QK ? 0 : 16, N1 = DO_PV ? 32 : 16;
    bf16x8 fr[32];
#pragma unroll
    for (int i = 0; i < ATT_W; ++i) fr[N0 + i] = pre[i];
#pragma unroll
    for (int i = N0; i < N1; ++i) {
        const int n = i + ATT_W;
        if (n < N1) { if (n < 16) fr[n] = ldsf(kbase + KP * 16384 + (n >> 1) * 2048 + (n & 1) * 512); else fr[n] = ldsf(vbase + VP * 16384 + ((n - 16) >> 2) * 4096 + ((n - 16) & 3) * 512); }
        SBAR();
        if (i < 16) { const int s = i >> 1;
            if (i == 0) asm volatile("v_mfma_f32_32x32x16_bf16 %0, %2, %3, %1" : "=&v"(st.s0), "+v"(st.negm) : "v"(fr[i]), "v"(st.q[0]));
            else if (i == 1) asm volatile("v_mfma_f32_32x32x16_bf16 %0, %2, %3, %1" : "=&v"(st.s1), "+v"(st.negm) : "v"(fr[i]), "v"(st.q[0]));
            else if ((i & 1) == 0) st.s0 = MFMA32(fr[i], st.q[s], st.s0);
            else              st.s1 = MFMA32(fr[i], st.q[s], st.s1);
        } else { const int ks = (i - 16) >> 2, db = (i - 16) & 3;
            st.o[db] = MFMA32(fr[i], __builtin_bit_cast(bf16x8, st.p[ks]), st.o[db]); }
        SBAR();
    }
    PIN(st.negm);
}

DI void store_ctx(const f32x16 (&o)[4], float inv, srd_t srd, unsigned voff) {
#pragma unroll
    for (int db = 0; db < 4; ++db)
#pragma unroll
        for (int s = 0; s < 2; ++s) {
            u32x4 w;
#pragma unroll
            for (int e = 0; e < 4; ++e) w[e] = cvtpk(o[db][8 * s + 2 * e] * inv, o[db][8 * s + 2 * e + 1] * inv);
            __builtin_amdgcn_raw_buffer_store_b128(w, srd, voff + (4 * db + 2 * s) * 512, 0, 16);
        }
}

template <bool LAST, int PAR>
DI void v_phase(AttnState& st, AttnCtx& c, const int t) {
    const bool sw = (t == c.nl);
    const unsigned vm0 = c.vslot0 + (PAR ^ 1) * 16384, km0 = c.kslot0 + PAR * 16384;
    const unsigned vso = c.soA, kso = c.soB;
    SBAR();
    if (__builtin_expect(sw, 0)) { const float lt = swap_add(st.l); store_ctx(st.o, __builtin_amdgcn_rcpf(lt), c.srdC, c.lane16 + c.coffL);
#pragma unroll
        for (int d = 0; d < 4; ++d) st.o[d] = f32x16{};
        st.l = 0.f; c.rel = c.relH; }
    if (__builtin_expect(c.rel > c.thrg, 0)) {
        const int u = 32 * c.g - c.rel; const float NEG = -__builtin_inff();
        if (u == 32) {
#pragma unroll
            for (int r = 0; r < 16; ++r) if ((r & 3) + 8 * (r >> 2) > c.dq0) st.s1[r] = NEG;
        } else {
            if (u == 0) {
#pragma unroll
                for (int r = 0; r < 16; ++r) if ((r & 3) + 8 * (r >> 2) > c.dq0) st.s0[r] = NEG;
            } else {
#pragma unroll
                for (int r = 0; r < 16; ++r) st.s0[r] = NEG; }
#pragma unroll
            for (int r = 0; r < 16; ++r) st.s1[r] = NEG;
        } }
    SBAR();
    if (!LAST) dma_piece<0, true>(vm0, c.voff, c.srdV, vso);
#define MX3(a, b, c) __builtin_fmaxf(__builtin_fmaxf((a), (b)), (c))
    float a = MX3(st.s0[0], st.s0[1], st.s1[0]), b = MX3(st.s0[2], st.s0[3], st.s1[1]);
    a = MX3(a, st.s1[2], st.s1[3]);
#pragma unroll
    for (int r = 4; r < 16; r += 4) {
        a = MX3(a, st.s0[r], st.s0[r + 1]); b = MX3(b, st.s0[r + 2], st.s0[r + 3]);
        a = MX3(a, st.s1[r], st.s1[r + 1]); b = MX3(b, st.s1[r + 2], st.s1[r + 3]); }
    const float rmax = __builtin_fmaxf(a, b);
    const unsigned long long firstm = (t == 0 || sw) ? ~0ull : 0ull;
    if (__builtin_expect((__builtin_amdgcn_ballot_w64(rmax > ATT_THR) | firstm) != 0ull, 0)) {
        const bool first = firstm != 0ull;
        const float m = swap_max(rmax);
        const float delta = __builtin_fmaxf(m, first ? -1e30f : 0.f);
        st.mhat = (first ? 0.f : st.mhat) + delta;
#pragma unroll
        for (int r = 0; r < 16; ++r) { st.s0[r] -= delta; st.s1[r] -= delta; st.negm[r] = -st.mhat; }
        if (!first) { const float f = __builtin_amdgcn_exp2f(-delta); st.l *= f;
#pragma unroll
            for (int d = 0; d < 4; ++d)
#pragma unroll
                for (int r = 0; r < 16; ++r) st.o[d][r] *= f; }
    }
    SBAR();
    float e[32];
#define EXPG(k) do { _Pragma("unroll") for (int i_ = 0; i_ < 4; ++i_) e[4 * (k) + i_] = __builtin_amdgcn_exp2f((k) < 4 ? st.s0[4 * (k) + i_] : st.s1[4 * ((k) - 4) + i_]); } while (0)
    float l0 = st.l, l1 = 0.f, l2 = 0.f, l3 = 0.f;
#define FING(k) do { l0 += e[4 * (k)]; if ((k) == 0) { l1 = e[1]; l2 = e[2]; l3 = e[3]; } else { l1 += e[4 * (k) + 1]; l2 += e[4 * (k) + 2]; l3 += e[4 * (k) + 3]; } \
        unsigned w0_ = cvtpk(e[4 * (k)], e[4 * (k) + 1]), w1_ = cvtpk(e[4 * (k) + 2], e[4 * (k) + 3]); PIN(w0_); PIN(w1_); PIN(l0); PIN(l1); PIN(l2); PIN(l3); \
        st.p[(k) >> 1][2 * ((k) & 1)] = w0_; st.p[(k) >> 1][2 * ((k) & 1) + 1] = w1_; } while (0)
    EXPG(0); SBAR();
    if (!LAST) dma_piece<1024, false>(vm0, c.voff, c.srdV, vso);
    EXPG(1); FING(0); SBAR();
    if (!LAST) dma_piece<2048, false>(vm0, c.voff, c.srdV, vso);
    EXPG(2); FING(1); SBAR();
    if (!LAST) dma_piece<3072, false>(vm0, c.voff, c.srdV, vso);
    EXPG(3); FING(2); SBAR();
    if (!LAST) dma_piece<0, true>(km0, c.voff, c.srdK, kso);
    EXPG(4); FING(3); SBAR();
    if (!LAST) dma_piece<1024, false>(km0, c.voff, c.srdK, kso);
    EXPG(5); FING(4); SBAR();
    if (!LAST) dma_piece<2048, false>(km0, c.voff, c.srdK, kso);
    EXPG(6); FING(5); SBAR();
    if (!LAST) dma_piece<3072, false>(km0, c.voff, c.srdK, kso);
    EXPG(7); FING(6); SBAR();
    FING(7);
    st.l = (l0 + l1) + (l2 + l3);
#undef EXPG
#undef FING
    if (!LAST) {
        if (__builtin_expect(t == c.nl - 1, 0)) {
#pragma unroll
            for (int s = 0; s < 8; ++s) st.q[s] = ldsf(c.qstash + s * 1024);
            st.negm = f32x16{};
        }
        c.soA = c.soB; c.soB += (t + 3 == c.nl) ? c.jump : 16384; c.rel += 64;
    }
    SBAR();
}

DI void attn_setup(AttnCtx& c, const u16* Kf, const u16* Vf, u16* Cf, unsigned lds0, int h, int j, int wid, int lane) {
    const int grp = wid >> 2; c.g = wid & 3; c.dq0 = (lane & 31) - 4 * (lane >> 5);
    const int qbH = 15 - j, qbL = j, nl = 2 * j + 2;
    c.nl = grp ? nl : 1000; c.thrg = 32 * c.g - 63; c.jump = (18 - nl) * 16384; c.relH = 64 * (17 - 2 * qbH);
    c.rel = grp ? -128 * qbL : -128 * qbH;
    c.soA = 16384; c.soB = (c.nl == 2) ? 17 * 16384 : 2 * 16384;
    c.srdK = mk_srd((const char*)Kf + (size_t)h * 524288, 524288);
    c.srdV = mk_srd((const char*)Vf + (size_t)h * 524288, 524288);
    c.srdC = mk_srd((const char*)Cf, 16777216);
    c.lane16 = lane * 16; c.voff = lane * 16 + c.g * 4096;
    c.kslot0 = lds0 + grp * 65536 + c.g * 4096; c.vslot0 = c.kslot0 + 32768;
    c.coffL = ((qbL * 4 + c.g) * 32 + h) * 8192;
    c.qstash = lds0 + 131072 + c.g * 8192 + lane * 16;
}
#define DMA4(m_, vo_, srd, so_) do { dma_piece<0, true>(m_, vo_, srd, so_); dma_piece<1024, false>(m_, vo_, srd, so_); dma_piece<2048, false>(m_, vo_, srd, so_); dma_piece<3072, false>(m_, vo_, srd, so_); } while (0)
DI void attn_prime(const AttnCtx& c, int grp) { DMA4(grp ? c.vslot0 + 16384 : c.kslot0, c.voff, c.srdK, 0u); DMA4(c.vslot0, c.voff, c.srdV, 0u); if (grp == 0) DMA4(c.kslot0 + 16384, c.voff, c.srdK, 16384u); }

template <class First, class G0Tail>
DI void attn_run(AttnState& st, AttnCtx& c, unsigned lds0, int h, int j, int wid, int lane, First afterFirst, G0Tail g0tail, Stamper& stamp) {
    const int grp = wid >> 2;
    const unsigned kbase = lds0 + grp * 65536 + (lane >> 5) * 1024 + (lane & 31) * 16;
    const unsigned vbase = lds0 + grp * 65536 + 32768 + (lane >> 5) * 2048 + (lane & 31) * 16;
#pragma unroll
    for (int d = 0; d < 4; ++d) st.o[d] = f32x16{};
    st.negm = f32x16{}; st.l = 0.f; st.mhat = 0.f;
    PIN(st.negm);
    if (grp == 1) { DMA4(c.kslot0 + 16384, c.voff, c.srdK, 16384u); BARRIER(); }
    wait_bar<8>();
    stamp();
    bf16x8 pre[ATT_W];
#define PRE_K(P) do { _Pragma("unroll") for (int i = 0; i < ATT_W; ++i) pre[i] = ldsf(kbase + (P) * 16384 + (i >> 1) * 2048 + (i & 1) * 512); } while (0)
    const unsigned kbase0 = grp ? kbase + 49152 : kbase;
#pragma unroll
    for (int i = 0; i < ATT_W; ++i) pre[i] = ldsf(kbase0 + (i >> 1) * 2048 + (i & 1) * 512);
    __builtin_amdgcn_s_setprio(1);
    m_phase<true, false, 0, 0>(st, kbase0, vbase, pre);
    __builtin_amdgcn_s_setprio(0);
    wait_bar<0>();
    afterFirst();
#define STEP(PAR, t) do { \
        v_phase<false, PAR>(st, c, t); \
        PRE_K(PAR ^ 1); \
        asm volatile("s_barrier" ::: "memory"); \
        __builtin_amdgcn_s_setprio(1); \
        m_phase<true, true, PAR ^ 1, PAR>(st, kbase, vbase, pre); \
        __builtin_amdgcn_s_setprio(0); \
        wait_bar<0>(); } while (0)
    for (int t = 0; t < 16; t += 2) { stamp(); STEP(0, t); STEP(1, t + 1); }
    stamp();
    v_phase<true, 0>(st, c, 16);
#pragma unroll
    for (int i = 0; i < ATT_W; ++i) pre[i] = ldsf(vbase + (i >> 2) * 4096 + (i & 3) * 512);
    asm volatile("s_barrier" ::: "memory");
    __builtin_amdgcn_s_setprio(1);
    m_phase<false, true, 0, 0>(st, kbase, vbase, pre);
    __builtin_amdgcn_s_setprio(0);
    wait_bar<0>();
    stamp();
    const float lt = swap_add(st.l);
    const unsigned xo = lds0 + c.g * 16384 + lane * 16;
    LAS f32x2* xs = (LAS f32x2*)(lds0 + 65536 + c.g * 512 + lane * 8);
    if (grp == 0) {
#pragma unroll
        for (int d = 0; d < 4; ++d)
#pragma unroll
            for (int rq = 0; rq < 4; ++rq) *(LAS f32x4*)(xo + (d * 4 + rq) * 1024) = (f32x4){st.o[d][4 * rq], st.o[d][4 * rq + 1], st.o[d][4 * rq + 2], st.o[d][4 * rq + 3]};
        *xs = (f32x2){st.mhat, lt};
        BARRIER();
        g0tail();
    } else {
        const f32x2 o0 = *xs;
        const float m = __builtin_fmaxf(o0[0], st.mhat);
        const float f0 = __builtin_amdgcn_exp2f(o0[0] - m), f1 = __builtin_amdgcn_exp2f(st.mhat - m);
        const float inv = __builtin_amdgcn_rcpf(o0[1] * f0 + lt * f1), a0 = f0 * inv, a1 = f1 * inv;
#pragma unroll
        for (int d = 0; d < 4; ++d)
#pragma unroll
            for (int rq = 0; rq < 4; ++rq) { const f32x4 x = *(const LAS f32x4*)(xo + (d * 4 + rq) * 1024);
#pragma unroll
                for (int e = 0; e < 4; ++e) st.o[d][4 * rq + e] = st.o[d][4 * rq + e] * a1 + x[e] * a0; }
        store_ctx(st.o, 1.0f, c.srdC, c.lane16 + (((15 - j) * 4 + c.g) * 32 + h) * 8192);
    }
    stamp();
#undef PRE_K
#undef STEP
}
constexpr int OUT_LDS = 163840;
constexpr unsigned OUT_O = 131072;
struct OutCtx { srd_t srdC, srdW, srdB; unsigned lane16, vw0, vwA, sC, sW, sB, lds0; int wid, rot; };
DI void out_issueA(const OutCtx& c, int kt, int i) {
    const unsigned st = c.lds0 + (kt & 3) * 32768;
    const int kr = (kt + c.rot) & 15;
    if (i < 2) { const int p = 2 * c.wid + i;
        dma1s(st + p * 1024, c.lane16, c.srdC, c.sC + (p >> 2) * 262144 + (p & 3) * 1024 + kr * 4096); }
    else dma1(st + 16384 + (2 * c.wid + (i - 2)) * 1024, c.vwA, c.srdW, c.sW + (8 * c.wid) * 1024 + (i - 2) * 128 + kr * 65536);
}
DI void out_issueB(const OutCtx& c, int ch, int i) {
    const int rg = 2 * c.wid + (i >> 1);
    dma1(c.lds0 + ch * 32768 + rg * 2048 + (i & 1) * 1024, (i >> 1) ? (c.vw0 ^ 32u) : c.vw0, c.srdB, c.sB + (ch * 128 + 8 * rg) * 1024 + (i & 1) * 128);
}
DI u32x4 out_pack(f32x4 s0, f32x4 s1) {
    unsigned a0 = cvtpk(s0[0], s0[1]), a1 = cvtpk(s0[2], s0[3]), b0 = cvtpk(s1[0], s1[1]), b1 = cvtpk(s1[2], s1[3]);
    auto x0 = __builtin_amdgcn_permlane32_swap(a0, b0, false, false); auto x1 = __builtin_amdgcn_permlane32_swap(a1, b1, false, false);
    return (u32x4){x0[0], x1[0], x0[1], x1[1]};
}
DI f32x4 quad(const f32x16& x, int rq) { return (f32x4){x[4 * rq], x[4 * rq + 1], x[4 * rq + 2], x[4 * rq + 3]}; }

DI void out_phase(const u16* Wo, const u16* Bwo, u16* out, char* ws, u64 tok, unsigned lds0, int bid, int wid, int lane, Stamper& stamp) {
    const int tid = wid * 64 + lane, r32 = lane & 31, hi = lane >> 5, wr = wid >> 2, wc = wid & 3;
    const int x = bid & 7, u = bid >> 3, s = x >> 1, rb = 8 * (x & 1) + (u >> 2), cb = u & 3, tile = rb * 4 + cb;
    OutCtx c; c.wid = wid; c.lds0 = lds0; c.lane16 = lane * 16;
    c.srdC = mk_srd(ws + WS_CF + (size_t)rb * 4 * 262144, 4 * 262144); c.srdW = mk_srd(Wo, 4194304); c.srdB = mk_srd(Bwo, 524288);
    c.vw0 = ((lane >> 2) & 7) * 1024 + (4 * (lane >> 5) + ((lane & 3) ^ ((lane >> 4) & 1))) * 16; c.vwA = (wid & 1) ? (c.vw0 ^ 32u) : c.vw0;
    c.rot = 4 * cb + 2 * (rb & 7);
    c.sC = s * 8 * 8192; c.sW = s * 1024 * 1024 + cb * 256; c.sB = cb * 256;
#pragma unroll
    for (int kt = 0; kt < 4; ++kt) { out_issueA(c, kt, 2); out_issueA(c, kt, 3); }
    u64* flags = (u64*)(ws + WS_FLAG);
    {
        const int l = lane & 15, hh = 8 * s + (l & 7), jj = rb < 8 ? rb : 15 - rb;
        const int src = l < 8 ? ((hh >> 2) | (jj << 3) | ((hh & 3) << 6)) : (8 * (2 * rb + (s >> 1)) + (l & 7));
        const u64* fp = flags + (FL_C + src) * FL_STRIDE;
        for (unsigned spins = 0; spins < (1u << 15); ++spins) { const u64 v = ld_agent(fp); if (__all(v == tok)) break; __builtin_amdgcn_s_sleep(1); }
        asm volatile("" ::: "memory"); }
    stamp();
#pragma unroll
    for (int kt = 0; kt < 4; ++kt) { out_issueA(c, kt, 0); out_issueA(c, kt, 1); }
    { const srd_t srdO = mk_srd(ws + WS_OF + (size_t)(rb * 4 + s) * 32768, 32768);
#pragma unroll
      for (int i = 0; i < 4; ++i) dma1s(lds0 + OUT_O + (4 * wid + i) * 1024, c.lane16, srdO, (unsigned)((4 * wid + i) * 1024)); }
    const int q4 = (lane & 15) >> 2, p4 = lane & 3, blk = (lane >> 4) & 1;
    const unsigned aaddr = lds0 + (2 * wr) * 4096 + c.lane16;
    unsigned waddr[2];
#pragma unroll
    for (int t = 0; t < 2; ++t) waddr[t] = lds0 + 2048 * t + 512 * wc + 64 * (4 * hi + q4) + 16 * ((2 * blk + (p4 >> 1)) ^ ((2 * t + hi) & 3)) + 8 * (p4 & 1);
    f32x16 acc[2] = {f32x16{}, f32x16{}};
    stamp();
    struct Fr { bf16x8 a[2][2]; bf16x4 w[2][2]; };
    auto rd = [&](Fr& f, unsigned st, int half) {
#pragma unroll
        for (int k = 0; k < 2; ++k) { const int ks = 2 * half + k;
            f.w[k][0] = ldstr(waddr[0] + st + 16384 + ks * 4096); f.w[k][1] = ldstr(waddr[1] + st + 16384 + ks * 4096);
            f.a[k][0] = ldsf(aaddr + st + ks * 1024); f.a[k][1] = ldsf(aaddr + st + 4096 + ks * 1024); } };
    Fr f0, f1;
    wait_bar<10>();
    rd(f0, 0u, 0);
    auto step = [&](int kt, auto late, auto early) {
        const unsigned st = (kt & 3) * 32768, stn = ((kt + 1) & 3) * 32768;
        rd(f1, st, 1);
        SBAR();
#pragma unroll
        for (int k = 0; k < 2; ++k) { const bf16x8 wf = cat4(f0.w[k][0], f0.w[k][1]);
            acc[0] = MFMA32(wf, f0.a[k][0], acc[0]); acc[1] = MFMA32(wf, f0.a[k][1], acc[1]); SBAR(); late(2 + k); SBAR(); }
        wait_bar<8>();
        rd(f0, stn, 0);
        SBAR();
#pragma unroll
        for (int k = 0; k < 2; ++k) { const bf16x8 wf = cat4(f1.w[k][0], f1.w[k][1]);
            acc[0] = MFMA32(wf, f1.a[k][0], acc[0]); acc[1] = MFMA32(wf, f1.a[k][1], acc[1]); SBAR(); early(k); SBAR(); }
    };
    step(0, [&](int) {}, [&](int i) { out_issueA(c, 4, i); });
    for (int kt = 1; kt < 12; ++kt) step(kt, [&](int i) { out_issueA(c, kt + 3, i); }, [&](int i) { out_issueA(c, kt + 4, i); });
    step(12, [&](int i) { out_issueA(c, 15, i); }, [&](int i) { out_issueB(c, 0, i); });
    step(13, [&](int i) { out_issueB(c, 0, i); }, [&](int i) { out_issueB(c, 1, i); });
    step(14, [&](int i) { out_issueB(c, 1, i); }, [&](int i) { out_issueB(c, 2, i); });
    step(15, [&](int i) { out_issueB(c, 2, i); }, [&](int i) { out_issueB(c, 3, i); });
    out_issueB(c, 3, 2); out_issueB(c, 3, 3);
    stamp();
    const srd_t srdX = mk_srd(ws + WS_XCH + (size_t)tile * 262144, 262144);
#pragma unroll
    for (int mb = 0; mb < 2; ++mb) { const int q = 2 * wr + mb;
        if (q != s) {
#pragma unroll
            for (int rq = 0; rq < 4; ++rq) store16_sc1(__builtin_bit_cast(u32x4, quad(acc[mb], rq)), c.lane16, srdX, (unsigned)(((q * 4 + s) * 4 + wc) * 4096 + rq * 1024)); } }
    const int kh = (wr == (s >> 1)) ? 1 : 0;
    f32x16 accB = f32x16{};
#pragma unroll
    for (int ch = 0; ch < 4; ++ch) {
        if (ch == 0) { wait_bar<0>(); if (tid == 0) st_agent(flags + (FL_XF + tile * 4 + s) * FL_STRIDE, tok); }
        const unsigned st = ((16 + ch) & 3) * 32768;
        bf16x8 a[4]; bf16x4 w[4][2];
#pragma unroll
        for (int ks = 0; ks < 4; ++ks) { const int k = 4 * kh + ks;
            w[ks][0] = ldstr(waddr[0] + st + k * 4096); w[ks][1] = ldstr(waddr[1] + st + k * 4096); a[ks] = ldsf(lds0 + OUT_O + (8 * ch + k) * 1024 + c.lane16); }
#pragma unroll
        for (int ks = 0; ks < 4; ++ks) accB = MFMA32(cat4(w[ks][0], w[ks][1]), a[ks], accB);
    }
    stamp();
    BARRIER();
    const int row = rb * 128 + 32 * s + r32;
    if (kh == 1) {
#pragma unroll
        for (int rq = 0; rq < 4; ++rq) *(LAS f32x4*)(lds0 + (wc * 4 + rq) * 1024 + c.lane16) = quad(accB, rq);
        BARRIER();
        const u64* fp = flags + (FL_XF + tile * 4 + (lane & 3)) * FL_STRIDE;
        for (unsigned spins = 0; spins < (1u << 15); ++spins) { const u64 v = ld_agent(fp); if (__all(v == tok)) break; __builtin_amdgcn_s_sleep(1); }
        asm volatile("" ::: "memory");
        f32x4 p[4][4];
#pragma unroll
        for (int q = 0; q < 4; ++q)
#pragma unroll
            for (int rq = 0; rq < 4; ++rq) p[q][rq] = __builtin_bit_cast(f32x4, __builtin_amdgcn_raw_buffer_load_b128(srdX, (q == s ? 0x7ffffff0u : c.lane16) + rq * 1024, ((s * 4 + q) * 4 + wc) * 4096, 16));
        f32x4 r[4];
#pragma unroll
        for (int rq = 0; rq < 4; ++rq) r[rq] = ((p[0][rq] + p[1][rq]) + (p[2][rq] + p[3][rq])) + ((s & 1) ? quad(acc[1], rq) : quad(acc[0], rq));
        u16* o = out + (size_t)row * 1024 + cb * 128 + 32 * wc + 8 * hi;
        *(u32x4*)o = out_pack(r[0], r[1]); *(u32x4*)(o + 16) = out_pack(r[2], r[3]);
    } else {
        BARRIER();
        f32x4 r[4];
#pragma unroll
        for (int rq = 0; rq < 4; ++rq) r[rq] = quad(accB, rq) + *(const LAS f32x4*)(lds0 + (wc * 4 + rq) * 1024 + c.lane16);
        u16* o = out + (size_t)row * 1024 + 512 + cb * 128 + 32 * wc + 8 * hi;
        *(u32x4*)o = out_pack(r[0], r[1]); *(u32x4*)(o + 16) = out_pack(r[2], r[3]);
    }
}
constexpr int FUSED_LDS = 163840;
constexpr unsigned WPHI_LDS = 151552;
struct FusedArgs { const u16 *X, *Ckv, *Wq, *Wuk, *Wuv, *Wo, *Bx, *Wqkv, *Wphi, *Bwo; char* ws; u16* out; };

__global__ __launch_bounds__(512, 2) void fused_kernel(FusedArgs a) {
    extern __shared__ __attribute__((aligned(16))) char lds_[];
    const int tid = threadIdx.x, lane = tid & 63;
    const int wid = rfl(tid >> 6);
    const int bid = blockIdx.x, h = (bid & 7) * 4 + (bid >> 6), j = (bid >> 3) & 7;
    const unsigned lds0 = (unsigned)(uintptr_t)lds_;
    Stamper stamp{0u, 0, lane};
    stamp();
    u16* Kf = (u16*)(a.ws + WS_KF); u16* Vf = (u16*)(a.ws + WS_VF); u16* Cf = (u16*)(a.ws + WS_CF);
    u16* PhiQ = (u16*)(a.ws + WS_PHIQ); u16* Of = (u16*)(a.ws + WS_OF); float* Bst = (float*)(a.ws + WS_BST);
    u64* flags = (u64*)(a.ws + WS_FLAG);
    float* Bred = (float*)(a.ws + WS_BRED);
    const int hg = bid & 7, rc = bid >> 3;
    if (wid < 2) dma1(lds0 + WPHI_LDS + wid * 1024, lane * 16, mk_srd(a.Wphi, 16384), (unsigned)(hg * 2048 + wid * 1024));
    ProjCtx pc; proj_setup(pc, a.X, a.Ckv, a.Wq, a.Wuk, a.Wuv, Kf, Vf, lds0, h, j, wid, lane);
#pragma unroll
    for (int i = 0; i < 8; ++i) issueKV(pc, 0, i);
#pragma unroll
    for (int i = 0; i < 8; ++i) issueKV(pc, 1, i);
    const unsigned tsc = lds0 + TOUCH_LDS;
    { const unsigned vW = (lane >> 4) * 524288 + (lane & 15) * 128;
      const char* base; unsigned vo = pc.l128;
      if (wid < 4) { base = (const char*)((wid & 1) ? a.Wuv : a.Wuk) + ((wid & 2) ? 4 : 2) * 524288 + bid * 2048 - ((wid & 2) ? 0 : 0); vo = vW; }
      else if (wid == 4) base = (const char*)a.Ckv + bid * 8192;
      else { base = (const char*)a.Ckv; vo = 0; }
      touch_g<1>(tsc, vo, base); }
    const u64 epoch = *(const u64 __attribute__((address_space(4)))*)(flags + FL_EPOCH * FL_STRIDE);
    u64 tok = mix64(epoch); if (tok == epoch) tok ^= 1ull;
    B1Ctx bc; b1_setup(bc, a.Bx, a.Wqkv, lds0, rc, hg, wid, lane);
    stamp();
    auto touchk = [&](int kt) {
        const char* base = (const char*)a.Ckv; unsigned vo = 0;
        if (kt == 0) {
            if (wid == 0) { base = (const char*)a.Bx + bid * 8192; vo = pc.l128; }
            else if (wid == 1) { base = (const char*)a.X + bid * 8192; vo = pc.l128; }
            else if (wid == 2) { base = (const char*)a.Wqkv + (bid < 192 ? bid : 0) * 8192; vo = pc.l128; }
        } else if (kt == 1) {
            if (wid < 2) { base = (const char*)a.Wq + (bid * 2 + wid) * 8192; vo = pc.l128; }
        } else if (kt == 2) {
            if (wid < 2) { base = (const char*)a.Wo + (bid * 2 + wid) * 8192; vo = pc.l128; }
            else if (wid == 2) { base = (const char*)a.Bwo + (bid & 63) * 8192; vo = pc.l128; }
        }
        touch_g<2>(tsc, vo, base); };
    u32x4 pk[16];
    kv_run(pc, lane, pk, touchk, [&](int kt) { b1_issue(bc, kt); }, stamp);
    stamp();
    B1Acc ba;
    b1_gemm(bc, ba, lane, [&](int n) { kv_store(pc, pk, n); },
            [&]() {
#pragma unroll
                for (int i = 0; i < 6; ++i) issueQ(pc, 0, i); },
            [&]() {
#pragma unroll
                for (int i = 0; i < 6; ++i) issueQ(pc, 1, i); }, stamp);
    stamp();
    u32x2 wp; { const int l16 = lane & 15, g = lane >> 4; const LAS u16* w = (const LAS u16*)(lds0 + WPHI_LDS + (wid & 3) * 512);
        wp[0] = (unsigned)w[(4 * g) * 16 + l16] | ((unsigned)w[(4 * g + 1) * 16 + l16] << 16); wp[1] = (unsigned)w[(4 * g + 2) * 16 + l16] | ((unsigned)w[(4 * g + 3) * 16 + l16] << 16); }
    b1_phi(ba, wp, PhiQ, Bst, lds0, rc, hg, wid, lane, stamp);
    stamp();
    wait_bar<10>();
    stamp();
#pragma unroll
    for (int i = 0; i < 6; ++i) issueQ(pc, 2, i);
    stamp();
    AttnState st;
    AttnCtx c; attn_setup(c, Kf, Vf, Cf, lds0, h, j, wid, lane);
    B2a b2;
    auto spin = [&](const u64* fp) {
        for (unsigned spins = 0; spins < (1u << 15); ++spins) { const u64 v = ld_agent(fp); if (__all(v == tok)) break; __builtin_amdgcn_s_sleep(1); }
        asm volatile("" ::: "memory"); };
    const u64* fkv = flags + (FL_KV + 8 * h + (lane & 7)) * FL_STRIDE;
    const u64* fbs = flags + (FL_BS + 32 * hg + (lane & 31)) * FL_STRIDE;
    u64 early = 0;
    q_run(pc, lane, pk, st.q, [&](int kt) {
        if (kt == 1) { if (tid == 0) st_agent(flags + (FL_BS + 32 * hg + rc) * FL_STRIDE, tok); }
        if (kt == 2) { if (wid == 7) early = ld_agent(fbs); }
        if (kt == 4) { if (tid == 0) st_agent(flags + (FL_KV + 8 * h + j) * FL_STRIDE, tok); }
        if (kt == 3) {
            if (wid == 7) {
                if (!__all(early == tok)) spin(fbs);
                asm volatile("" ::: "memory");
                b2a_load(b2, Bst, rc, hg, lane); } }
        if (kt == 5) { if (wid == 7) b2a_finish(b2, Bred, rc, hg, lane); early = ld_agent(fkv); }
        if (kt == 6) {
            if (tid == 448) st_agent(flags + (FL_BR + 32 * hg + rc) * FL_STRIDE, tok);
            if (!__all(early == tok)) spin(fkv);
            asm volatile("" ::: "memory");
            attn_prime(c, wid >> 2);
        } }, stamp);
    stamp();
    BARRIER();
    if (wid < 4) {
#pragma unroll
        for (int s = 0; s < 8; ++s) *(LAS bf16x8*)(c.qstash + s * 1024) = st.q[s]; }
    stamp();
    attn_run(st, c, lds0, h, j, wid, lane,
             [&]() {},
             [&]() { b2b_body<4>(PhiQ, Bred, Of, flags + (FL_BR + 32 * hg + 8 * wid) * FL_STRIDE, tok, 4 * hg + wid, 4 * rc, lane); }, stamp);
    wait_bar<0>();
    if (tid == 0) st_agent(flags + (FL_C + bid) * FL_STRIDE, tok);
    stamp();
    out_phase(a.Wo, a.Bwo, a.out, a.ws, tok, lds0, bid, wid, lane, stamp);
    if (bid == 0 && wid == 7) {
        bool ok = false;
        for (unsigned spins = 0; spins < (1u << 15) && !ok; ++spins) { bool all = true;
#pragma unroll
            for (int i = 0; i < 4; ++i) all &= ld_agent(flags + (FL_C + lane + 64 * i) * FL_STRIDE) == tok;
            ok = __all(all); if (!ok) __builtin_amdgcn_s_sleep(1); }
        if (lane == 0) st_agent(flags + FL_EPOCH * FL_STRIDE, tok);
    }
    stamp();
}

extern "C" void kernel_launch(void* const* d_in, const int* in_sizes, int n_in, void* d_out, int out_size, void* d_ws, size_t ws_size, hipStream_t stream) {
    FusedArgs a;
    a.X = (const u16*)d_in[0]; a.Ckv = (const u16*)d_in[1]; a.Wq = (const u16*)d_in[2]; a.Wuk = (const u16*)d_in[3]; a.Wuv = (const u16*)d_in[4]; a.Wo = (const u16*)d_in[5];
    a.Bx = (const u16*)d_in[6]; a.Wqkv = (const u16*)d_in[7]; a.Wphi = (const u16*)d_in[8]; a.Bwo = (const u16*)d_in[9]; a.ws = (char*)d_ws; a.out = (u16*)d_out;
    (void)hipFuncSetAttribute((const void*)fused_kernel, hipFuncAttributeMaxDynamicSharedMemorySize, FUSED_LDS);
    fused_kernel<<<256, 512, FUSED_LDS, stream>>>(a);
}
```

```cpp
#include <hip/hip_runtime.h>
#include <stdint.h>

typedef unsigned short u16;
typedef unsigned long long u64;
typedef __bf16 bf16x8 __attribute__((ext_vector_type(8)));
typedef __bf16 bf16x4 __attribute__((ext_vector_type(4)));
typedef __bf16 bf16x2 __attribute__((ext_vector_type(2)));
typedef short s16x4 __attribute__((ext_vector_type(4)));
typedef float f32x16 __attribute__((ext_vector_type(16)));
typedef float f32x4 __attribute__((ext_vector_type(4)));
typedef float f32x2 __attribute__((ext_vector_type(2)));
typedef unsigned u32x4 __attribute__((ext_vector_type(4)));
typedef unsigned u32x2 __attribute__((ext_vector_type(2)));
typedef __amdgpu_buffer_rsrc_t srd_t;
#define LAS __attribute__((address_space(3)))
#define GAS __attribute__((address_space(1)))
#define SBAR() __builtin_amdgcn_sched_barrier(0)
#define PIN(x) asm volatile("" : "+v"(x))
#define MFMA32(a, b, c) __builtin_amdgcn_mfma_f32_32x32x16_bf16((a), (b), (c), 0, 0, 0)
#define MFMA16(a, b, c) __builtin_amdgcn_mfma_f32_16x16x32_bf16((a), (b), (c), 0, 0, 0)
#define DI __device__ __forceinline__
#pragma clang diagnostic ignored "-Wint-to-pointer-cast"

DI unsigned cvtpk(float lo, float hi) { f32x2 v = {lo, hi}; bf16x2 b = __builtin_convertvector(v, bf16x2); return __builtin_bit_cast(unsigned, b); }
DI unsigned rfl(unsigned x) { return __builtin_amdgcn_readfirstlane(x); }
DI float swap_max(float x) { auto rr = __builtin_amdgcn_permlane32_swap(__float_as_uint(x), __float_as_uint(x), false, false); return __builtin_fmaxf(__uint_as_float(rr[0]), __uint_as_float(rr[1])); }
DI float swap_add(float x) { auto rr = __builtin_amdgcn_permlane32_swap(__float_as_uint(x), __float_as_uint(x), false, false); return __uint_as_float(rr[0]) + __uint_as_float(rr[1]); }
DI srd_t mk_srd(const void* p, int bytes) { return __builtin_amdgcn_make_buffer_rsrc((void*)p, (short)0, bytes, 0x00020000); }
DI bf16x4 ldstr(unsigned a) { return __builtin_amdgcn_ds_read_tr16_b64_v4bf16((LAS bf16x4*)a); }
DI bf16x8 ldsf(unsigned a) { return *(const LAS bf16x8*)a; }
DI bf16x8 cat4(bf16x4 a, bf16x4 b) { return __builtin_shufflevector(a, b, 0, 1, 2, 3, 4, 5, 6, 7); }
#define SCALARIZE2(a, b) do {} while (0)
DI void dma1(unsigned m0v, unsigned voff, srd_t srd, unsigned soff) {
    SCALARIZE2(m0v, soff);
    asm volatile("s_mov_b32 m0, %0\n\ts_nop 0\n\tbuffer_load_dwordx4 %1, %2, %3 offen lds" :: "s"(m0v), "v"(voff), "s"(srd), "s"(soff) : "memory");
}
DI void dma1s(unsigned m0v, unsigned voff, srd_t srd, unsigned soff) {
    SCALARIZE2(m0v, soff);
    asm volatile("s_mov_b32 m0, %0\n\ts_nop 0\n\tbuffer_load_dwordx4 %1, %2, %3 offen sc1 lds" :: "s"(m0v), "v"(voff), "s"(srd), "s"(soff) : "memory");
}
DI void touch(unsigned m0v, unsigned voff, srd_t srd, unsigned soff) {
    SCALARIZE2(m0v, soff);
    asm volatile("s_mov_b32 m0, %0\n\ts_nop 0\n\tbuffer_load_dword %1, %2, %3 offen lds" :: "s"(m0v), "v"(voff), "s"(srd), "s"(soff) : "memory");
}
DI void store16_sc1(u32x4 w, unsigned voff, srd_t srd, unsigned soff) {
    unsigned z = soff; SCALARIZE2(soff, z);
    asm volatile("buffer_store_dwordx4 %0, %1, %2, %3 offen sc1\n\ts_nop 1" :: "v"(w), "v"(voff), "s"(srd), "s"(soff) : "memory");
}
template <int N> DI void wait_vm() { asm volatile("s_waitcnt vmcnt(%0)" :: "i"(N) : "memory"); }
template <int N> DI void wait_bar() { asm volatile("s_waitcnt vmcnt(%0) lgkmcnt(0)\n\ts_barrier" :: "i"(N) : "memory"); }
#define BARRIER() asm volatile("s_waitcnt lgkmcnt(0)\n\ts_barrier" ::: "memory")

struct Stamper { unsigned v; int n; int lane;
    DI void operator()() {
    } };

constexpr size_t WS_FLAG = 0;
constexpr size_t WS_KF = 1 << 20, WS_VF = WS_KF + (16 << 20), WS_CF = WS_VF + (16 << 20), WS_PHIQ = WS_CF + (16 << 20), WS_OF = WS_PHIQ + (4 << 20), WS_BST = WS_OF + (2 << 20), WS_BRED = WS_BST + (3 << 20), WS_XCH = WS_BRED + (1 << 20), WS_DBG = 77 << 20;
constexpr int FL_STRIDE = 16;
constexpr int FL_EPOCH = 0, FL_KV = 32, FL_BS = 32 + 256, FL_BR = 32 + 512, FL_XF = 32 + 768, FL_C = FL_XF + 256;
typedef GAS u64 gu64;
DI u64 ld_agent(const u64* p) { return __hip_atomic_load((gu64*)p, __ATOMIC_RELAXED, __HIP_MEMORY_SCOPE_AGENT); }
DI void st_agent(u64* p, u64 v) { __hip_atomic_store((gu64*)p, v, __ATOMIC_RELAXED, __HIP_MEMORY_SCOPE_AGENT); }
DI u64 mix64(u64 z) { z += 0x9E3779B97F4A7C15ull; z = (z ^ (z >> 30)) * 0xBF58476D1CE4E5B9ull; z = (z ^ (z >> 27)) * 0x94D049BB133111EBull; return z ^ (z >> 31); }
DI f32x4 mfma16_16(u32x2 a, u32x2 b, f32x4 c) { return __builtin_amdgcn_mfma_f32_16x16x16bf16_1k(__builtin_bit_cast(s16x4, a), __builtin_bit_cast(s16x4, b), c, 0, 0, 0); }
DI float rbf16(float x) { return __uint_as_float(cvtpk(x, 0.f) << 16); }
DI float x16_max(float x) { auto rr = __builtin_amdgcn_permlane16_swap(__float_as_uint(x), __float_as_uint(x), false, false); return __builtin_fmaxf(__uint_as_float(rr[0]), __uint_as_float(rr[1])); }
DI float x16_add(float x) { auto rr = __builtin_amdgcn_permlane16_swap(__float_as_uint(x), __float_as_uint(x), false, false); return __uint_as_float(rr[0]) + __uint_as_float(rr[1]); }
DI float grp_max(float x) { return swap_max(x16_max(x)); }
DI float grp_sum(float x) { return swap_add(x16_add(x)); }
constexpr float LOG2E = 1.4426950408889634f;
constexpr unsigned B1_RED = 147456;
struct B1Ctx { srd_t srdX, srdW; unsigned vx, vw[2], sx, sw; int wid; unsigned lds0; };
DI void b1_setup(B1Ctx& c, const u16* Bx, const u16* Wqkv, unsigned lds0, int rc, int hg, int wid, int lane) {
    c.srdX = mk_srd(Bx, 2097152); c.srdW = mk_srd(Wqkv, 512 * 1536 * 2); c.wid = wid; c.lds0 = lds0;
    c.vx = (lane >> 3) * 1024 + (((lane & 7) ^ ((4 * wid + (lane >> 4)) & 7)) * 16);
#pragma unroll
    for (int p = 0; p < 2; ++p) c.vw[p] = (lane >> 3) * 3072 + (((lane & 7) ^ (2 * (((lane >> 4) & 1) | (p << 1)))) * 16);
    c.sx = (64 * rc + 8 * wid) * 1024; c.sw = hg * 128;
}
DI void b1_issue(const B1Ctx& c, int kt) {
    const unsigned st = c.lds0 + (kt & 3) * 32768;
    dma1(st + c.wid * 1024, c.vx, c.srdX, c.sx + kt * 128);
#pragma unroll
    for (int i = 0; i < 3; ++i) { const int pw = 3 * c.wid + i, m = pw >> 3, pr = pw & 7;
        dma1(st + 8192 + pw * 1024, (pr & 1) ? c.vw[1] : c.vw[0], c.srdW, c.sw + (kt * 64 + 8 * pr) * 3072 + m * 1024); }
}
struct B1Acc { f32x4 aq[2], ak[2], av[2]; };
struct B1Fr { bf16x8 x[2][2]; bf16x4 w[2][3][2]; };
struct B1Ad { unsigned xa[2][2], wa[2]; };
template <int KT> DI void b1_read(B1Fr& f, const B1Ad& ad) {
    constexpr unsigned st = (KT & 1) * 32768; constexpr int sp = (KT >> 1) & 1;
#pragma unroll
    for (int ks = 0; ks < 2; ++ks) { const unsigned xa = ad.xa[sp][ks];
        f.x[ks][0] = ldsf(xa + st); f.x[ks][1] = ldsf(xa + st + 2048);
#pragma unroll
        for (int m = 0; m < 3; ++m) { f.w[ks][m][0] = ldstr(ad.wa[sp] + st + m * 8192 + ks * 4096); f.w[ks][m][1] = ldstr(ad.wa[sp] + st + m * 8192 + ks * 4096 + 512); } }
}
DI void b1_mma(B1Acc& a, const B1Fr& f) {
#pragma unroll
    for (int ks = 0; ks < 2; ++ks) {
        const bf16x8 wq = cat4(f.w[ks][0][0], f.w[ks][0][1]), wk = cat4(f.w[ks][1][0], f.w[ks][1][1]), wv = cat4(f.w[ks][2][0], f.w[ks][2][1]);
        a.aq[0] = MFMA16(wq, f.x[ks][0], a.aq[0]); a.aq[1] = MFMA16(wq, f.x[ks][1], a.aq[1]); a.ak[0] = MFMA16(wk, f.x[ks][0], a.ak[0]); a.ak[1] = MFMA16(wk, f.x[ks][1], a.ak[1]);
        a.av[0] = MFMA16(f.x[ks][0], wv, a.av[0]); a.av[1] = MFMA16(f.x[ks][1], wv, a.av[1]);
    }
}
template <class St, class H6, class HE>
DI void b1_gemm(const B1Ctx& c, B1Acc& a, int lane, bool early, St store, H6 afterB6, HE afterEnd) {
    const int l16 = lane & 15, g = lane >> 4, q4 = l16 >> 2, p4 = lane & 3, wid = c.wid;
    const int row = 32 * (wid >> 2) + l16;
    B1Ad ad;
#pragma unroll
    for (int sp = 0; sp < 2; ++sp) {
        ad.xa[sp][0] = c.lds0 + sp * 65536 + row * 128 + (((0 + g) ^ ((row >> 1) & 7)) << 4); ad.xa[sp][1] = c.lds0 + sp * 65536 + row * 128 + (((4 + g) ^ ((row >> 1) & 7)) << 4);
        ad.wa[sp] = c.lds0 + sp * 65536 + 8192 + (8 * g + q4) * 128 + (((wid & 3) ^ (((q4 >> 1) & 1) | ((g & 1) << 1))) * 32) + p4 * 8;
        PIN(ad.xa[sp][0]); PIN(ad.xa[sp][1]); PIN(ad.wa[sp]); }
#pragma unroll
    for (int r = 0; r < 2; ++r) { a.aq[r] = f32x4{}; a.ak[r] = f32x4{}; a.av[r] = f32x4{}; }
    B1Fr f;
#define B1T(KT, PRE) do { b1_read<KT>(f, ad); SBAR(); PRE; store(KT); SBAR(); b1_mma(a, f); SBAR(); } while (0)
#define B1W(E, L) do { if (early) wait_bar<E>(); else wait_bar<L>(); } while (0)
    B1T(0, (void)0);
    B1W(9, 8);   B1T(1, b1_issue(c, 4));
    B1W(10, 9);  B1T(2, b1_issue(c, 5));
    B1W(11, 10); B1T(3, b1_issue(c, 6));
    B1W(11, 11); B1T(4, b1_issue(c, 7));
    B1W(11, 11); B1T(5, (void)0);
    B1W(7, 7);   B1T(6, afterB6());
    B1W(9, 9);   B1T(7, (void)0);
#undef B1W
#undef B1T
    BARRIER(); afterEnd();
}
template <int N, class Tick> DI void phi_map(const f32x4 (&p)[N], u32x4 (&out)[N], Tick tick) {
    float x[N][4], mx[N], nm[N], ep[N][4], en[N][4], sp[N], sn[N];
#pragma unroll
    for (int i = 0; i < N; ++i) {
#pragma unroll
        for (int e = 0; e < 4; ++e) x[i][e] = rbf16(p[i][e]) * LOG2E;
        mx[i] = __builtin_fmaxf(__builtin_fmaxf(x[i][0], x[i][1]), __builtin_fmaxf(x[i][2], x[i][3])); nm[i] = -__builtin_fminf(__builtin_fminf(x[i][0], x[i][1]), __builtin_fminf(x[i][2], x[i][3]));
        if (i & 1) tick(i >> 1); }
#pragma unroll
    for (int i = 0; i < N; ++i) { mx[i] = x16_max(mx[i]); nm[i] = x16_max(nm[i]); if (i & 1) tick(2 + (i >> 1)); }
#pragma unroll
    for (int i = 0; i < N; ++i) { mx[i] = swap_max(mx[i]); nm[i] = swap_max(nm[i]); if (i & 1) tick(4 + (i >> 1)); }
#pragma unroll
    for (int i = 0; i < N; ++i) {
#pragma unroll
        for (int e = 0; e < 4; ++e) { ep[i][e] = __builtin_amdgcn_exp2f(x[i][e] - mx[i]); en[i][e] = __builtin_amdgcn_exp2f(-x[i][e] - nm[i]); }
        sp[i] = (ep[i][0] + ep[i][1]) + (ep[i][2] + ep[i][3]); sn[i] = (en[i][0] + en[i][1]) + (en[i][2] + en[i][3]); tick(6 + i); }
#pragma unroll
    for (int i = 0; i < N; ++i) { sp[i] = x16_add(sp[i]); sn[i] = x16_add(sn[i]); if (i & 1) tick(10 + (i >> 1)); }
#pragma unroll
    for (int i = 0; i < N; ++i) { sp[i] = __builtin_amdgcn_rcpf(swap_add(sp[i])); sn[i] = __builtin_amdgcn_rcpf(swap_add(sn[i])); if (i & 1) tick(12 + (i >> 1)); }
#pragma unroll
    for (int i = 0; i < N; ++i) { out[i] = (u32x4){cvtpk(ep[i][0] * sp[i], ep[i][1] * sp[i]), cvtpk(ep[i][2] * sp[i], ep[i][3] * sp[i]), cvtpk(en[i][0] * sn[i], en[i][1] * sn[i]), cvtpk(en[i][2] * sn[i], en[i][3] * sn[i])};
        if (i & 1) tick(14 + (i >> 1)); }
}
template <class Tick>
DI void b1_phi(const B1Acc& a, Tick tick, u32x2 wp, u16* PhiQ, float* Bst, unsigned lds0, int rc, int hg, int wid, int lane, Stamper& stamp) {
    const int h = 4 * hg + (wid & 3), row0 = 64 * rc + 32 * (wid >> 2);
    const int l16 = lane & 15, g = lane >> 4;
    f32x4 sp = f32x4{}, sn = f32x4{}; float zp = 0.f, zn = 0.f;
    f32x4 pin[4]; u32x4 pout[4];
#pragma unroll
    for (int r = 0; r < 2; ++r) {
        const u32x2 qb = {cvtpk(a.aq[r][0], a.aq[r][1]), cvtpk(a.aq[r][2], a.aq[r][3])}, kb = {cvtpk(a.ak[r][0], a.ak[r][1]), cvtpk(a.ak[r][2], a.ak[r][3])};
        pin[r] = mfma16_16(wp, qb, f32x4{}); pin[2 + r] = mfma16_16(wp, kb, f32x4{});
    }
    phi_map<4>(pin, pout, tick);
    const u32x4 wq[2] = {pout[0], pout[1]}, wk[2] = {pout[2], pout[3]};
    const unsigned tb = lds0 + B1_RED + wid * 1024;
#pragma unroll
    for (int r = 0; r < 2; ++r) {
        *(LAS u32x2*)(tb + l16 * 32 + g * 8) = (u32x2){wk[r][0], wk[r][1]};
        *(LAS u32x2*)(tb + 512 + l16 * 32 + g * 8) = (u32x2){wk[r][2], wk[r][3]};
        __builtin_amdgcn_raw_buffer_store_b128(wq[r], mk_srd(PhiQ, 4194304), ((h * 128 + (row0 >> 4) + r) * 64 + lane) * 16, 0, 0);
        asm volatile("s_waitcnt lgkmcnt(0)" ::: "memory");
        const unsigned ta = tb + (4 * g + (l16 >> 2)) * 32 + (lane & 3) * 8;
        u32x2 pp = __builtin_bit_cast(u32x2, ldstr(ta)), pn = __builtin_bit_cast(u32x2, ldstr(ta + 512));
        asm volatile("s_waitcnt lgkmcnt(0)" : "+v"(pp), "+v"(pn) :: "memory");
        const u32x2 vb = {cvtpk(a.av[r][0], a.av[r][1]), cvtpk(a.av[r][2], a.av[r][3])};
        sp = mfma16_16(pp, vb, sp); sn = mfma16_16(pn, vb, sn);
        zp += (__uint_as_float(pp[0] << 16) + __uint_as_float(pp[0] & 0xffff0000u)) + (__uint_as_float(pp[1] << 16) + __uint_as_float(pp[1] & 0xffff0000u));
        zn += (__uint_as_float(pn[0] << 16) + __uint_as_float(pn[0] & 0xffff0000u)) + (__uint_as_float(pn[1] << 16) + __uint_as_float(pn[1] & 0xffff0000u));
    }
    zp = grp_sum(zp); zn = grp_sum(zn);
    BARRIER();
    LAS char* red = (LAS char*)(lds0 + B1_RED + (wid & 3) * 2176);
    if (wid >= 4) { *(LAS f32x4*)(red + lane * 32) = sp; *(LAS f32x4*)(red + lane * 32 + 16) = sn; if (lane < 16) *(LAS f32x2*)(red + 2048 + lane * 8) = (f32x2){zp, zn}; }
    BARRIER();
    sp += *(const LAS f32x4*)(red + lane * 32); sn += *(const LAS f32x4*)(red + lane * 32 + 16);
    const f32x2 zo = *(const LAS f32x2*)(red + 2048 + l16 * 8);
    const srd_t srd = mk_srd(Bst + (size_t)(h * 32 + rc) * 576, wid < 4 ? 2304 : 0);
    store16_sc1(__builtin_bit_cast(u32x4, sp), lane * 32, srd, 0u); store16_sc1(__builtin_bit_cast(u32x4, sn), lane * 32 + 16, srd, 0u);
    __builtin_amdgcn_raw_buffer_store_b64((u32x2){__float_as_uint(zp + zo[0]), __float_as_uint(zn + zo[1])}, srd, lane < 16 ? 2048 + lane * 8 : 0x7fffffff, 0, 16);
}
template <int N> DI void waitN(const u64* flags, u64 tok, int lane) {
    const u64* p = flags + (lane & (N - 1)) * FL_STRIDE;
    for (unsigned spins = 0; spins < (1u << 15); ++spins) { const u64 v = ld_agent(p); if (__all(v == tok)) break; __builtin_amdgcn_s_sleep(1); }
    asm volatile("" ::: "memory");
}
struct B2a { f32x4 v[16]; };
DI void b2a_load(B2a& b, const float* Bst, int rc, int hg, int lane) {
    const int h = 4 * hg + (rc >> 3), c = lane & 31, half = lane >> 5;
    const srd_t srd = mk_srd(Bst + (size_t)h * 32 * 576, 32 * 2304);
    const unsigned vo = (c < 18 ? (18 * (rc & 7) + c) * 16 : 0x7fffffff) + half * 16 * 2304;
#pragma unroll
    for (int i = 0; i < 16; ++i) b.v[i] = __builtin_bit_cast(f32x4, __builtin_amdgcn_raw_buffer_load_b128(srd, vo, i * 2304, 16));
}
DI void b2a_finish(const B2a& b, float* Bred, int rc, int hg, int lane) {
    const int h = 4 * hg + (rc >> 3), c = lane & 31, half = lane >> 5;
    f32x4 s = b.v[0];
#pragma unroll
    for (int i = 1; i < 16; ++i) s += b.v[i];
#pragma unroll
    for (int e = 0; e < 4; ++e) s[e] = swap_add(s[e]);
    const srd_t srdR = mk_srd(Bred + (size_t)h * 576, 2304);
    store16_sc1(__builtin_bit_cast(u32x4, s), (half == 0 && c < 18) ? (18 * (rc & 7) + c) * 16 : 0x7fffffffu, srdR, 0u);
}
template <int NRB>
DI void b2b_body(const u16* PhiQ, const float* Bred, u16* Of, const u64* flagsR, u64 tok, int h, int rb0, int lane) {
    PIN(lane);
    const int l16 = lane & 15, g = lane >> 4;
    bf16x8 ph[NRB];
#pragma unroll
    for (int r = 0; r < NRB; ++r) ph[r] = *(const bf16x8*)((const char*)PhiQ + ((size_t)(h * 128 + rb0 + r) * 64 + lane) * 16);
    waitN<8>(flagsR, tok, lane);
    const srd_t srd = mk_srd(Bred + (size_t)h * 576, 2304);
    const f32x4 sp = __builtin_bit_cast(f32x4, __builtin_amdgcn_raw_buffer_load_b128(srd, lane * 32, 0, 16)), sn = __builtin_bit_cast(f32x4, __builtin_amdgcn_raw_buffer_load_b128(srd, lane * 32 + 16, 0, 16));
    const f32x4 za = __builtin_bit_cast(f32x4, __builtin_amdgcn_raw_buffer_load_b128(srd, 2048 + g * 32, 0, 16)), zb = __builtin_bit_cast(f32x4, __builtin_amdgcn_raw_buffer_load_b128(srd, 2048 + g * 32 + 16, 0, 16));
    const u32x4 kvw = {cvtpk(sp[0], sp[1]), cvtpk(sp[2], sp[3]), cvtpk(sn[0], sn[1]), cvtpk(sn[2], sn[3])};
    const u32x4 zw = {cvtpk(za[0], za[2]), cvtpk(zb[0], zb[2]), cvtpk(za[1], za[3]), cvtpk(zb[1], zb[3])};
    const srd_t srdO = mk_srd(Of, 2097152);
#pragma unroll
    for (int r = 0; r < NRB; ++r) { const int rb16 = rb0 + r;
        const f32x4 num = MFMA16(__builtin_bit_cast(bf16x8, kvw), ph[r], f32x4{});
        const f32x4 den = MFMA16(__builtin_bit_cast(bf16x8, zw), ph[r], f32x4{});
        const float dn = rbf16(rbf16(den[0]) + 1e-6f);
        const u32x2 w = {cvtpk(rbf16(num[0]) / dn, rbf16(num[1]) / dn), cvtpk(rbf16(num[2]) / dn, rbf16(num[3]) / dn)};
        __builtin_amdgcn_raw_buffer_store_b64(w, srdO, (((rb16 >> 1) * 32 + h) * 2 + (g & 1)) * 512 + ((rb16 & 1) * 16 + l16) * 16 + (g >> 1) * 8, 0, 16); }
}
template <int MB, int NB> struct Frags { bf16x8 a[MB]; bf16x4 w0[NB], w1[NB]; };
struct GAddr { unsigned a[4]; unsigned w[2]; };

template <int MB, int NB, int S> DI void frag_read(Frags<MB, NB>& f, const GAddr& g, int i) {
    if (i == 2) { f.a[0] = ldsf(g.a[S]); return; }
    const int k = i < 2 ? i : i - 1;
    if (k < 2 * NB) { const int nb = k >> 1; if (k & 1) f.w1[nb] = ldstr(g.w[1] + S * 4096 + nb * 512); else f.w0[nb] = ldstr(g.w[0] + S * 4096 + nb * 512); }
    else { const int mb = k - 2 * NB + 1; if (mb < MB) f.a[mb] = ldsf(g.a[S] + mb * 4096); }
}
template <int MB, int NB, bool ACT_A, int SN, int RPG, class Hook>
DI void kstep(f32x16 (&acc)[MB][NB], const Frags<MB, NB>& cur, Frags<MB, NB>& nxt, const GAddr& g, Hook hook) {
    constexpr int NM = MB * NB, NR = MB + 2 * NB;
#pragma unroll
    for (int i = 0; i < NM; ++i) {
        const int mb = i / NB, nb = i % NB;
        const bf16x8 w = cat4(cur.w0[nb], cur.w1[nb]);
        acc[mb][nb] = ACT_A ? MFMA32(cur.a[mb], w, acc[mb][nb]) : MFMA32(w, cur.a[mb], acc[mb][nb]);
        SBAR();
        if constexpr (SN >= 0) {
#pragma unroll
            for (int r = 0; r < RPG; ++r) if (i * RPG + r < NR) frag_read<MB, NB, SN < 0 ? 0 : SN>(nxt, g, i * RPG + r);
        }
        hook(i);
        SBAR();
    }
}
struct NoHook { DI void operator()(int) const {} };

struct ProjCtx {
    srd_t srdC, srdX, srdW, srdWq, srdO;
    unsigned va[2], vw[2], vwq, vo, lds0, l128;
    unsigned sA, sW, sWq, sXa;
    int wid, isV;
};
constexpr unsigned TOUCH_LDS = 163840 - 256;
DI void issueKV(const ProjCtx& c, int kt, int i) {
    const unsigned st = c.lds0 + (kt & 1) * 65536;
    if (i < 4) dma1(st + (4 * c.wid + i) * 1024, c.va[i & 1], c.srdC, c.sA + i * 8192 + kt * 128);
    else dma1(st + 32768 + (c.wid >> 2) * 16384 + (4 * (c.wid & 3) + (i - 4)) * 1024, c.vw[(i - 4) >> 1], c.srdW, c.sW + ((i - 4) >> 1) * 65536 + ((i - 4) & 1) * 128 + kt * 524288);
}
DI void issueQ(const ProjCtx& c, int kt, int i) {
    const unsigned st = c.lds0 + (kt % 3) * 49152;
    if (i < 4) dma1(st + (4 * c.wid + i) * 1024, c.va[i & 1], c.srdX, c.sXa + i * 8192 + kt * 128);
    else dma1(st + 32768 + (2 * c.wid + (i - 4)) * 1024, c.vwq, c.srdWq, c.sWq + (i - 4) * 128 + kt * 524288);
}

#define TOUCHMODE 3
template <int WHICH> DI void touch_g(unsigned m0v, unsigned voff, const void* base) {
    if (!(TOUCHMODE & WHICH)) voff = 0;
    const u64 b = (u64)base; const u64 sb = ((u64)rfl((unsigned)(b >> 32)) << 32) | rfl((unsigned)b);
    asm volatile("s_nop 4\n\ts_mov_b32 m0, %0\n\ts_nop 0\n\tglobal_load_lds_dword %1, %2" :: "s"(rfl(m0v)), "v"(voff), "s"(sb) : "memory");
}
template <bool ISV, class Touch, class IssueB1>
DI void kv_part(const ProjCtx& c, GAddr g, u32x4 (&pk)[16], Touch touchk, IssueB1 issueB1, Stamper& stamp) {
    f32x16 acc[4][2];
#pragma unroll
    for (int a = 0; a < 4; ++a) { acc[a][0] = f32x16{}; acc[a][1] = f32x16{}; }
    Frags<4, 2> f0, f1;
    wait_bar<9>();
    stamp();
#pragma unroll
    for (int i = 0; i < 8; ++i) frag_read<4, 2, 0>(f0, g, i);
    for (int kt = 0; kt < 6; ++kt) {
        kstep<4, 2, ISV, 1, 2>(acc, f0, f1, g, [&](int i) { if (kt > 0 && (i & 1)) issueKV(c, kt + 1, 4 + (i >> 1)); });
        kstep<4, 2, ISV, 2, 2>(acc, f1, f0, g, [&](int i) { if (i == 1) touchk(kt); });
        kstep<4, 2, ISV, 3, 2>(acc, f0, f1, g, NoHook{});
        wait_bar<1>();
#pragma unroll
        for (int i = 0; i < 4; ++i) g.a[i] ^= 65536u;
        g.w[0] ^= 65536u; g.w[1] ^= 65536u;
        kstep<4, 2, ISV, 0, 2>(acc, f1, f0, g, [&](int i) { if (i & 1) issueKV(c, kt + 2, i >> 1); });
    }
    kstep<4, 2, ISV, 1, 2>(acc, f0, f1, g, [&](int i) { if (i & 1) issueKV(c, 7, 4 + (i >> 1)); });
    kstep<4, 2, ISV, 2, 2>(acc, f1, f0, g, NoHook{});
    kstep<4, 2, ISV, 3, 2>(acc, f0, f1, g, NoHook{});
    stamp();
    wait_bar<0>();
    stamp();
#pragma unroll
    for (int i = 0; i < 4; ++i) g.a[i] ^= 65536u;
    g.w[0] ^= 65536u; g.w[1] ^= 65536u;
    kstep<4, 2, ISV, 0, 2>(acc, f1, f0, g, [&](int i) { if (i == 1) issueB1(0); if (i == 5) issueB1(1); });
    kstep<4, 2, ISV, 1, 2>(acc, f0, f1, g, NoHook{});
    kstep<4, 2, ISV, 2, 2>(acc, f1, f0, g, NoHook{});
    kstep<4, 2, ISV, 3, 2>(acc, f0, f1, g, NoHook{});
    stamp();
    wait_bar<4>();
    stamp();
    kstep<4, 2, ISV, -1, 2>(acc, f1, f0, g, [&](int i) { if (i == 1) issueB1(2); if (i == 5) issueB1(3); });
#pragma unroll
    for (int mb = 0; mb < 4; ++mb)
#pragma unroll
        for (int nb = 0; nb < 2; ++nb)
#pragma unroll
            for (int s = 0; s < 2; ++s)
#pragma unroll
                for (int e = 0; e < 4; ++e) pk[(mb * 2 + nb) * 2 + s][e] = cvtpk(acc[mb][nb][8 * s + 2 * e], acc[mb][nb][8 * s + 2 * e + 1]);
#pragma unroll
    for (int n = 0; n < 16; ++n) PIN(pk[n]);
}
DI void kv_store(const ProjCtx& c, const u32x4 (&pk)[16], int n) {
    const int mb = n >> 2, nb = (n >> 1) & 1, s = n & 1;
    const unsigned offK = (mb >> 1) * 16384 + (mb & 1) * 512 + (4 * nb + 2 * s) * 1024, offV = (mb >> 1) * 16384 + (2 * (mb & 1) + s) * 4096 + nb * 512;
    store16_sc1(pk[n], c.vo, c.srdO, c.isV ? offV : offK);
}
template <int KT, class Pub>
DI void q_tile(const ProjCtx& c, bool early, GAddr& g, f32x16 (&acc)[1][4], Frags<1, 4>& f0, Frags<1, 4>& f1, const u32x4 (&pk)[16], Pub after, Stamper& stamp) {
    kstep<1, 4, false, 1, 3>(acc, f0, f1, g, [&](int i) { if (KT >= 1 && KT + 2 < 8 && i < 3) issueQ(c, KT + 2, 3 + i); });
    kstep<1, 4, false, 2, 3>(acc, f1, f0, g, [&](int i) {
        if (i == 1) { if (early) { if (KT < 2) kv_store(c, pk, 12 + 2 * (KT < 2 ? KT : 0)); } else if (KT < 5) kv_store(c, pk, 11 + (KT < 5 ? KT : 0)); } });
    kstep<1, 4, false, 3, 3>(acc, f0, f1, g, [&](int i) { if (i == 1 && early && KT < 2) kv_store(c, pk, 13 + 2 * (KT < 2 ? KT : 0)); });
    if constexpr (KT < 7) {
        constexpr int WE = KT == 0 ? 17 : (KT == 1 ? 19 : (KT == 2 ? 8 : (KT <= 5 ? 6 : 0)));
        constexpr int WL = KT == 0 ? 17 : (KT == 1 ? 18 : (KT <= 4 ? 8 : (KT == 5 ? 7 : 0)));
        const bool w7 = KT == 5 && c.wid == 7;
        if (early) { if (w7) wait_bar<WE + 16>(); else wait_bar<WE>(); } else { if (w7) wait_bar<WL + 16>(); else wait_bar<WL>(); }
        stamp();
        after(KT);
        constexpr int d = ((KT + 1) % 3 - KT % 3) * 49152;
#pragma unroll
        for (int i = 0; i < 4; ++i) { g.a[i] += d; PIN(g.a[i]); }
        g.w[0] += d; g.w[1] += d; PIN(g.w[0]); PIN(g.w[1]);
        kstep<1, 4, false, 0, 3>(acc, f1, f0, g, [&](int i) { if (KT + 3 < 8 && i < 3) issueQ(c, KT + 3, i); });
    } else kstep<1, 4, false, -1, 3>(acc, f1, f0, g, NoHook{});
}

DI void proj_setup(ProjCtx& c, const u16* X, const u16* Ckv, const u16* Wq, const u16* Wuk, const u16* Wuv, u16* Kf, u16* Vf, unsigned lds0, int h, int j, int wid, int lane) {
    const int r32 = lane & 31, hi = lane >> 5, wr = wid >> 2, wc = wid & 3;
    c.wid = wid; c.isV = wc >> 1; c.lds0 = lds0;
    c.srdC = mk_srd(Ckv, 2097152); c.srdX = mk_srd(X, 2097152);
    c.srdW = mk_srd((wid < 4 ? Wuk : Wuv), 4194304); c.srdWq = mk_srd(Wq, 4194304);
    c.srdO = mk_srd((const char*)(c.isV ? Vf : Kf) + (size_t)h * 524288, 524288);
#pragma unroll
    for (int p = 0; p < 2; ++p) {
        c.va[p] = (lane >> 3) * 1024 + (((lane & 7) ^ (4 * p + (lane >> 4))) * 16);
        c.vw[p] = ((lane >> 2) & 7) * 8192 + (4 * (lane >> 5) + ((lane & 3) ^ ((2 * p + ((lane >> 4) & 1)) & 3))) * 16;
    }
    c.vwq = (wid & 1) ? c.vw[1] : c.vw[0];
    c.l128 = lane * 128;
    c.sA = (256 * j + 32 * wid) * 1024;
    c.sW = (16 * (wid & 3)) * 8192 + h * 256;
    c.sXa = (wid < 4 ? 128 * (15 - j) + 32 * wid : 128 * j + 32 * (wid - 4)) * 1024;
    c.sWq = (8 * wid) * 8192 + h * 256;
    c.vo = c.isV ? hi * 2048 + r32 * 16 + (4 * j + 2 * wr) * 16384 + (wc & 1) * 1024 : hi * 1024 + r32 * 16 + (4 * j + 2 * wr) * 16384 + (wc & 1) * 8192;
}
template <class Touch, class IssueB1>
DI void kv_run(const ProjCtx& c, int lane, u32x4 (&pk)[16], Touch touchk, IssueB1 issueB1, Stamper& stamp) {
    const int r32 = lane & 31, hi = lane >> 5, wid = c.wid, wr = wid >> 2, wc = wid & 3;
    const int q4 = (lane & 15) >> 2, p4 = lane & 3, blk = (lane >> 4) & 1;
    GAddr g;
#pragma unroll
    for (int s = 0; s < 4; ++s) g.a[s] = c.lds0 + (128 * wr + r32) * 128 + (((2 * s + hi) ^ ((r32 >> 1) & 7)) << 4);
#pragma unroll
    for (int t = 0; t < 2; ++t) g.w[t] = c.lds0 + 32768 + (wc >> 1) * 16384 + 2048 * hi + 1024 * (wc & 1) + 64 * (4 * t + q4) + 16 * ((2 * blk + (p4 >> 1)) ^ ((2 * hi + t) & 3)) + 8 * (p4 & 1);
    if (c.isV) kv_part<true>(c, g, pk, touchk, issueB1, stamp); else kv_part<false>(c, g, pk, touchk, issueB1, stamp);
}
template <class Pub>
DI void q_run(const ProjCtx& c, bool early, int lane, const u32x4 (&pk)[16], bf16x8 (&q)[8], Pub publish, Stamper& stamp) {
    const int r32 = lane & 31, hi = lane >> 5, wid = c.wid;
    const int q4 = (lane & 15) >> 2, p4 = lane & 3, blk = (lane >> 4) & 1;
    GAddr g;
#pragma unroll
    for (int s = 0; s < 4; ++s) g.a[s] = c.lds0 + (32 * wid + r32) * 128 + (((2 * s + hi) ^ ((r32 >> 1) & 7)) << 4);
#pragma unroll
    for (int t = 0; t < 2; ++t) g.w[t] = c.lds0 + 32768 + 2048 * hi + 64 * (4 * t + q4) + 16 * ((2 * blk + (p4 >> 1)) ^ ((2 * hi + t) & 3)) + 8 * (p4 & 1);
#pragma unroll
    for (int i = 0; i < 4; ++i) PIN(g.a[i]);
    PIN(g.w[0]); PIN(g.w[1]);
    f32x16 acc[1][4];
#pragma unroll
    for (int a = 0; a < 4; ++a) acc[0][a] = f32x16{};
    Frags<1, 4> f0, f1;
#pragma unroll
    for (int i = 0; i < 9; ++i) frag_read<1, 4, 0>(f0, g, i);
    q_tile<0>(c, early, g, acc, f0, f1, pk, publish, stamp); q_tile<1>(c, early, g, acc, f0, f1, pk, publish, stamp); q_tile<2>(c, early, g, acc, f0, f1, pk, publish, stamp); q_tile<3>(c, early, g, acc, f0, f1, pk, publish, stamp);
    q_tile<4>(c, early, g, acc, f0, f1, pk, publish, stamp); q_tile<5>(c, early, g, acc, f0, f1, pk, publish, stamp); q_tile<6>(c, early, g, acc, f0, f1, pk, publish, stamp); q_tile<7>(c, early, g, acc, f0, f1, pk, publish, stamp);
    constexpr float QSC = 0.08838834764831845f * 1.4426950408889634f;
#pragma unroll
    for (int nb = 0; nb < 4; ++nb)
#pragma unroll
        for (int s = 0; s < 2; ++s) { u32x4 w;
#pragma unroll
            for (int e = 0; e < 4; ++e) w[e] = cvtpk(acc[0][nb][8 * s + 2 * e] * QSC, acc[0][nb][8 * s + 2 * e + 1] * QSC);
            q[2 * nb + s] = __builtin_bit_cast(bf16x8, w); }
}
template <int OFF, bool SETM0> DI void dma_piece(unsigned m0v, unsigned voff, srd_t srd, unsigned soff) {
    if constexpr (SETM0) asm volatile("s_mov_b32 m0, %0\n\ts_nop 0\n\tbuffer_load_dwordx4 %1, %2, %3 offen offset:%4 sc1 lds" :: "s"(m0v), "v"(voff), "s"(srd), "s"(soff), "i"(OFF) : "memory");
    else asm volatile("buffer_load_dwordx4 %0, %1, %2 offen offset:%3 sc1 lds" :: "v"(voff), "s"(srd), "s"(soff), "i"(OFF) : "memory");
}
#define ATT_THRV 64.f
constexpr float ATT_THR = ATT_THRV;
#define ATT_WIN 6
constexpr int ATT_W = ATT_WIN;

struct AttnState { f32x16 o[4]; f32x16 s0, s1, negm; bf16x8 q[8]; u32x4 p[4]; float l, mhat; };
struct AttnCtx {
    srd_t srdK, srdV, srdC; unsigned voff, lane16, kslot0, vslot0, coffL;
    int g, nl  , thrg, dq0, relH, jump; unsigned qstash;
    unsigned soA, soB; int rel;
    srd_t srdF; u64 tok, prev; unsigned known;
};

DI u64 ld_flag8(const AttnCtx& c) { return __builtin_bit_cast(u64, __builtin_amdgcn_raw_buffer_load_b64(c.srdF, (c.lane16 & 0x70u) << 3, 0, 16)); }
template <bool DO_QK, bool DO_PV, int KP, int VP>
DI void m_phase(AttnState& st, unsigned kbase, unsigned vbase, bf16x8 (&pre)[ATT_W]) {
    constexpr int N0 = DO_QK ? 0 : 16, N1 = DO_PV ? 32 : 16;
    bf16x8 fr[32];
#pragma unroll
    for (int i = 0; i < ATT_W; ++i) fr[N0 + i] = pre[i];
#pragma unroll
    for (int i = N0; i < N1; ++i) {
        const int n = i + ATT_W;
        if (n < N1) { if (n < 16) fr[n] = ldsf(kbase + KP * 16384 + (n >> 1) * 2048 + (n & 1) * 512); else fr[n] = ldsf(vbase + VP * 16384 + ((n - 16) >> 2) * 4096 + ((n - 16) & 3) * 512); }
        SBAR();
        if (i < 16) { const int s = i >> 1;
            if (i == 0) asm volatile("v_mfma_f32_32x32x16_bf16 %0, %2, %3, %1" : "=&v"(st.s0), "+v"(st.negm) : "v"(fr[i]), "v"(st.q[0]));
            else if (i == 1) asm volatile("v_mfma_f32_32x32x16_bf16 %0, %2, %3, %1" : "=&v"(st.s1), "+v"(st.negm) : "v"(fr[i]), "v"(st.q[0]));
            else if ((i & 1) == 0) st.s0 = MFMA32(fr[i], st.q[s], st.s0);
            else              st.s1 = MFMA32(fr[i], st.q[s], st.s1);
        } else { const int ks = (i - 16) >> 2, db = (i - 16) & 3;
            st.o[db] = MFMA32(fr[i], __builtin_bit_cast(bf16x8, st.p[ks]), st.o[db]); }
        SBAR();
    }
    PIN(st.negm);
}

DI void store_ctx(const f32x16 (&o)[4], float inv, srd_t srd, unsigned voff) {
    PIN(voff);
#pragma unroll
    for (int db = 0; db < 4; ++db)
#pragma unroll
        for (int s = 0; s < 2; ++s) {
            u32x4 w;
#pragma unroll
            for (int e = 0; e < 4; ++e) w[e] = cvtpk(o[db][8 * s + 2 * e] * inv, o[db][8 * s + 2 * e + 1] * inv);
            __builtin_amdgcn_raw_buffer_store_b128(w, srd, voff + (4 * db + 2 * s) * 512, 0, 16);
        }
}

template <bool LAST, int PAR>
DI void v_phase(AttnState& st, AttnCtx& c, const int t) {
    const bool sw = (t == c.nl);
    const unsigned vm0 = c.vslot0 + (PAR ^ 1) * 16384, km0 = c.kslot0 + PAR * 16384;
    const unsigned vso = c.soA, kso = c.soB;
    if (!LAST && __builtin_expect(c.known != 0xffu, 0)) {
        c.known |= (unsigned)__builtin_amdgcn_ballot_w64(c.prev == c.tok) & 0xffu;
        const unsigned need = 1u << ((kso >> 16) & 7);
        if (kso < 524288u && !(c.known & need)) {
            for (unsigned spins = 0; spins < (1u << 15); ++spins) { const u64 v = ld_flag8(c); c.known |= (unsigned)__builtin_amdgcn_ballot_w64(v == c.tok) & 0xffu; if (c.known & need) break; __builtin_amdgcn_s_sleep(1); } }
        asm volatile("" ::: "memory");
        c.prev = ld_flag8(c);
    }
    SBAR();
    if (__builtin_expect(sw, 0)) { const float lt = swap_add(st.l); store_ctx(st.o, __builtin_amdgcn_rcpf(lt), c.srdC, c.lane16 + c.coffL);
#pragma unroll
        for (int d = 0; d < 4; ++d) st.o[d] = f32x16{};
        st.l = 0.f; c.rel = c.relH; }
    if (__builtin_expect(c.rel > c.thrg, 0)) {
        const int u = 32 * c.g - c.rel; const float NEG = -__builtin_inff(); int dq0 = c.dq0; PIN(dq0);
        if (u == 32) {
#pragma unroll
            for (int r = 0; r < 16; ++r) if ((r & 3) + 8 * (r >> 2) > dq0) st.s1[r] = NEG;
        } else {
            if (u == 0) {
#pragma unroll
                for (int r = 0; r < 16; ++r) if ((r & 3) + 8 * (r >> 2) > dq0) st.s0[r] = NEG;
            } else {
#pragma unroll
                for (int r = 0; r < 16; ++r) st.s0[r] = NEG; }
#pragma unroll
            for (int r = 0; r < 16; ++r) st.s1[r] = NEG;
        } }
    SBAR();
    if (!LAST) dma_piece<0, true>(vm0, c.voff, c.srdV, vso);
#define MX3(a, b, c) __builtin_fmaxf(__builtin_fmaxf((a), (b)), (c))
    float a = MX3(st.s0[0], st.s0[1], st.s1[0]), b = MX3(st.s0[2], st.s0[3], st.s1[1]);
    a = MX3(a, st.s1[2], st.s1[3]);
#pragma unroll
    for (int r = 4; r < 16; r += 4) {
        a = MX3(a, st.s0[r], st.s0[r + 1]); b = MX3(b, st.s0[r + 2], st.s0[r + 3]);
        a = MX3(a, st.s1[r], st.s1[r + 1]); b = MX3(b, st.s1[r + 2], st.s1[r + 3]); }
    const float rmax = __builtin_fmaxf(a, b);
    const unsigned long long firstm = (t == 0 || sw) ? ~0ull : 0ull;
    if (__builtin_expect((__builtin_amdgcn_ballot_w64(rmax > ATT_THR) | firstm) != 0ull, 0)) {
        const bool first = firstm != 0ull;
        const float m = swap_max(rmax);
        const float delta = __builtin_fmaxf(m, first ? -1e30f : 0.f);
        st.mhat = (first ? 0.f : st.mhat) + delta;
#pragma unroll
        for (int r = 0; r < 16; ++r) { st.s0[r] -= delta; st.s1[r] -= delta; st.negm[r] = -st.mhat; }
        if (!first) { const float f = __builtin_amdgcn_exp2f(-delta); st.l *= f;
#pragma unroll
            for (int d = 0; d < 4; ++d)
#pragma unroll
                for (int r = 0; r < 16; ++r) st.o[d][r] *= f; }
    }
    SBAR();
    float e[32];
#define EXPG(k) do { _Pragma("unroll") for (int i_ = 0; i_ < 4; ++i_) e[4 * (k) + i_] = __builtin_amdgcn_exp2f((k) < 4 ? st.s0[4 * (k) + i_] : st.s1[4 * ((k) - 4) + i_]); } while (0)
    float l0 = st.l, l1 = 0.f, l2 = 0.f, l3 = 0.f;
#define FING(k) do { l0 += e[4 * (k)]; if ((k) == 0) { l1 = e[1]; l2 = e[2]; l3 = e[3]; } else { l1 += e[4 * (k) + 1]; l2 += e[4 * (k) + 2]; l3 += e[4 * (k) + 3]; } \
        unsigned w0_ = cvtpk(e[4 * (k)], e[4 * (k) + 1]), w1_ = cvtpk(e[4 * (k) + 2], e[4 * (k) + 3]); PIN(w0_); PIN(w1_); PIN(l0); PIN(l1); PIN(l2); PIN(l3); \
        st.p[(k) >> 1][2 * ((k) & 1)] = w0_; st.p[(k) >> 1][2 * ((k) & 1) + 1] = w1_; } while (0)
    EXPG(0); SBAR();
    if (!LAST) dma_piece<1024, false>(vm0, c.voff, c.srdV, vso);
    EXPG(1); FING(0); SBAR();
    if (!LAST) dma_piece<2048, false>(vm0, c.voff, c.srdV, vso);
    EXPG(2); FING(1); SBAR();
    if (!LAST) dma_piece<3072, false>(vm0, c.voff, c.srdV, vso);
    EXPG(3); FING(2); SBAR();
    if (!LAST) dma_piece<0, true>(km0, c.voff, c.srdK, kso);
    EXPG(4); FING(3); SBAR();
    if (!LAST) dma_piece<1024, false>(km0, c.voff, c.srdK, kso);
    EXPG(5); FING(4); SBAR();
    if (!LAST) dma_piece<2048, false>(km0, c.voff, c.srdK, kso);
    EXPG(6); FING(5); SBAR();
    if (!LAST) dma_piece<3072, false>(km0, c.voff, c.srdK, kso);
    EXPG(7); FING(6); SBAR();
    FING(7);
    st.l = (l0 + l1) + (l2 + l3);
#undef EXPG
#undef FING
    if (!LAST) {
        if (__builtin_expect(t == c.nl - 1, 0)) {
            unsigned qs = c.qstash; PIN(qs);
#pragma unroll
            for (int s = 0; s < 8; ++s) st.q[s] = ldsf(qs + s * 1024);
            st.negm = f32x16{};
        }
        c.soA = c.soB; c.soB += (t + 3 == c.nl) ? c.jump : 16384; c.rel += 64;
    }
    SBAR();
}

DI void attn_setup(AttnCtx& c, const u16* Kf, const u16* Vf, u16* Cf, unsigned lds0, int h, int j, int wid, int lane) {
    PIN(lane);
    const int grp = wid >> 2; c.g = wid & 3; c.dq0 = (lane & 31) - 4 * (lane >> 5);
    const int qbH = 15 - j, qbL = j, nl = 2 * j + 2;
    c.nl = grp ? nl : 1000; c.thrg = 32 * c.g - 63; c.jump = (18 - nl) * 16384; c.relH = 64 * (17 - 2 * qbH);
    c.rel = grp ? -128 * qbL : -128 * qbH;
    c.soA = 16384; c.soB = (c.nl == 2) ? 17 * 16384 : 2 * 16384;
    c.srdK = mk_srd((const char*)Kf + (size_t)h * 524288, 524288);
    c.srdV = mk_srd((const char*)Vf + (size_t)h * 524288, 524288);
    c.srdC = mk_srd((const char*)Cf, 16777216);
    c.lane16 = lane * 16; c.voff = lane * 16 + c.g * 4096;
    c.kslot0 = lds0 + grp * 65536 + c.g * 4096; c.vslot0 = c.kslot0 + 32768;
    c.coffL = ((qbL * 4 + c.g) * 32 + h) * 8192;
    c.qstash = lds0 + 131072 + c.g * 8192 + lane * 16;
}
#define DMA4(m_, vo_, srd, so_) do { dma_piece<0, true>(m_, vo_, srd, so_); dma_piece<1024, false>(m_, vo_, srd, so_); dma_piece<2048, false>(m_, vo_, srd, so_); dma_piece<3072, false>(m_, vo_, srd, so_); } while (0)
DI void attn_prime(const AttnCtx& c, int grp) { DMA4(grp ? c.vslot0 + 16384 : c.kslot0, c.voff, c.srdK, 0u); DMA4(c.vslot0, c.voff, c.srdV, 0u); if (grp == 0) DMA4(c.kslot0 + 16384, c.voff, c.srdK, 16384u); }

template <class First, class G0Tail>
DI void attn_run(AttnState& st, AttnCtx& c, unsigned lds0, int h, int j, int wid, int lane, First afterFirst, G0Tail g0tail, Stamper& stamp) {
    PIN(lane);
    const int grp = wid >> 2;
    const unsigned kbase = lds0 + grp * 65536 + (lane >> 5) * 1024 + (lane & 31) * 16;
    const unsigned vbase = lds0 + grp * 65536 + 32768 + (lane >> 5) * 2048 + (lane & 31) * 16;
#pragma unroll
    for (int d = 0; d < 4; ++d) st.o[d] = f32x16{};
    st.negm = f32x16{}; st.l = 0.f; st.mhat = 0.f;
    PIN(st.negm);
    if (grp == 1) { DMA4(c.kslot0 + 16384, c.voff, c.srdK, 16384u); BARRIER(); }
    wait_bar<8>();
    stamp();
    bf16x8 pre[ATT_W];
#define PRE_K(P) do { _Pragma("unroll") for (int i = 0; i < ATT_W; ++i) pre[i] = ldsf(kbase + (P) * 16384 + (i >> 1) * 2048 + (i & 1) * 512); } while (0)
    const unsigned kbase0 = grp ? kbase + 49152 : kbase;
#pragma unroll
    for (int i = 0; i < ATT_W; ++i) pre[i] = ldsf(kbase0 + (i >> 1) * 2048 + (i & 1) * 512);
    __builtin_amdgcn_s_setprio(1);
    m_phase<true, false, 0, 0>(st, kbase0, vbase, pre);
    __builtin_amdgcn_s_setprio(0);
    wait_bar<0>();
    afterFirst();
#define STEP(PAR, t) do { \
        v_phase<false, PAR>(st, c, t); \
        PRE_K(PAR ^ 1); \
        asm volatile("s_barrier" ::: "memory"); \
        __builtin_amdgcn_s_setprio(1); \
        m_phase<true, true, PAR ^ 1, PAR>(st, kbase, vbase, pre); \
        __builtin_amdgcn_s_setprio(0); \
        wait_bar<0>(); } while (0)
    for (int t = 0; t < 16; t += 2) { stamp(); STEP(0, t); STEP(1, t + 1); }
    stamp();
    v_phase<true, 0>(st, c, 16);
#pragma unroll
    for (int i = 0; i < ATT_W; ++i) pre[i] = ldsf(vbase + (i >> 2) * 4096 + (i & 3) * 512);
    asm volatile("s_barrier" ::: "memory");
    __builtin_amdgcn_s_setprio(1);
    m_phase<false, true, 0, 0>(st, kbase, vbase, pre);
    __builtin_amdgcn_s_setprio(0);
    wait_bar<0>();
    stamp();
    PIN(lane);
    const float lt = swap_add(st.l);
    const unsigned xo = lds0 + c.g * 16384 + lane * 16;
    LAS f32x2* xs = (LAS f32x2*)(lds0 + 65536 + c.g * 512 + lane * 8);
    if (grp == 0) {
#pragma unroll
        for (int d = 0; d < 4; ++d)
#pragma unroll
            for (int rq = 0; rq < 4; ++rq) *(LAS f32x4*)(xo + (d * 4 + rq) * 1024) = (f32x4){st.o[d][4 * rq], st.o[d][4 * rq + 1], st.o[d][4 * rq + 2], st.o[d][4 * rq + 3]};
        *xs = (f32x2){st.mhat, lt};
        BARRIER();
        g0tail();
    } else {
        const f32x2 o0 = *xs;
        const float m = __builtin_fmaxf(o0[0], st.mhat);
        const float f0 = __builtin_amdgcn_exp2f(o0[0] - m), f1 = __builtin_amdgcn_exp2f(st.mhat - m);
        const float inv = __builtin_amdgcn_rcpf(o0[1] * f0 + lt * f1), a0 = f0 * inv, a1 = f1 * inv;
#pragma unroll
        for (int d = 0; d < 4; ++d)
#pragma unroll
            for (int rq = 0; rq < 4; ++rq) { const f32x4 x = *(const LAS f32x4*)(xo + (d * 4 + rq) * 1024);
#pragma unroll
                for (int e = 0; e < 4; ++e) st.o[d][4 * rq + e] = st.o[d][4 * rq + e] * a1 + x[e] * a0; }
        store_ctx(st.o, 1.0f, c.srdC, c.lane16 + (((15 - j) * 4 + c.g) * 32 + h) * 8192);
    }
    stamp();
#undef PRE_K
#undef STEP
}
constexpr int OUT_LDS = 163840;
constexpr unsigned OUT_O = 131072;
struct OutCtx { srd_t srdC, srdW, srdB; unsigned lane16, vw0, vwA, sC, sW, sB, lds0; int wid, rot; };
DI void out_issueA(const OutCtx& c, int kt, int i) {
    const unsigned st = c.lds0 + (kt & 3) * 32768;
    const int kr = (kt + c.rot) & 15;
    if (i < 2) { const int p = 2 * c.wid + i;
        dma1s(st + p * 1024, c.lane16, c.srdC, c.sC + (p >> 2) * 262144 + (p & 3) * 1024 + kr * 4096); }
    else dma1(st + 16384 + (2 * c.wid + (i - 2)) * 1024, c.vwA, c.srdW, c.sW + (8 * c.wid) * 1024 + (i - 2) * 128 + kr * 65536);
}
DI void out_issueB(const OutCtx& c, int ch, int i) {
    const int rg = 2 * c.wid + (i >> 1);
    dma1(c.lds0 + ch * 32768 + rg * 2048 + (i & 1) * 1024, (i >> 1) ? (c.vw0 ^ 32u) : c.vw0, c.srdB, c.sB + (ch * 128 + 8 * rg) * 1024 + (i & 1) * 128);
}
DI u32x4 out_pack(f32x4 s0, f32x4 s1) {
    unsigned a0 = cvtpk(s0[0], s0[1]), a1 = cvtpk(s0[2], s0[3]), b0 = cvtpk(s1[0], s1[1]), b1 = cvtpk(s1[2], s1[3]);
    auto x0 = __builtin_amdgcn_permlane32_swap(a0, b0, false, false); auto x1 = __builtin_amdgcn_permlane32_swap(a1, b1, false, false);
    return (u32x4){x0[0], x1[0], x0[1], x1[1]};
}
DI f32x4 quad(const f32x16& x, int rq) { return (f32x4){x[4 * rq], x[4 * rq + 1], x[4 * rq + 2], x[4 * rq + 3]}; }

DI void out_phase(const u16* Wo, const u16* Bwo, u16* out, char* ws, u64 tok, unsigned lds0, int bid, int wid, int lane, Stamper& stamp) {
    PIN(lane);
    const int tid = wid * 64 + lane, r32 = lane & 31, hi = lane >> 5, wr = wid >> 2, wc = wid & 3;
    const int x = bid & 7, u = bid >> 3, s = x >> 1, rb = 8 * (x & 1) + (u >> 2), cb = u & 3, tile = rb * 4 + cb;
    OutCtx c; c.wid = wid; c.lds0 = lds0; c.lane16 = lane * 16;
    c.srdC = mk_srd(ws + WS_CF + (size_t)rb * 4 * 262144, 4 * 262144); c.srdW = mk_srd(Wo, 4194304); c.srdB = mk_srd(Bwo, 524288);
    c.vw0 = ((lane >> 2) & 7) * 1024 + (4 * (lane >> 5) + ((lane & 3) ^ ((lane >> 4) & 1))) * 16; c.vwA = (wid & 1) ? (c.vw0 ^ 32u) : c.vw0;
    c.rot = 4 * cb + 2 * (rb & 7);
    c.sC = s * 8 * 8192; c.sW = s * 1024 * 1024 + cb * 256; c.sB = cb * 256;
#pragma unroll
    for (int kt = 0; kt < 4; ++kt) { out_issueA(c, kt, 2); out_issueA(c, kt, 3); }
    u64* flags = (u64*)(ws + WS_FLAG);
    {
        const int l = lane & 15, hh = 8 * s + (l & 7), jj = rb < 8 ? rb : 15 - rb;
        const int src = l < 8 ? ((hh >> 2) | (jj << 3) | ((hh & 3) << 6)) : (8 * (2 * rb + (s >> 1)) + (l & 7));
        const u64* fp = flags + (FL_C + src) * FL_STRIDE;
        for (unsigned spins = 0; spins < (1u << 15); ++spins) { const u64 v = ld_agent(fp); if (__all(v == tok)) break; __builtin_amdgcn_s_sleep(1); }
        asm volatile("" ::: "memory"); }
    stamp();
#pragma unroll
    for (int kt = 0; kt < 4; ++kt) { out_issueA(c, kt, 0); out_issueA(c, kt, 1); }
    { const srd_t srdO = mk_srd(ws + WS_OF + (size_t)(rb * 4 + s) * 32768, 32768);
#pragma unroll
      for (int i = 0; i < 4; ++i) dma1s(lds0 + OUT_O + (4 * wid + i) * 1024, c.lane16, srdO, (unsigned)((4 * wid + i) * 1024)); }
    const int q4 = (lane & 15) >> 2, p4 = lane & 3, blk = (lane >> 4) & 1;
    const unsigned aaddr = lds0 + (2 * wr) * 4096 + c.lane16;
    unsigned waddr[2];
#pragma unroll
    for (int t = 0; t < 2; ++t) waddr[t] = lds0 + 2048 * t + 512 * wc + 64 * (4 * hi + q4) + 16 * ((2 * blk + (p4 >> 1)) ^ ((2 * t + hi) & 3)) + 8 * (p4 & 1);
    f32x16 acc[2] = {f32x16{}, f32x16{}};
    stamp();
    struct Fr { bf16x8 a[2][2]; bf16x4 w[2][2]; };
    auto rd = [&](Fr& f, unsigned st, int half) {
        unsigned w0 = waddr[0] + st + 16384, w1 = waddr[1] + st + 16384, aa = aaddr + st; PIN(w0); PIN(w1); PIN(aa);
#pragma unroll
        for (int k = 0; k < 2; ++k) { const int ks = 2 * half + k;
            f.w[k][0] = ldstr(w0 + ks * 4096); f.w[k][1] = ldstr(w1 + ks * 4096);
            f.a[k][0] = ldsf(aa + ks * 1024); f.a[k][1] = ldsf(aa + 4096 + ks * 1024); } };
    Fr f0, f1;
    wait_bar<10>();
    rd(f0, 0u, 0);
    auto step = [&](int kt, auto late, auto early) {
        const unsigned st = (kt & 3) * 32768, stn = ((kt + 1) & 3) * 32768;
        rd(f1, st, 1);
        SBAR();
#pragma unroll
        for (int k = 0; k < 2; ++k) { const bf16x8 wf = cat4(f0.w[k][0], f0.w[k][1]);
            acc[0] = MFMA32(wf, f0.a[k][0], acc[0]); acc[1] = MFMA32(wf, f0.a[k][1], acc[1]); SBAR(); late(2 + k); SBAR(); }
        wait_bar<8>();
        rd(f0, stn, 0);
        SBAR();
#pragma unroll
        for (int k = 0; k < 2; ++k) { const bf16x8 wf = cat4(f1.w[k][0], f1.w[k][1]);
            acc[0] = MFMA32(wf, f1.a[k][0], acc[0]); acc[1] = MFMA32(wf, f1.a[k][1], acc[1]); SBAR(); early(k); SBAR(); }
    };
    step(0, [&](int) {}, [&](int i) { out_issueA(c, 4, i); });
    for (int kt = 1; kt < 12; ++kt) step(kt, [&](int i) { out_issueA(c, kt + 3, i); }, [&](int i) { out_issueA(c, kt + 4, i); });
    step(12, [&](int i) { out_issueA(c, 15, i); }, [&](int i) { out_issueB(c, 0, i); });
    step(13, [&](int i) { out_issueB(c, 0, i); }, [&](int i) { out_issueB(c, 1, i); });
    step(14, [&](int i) { out_issueB(c, 1, i); }, [&](int i) { out_issueB(c, 2, i); });
    step(15, [&](int i) { out_issueB(c, 2, i); }, [&](int i) { out_issueB(c, 3, i); });
    out_issueB(c, 3, 2); out_issueB(c, 3, 3);
    stamp();
    const srd_t srdX = mk_srd(ws + WS_XCH + (size_t)tile * 262144, 262144);
#pragma unroll
    for (int mb = 0; mb < 2; ++mb) { const int q = 2 * wr + mb;
        if (q != s) {
#pragma unroll
            for (int rq = 0; rq < 4; ++rq) store16_sc1(__builtin_bit_cast(u32x4, quad(acc[mb], rq)), c.lane16, srdX, (unsigned)(((q * 4 + s) * 4 + wc) * 4096 + rq * 1024)); } }
    const int kh = (wr == (s >> 1)) ? 1 : 0;
    f32x16 accB = f32x16{};
#pragma unroll
    for (int ch = 0; ch < 4; ++ch) {
        if (ch == 0) { wait_bar<0>(); if (tid == 0) st_agent(flags + (FL_XF + tile * 4 + s) * FL_STRIDE, tok); }
        unsigned w0 = waddr[0] + ch * 32768 + kh * 16384, w1 = waddr[1] + ch * 32768 + kh * 16384, oa = lds0 + OUT_O + (8 * ch + 4 * kh) * 1024 + c.lane16; PIN(w0); PIN(w1); PIN(oa);
        bf16x8 a[4]; bf16x4 w[4][2];
#pragma unroll
        for (int ks = 0; ks < 4; ++ks) { w[ks][0] = ldstr(w0 + ks * 4096); w[ks][1] = ldstr(w1 + ks * 4096); a[ks] = ldsf(oa + ks * 1024); }
#pragma unroll
        for (int ks = 0; ks < 4; ++ks) accB = MFMA32(cat4(w[ks][0], w[ks][1]), a[ks], accB);
    }
    stamp();
    BARRIER();
    const int row = rb * 128 + 32 * s + r32;
    if (kh == 1) {
#pragma unroll
        for (int rq = 0; rq < 4; ++rq) *(LAS f32x4*)(lds0 + (wc * 4 + rq) * 1024 + c.lane16) = quad(accB, rq);
        BARRIER();
        const u64* fp = flags + (FL_XF + tile * 4 + (lane & 3)) * FL_STRIDE;
        for (unsigned spins = 0; spins < (1u << 15); ++spins) { const u64 v = ld_agent(fp); if (__all(v == tok)) break; __builtin_amdgcn_s_sleep(1); }
        asm volatile("" ::: "memory");
        f32x4 p[4][4];
#pragma unroll
        for (int q = 0; q < 4; ++q)
#pragma unroll
            for (int rq = 0; rq < 4; ++rq) p[q][rq] = __builtin_bit_cast(f32x4, __builtin_amdgcn_raw_buffer_load_b128(srdX, (q == s ? 0x7ffffff0u : c.lane16) + rq * 1024, ((s * 4 + q) * 4 + wc) * 4096, 16));
        f32x4 r[4];
#pragma unroll
        for (int rq = 0; rq < 4; ++rq) r[rq] = ((p[0][rq] + p[1][rq]) + (p[2][rq] + p[3][rq])) + ((s & 1) ? quad(acc[1], rq) : quad(acc[0], rq));
        u16* o = out + (size_t)row * 1024 + cb * 128 + 32 * wc + 8 * hi;
        *(u32x4*)o = out_pack(r[0], r[1]); *(u32x4*)(o + 16) = out_pack(r[2], r[3]);
    } else {
        BARRIER();
        f32x4 r[4];
#pragma unroll
        for (int rq = 0; rq < 4; ++rq) r[rq] = quad(accB, rq) + *(const LAS f32x4*)(lds0 + (wc * 4 + rq) * 1024 + c.lane16);
        u16* o = out + (size_t)row * 1024 + 512 + cb * 128 + 32 * wc + 8 * hi;
        *(u32x4*)o = out_pack(r[0], r[1]); *(u32x4*)(o + 16) = out_pack(r[2], r[3]);
    }
}
constexpr int FUSED_LDS = 163840;
constexpr unsigned WPHI_LDS = 156160;
struct FusedArgs { const u16 *X, *Ckv, *Wq, *Wuk, *Wuv, *Wo, *Bx, *Wqkv, *Wphi, *Bwo; char* ws; u16* out; };

__global__ __launch_bounds__(512, 2) void fused_kernel(FusedArgs a) {
    const int tid = threadIdx.x, lane = tid & 63;
    const int wid = rfl(tid >> 6);
    const int bid = blockIdx.x, h = (bid & 7) * 4 + (bid >> 6), j = (bid >> 3) & 7;
    const unsigned lds0 = 0u;
    Stamper stamp{0u, 0, lane};
    stamp();
    u16* Kf = (u16*)(a.ws + WS_KF); u16* Vf = (u16*)(a.ws + WS_VF); u16* Cf = (u16*)(a.ws + WS_CF);
    u16* PhiQ = (u16*)(a.ws + WS_PHIQ); u16* Of = (u16*)(a.ws + WS_OF); float* Bst = (float*)(a.ws + WS_BST);
    u64* flags = (u64*)(a.ws + WS_FLAG);
    float* Bred = (float*)(a.ws + WS_BRED);
    const int hg = bid & 7, rc = bid >> 3;
    if (wid < 2) dma1(lds0 + WPHI_LDS + wid * 1024, lane * 16, mk_srd(a.Wphi, 16384), (unsigned)(hg * 2048 + wid * 1024));
    ProjCtx pc; proj_setup(pc, a.X, a.Ckv, a.Wq, a.Wuk, a.Wuv, Kf, Vf, lds0, h, j, wid, lane);
#pragma unroll
    for (int i = 0; i < 8; ++i) issueKV(pc, 0, i);
#pragma unroll
    for (int i = 0; i < 8; ++i) issueKV(pc, 1, i);
    const unsigned tsc = lds0 + TOUCH_LDS;
    { const unsigned vW = (lane >> 4) * 524288 + (lane & 15) * 128;
      const char* base; unsigned vo = pc.l128;
      if (wid < 4) { base = (const char*)((wid & 1) ? a.Wuv : a.Wuk) + ((wid & 2) ? 4 : 2) * 524288 + bid * 2048 - ((wid & 2) ? 0 : 0); vo = vW; }
      else if (wid == 4) base = (const char*)a.Ckv + bid * 8192;
      else { base = (const char*)a.Ckv; vo = 0; }
      touch_g<1>(tsc, vo, base); }
    const u64 epoch = *(const u64 __attribute__((address_space(4)))*)(flags + FL_EPOCH * FL_STRIDE);
    u64 tok = mix64(epoch); if (tok == epoch) tok ^= 1ull;
    tok = ((u64)rfl((unsigned)(tok >> 32)) << 32) | rfl((unsigned)tok); asm volatile("" : "+s"(tok));
    B1Ctx bc; b1_setup(bc, a.Bx, a.Wqkv, lds0, rc, hg, wid, lane);
    stamp();
    auto touchk = [&](int kt) {
        const char* base = (const char*)a.Ckv; unsigned vo = 0;
        if (kt == 0) {
            if (wid == 0) { base = (const char*)a.Bx + bid * 8192; vo = pc.l128; }
            else if (wid == 1) { base = (const char*)a.X + bid * 8192; vo = pc.l128; }
            else if (wid == 2) { base = (const char*)a.Wqkv + (bid < 192 ? bid : 0) * 8192; vo = pc.l128; }
        } else if (kt == 1) {
            if (wid < 2) { base = (const char*)a.Wq + (bid * 2 + wid) * 8192; vo = pc.l128; }
        } else if (kt == 2) {
            if (wid < 2) { base = (const char*)a.Wo + (bid * 2 + wid) * 8192; vo = pc.l128; }
            else if (wid == 2) { base = (const char*)a.Bwo + (bid & 63) * 8192; vo = pc.l128; }
        }
        touch_g<2>(tsc, vo, base); };
    u32x4 pk[16];
    kv_run(pc, lane, pk, touchk, [&](int kt) { b1_issue(bc, kt); }, stamp);
    stamp();
    B1Acc ba;
    const bool early = (j & 3) == 0;
    b1_gemm(bc, ba, lane, early, [&](int n) { if (early) kv_store(pc, pk, n); else if (n >= 1 && n <= 6) kv_store(pc, pk, n - 1); },
            [&]() {
#pragma unroll
                for (int i = 0; i < 6; ++i) issueQ(pc, 0, i); },
            [&]() {
#pragma unroll
                for (int i = 0; i < 6; ++i) issueQ(pc, 1, i);
#pragma unroll
                for (int i = 0; i < 6; ++i) issueQ(pc, 2, i); });
    stamp();
    u32x2 wp; { const int l16 = lane & 15, g = lane >> 4; const LAS u16* w = (const LAS u16*)(lds0 + WPHI_LDS + (wid & 3) * 512);
        wp[0] = (unsigned)w[(4 * g) * 16 + l16] | ((unsigned)w[(4 * g + 1) * 16 + l16] << 16); wp[1] = (unsigned)w[(4 * g + 2) * 16 + l16] | ((unsigned)w[(4 * g + 3) * 16 + l16] << 16); }
    b1_phi(ba, [&](int n) { if (early) { if (n % 4 == 2) kv_store(pc, pk, 8 + n / 4); } else if (n % 3 == 1 && n < 14) kv_store(pc, pk, 6 + n / 3); }, wp, PhiQ, Bst, lds0, rc, hg, wid, lane, stamp);
    stamp();
    wait_bar<21>();
    stamp();
    AttnState st;
    AttnCtx c; attn_setup(c, Kf, Vf, Cf, lds0, h, j, wid, lane);
    B2a b2;
    auto spin = [&](const u64* fp) {
        for (unsigned spins = 0; spins < (1u << 15); ++spins) { const u64 v = ld_agent(fp); if (__all(v == tok)) break; __builtin_amdgcn_s_sleep(1); }
        asm volatile("" ::: "memory"); };
    const u64* fkv = flags + (FL_KV + 8 * h + (lane & 7)) * FL_STRIDE;
    const u64* fbs = flags + (FL_BS + 32 * hg + (lane & 31)) * FL_STRIDE;
    u64 pre = 0;
    q_run(pc, early, lane, pk, st.q, [&](int kt) {
        if (kt == 2) { if (tid == 0) st_agent(flags + (FL_BS + 32 * hg + rc) * FL_STRIDE, tok); }
        if (kt == 3) { if (early && tid == 0) st_agent(flags + (FL_KV + 8 * h + j) * FL_STRIDE, tok); }
        if (kt == 3) { if (wid == 7) pre = ld_agent(fbs); }
        if (kt == 4) {
            if (wid == 7) {
                if (!__all(pre == tok)) spin(fbs);
                asm volatile("" ::: "memory");
                b2a_load(b2, Bst, rc, hg, lane); } }
        if (kt == 5) { if (wid == 7) b2a_finish(b2, Bred, rc, hg, lane); pre = ld_agent(fkv); }
        if (kt == 6) {
            if (!early && tid == 0) st_agent(flags + (FL_KV + 8 * h + j) * FL_STRIDE, tok);
            if (tid == 448) st_agent(flags + (FL_BR + 32 * hg + rc) * FL_STRIDE, tok);
            c.known = (unsigned)__builtin_amdgcn_ballot_w64(pre == tok) & 0xffu;
            for (unsigned spins = 0; spins < (1u << 15) && !(c.known & 1u); ++spins) { const u64 v = ld_agent(fkv); c.known = (unsigned)__builtin_amdgcn_ballot_w64(v == tok) & 0xffu; if (!(c.known & 1u)) __builtin_amdgcn_s_sleep(1); }
            asm volatile("" ::: "memory");
            attn_prime(c, wid >> 2);
        } }, stamp);
    c.srdF = mk_srd(flags + (FL_KV + 8 * h) * FL_STRIDE, 1024); c.tok = tok; c.prev = 0;
    stamp();
    BARRIER();
    if (wid < 4) {
#pragma unroll
        for (int s = 0; s < 8; ++s) *(LAS bf16x8*)(c.qstash + s * 1024) = st.q[s]; }
    stamp();
    attn_run(st, c, lds0, h, j, wid, lane,
             [&]() {},
             [&]() { b2b_body<4>(PhiQ, Bred, Of, flags + (FL_BR + 32 * hg + 8 * wid) * FL_STRIDE, tok, 4 * hg + wid, 4 * rc, lane); }, stamp);
    wait_bar<0>();
    if (tid == 0) st_agent(flags + (FL_C + bid) * FL_STRIDE, tok);
    stamp();
    out_phase(a.Wo, a.Bwo, a.out, a.ws, tok, lds0, bid, wid, lane, stamp);
    if (bid == 0 && wid == 7) {
        bool ok = false;
        for (unsigned spins = 0; spins < (1u << 15) && !ok; ++spins) { bool all = true;
#pragma unroll
            for (int i = 0; i < 4; ++i) all &= ld_agent(flags + (FL_C + lane + 64 * i) * FL_STRIDE) == tok;
            ok = __all(all); if (!ok) __builtin_amdgcn_s_sleep(1); }
        if (lane == 0) st_agent(flags + FL_EPOCH * FL_STRIDE, tok);
    }
    stamp();
}

#define WS_END 1
#define WS_SHIFT(n) (WS_END && (n) >= ((size_t)160 << 20) ? (((n) - ((size_t)80 << 20)) & ~(size_t)0x1fffff) : (size_t)0)
extern "C" void kernel_launch(void* const* d_in, const int* in_sizes, int n_in, void* d_out, int out_size, void* d_ws, size_t ws_size, hipStream_t stream) {
    FusedArgs a;
    a.X = (const u16*)d_in[0]; a.Ckv = (const u16*)d_in[1]; a.Wq = (const u16*)d_in[2]; a.Wuk = (const u16*)d_in[3]; a.Wuv = (const u16*)d_in[4]; a.Wo = (const u16*)d_in[5];
    a.Bx = (const u16*)d_in[6]; a.Wqkv = (const u16*)d_in[7]; a.Wphi = (const u16*)d_in[8]; a.Bwo = (const u16*)d_in[9]; a.ws = (char*)d_ws + WS_SHIFT(ws_size); a.out = (u16*)d_out;
    (void)hipFuncSetAttribute((const void*)fused_kernel, hipFuncAttributeMaxDynamicSharedMemorySize, FUSED_LDS);
    fused_kernel<<<256, 512, FUSED_LDS, stream>>>(a);
}
```

```cpp
#include <hip/hip_runtime.h>
#include <stdint.h>

typedef unsigned short u16;
typedef unsigned long long u64;
typedef __bf16 bf16x8 __attribute__((ext_vector_type(8)));
typedef __bf16 bf16x4 __attribute__((ext_vector_type(4)));
typedef __bf16 bf16x2 __attribute__((ext_vector_type(2)));
typedef short s16x4 __attribute__((ext_vector_type(4)));
typedef float f32x16 __attribute__((ext_vector_type(16)));
typedef float f32x4 __attribute__((ext_vector_type(4)));
typedef float f32x2 __attribute__((ext_vector_type(2)));
typedef unsigned u32x4 __attribute__((ext_vector_type(4)));
typedef unsigned u32x2 __attribute__((ext_vector_type(2)));
typedef __amdgpu_buffer_rsrc_t srd_t;
#define LAS __attribute__((address_space(3)))
#define GAS __attribute__((address_space(1)))
#define SBAR() __builtin_amdgcn_sched_barrier(0)
#define PIN(x) asm volatile("" : "+v"(x))
#define MFMA32(a, b, c) __builtin_amdgcn_mfma_f32_32x32x16_bf16((a), (b), (c), 0, 0, 0)
#define MFMA16(a, b, c) __builtin_amdgcn_mfma_f32_16x16x32_bf16((a), (b), (c), 0, 0, 0)
#define DI __device__ __forceinline__
#define MFMA16I(acc, a, b) asm volatile("v_mfma_f32_16x16x32_bf16 %0, %1, %2, %0" : "+v"(acc) : "v"(a), "v"(b))
#define MFMA_DRAIN() asm volatile("s_nop 7")
#pragma clang diagnostic ignored "-Wint-to-pointer-cast"

DI unsigned cvtpk(float lo, float hi) { f32x2 v = {lo, hi}; bf16x2 b = __builtin_convertvector(v, bf16x2); return __builtin_bit_cast(unsigned, b); }
DI unsigned rfl(unsigned x) { return __builtin_amdgcn_readfirstlane(x); }
DI float swap_max(float x) { auto rr = __builtin_amdgcn_permlane32_swap(__float_as_uint(x), __float_as_uint(x), false, false); return __builtin_fmaxf(__uint_as_float(rr[0]), __uint_as_float(rr[1])); }
DI float swap_add(float x) { auto rr = __builtin_amdgcn_permlane32_swap(__float_as_uint(x), __float_as_uint(x), false, false); return __uint_as_float(rr[0]) + __uint_as_float(rr[1]); }
DI srd_t mk_srd(const void* p, int bytes) { return __builtin_amdgcn_make_buffer_rsrc((void*)p, (short)0, bytes, 0x00020000); }
DI bf16x4 ldstr(unsigned a) { return __builtin_amdgcn_ds_read_tr16_b64_v4bf16((LAS bf16x4*)a); }
DI bf16x8 ldsf(unsigned a) { return *(const LAS bf16x8*)a; }
DI bf16x8 cat4(bf16x4 a, bf16x4 b) { return __builtin_shufflevector(a, b, 0, 1, 2, 3, 4, 5, 6, 7); }
#define SCALARIZE2(a, b) do {} while (0)
DI void dma1(unsigned m0v, unsigned voff, srd_t srd, unsigned soff) {
    SCALARIZE2(m0v, soff);
    asm volatile("s_mov_b32 m0, %0\n\ts_nop 0\n\tbuffer_load_dwordx4 %1, %2, %3 offen lds" :: "s"(m0v), "v"(voff), "s"(srd), "s"(soff) : "memory");
}
DI void dma1s(unsigned m0v, unsigned voff, srd_t srd, unsigned soff) {
    SCALARIZE2(m0v, soff);
    asm volatile("s_mov_b32 m0, %0\n\ts_nop 0\n\tbuffer_load_dwordx4 %1, %2, %3 offen sc1 lds" :: "s"(m0v), "v"(voff), "s"(srd), "s"(soff) : "memory");
}
DI void touch(unsigned m0v, unsigned voff, srd_t srd, unsigned soff) {
    SCALARIZE2(m0v, soff);
    asm volatile("s_mov_b32 m0, %0\n\ts_nop 0\n\tbuffer_load_dword %1, %2, %3 offen lds" :: "s"(m0v), "v"(voff), "s"(srd), "s"(soff) : "memory");
}
DI void store16_sc1(u32x4 w, unsigned voff, srd_t srd, unsigned soff) {
    unsigned z = soff; SCALARIZE2(soff, z);
    asm volatile("buffer_store_dwordx4 %0, %1, %2, %3 offen sc1\n\ts_nop 1" :: "v"(w), "v"(voff), "s"(srd), "s"(soff) : "memory");
}
template <int N> DI void wait_vm() { asm volatile("s_waitcnt vmcnt(%0)" :: "i"(N) : "memory"); }
template <int N> DI void wait_bar() { asm volatile("s_waitcnt vmcnt(%0) lgkmcnt(0)\n\ts_barrier" :: "i"(N) : "memory"); }
template <int N> DI void wait_bar_keep() { asm volatile("s_waitcnt vmcnt(%0)\n\ts_barrier" :: "i"(N) : "memory"); }
#define BARRIER() asm volatile("s_waitcnt lgkmcnt(0)\n\ts_barrier" ::: "memory")

struct Stamper { unsigned v; int n; int lane;
    DI void operator()() {
    } };

constexpr size_t WS_FLAG = 0;
constexpr size_t WS_KF = 1 << 20, WS_VF = WS_KF + (16 << 20), WS_CF = WS_VF + (16 << 20), WS_PHIQ = WS_CF + (16 << 20), WS_OF = WS_PHIQ + (4 << 20), WS_BST = WS_OF + (2 << 20), WS_BRED = WS_BST + (3 << 20), WS_XCH = WS_BRED + (1 << 20), WS_DBG = 77 << 20;
constexpr int FL_STRIDE = 16;
constexpr int FL_EPOCH = 0, FL_KV = 32, FL_BS = 32 + 256, FL_BR = 32 + 512, FL_XF = 32 + 768, FL_C = FL_XF + 256;
typedef GAS u64 gu64;
DI u64 ld_agent(const u64* p) { return __hip_atomic_load((gu64*)p, __ATOMIC_RELAXED, __HIP_MEMORY_SCOPE_AGENT); }
DI void st_agent(u64* p, u64 v) { __hip_atomic_store((gu64*)p, v, __ATOMIC_RELAXED, __HIP_MEMORY_SCOPE_AGENT); }
DI u64 mix64(u64 z) { z += 0x9E3779B97F4A7C15ull; z = (z ^ (z >> 30)) * 0xBF58476D1CE4E5B9ull; z = (z ^ (z >> 27)) * 0x94D049BB133111EBull; return z ^ (z >> 31); }
DI f32x4 mfma16_16(u32x2 a, u32x2 b, f32x4 c) { return __builtin_amdgcn_mfma_f32_16x16x16bf16_1k(__builtin_bit_cast(s16x4, a), __builtin_bit_cast(s16x4, b), c, 0, 0, 0); }
DI float rbf16(float x) { return __uint_as_float(cvtpk(x, 0.f) << 16); }
DI float x16_max(float x) { auto rr = __builtin_amdgcn_permlane16_swap(__float_as_uint(x), __float_as_uint(x), false, false); return __builtin_fmaxf(__uint_as_float(rr[0]), __uint_as_float(rr[1])); }
DI float x16_add(float x) { auto rr = __builtin_amdgcn_permlane16_swap(__float_as_uint(x), __float_as_uint(x), false, false); return __uint_as_float(rr[0]) + __uint_as_float(rr[1]); }
DI float grp_max(float x) { return swap_max(x16_max(x)); }
DI float grp_sum(float x) { return swap_add(x16_add(x)); }
constexpr float LOG2E = 1.4426950408889634f;
constexpr unsigned B1_RED = 147456;
struct B1Ctx { srd_t srdX, srdW; unsigned vx, vw[2], sx, sw; int wid; unsigned lds0; };
DI void b1_setup(B1Ctx& c, const u16* Bx, const u16* Wqkv, unsigned lds0, int rc, int hg, int wid, int lane) {
    c.srdX = mk_srd(Bx, 2097152); c.srdW = mk_srd(Wqkv, 512 * 1536 * 2); c.wid = wid; c.lds0 = lds0;
    c.vx = (lane >> 3) * 1024 + (((lane & 7) ^ ((4 * wid + (lane >> 4)) & 7)) * 16);
#pragma unroll
    for (int p = 0; p < 2; ++p) c.vw[p] = (lane >> 3) * 3072 + (((lane & 7) ^ (2 * (((lane >> 4) & 1) | (p << 1)))) * 16);
    c.sx = (64 * rc + 8 * wid) * 1024; c.sw = hg * 128;
}
DI void b1_issue(const B1Ctx& c, int kt) {
    const unsigned st = c.lds0 + (kt & 3) * 32768;
    dma1(st + c.wid * 1024, c.vx, c.srdX, c.sx + kt * 128);
#pragma unroll
    for (int i = 0; i < 3; ++i) { const int pw = 3 * c.wid + i, m = pw >> 3, pr = pw & 7;
        dma1(st + 8192 + pw * 1024, (pr & 1) ? c.vw[1] : c.vw[0], c.srdW, c.sw + (kt * 64 + 8 * pr) * 3072 + m * 1024); }
}
struct B1Acc { f32x4 aq[2], ak[2], av[2]; };
struct B1Fr { bf16x8 x[2][2]; bf16x4 w[2][3][2]; };
struct B1Ad { unsigned xa[2][2], wa[2]; };
template <int KT> DI void b1_read(B1Fr& f, const B1Ad& ad) {
    constexpr unsigned st = (KT & 1) * 32768; constexpr int sp = (KT >> 1) & 1;
#pragma unroll
    for (int ks = 0; ks < 2; ++ks) { const unsigned xa = ad.xa[sp][ks];
        f.x[ks][0] = ldsf(xa + st); f.x[ks][1] = ldsf(xa + st + 2048);
#pragma unroll
        for (int m = 0; m < 3; ++m) { f.w[ks][m][0] = ldstr(ad.wa[sp] + st + m * 8192 + ks * 4096); f.w[ks][m][1] = ldstr(ad.wa[sp] + st + m * 8192 + ks * 4096 + 512); } }
}
DI void b1_mma(B1Acc& a, const B1Fr& f) {
#pragma unroll
    for (int ks = 0; ks < 2; ++ks) {
        const bf16x8 wq = cat4(f.w[ks][0][0], f.w[ks][0][1]), wk = cat4(f.w[ks][1][0], f.w[ks][1][1]), wv = cat4(f.w[ks][2][0], f.w[ks][2][1]);
        a.aq[0] = MFMA16(wq, f.x[ks][0], a.aq[0]); a.aq[1] = MFMA16(wq, f.x[ks][1], a.aq[1]); a.ak[0] = MFMA16(wk, f.x[ks][0], a.ak[0]); a.ak[1] = MFMA16(wk, f.x[ks][1], a.ak[1]);
        a.av[0] = MFMA16(f.x[ks][0], wv, a.av[0]); a.av[1] = MFMA16(f.x[ks][1], wv, a.av[1]);
    }
}
template <class St, class H6, class HE>
DI void b1_gemm(const B1Ctx& c, B1Acc& a, int lane, bool early, St store, H6 afterB6, HE afterEnd) {
    const int l16 = lane & 15, g = lane >> 4, q4 = l16 >> 2, p4 = lane & 3, wid = c.wid;
    const int row = 32 * (wid >> 2) + l16;
    B1Ad ad;
#pragma unroll
    for (int sp = 0; sp < 2; ++sp) {
        ad.xa[sp][0] = c.lds0 + sp * 65536 + row * 128 + (((0 + g) ^ ((row >> 1) & 7)) << 4); ad.xa[sp][1] = c.lds0 + sp * 65536 + row * 128 + (((4 + g) ^ ((row >> 1) & 7)) << 4);
        ad.wa[sp] = c.lds0 + sp * 65536 + 8192 + (8 * g + q4) * 128 + (((wid & 3) ^ (((q4 >> 1) & 1) | ((g & 1) << 1))) * 32) + p4 * 8;
        PIN(ad.xa[sp][0]); PIN(ad.xa[sp][1]); PIN(ad.wa[sp]); }
#pragma unroll
    for (int r = 0; r < 2; ++r) { a.aq[r] = f32x4{}; a.ak[r] = f32x4{}; a.av[r] = f32x4{}; }
    B1Fr f;
#define B1T(KT, PRE) do { b1_read<KT>(f, ad); SBAR(); PRE; store(KT); SBAR(); b1_mma(a, f); SBAR(); } while (0)
#define B1W(E, L) do { if (early) wait_bar<E>(); else wait_bar<L>(); } while (0)
    B1T(0, (void)0);
    B1W(9, 8);   B1T(1, b1_issue(c, 4));
    B1W(10, 9);  B1T(2, b1_issue(c, 5));
    B1W(11, 10); B1T(3, b1_issue(c, 6));
    B1W(11, 11); B1T(4, b1_issue(c, 7));
    B1W(11, 11); B1T(5, (void)0);
    B1W(7, 7);   B1T(6, afterB6());
    B1W(9, 9);   B1T(7, (void)0);
#undef B1W
#undef B1T
    BARRIER(); afterEnd();
}
template <int N, class Tick> DI void phi_map(const f32x4 (&p)[N], u32x4 (&out)[N], Tick tick) {
    float x[N][4], mx[N], nm[N], ep[N][4], en[N][4], sp[N], sn[N];
#pragma unroll
    for (int i = 0; i < N; ++i) {
#pragma unroll
        for (int e = 0; e < 4; ++e) x[i][e] = rbf16(p[i][e]) * LOG2E;
        mx[i] = __builtin_fmaxf(__builtin_fmaxf(x[i][0], x[i][1]), __builtin_fmaxf(x[i][2], x[i][3])); nm[i] = -__builtin_fminf(__builtin_fminf(x[i][0], x[i][1]), __builtin_fminf(x[i][2], x[i][3]));
        if (i & 1) tick(i >> 1); }
#pragma unroll
    for (int i = 0; i < N; ++i) { mx[i] = x16_max(mx[i]); nm[i] = x16_max(nm[i]); if (i & 1) tick(2 + (i >> 1)); }
#pragma unroll
    for (int i = 0; i < N; ++i) { mx[i] = swap_max(mx[i]); nm[i] = swap_max(nm[i]); if (i & 1) tick(4 + (i >> 1)); }
#pragma unroll
    for (int i = 0; i < N; ++i) {
#pragma unroll
        for (int e = 0; e < 4; ++e) { ep[i][e] = __builtin_amdgcn_exp2f(x[i][e] - mx[i]); en[i][e] = __builtin_amdgcn_exp2f(-x[i][e] - nm[i]); }
        sp[i] = (ep[i][0] + ep[i][1]) + (ep[i][2] + ep[i][3]); sn[i] = (en[i][0] + en[i][1]) + (en[i][2] + en[i][3]); tick(6 + i); }
#pragma unroll
    for (int i = 0; i < N; ++i) { sp[i] = x16_add(sp[i]); sn[i] = x16_add(sn[i]); if (i & 1) tick(10 + (i >> 1)); }
#pragma unroll
    for (int i = 0; i < N; ++i) { sp[i] = __builtin_amdgcn_rcpf(swap_add(sp[i])); sn[i] = __builtin_amdgcn_rcpf(swap_add(sn[i])); if (i & 1) tick(12 + (i >> 1)); }
#pragma unroll
    for (int i = 0; i < N; ++i) { out[i] = (u32x4){cvtpk(ep[i][0] * sp[i], ep[i][1] * sp[i]), cvtpk(ep[i][2] * sp[i], ep[i][3] * sp[i]), cvtpk(en[i][0] * sn[i], en[i][1] * sn[i]), cvtpk(en[i][2] * sn[i], en[i][3] * sn[i])};
        if (i & 1) tick(14 + (i >> 1)); }
}
template <class Tick>
DI void b1_phi(const B1Acc& a, Tick tick, u32x2 wp, u16* PhiQ, float* Bst, unsigned lds0, int rc, int hg, int wid, int lane, Stamper& stamp) {
    const int h = 4 * hg + (wid & 3), row0 = 64 * rc + 32 * (wid >> 2);
    const int l16 = lane & 15, g = lane >> 4;
    f32x4 sp = f32x4{}, sn = f32x4{}; float zp = 0.f, zn = 0.f;
    f32x4 pin[4]; u32x4 pout[4];
#pragma unroll
    for (int r = 0; r < 2; ++r) {
        const u32x2 qb = {cvtpk(a.aq[r][0], a.aq[r][1]), cvtpk(a.aq[r][2], a.aq[r][3])}, kb = {cvtpk(a.ak[r][0], a.ak[r][1]), cvtpk(a.ak[r][2], a.ak[r][3])};
        pin[r] = mfma16_16(wp, qb, f32x4{}); pin[2 + r] = mfma16_16(wp, kb, f32x4{});
    }
    phi_map<4>(pin, pout, tick);
    const u32x4 wq[2] = {pout[0], pout[1]}, wk[2] = {pout[2], pout[3]};
    const unsigned tb = lds0 + B1_RED + wid * 1024;
#pragma unroll
    for (int r = 0; r < 2; ++r) {
        *(LAS u32x2*)(tb + l16 * 32 + g * 8) = (u32x2){wk[r][0], wk[r][1]};
        *(LAS u32x2*)(tb + 512 + l16 * 32 + g * 8) = (u32x2){wk[r][2], wk[r][3]};
        __builtin_amdgcn_raw_buffer_store_b128(wq[r], mk_srd(PhiQ, 4194304), ((h * 128 + (row0 >> 4) + r) * 64 + lane) * 16, 0, 0);
        asm volatile("s_waitcnt lgkmcnt(0)" ::: "memory");
        const unsigned ta = tb + (4 * g + (l16 >> 2)) * 32 + (lane & 3) * 8;
        u32x2 pp = __builtin_bit_cast(u32x2, ldstr(ta)), pn = __builtin_bit_cast(u32x2, ldstr(ta + 512));
        asm volatile("s_waitcnt lgkmcnt(0)" : "+v"(pp), "+v"(pn) :: "memory");
        const u32x2 vb = {cvtpk(a.av[r][0], a.av[r][1]), cvtpk(a.av[r][2], a.av[r][3])};
        sp = mfma16_16(pp, vb, sp); sn = mfma16_16(pn, vb, sn);
        zp += (__uint_as_float(pp[0] << 16) + __uint_as_float(pp[0] & 0xffff0000u)) + (__uint_as_float(pp[1] << 16) + __uint_as_float(pp[1] & 0xffff0000u));
        zn += (__uint_as_float(pn[0] << 16) + __uint_as_float(pn[0] & 0xffff0000u)) + (__uint_as_float(pn[1] << 16) + __uint_as_float(pn[1] & 0xffff0000u));
    }
    zp = grp_sum(zp); zn = grp_sum(zn);
    BARRIER();
    LAS char* red = (LAS char*)(lds0 + B1_RED + (wid & 3) * 2176);
    if (wid >= 4) { *(LAS f32x4*)(red + lane * 32) = sp; *(LAS f32x4*)(red + lane * 32 + 16) = sn; if (lane < 16) *(LAS f32x2*)(red + 2048 + lane * 8) = (f32x2){zp, zn}; }
    BARRIER();
    sp += *(const LAS f32x4*)(red + lane * 32); sn += *(const LAS f32x4*)(red + lane * 32 + 16);
    const f32x2 zo = *(const LAS f32x2*)(red + 2048 + l16 * 8);
    const srd_t srd = mk_srd(Bst + (size_t)(h * 32 + rc) * 576, wid < 4 ? 2304 : 0);
    store16_sc1(__builtin_bit_cast(u32x4, sp), lane * 32, srd, 0u); store16_sc1(__builtin_bit_cast(u32x4, sn), lane * 32 + 16, srd, 0u);
    __builtin_amdgcn_raw_buffer_store_b64((u32x2){__float_as_uint(zp + zo[0]), __float_as_uint(zn + zo[1])}, srd, lane < 16 ? 2048 + lane * 8 : 0x7fffffff, 0, 16);
}
template <int N> DI void waitN(const u64* flags, u64 tok, int lane) {
    const u64* p = flags + (lane & (N - 1)) * FL_STRIDE;
    for (unsigned spins = 0; spins < (1u << 15); ++spins) { const u64 v = ld_agent(p); if (__all(v == tok)) break; __builtin_amdgcn_s_sleep(1); }
    asm volatile("" ::: "memory");
}
struct B2a { f32x4 v[16]; };
DI void b2a_load(B2a& b, const float* Bst, int rc, int hg, int lane) {
    const int h = 4 * hg + (rc >> 3), c = lane & 31, half = lane >> 5;
    const srd_t srd = mk_srd(Bst + (size_t)h * 32 * 576, 32 * 2304);
    const unsigned vo = (c < 18 ? (18 * (rc & 7) + c) * 16 : 0x7fffffff) + half * 16 * 2304;
#pragma unroll
    for (int i = 0; i < 16; ++i) b.v[i] = __builtin_bit_cast(f32x4, __builtin_amdgcn_raw_buffer_load_b128(srd, vo, i * 2304, 16));
}
DI void b2a_finish(const B2a& b, float* Bred, int rc, int hg, int lane) {
    const int h = 4 * hg + (rc >> 3), c = lane & 31, half = lane >> 5;
    f32x4 s = b.v[0];
#pragma unroll
    for (int i = 1; i < 16; ++i) s += b.v[i];
#pragma unroll
    for (int e = 0; e < 4; ++e) s[e] = swap_add(s[e]);
    const srd_t srdR = mk_srd(Bred + (size_t)h * 576, 2304);
    store16_sc1(__builtin_bit_cast(u32x4, s), (half == 0 && c < 18) ? (18 * (rc & 7) + c) * 16 : 0x7fffffffu, srdR, 0u);
}
template <int NRB>
DI void b2b_body(const u16* PhiQ, const float* Bred, u16* Of, const u64* flagsR, u64 tok, int h, int rb0, int lane) {
    PIN(lane);
    const int l16 = lane & 15, g = lane >> 4;
    bf16x8 ph[NRB];
#pragma unroll
    for (int r = 0; r < NRB; ++r) ph[r] = *(const bf16x8*)((const char*)PhiQ + ((size_t)(h * 128 + rb0 + r) * 64 + lane) * 16);
    waitN<8>(flagsR, tok, lane);
    const srd_t srd = mk_srd(Bred + (size_t)h * 576, 2304);
    const f32x4 sp = __builtin_bit_cast(f32x4, __builtin_amdgcn_raw_buffer_load_b128(srd, lane * 32, 0, 16)), sn = __builtin_bit_cast(f32x4, __builtin_amdgcn_raw_buffer_load_b128(srd, lane * 32 + 16, 0, 16));
    const f32x4 za = __builtin_bit_cast(f32x4, __builtin_amdgcn_raw_buffer_load_b128(srd, 2048 + g * 32, 0, 16)), zb = __builtin_bit_cast(f32x4, __builtin_amdgcn_raw_buffer_load_b128(srd, 2048 + g * 32 + 16, 0, 16));
    const u32x4 kvw = {cvtpk(sp[0], sp[1]), cvtpk(sp[2], sp[3]), cvtpk(sn[0], sn[1]), cvtpk(sn[2], sn[3])};
    const u32x4 zw = {cvtpk(za[0], za[2]), cvtpk(zb[0], zb[2]), cvtpk(za[1], za[3]), cvtpk(zb[1], zb[3])};
    const srd_t srdO = mk_srd(Of, 2097152);
#pragma unroll
    for (int r = 0; r < NRB; ++r) { const int rb16 = rb0 + r;
        const f32x4 num = MFMA16(__builtin_bit_cast(bf16x8, kvw), ph[r], f32x4{});
        const f32x4 den = MFMA16(__builtin_bit_cast(bf16x8, zw), ph[r], f32x4{});
        const float dn = rbf16(rbf16(den[0]) + 1e-6f);
        const u32x2 w = {cvtpk(rbf16(num[0]) / dn, rbf16(num[1]) / dn), cvtpk(rbf16(num[2]) / dn, rbf16(num[3]) / dn)};
        __builtin_amdgcn_raw_buffer_store_b64(w, srdO, (((rb16 >> 1) * 32 + h) * 2 + (g & 1)) * 512 + ((rb16 & 1) * 16 + l16) * 16 + (g >> 1) * 8, 0, 16); }
}
struct ProjCtx {
    srd_t srdC, srdX, srdW, srdWq, srdO;
    unsigned va[2], vw[2], vwq, vo, lds0, l128;
    unsigned sA, sW, sWq, sXa;
    int wid, isV;
};
constexpr unsigned TOUCH_LDS = 163840 - 256;
DI void issueKV(const ProjCtx& c, int kt, int i) {
    const unsigned st = c.lds0 + (kt & 1) * 65536;
    if (i < 4) dma1(st + (4 * c.wid + i) * 1024, c.va[i & 1], c.srdC, c.sA + i * 8192 + kt * 128);
    else dma1(st + 32768 + (c.wid >> 2) * 16384 + (4 * (c.wid & 3) + (i - 4)) * 1024, c.vw[(i - 4) >> 1], c.srdW, c.sW + ((i - 4) >> 1) * 65536 + ((i - 4) & 1) * 128 + kt * 524288);
}
DI void issueQ(const ProjCtx& c, int kt, int i) {
    const unsigned st = c.lds0 + (kt % 3) * 49152;
    if (i < 4) dma1(st + (4 * c.wid + i) * 1024, c.va[i & 1], c.srdX, c.sXa + i * 8192 + kt * 128);
    else dma1(st + 32768 + (2 * c.wid + (i - 4)) * 1024, c.vwq, c.srdWq, c.sWq + (i - 4) * 128 + kt * 524288);
}
#define TOUCHMODE 3
template <int WHICH> DI void touch_g(unsigned m0v, unsigned voff, const void* base) {
    if (!(TOUCHMODE & WHICH)) voff = 0;
    const u64 b = (u64)base; const u64 sb = ((u64)rfl((unsigned)(b >> 32)) << 32) | rfl((unsigned)b);
    asm volatile("s_nop 4\n\ts_mov_b32 m0, %0\n\ts_nop 0\n\tglobal_load_lds_dword %1, %2" :: "s"(rfl(m0v)), "v"(voff), "s"(sb) : "memory");
}

struct GAddr { unsigned a[2]; unsigned w[2][2]; };
DI void gaddr_add(GAddr& g, unsigned d) { g.a[0] += d; g.a[1] += d; g.w[0][0] += d; g.w[0][1] += d; g.w[1][0] += d; g.w[1][1] += d; PIN(g.a[0]); PIN(g.a[1]); PIN(g.w[0][0]); PIN(g.w[0][1]); PIN(g.w[1][0]); PIN(g.w[1][1]); }
DI void gaddr_xor(GAddr& g, unsigned d) { g.a[0] ^= d; g.a[1] ^= d; g.w[0][0] ^= d; g.w[0][1] ^= d; g.w[1][0] ^= d; g.w[1][1] ^= d; }
DI bf16x8 wfrag(const GAddr& g, int ks, int nb) { return cat4(ldstr(g.w[nb & 1][0] + ks * 8192 + (nb >> 1) * 512), ldstr(g.w[nb & 1][1] + ks * 8192 + (nb >> 1) * 512)); }
DI void gaddr_w(GAddr& g, unsigned base, int lane) {
    const int gq = lane >> 4, q4 = (lane & 15) >> 2, p4 = lane & 3;
#pragma unroll
    for (int o = 0; o < 2; ++o)
#pragma unroll
        for (int t = 0; t < 2; ++t) g.w[o][t] = base + 2048 * gq + 64 * (4 * t + q4) + 16 * ((2 * o + (p4 >> 1)) ^ ((2 * gq + t) & 3)) + 8 * (p4 & 1);
}
DI void gaddr_a(GAddr& g, unsigned base, int row0, int lane) {
    const int row = row0 + (lane & 15), gq = lane >> 4;
#pragma unroll
    for (int ks = 0; ks < 2; ++ks) g.a[ks] = base + row * 128 + (((4 * ks + gq) ^ ((row >> 1) & 7)) << 4);
}

constexpr int AH = 3;
#define KV_MMA(mb, W) do { _Pragma("unroll") for (int nb = 0; nb < 4; ++nb) { if (ISV) MFMA16I(acc[mb][nb], a[mb], W[nb]); else MFMA16I(acc[mb][nb], W[nb], a[mb]); } } while (0)
template <bool ISV, class Hook>
DI void kv_step0(f32x4 (&acc)[8][4], const bf16x8 (&w)[4], bf16x8 (&wn)[4], bf16x8 (&an)[AH], const GAddr& g, Hook hook) {
    bf16x8 a[8 + AH];
#pragma unroll
    for (int i = 0; i < AH; ++i) a[i] = an[i];
#pragma unroll
    for (int mb = 0; mb < 8; ++mb) {
        a[mb + AH] = mb + AH < 8 ? ldsf(g.a[0] + (mb + AH) * 2048) : ldsf(g.a[1] + (mb + AH - 8) * 2048);
        if (mb < 4) wn[mb] = wfrag(g, 1, mb);
        SBAR();
        KV_MMA(mb, w);
        SBAR();
        hook(mb);
        SBAR();
    }
#pragma unroll
    for (int i = 0; i < AH; ++i) an[i] = a[8 + i];
}
struct NoHook { DI void operator()(int) const {} };

template <bool ISV, class Touch, class IssueB1>
DI void kv_part(const ProjCtx& c, GAddr g, u32x4 (&pk)[16], Touch touchk, IssueB1 issueB1, Stamper& stamp) {
    f32x4 acc[8][4];
#pragma unroll
    for (int a = 0; a < 8; ++a)
#pragma unroll
        for (int b = 0; b < 4; ++b) { acc[a][b] = f32x4{}; PIN(acc[a][b]); }
    bf16x8 w0[4], w1[4], an[AH];
    wait_bar<9>();
    stamp();
#pragma unroll
    for (int nb = 0; nb < 4; ++nb) w0[nb] = wfrag(g, 0, nb);
#pragma unroll
    for (int i = 0; i < AH; ++i) an[i] = ldsf(g.a[0] + i * 2048);
    auto tile = [&](int kt, auto late, auto mid, auto waitB, auto early, bool next) {
        kv_step0<ISV>(acc, w0, w1, an, g, late);
        bf16x8 a[8];
#pragma unroll
        for (int i = 0; i < AH; ++i) a[i] = an[i];
#pragma unroll
        for (int mb = 0; mb < 8 - AH; ++mb) {
            a[mb + AH] = ldsf(g.a[1] + (mb + AH) * 2048);
            SBAR();
            KV_MMA(mb, w1);
            SBAR();
            if (mb == 0) { mid(); SBAR(); }
        }
        waitB();
        if (next) { gaddr_xor(g, 65536u);
#pragma unroll
            for (int nb = 0; nb < 4; ++nb) w0[nb] = wfrag(g, 0, nb);
#pragma unroll
            for (int i = 0; i < AH; ++i) an[i] = ldsf(g.a[0] + i * 2048); }
        SBAR();
#pragma unroll
        for (int mb = 8 - AH; mb < 8; ++mb) { KV_MMA(mb, w1); SBAR(); early(mb - (8 - AH)); SBAR(); }
    };
    for (int kt = 0; kt < 6; ++kt)
        tile(kt, [&](int mb) { if (kt > 0 && (mb & 1)) issueKV(c, kt + 1, 4 + (mb >> 1)); }, [&]() { touchk(kt); }, [&]() { wait_bar<1>(); },
             [&](int i) { if (i == 0) { issueKV(c, kt + 2, 0); issueKV(c, kt + 2, 1); } else issueKV(c, kt + 2, 1 + i); }, true);
    tile(6, [&](int mb) { if (mb & 1) issueKV(c, 7, 4 + (mb >> 1)); }, [&]() {}, [&]() { wait_bar<0>(); }, [&](int i) { if (i < 2) issueB1(i); }, true);
    tile(7, NoHook{}, [&]() {}, [&]() { stamp(); wait_bar<4>(); stamp(); }, [&](int i) { if (i < 2) issueB1(2 + i); }, false);
    MFMA_DRAIN();
#pragma unroll
    for (int n = 0; n < 16; ++n) {
        const f32x4 lo = ISV ? acc[2 * (n >> 2)][n & 3] : acc[n >> 1][2 * (n & 1)], hi = ISV ? acc[2 * (n >> 2) + 1][n & 3] : acc[n >> 1][2 * (n & 1) + 1];
        pk[n] = (u32x4){cvtpk(lo[0], lo[1]), cvtpk(lo[2], lo[3]), cvtpk(hi[0], hi[1]), cvtpk(hi[2], hi[3])}; PIN(pk[n]); }
}
DI void kv_store(const ProjCtx& c, const u32x4 (&pk)[16], int n) {
    const unsigned offV = (n >> 3) * 16384 + ((n & 3) * 2 + ((n >> 2) & 1)) * 1024;
    const unsigned offK = (n >> 3) * 16384 + (((n >> 1) & 3) * 4 + (n & 1)) * 1024;
    store16_sc1(pk[n], c.vo, c.srdO, c.isV ? offV : offK);
}

struct QFr { bf16x8 x[2]; bf16x8 w[8]; };
template <int KS> DI void q_read(QFr& f, const GAddr& g, int i) {
    if (i == 1) f.x[0] = ldsf(g.a[KS]); else if (i == 2) f.x[1] = ldsf(g.a[KS] + 2048); else { const int nb = i == 0 ? 0 : i - 2; f.w[nb] = wfrag(g, KS, nb); }
}
template <int KSN, bool NEXT, class Hook>
DI void q_kstep(f32x4 (&acc)[8][2], const QFr& cur, QFr& nxt, const GAddr& gn, Hook hook) {
#pragma unroll
    for (int nb = 0; nb < 8; ++nb) {
        MFMA16I(acc[nb][0], cur.w[nb], cur.x[0]); MFMA16I(acc[nb][1], cur.w[nb], cur.x[1]);
        SBAR();
        if (NEXT) { if (nb < 2) { q_read<KSN>(nxt, gn, 2 * nb); q_read<KSN>(nxt, gn, 2 * nb + 1); } else q_read<KSN>(nxt, gn, nb + 2); }
        hook(nb);
        SBAR();
    }
}
template <int KT, class Pub>
DI void q_tile(const ProjCtx& c, bool early, GAddr& g, f32x4 (&acc)[8][2], QFr& f0, QFr& f1, const u32x4 (&pk)[16], Pub after, Stamper& stamp) {
    q_kstep<1, true>(acc, f0, f1, g, [&](int nb) {
        if (KT >= 1 && KT + 2 < 8 && nb < 3) issueQ(c, KT + 2, 3 + nb);
        if (nb == 4) { if (early) { if (KT < 2) kv_store(c, pk, 12 + 2 * (KT < 2 ? KT : 0)); } else if (KT < 5) kv_store(c, pk, 11 + (KT < 5 ? KT : 0)); }
        if (nb == 7 && early && KT < 2) kv_store(c, pk, 13 + 2 * (KT < 2 ? KT : 0)); });
    if constexpr (KT < 7) {
        constexpr int WE = KT == 0 ? 17 : (KT == 1 ? 19 : (KT == 2 ? 8 : (KT <= 5 ? 6 : 0)));
        constexpr int WL = KT == 0 ? 17 : (KT == 1 ? 18 : (KT <= 4 ? 8 : (KT == 5 ? 7 : 0)));
        const bool w7 = KT == 5 && c.wid == 7;
        if (early) { if (w7) wait_bar<WE + 16>(); else wait_bar<WE>(); } else { if (w7) wait_bar<WL + 16>(); else wait_bar<WL>(); }
        stamp();
        after(KT);
        gaddr_add(g, (unsigned)(((KT + 1) % 3 - KT % 3) * 49152));
        q_kstep<0, true>(acc, f1, f0, g, [&](int nb) { if (KT + 3 < 8 && nb < 3) issueQ(c, KT + 3, nb); });
    } else q_kstep<0, false>(acc, f1, f0, g, NoHook{});
}

DI void proj_setup(ProjCtx& c, const u16* X, const u16* Ckv, const u16* Wq, const u16* Wuk, const u16* Wuv, u16* Kf, u16* Vf, unsigned lds0, int h, int j, int wid, int lane) {
    const int wr = wid >> 2, wc = wid & 3;
    c.wid = wid; c.isV = wc >> 1; c.lds0 = lds0;
    c.srdC = mk_srd(Ckv, 2097152); c.srdX = mk_srd(X, 2097152);
    c.srdW = mk_srd((wid < 4 ? Wuk : Wuv), 4194304); c.srdWq = mk_srd(Wq, 4194304);
    c.srdO = mk_srd((const char*)(c.isV ? Vf : Kf) + (size_t)h * 524288, 524288);
#pragma unroll
    for (int p = 0; p < 2; ++p) {
        c.va[p] = (lane >> 3) * 1024 + (((lane & 7) ^ (4 * p + (lane >> 4))) * 16);
        c.vw[p] = ((lane >> 2) & 7) * 8192 + (4 * (lane >> 5) + ((lane & 3) ^ ((2 * p + ((lane >> 4) & 1)) & 3))) * 16;
    }
    c.vwq = (wid & 1) ? c.vw[1] : c.vw[0];
    c.l128 = lane * 128;
    c.sA = (256 * j + 32 * wid) * 1024;
    c.sW = (16 * (wid & 3)) * 8192 + h * 256;
    c.sXa = (wid < 4 ? 128 * (15 - j) + 32 * wid : 128 * j + 32 * (wid - 4)) * 1024;
    c.sWq = (8 * wid) * 8192 + h * 256;
    c.vo = lane * 16 + (4 * j + 2 * wr) * 16384 + (wc & 1) * (c.isV ? 8192 : 2048);
}
template <class Touch, class IssueB1>
DI void kv_run(const ProjCtx& c, int lane, u32x4 (&pk)[16], Touch touchk, IssueB1 issueB1, Stamper& stamp) {
    const int wid = c.wid, wr = wid >> 2, wc = wid & 3;
    GAddr g; gaddr_a(g, c.lds0, 128 * wr, lane); gaddr_w(g, c.lds0 + 32768 + (wc >> 1) * 16384 + (wc & 1) * 1024, lane);
    if (c.isV) kv_part<true>(c, g, pk, touchk, issueB1, stamp); else kv_part<false>(c, g, pk, touchk, issueB1, stamp);
}
template <class Pub>
DI void q_run(const ProjCtx& c, bool early, int lane, const u32x4 (&pk)[16], bf16x8 (&q)[8], Pub after, Stamper& stamp) {
    GAddr g; gaddr_a(g, c.lds0, 32 * c.wid, lane); gaddr_w(g, c.lds0 + 32768, lane); gaddr_add(g, 0u);
    f32x4 acc[8][2];
#pragma unroll
    for (int a = 0; a < 8; ++a) { acc[a][0] = f32x4{}; acc[a][1] = f32x4{}; PIN(acc[a][0]); PIN(acc[a][1]); }
    QFr f0, f1;
#pragma unroll
    for (int i = 0; i < 10; ++i) q_read<0>(f0, g, i);
    q_tile<0>(c, early, g, acc, f0, f1, pk, after, stamp); q_tile<1>(c, early, g, acc, f0, f1, pk, after, stamp); q_tile<2>(c, early, g, acc, f0, f1, pk, after, stamp); q_tile<3>(c, early, g, acc, f0, f1, pk, after, stamp);
    q_tile<4>(c, early, g, acc, f0, f1, pk, after, stamp); q_tile<5>(c, early, g, acc, f0, f1, pk, after, stamp); q_tile<6>(c, early, g, acc, f0, f1, pk, after, stamp); q_tile<7>(c, early, g, acc, f0, f1, pk, after, stamp);
    MFMA_DRAIN();
    constexpr float QSC = 0.08838834764831845f * 1.4426950408889634f;
#pragma unroll
    for (int qb = 0; qb < 2; ++qb)
#pragma unroll
        for (int ds = 0; ds < 4; ++ds) { const f32x4 lo = acc[2 * ds][qb] * QSC, hi = acc[2 * ds + 1][qb] * QSC;
            q[qb * 4 + ds] = __builtin_bit_cast(bf16x8, (u32x4){cvtpk(lo[0], lo[1]), cvtpk(lo[2], lo[3]), cvtpk(hi[0], hi[1]), cvtpk(hi[2], hi[3])}); }
}
template <int OFF, bool SETM0> DI void dma_piece(unsigned m0v, unsigned voff, srd_t srd, unsigned soff) {
    if constexpr (SETM0) asm volatile("s_mov_b32 m0, %0\n\ts_nop 0\n\tbuffer_load_dwordx4 %1, %2, %3 offen offset:%4 sc1 lds" :: "s"(m0v), "v"(voff), "s"(srd), "s"(soff), "i"(OFF) : "memory");
    else asm volatile("buffer_load_dwordx4 %0, %1, %2 offen offset:%3 sc1 lds" :: "v"(voff), "s"(srd), "s"(soff), "i"(OFF) : "memory");
}
#define ATT_THRV 64.f
constexpr float ATT_THR = ATT_THRV;
#define KSPLIT 0
#define ATT_WIN 5
constexpr int ATT_W = ATT_WIN;

struct AttnState { f32x4 o[8][2]; f32x4 s[4][2]; f32x4 negm[2]; bf16x8 q[8]; u32x4 p[2][2]; f32x4 l[2]; float mhat[2]; u32x4 ones; };
struct AttnCtx {
    srd_t srdK, srdV, srdC; unsigned voff, lane16, kslot0, vslot0, coffL;
    int g, nl  , thrg, dq0, relH, jump; unsigned qstash;
    unsigned soA, soB, ksoM; int rel;
    srd_t srdF; u64 tok, prev; unsigned known;
};
DI u64 ld_flag8(const AttnCtx& c) { return __builtin_bit_cast(u64, __builtin_amdgcn_raw_buffer_load_b64(c.srdF, (c.lane16 & 0x70u) << 3, 0, 16)); }

DI unsigned m_off(int i) { return i < 16 ? (unsigned)(((i & 7) * 2 + (i >> 3)) * 1024) : (unsigned)((((i - 16) & 3) * 4 + ((i - 16) >> 2)) * 1024); }
template <bool DO_PV, bool DO_QK, int KP, int VP, class Hook>
DI void m_phase(AttnState& st, unsigned kbase, unsigned vbase, bf16x8 (&pre)[ATT_W], Hook hook) {
    constexpr int N0 = DO_PV ? 0 : 16, N1 = DO_QK ? 32 : 16;
    bf16x8 fr[32];
#pragma unroll
    for (int i = 0; i < ATT_W; ++i) fr[N0 + i] = pre[i];
#pragma unroll
    for (int i = N0; i < N1; ++i) {
        const int n = i + ATT_W;
        if (n < N1) fr[n] = ldsf((n < 16 ? vbase + VP * 16384 : kbase + KP * 16384) + m_off(n));
        SBAR();
        if (i >= 16) { const int ds = (i - 16) >> 2, kb = (i - 16) & 3;
            if (ds == 0) { asm volatile("v_mfma_f32_16x16x32_bf16 %0, %2, %3, %1" : "=&v"(st.s[kb][0]), "+v"(st.negm[0]) : "v"(fr[i]), "v"(st.q[0]));
                           asm volatile("v_mfma_f32_16x16x32_bf16 %0, %2, %3, %1" : "=&v"(st.s[kb][1]), "+v"(st.negm[1]) : "v"(fr[i]), "v"(st.q[4])); }
            else { MFMA16I(st.s[kb][0], fr[i], st.q[ds]); MFMA16I(st.s[kb][1], fr[i], st.q[4 + ds]); }
        } else { const int ks = i >> 3, db = i & 7;
            MFMA16I(st.o[db][0], fr[i], st.p[ks][0]); MFMA16I(st.o[db][1], fr[i], st.p[ks][1]);
            if (db == 3) { MFMA16I(st.l[0], st.ones, st.p[ks][0]); MFMA16I(st.l[1], st.ones, st.p[ks][1]); } }
        SBAR();
        hook(i);
        SBAR();
    }
    MFMA_DRAIN();
    PIN(st.negm[0]); PIN(st.negm[1]);
}
struct NoHookI { DI void operator()(int) const {} };

DI void store_ctx(const f32x4 (&o)[8][2], float inv0, float inv1, srd_t srd, unsigned voff) {
    PIN(voff);
#pragma unroll
    for (int p = 0; p < 4; ++p)
#pragma unroll
        for (int qb = 0; qb < 2; ++qb) { const float inv = qb ? inv1 : inv0; const f32x4 lo = o[2 * p][qb] * inv, hi = o[2 * p + 1][qb] * inv;
            __builtin_amdgcn_raw_buffer_store_b128((u32x4){cvtpk(lo[0], lo[1]), cvtpk(lo[2], lo[3]), cvtpk(hi[0], hi[1]), cvtpk(hi[2], hi[3])}, srd, voff + p * 2048 + qb * 256, 0, 16); }
}
DI float row_add(float x) { return swap_add(x16_add(x)); }
DI float row_max4(float x) { return swap_max(x16_max(x)); }

template <bool LAST, int PAR>
DI void v_phase(AttnState& st, AttnCtx& c, const int t) {
    const bool sw = (t == c.nl);
    const unsigned vm0 = c.vslot0 + (PAR ^ 1) * 16384;
    const unsigned vso = c.soA, kso = c.soB;
    if (!LAST && __builtin_expect(c.known != 0xffu, 0)) {
        c.known |= (unsigned)__builtin_amdgcn_ballot_w64(c.prev == c.tok) & 0xffu;
        const unsigned need = 1u << ((kso >> 16) & 7);
        if (kso < 524288u && !(c.known & need)) {
            for (unsigned spins = 0; spins < (1u << 15); ++spins) { const u64 v = ld_flag8(c); c.known |= (unsigned)__builtin_amdgcn_ballot_w64(v == c.tok) & 0xffu; if (c.known & need) break; __builtin_amdgcn_s_sleep(1); } }
        asm volatile("" ::: "memory");
        c.prev = ld_flag8(c);
    }
    SBAR();
    if (__builtin_expect(sw, 0)) { store_ctx(st.o, __builtin_amdgcn_rcpf(st.l[0][0]), __builtin_amdgcn_rcpf(st.l[1][0]), c.srdC, ((c.lane16 >> 8) << 9) + (c.lane16 & 0xf0u) + c.coffL);
#pragma unroll
        for (int d = 0; d < 8; ++d) { st.o[d][0] = f32x4{}; st.o[d][1] = f32x4{}; PIN(st.o[d][0]); PIN(st.o[d][1]); }
        st.l[0] = f32x4{}; st.l[1] = f32x4{}; PIN(st.l[0]); PIN(st.l[1]); c.rel = c.relH; }
    if (__builtin_expect(c.rel > c.thrg, 0)) {
        int thr = 32 * c.g - c.rel + c.dq0; PIN(thr); const float NEG = -__builtin_inff();
#pragma unroll
        for (int kb = 0; kb < 4; ++kb)
#pragma unroll
            for (int qb = 0; qb < 2; ++qb)
#pragma unroll
                for (int e = 0; e < 4; ++e) if (16 * kb - 16 * qb + e > thr) st.s[kb][qb][e] = NEG;
    }
    SBAR();
    if (!LAST) dma_piece<0, true>(vm0, c.voff, c.srdV, vso);
#define MX3(a, b, c) __builtin_fmaxf(__builtin_fmaxf((a), (b)), (c))
    float mx[2];
#pragma unroll
    for (int qb = 0; qb < 2; ++qb) {
        float a = MX3(st.s[0][qb][0], st.s[0][qb][1], st.s[0][qb][2]), b = MX3(st.s[0][qb][3], st.s[1][qb][0], st.s[1][qb][1]);
        a = MX3(a, st.s[1][qb][2], st.s[1][qb][3]); b = MX3(b, st.s[2][qb][0], st.s[2][qb][1]); a = MX3(a, st.s[2][qb][2], st.s[2][qb][3]);
        b = MX3(b, st.s[3][qb][0], st.s[3][qb][1]); a = MX3(a, st.s[3][qb][2], st.s[3][qb][3]); mx[qb] = __builtin_fmaxf(a, b); }
    const unsigned long long firstm = (t == 0 || sw) ? ~0ull : 0ull;
    if (__builtin_expect((__builtin_amdgcn_ballot_w64(__builtin_fmaxf(mx[0], mx[1]) > ATT_THR) | firstm) != 0ull, 0)) {
        const bool first = firstm != 0ull;
#pragma unroll
        for (int qb = 0; qb < 2; ++qb) {
            const float delta = __builtin_fmaxf(row_max4(mx[qb]), first ? -1e30f : 0.f);
            st.mhat[qb] = (first ? 0.f : st.mhat[qb]) + delta;
#pragma unroll
            for (int kb = 0; kb < 4; ++kb) st.s[kb][qb] -= delta;
            st.negm[qb] = (f32x4){-st.mhat[qb], -st.mhat[qb], -st.mhat[qb], -st.mhat[qb]};
            if (!first) { const float f = __builtin_amdgcn_exp2f(-delta); st.l[qb] *= f;
#pragma unroll
                for (int d = 0; d < 8; ++d) st.o[d][qb] *= f; } }
    }
    SBAR();
    float e[8][4];
#define EXPG(k) do { _Pragma("unroll") for (int i_ = 0; i_ < 4; ++i_) e[k][i_] = __builtin_amdgcn_exp2f(st.s[(k) >> 1][(k) & 1][i_]); } while (0)
#define FING(k) do { constexpr int kb_ = (k) >> 1, qb_ = (k) & 1; \
        unsigned w0_ = cvtpk(e[k][0], e[k][1]), w1_ = cvtpk(e[k][2], e[k][3]); PIN(w0_); PIN(w1_); \
        st.p[kb_ >> 1][qb_][2 * (kb_ & 1)] = w0_; st.p[kb_ >> 1][qb_][2 * (kb_ & 1) + 1] = w1_; } while (0)
    EXPG(0); SBAR();
    EXPG(1); FING(0); SBAR();
    if (!LAST) dma_piece<1024, false>(vm0, c.voff, c.srdV, vso);
    EXPG(2); FING(1); SBAR();
    EXPG(3); FING(2); SBAR();
    if (!LAST) dma_piece<2048, false>(vm0, c.voff, c.srdV, vso);
    EXPG(4); FING(3); SBAR();
    EXPG(5); FING(4); SBAR();
    if (!LAST) dma_piece<3072, false>(vm0, c.voff, c.srdV, vso);
    EXPG(6); FING(5); SBAR();
    EXPG(7); FING(6); SBAR();
    FING(7);
#undef EXPG
#undef FING
    if (!LAST) {
        if (__builtin_expect(t == c.nl - 1, 0)) {
            unsigned qs = c.qstash; PIN(qs);
#pragma unroll
            for (int s = 0; s < 8; ++s) st.q[s] = ldsf(qs + s * 1024);
            st.negm[0] = f32x4{}; st.negm[1] = f32x4{}; PIN(st.negm[0]); PIN(st.negm[1]);
        }
        c.ksoM = kso;
        c.soA = c.soB; c.soB += (t + 3 == c.nl) ? c.jump : 16384; c.rel += 64;
    }
    SBAR();
}

DI void attn_setup(AttnCtx& c, const u16* Kf, const u16* Vf, u16* Cf, unsigned lds0, int h, int j, int wid, int lane) {
    PIN(lane);
    const int grp = wid >> 2; c.g = wid & 3; c.dq0 = (lane & 15) - 4 * (lane >> 4);
    const int qbH = 15 - j, qbL = j, nl = 2 * j + 2;
    c.nl = grp ? nl : 1000; c.thrg = 32 * c.g - 63; c.jump = (18 - nl) * 16384; c.relH = 64 * (17 - 2 * qbH);
    c.rel = grp ? -128 * qbL : -128 * qbH;
    c.soA = 16384; c.soB = (c.nl == 2) ? 17 * 16384 : 2 * 16384;
    c.srdK = mk_srd((const char*)Kf + (size_t)h * 524288, 524288);
    c.srdV = mk_srd((const char*)Vf + (size_t)h * 524288, 524288);
    c.srdC = mk_srd((const char*)Cf, 16777216);
    c.lane16 = lane * 16; c.voff = lane * 16 + c.g * 4096;
    c.kslot0 = lds0 + grp * 65536 + c.g * 4096; c.vslot0 = c.kslot0 + 32768;
    c.coffL = ((qbL * 4 + c.g) * 32 + h) * 8192;
    c.qstash = lds0 + 131072 + c.g * 8192 + lane * 16;
}
#define DMA4(m_, vo_, srd, so_) do { dma_piece<0, true>(m_, vo_, srd, so_); dma_piece<1024, false>(m_, vo_, srd, so_); dma_piece<2048, false>(m_, vo_, srd, so_); dma_piece<3072, false>(m_, vo_, srd, so_); } while (0)
DI void attn_prime(const AttnCtx& c, int grp) { DMA4(grp ? c.vslot0 + 16384 : c.kslot0, c.voff, c.srdK, 0u); DMA4(c.vslot0, c.voff, c.srdV, 0u); if (grp == 0) DMA4(c.kslot0 + 16384, c.voff, c.srdK, 16384u); }

template <class First, class G0Tail>
DI void attn_run(AttnState& st, AttnCtx& c, unsigned lds0, int h, int j, int wid, int lane, First afterFirst, G0Tail g0tail, Stamper& stamp) {
    PIN(lane);
    const int grp = wid >> 2;
    const unsigned kbase = lds0 + grp * 65536 + lane * 16;
    const unsigned vbase = lds0 + grp * 65536 + 32768 + lane * 16;
#pragma unroll
    for (int d = 0; d < 8; ++d) { st.o[d][0] = f32x4{}; st.o[d][1] = f32x4{}; PIN(st.o[d][0]); PIN(st.o[d][1]); }
    st.negm[0] = f32x4{}; st.negm[1] = f32x4{}; st.l[0] = f32x4{}; st.l[1] = f32x4{}; st.mhat[0] = 0.f; st.mhat[1] = 0.f;
    st.ones = (u32x4){0x3f803f80u, 0x3f803f80u, 0x3f803f80u, 0x3f803f80u}; PIN(st.ones); PIN(st.l[0]); PIN(st.l[1]);
    PIN(st.negm[0]); PIN(st.negm[1]);
    if (grp == 1) { DMA4(c.kslot0 + 16384, c.voff, c.srdK, 16384u); BARRIER(); }
    wait_bar<8>();
    stamp();
    bf16x8 pre[ATT_W];
#define PRE_V(P) do { _Pragma("unroll") for (int i = 0; i < ATT_W; ++i) pre[i] = ldsf(vbase + (P) * 16384 + m_off(i)); } while (0)
    const unsigned kbase0 = grp ? kbase + 49152 : kbase;
#pragma unroll
    for (int i = 0; i < ATT_W; ++i) pre[i] = ldsf(kbase0 + m_off(16 + i));
    __builtin_amdgcn_s_setprio(1);
    m_phase<false, true, 0, 0>(st, kbase0, vbase, pre, NoHookI{});
    __builtin_amdgcn_s_setprio(0);
    wait_bar<0>();
    afterFirst();
#define PSTAMP(t) do {} while (0)
#define STEP(PAR, t) do { \
        v_phase<false, PAR>(st, c, t); \
        PRE_V(PAR); \
        PSTAMP(t); \
        wait_bar_keep<(KSPLIT ? 6 : 4)>(); \
        PSTAMP(t); \
        __builtin_amdgcn_s_setprio(1); \
        m_phase<true, true, PAR ^ 1, PAR>(st, kbase, vbase, pre, [&](int i) { \
            if (!KSPLIT) { if (i == 3) dma_piece<0, true>(c.kslot0 + PAR * 16384, c.voff, c.srdK, c.ksoM); if (i == 9) dma_piece<1024, false>(c.kslot0 + PAR * 16384, c.voff, c.srdK, c.ksoM); } \
            if (i == (KSPLIT ? 5 : 15)) dma_piece<2048, KSPLIT>(c.kslot0 + PAR * 16384, c.voff, c.srdK, c.ksoM); if (i == (KSPLIT ? 17 : 21)) dma_piece<3072, false>(c.kslot0 + PAR * 16384, c.voff, c.srdK, c.ksoM); }); \
        __builtin_amdgcn_s_setprio(0); \
        PSTAMP(t); \
        wait_bar<4>(); \
        PSTAMP(t); } while (0)
    for (int t = 0; t < 16; t += 2) { stamp(); STEP(0, t); STEP(1, t + 1); }
    stamp();
    v_phase<true, 0>(st, c, 16);
    PRE_V(0);
    asm volatile("s_barrier" ::: "memory");
    __builtin_amdgcn_s_setprio(1);
    m_phase<true, false, 0, 0>(st, kbase, vbase, pre, NoHookI{});
    __builtin_amdgcn_s_setprio(0);
    wait_bar<0>();
    stamp();
    PIN(lane);
    const float lt0 = st.l[0][0], lt1 = st.l[1][0];
    const unsigned xo = lds0 + c.g * 16384 + lane * 16;
    LAS f32x4* xs = (LAS f32x4*)(lds0 + 65536 + c.g * 1024 + lane * 16);
    if (grp == 0) {
#pragma unroll
        for (int d = 0; d < 8; ++d) { *(LAS f32x4*)(xo + (2 * d) * 1024) = st.o[d][0]; *(LAS f32x4*)(xo + (2 * d + 1) * 1024) = st.o[d][1]; }
        *xs = (f32x4){st.mhat[0], lt0, st.mhat[1], lt1};
        BARRIER();
        g0tail();
    } else {
        const f32x4 o0 = *xs;
        float a0[2], a1[2];
#pragma unroll
        for (int qb = 0; qb < 2; ++qb) { const float m0 = o0[2 * qb], l0 = o0[2 * qb + 1], lt = qb ? lt1 : lt0;
            const float m = __builtin_fmaxf(m0, st.mhat[qb]), f0 = __builtin_amdgcn_exp2f(m0 - m), f1 = __builtin_amdgcn_exp2f(st.mhat[qb] - m);
            const float inv = __builtin_amdgcn_rcpf(l0 * f0 + lt * f1); a0[qb] = f0 * inv; a1[qb] = f1 * inv; }
#pragma unroll
        for (int d = 0; d < 8; ++d)
#pragma unroll
            for (int qb = 0; qb < 2; ++qb) st.o[d][qb] = st.o[d][qb] * a1[qb] + *(const LAS f32x4*)(xo + (2 * d + qb) * 1024) * a0[qb];
        store_ctx(st.o, 1.0f, 1.0f, c.srdC, (lane >> 4) * 512 + (lane & 15) * 16 + (((15 - j) * 4 + c.g) * 32 + h) * 8192);
    }
    stamp();
#undef PRE_V
#undef STEP
}
constexpr int OUT_LDS = 163840;
constexpr unsigned OUT_O = 131072;
struct OutCtx { srd_t srdC, srdW, srdB; unsigned lane16, vw0, vwA, sC, sW, sB, lds0; int wid, rot; };
DI void out_issueA(const OutCtx& c, int kt, int i) {
    const unsigned st = c.lds0 + (kt & 3) * 32768;
    const int kr = (kt + c.rot) & 15;
    if (i < 2) { const int p = 2 * c.wid + i;
        dma1s(st + p * 1024, c.lane16, c.srdC, c.sC + (p >> 2) * 262144 + (p & 3) * 1024 + kr * 4096); }
    else dma1(st + 16384 + (2 * c.wid + (i - 2)) * 1024, c.vwA, c.srdW, c.sW + (8 * c.wid) * 1024 + (i - 2) * 128 + kr * 65536);
}
DI void out_issueB(const OutCtx& c, int ch, int i) {
    const int rg = 2 * c.wid + (i >> 1);
    dma1(c.lds0 + ch * 32768 + rg * 2048 + (i & 1) * 1024, (i >> 1) ? (c.vw0 ^ 32u) : c.vw0, c.srdB, c.sB + (ch * 128 + 8 * rg) * 1024 + (i & 1) * 128);
}
DI u32x4 out_pack(f32x4 s0, f32x4 s1) {
    unsigned a0 = cvtpk(s0[0], s0[1]), a1 = cvtpk(s0[2], s0[3]), b0 = cvtpk(s1[0], s1[1]), b1 = cvtpk(s1[2], s1[3]);
    auto x0 = __builtin_amdgcn_permlane32_swap(a0, b0, false, false); auto x1 = __builtin_amdgcn_permlane32_swap(a1, b1, false, false);
    return (u32x4){x0[0], x1[0], x0[1], x1[1]};
}
DI f32x4 quad(const f32x16& x, int rq) { return (f32x4){x[4 * rq], x[4 * rq + 1], x[4 * rq + 2], x[4 * rq + 3]}; }

DI void out_phase(const u16* Wo, const u16* Bwo, u16* out, char* ws, u64 tok, unsigned lds0, int bid, int wid, int lane, Stamper& stamp) {
    PIN(lane);
    const int tid = wid * 64 + lane, r32 = lane & 31, hi = lane >> 5, wr = wid >> 2, wc = wid & 3;
    const int x = bid & 7, u = bid >> 3, s = x >> 1, rb = 8 * (x & 1) + (u >> 2), cb = u & 3, tile = rb * 4 + cb;
    OutCtx c; c.wid = wid; c.lds0 = lds0; c.lane16 = lane * 16;
    c.srdC = mk_srd(ws + WS_CF + (size_t)rb * 4 * 262144, 4 * 262144); c.srdW = mk_srd(Wo, 4194304); c.srdB = mk_srd(Bwo, 524288);
    c.vw0 = ((lane >> 2) & 7) * 1024 + (4 * (lane >> 5) + ((lane & 3) ^ ((lane >> 4) & 1))) * 16; c.vwA = (wid & 1) ? (c.vw0 ^ 32u) : c.vw0;
    c.rot = 4 * cb + 2 * (rb & 7);
    c.sC = s * 8 * 8192; c.sW = s * 1024 * 1024 + cb * 256; c.sB = cb * 256;
#pragma unroll
    for (int kt = 0; kt < 4; ++kt) { out_issueA(c, kt, 2); out_issueA(c, kt, 3); }
    u64* flags = (u64*)(ws + WS_FLAG);
    {
        const int l = lane & 15, hh = 8 * s + (l & 7), jj = rb < 8 ? rb : 15 - rb;
        const int src = l < 8 ? ((hh >> 2) | (jj << 3) | ((hh & 3) << 6)) : (8 * (2 * rb + (s >> 1)) + (l & 7));
        const u64* fp = flags + (FL_C + src) * FL_STRIDE;
        for (unsigned spins = 0; spins < (1u << 15); ++spins) { const u64 v = ld_agent(fp); if (__all(v == tok)) break; __builtin_amdgcn_s_sleep(1); }
        asm volatile("" ::: "memory"); }
    stamp();
#pragma unroll
    for (int kt = 0; kt < 4; ++kt) { out_issueA(c, kt, 0); out_issueA(c, kt, 1); }
    { const srd_t srdO = mk_srd(ws + WS_OF + (size_t)(rb * 4 + s) * 32768, 32768);
#pragma unroll
      for (int i = 0; i < 4; ++i) dma1s(lds0 + OUT_O + (4 * wid + i) * 1024, c.lane16, srdO, (unsigned)((4 * wid + i) * 1024)); }
    const int q4 = (lane & 15) >> 2, p4 = lane & 3, blk = (lane >> 4) & 1;
    const unsigned aaddr = lds0 + (2 * wr) * 4096 + c.lane16;
    unsigned waA[2], waddr[2];
#pragma unroll
    for (int t = 0; t < 2; ++t) { waA[t] = lds0 + 2048 * t + 512 * wc + 64 * (4 * hi + q4) + 16 * ((2 * blk + (p4 >> 1)) ^ ((2 * t + hi) & 3)) + 8 * (p4 & 1);
                                  waddr[t] = waA[t]; }
    f32x16 acc[2] = {f32x16{}, f32x16{}};
    stamp();
    struct Fr { bf16x8 a[2][2]; bf16x4 w[2][2]; };
    auto rd = [&](Fr& f, unsigned st, int half) {
        unsigned w0 = waA[0] + st + 16384 + half * 8192, w1 = waA[1] + st + 16384 + half * 8192, aa = aaddr + st; PIN(w0); PIN(w1); PIN(aa);
#pragma unroll
        for (int k = 0; k < 2; ++k) { const int ks = 2 * half + k; const unsigned wb = k ? w1 : w0;
            f.w[k][0] = ldstr(wb); f.w[k][1] = ldstr(wb + 4096);
            f.a[k][0] = ldsf(aa + ks * 1024); f.a[k][1] = ldsf(aa + 4096 + ks * 1024); } };
    Fr f0, f1;
    wait_bar<10>();
    rd(f0, 0u, 0);
    auto step = [&](int kt, auto late, auto early) {
        const unsigned st = (kt & 3) * 32768, stn = ((kt + 1) & 3) * 32768;
        rd(f1, st, 1);
        SBAR();
#pragma unroll
        for (int k = 0; k < 2; ++k) { const bf16x8 wf = cat4(f0.w[k][0], f0.w[k][1]);
            acc[0] = MFMA32(wf, f0.a[k][0], acc[0]); acc[1] = MFMA32(wf, f0.a[k][1], acc[1]); SBAR(); late(2 + k); SBAR(); }
        wait_bar<8>();
        rd(f0, stn, 0);
        SBAR();
#pragma unroll
        for (int k = 0; k < 2; ++k) { const bf16x8 wf = cat4(f1.w[k][0], f1.w[k][1]);
            acc[0] = MFMA32(wf, f1.a[k][0], acc[0]); acc[1] = MFMA32(wf, f1.a[k][1], acc[1]); SBAR(); early(k); SBAR(); }
    };
    step(0, [&](int) {}, [&](int i) { out_issueA(c, 4, i); });
    for (int kt = 1; kt < 12; ++kt) step(kt, [&](int i) { out_issueA(c, kt + 3, i); }, [&](int i) { out_issueA(c, kt + 4, i); });
    step(12, [&](int i) { out_issueA(c, 15, i); }, [&](int i) { out_issueB(c, 0, i); });
    step(13, [&](int i) { out_issueB(c, 0, i); }, [&](int i) { out_issueB(c, 1, i); });
    step(14, [&](int i) { out_issueB(c, 1, i); }, [&](int i) { out_issueB(c, 2, i); });
    step(15, [&](int i) { out_issueB(c, 2, i); }, [&](int i) { out_issueB(c, 3, i); });
    out_issueB(c, 3, 2); out_issueB(c, 3, 3);
    stamp();
    const srd_t srdX = mk_srd(ws + WS_XCH + (size_t)tile * 262144, 262144);
#pragma unroll
    for (int mb = 0; mb < 2; ++mb) { const int q = 2 * wr + mb;
        if (q != s) {
#pragma unroll
            for (int rq = 0; rq < 4; ++rq) store16_sc1(__builtin_bit_cast(u32x4, quad(acc[mb], rq)), c.lane16, srdX, (unsigned)(((q * 4 + s) * 4 + wc) * 4096 + rq * 1024)); } }
    const int kh = (wr == (s >> 1)) ? 1 : 0;
    f32x16 accB = f32x16{};
#pragma unroll
    for (int ch = 0; ch < 4; ++ch) {
        if (ch == 0) { wait_bar<0>(); if (tid == 0) st_agent(flags + (FL_XF + tile * 4 + s) * FL_STRIDE, tok); }
        unsigned w0 = waddr[0] + ch * 32768 + kh * 16384, w1 = waddr[1] + ch * 32768 + kh * 16384, oa = lds0 + OUT_O + (8 * ch + 4 * kh) * 1024 + c.lane16; PIN(w0); PIN(w1); PIN(oa);
        bf16x8 a[4]; bf16x4 w[4][2];
#pragma unroll
        for (int ks = 0; ks < 4; ++ks) { w[ks][0] = ldstr(w0 + ks * 4096); w[ks][1] = ldstr(w1 + ks * 4096); a[ks] = ldsf(oa + ks * 1024); }
#pragma unroll
        for (int ks = 0; ks < 4; ++ks) accB = MFMA32(cat4(w[ks][0], w[ks][1]), a[ks], accB);
    }
    stamp();
    BARRIER();
    const int row = rb * 128 + 32 * s + r32;
    if (kh == 1) {
#pragma unroll
        for (int rq = 0; rq < 4; ++rq) *(LAS f32x4*)(lds0 + (wc * 4 + rq) * 1024 + c.lane16) = quad(accB, rq);
        BARRIER();
        const u64* fp = flags + (FL_XF + tile * 4 + (lane & 3)) * FL_STRIDE;
        for (unsigned spins = 0; spins < (1u << 15); ++spins) { const u64 v = ld_agent(fp); if (__all(v == tok)) break; __builtin_amdgcn_s_sleep(1); }
        asm volatile("" ::: "memory");
        f32x4 p[4][4];
#pragma unroll
        for (int q = 0; q < 4; ++q)
#pragma unroll
            for (int rq = 0; rq < 4; ++rq) p[q][rq] = __builtin_bit_cast(f32x4, __builtin_amdgcn_raw_buffer_load_b128(srdX, (q == s ? 0x7ffffff0u : c.lane16) + rq * 1024, ((s * 4 + q) * 4 + wc) * 4096, 16));
        f32x4 r[4];
#pragma unroll
        for (int rq = 0; rq < 4; ++rq) r[rq] = ((p[0][rq] + p[1][rq]) + (p[2][rq] + p[3][rq])) + ((s & 1) ? quad(acc[1], rq) : quad(acc[0], rq));
        u16* o = out + (size_t)row * 1024 + cb * 128 + 32 * wc + 8 * hi;
        *(u32x4*)o = out_pack(r[0], r[1]); *(u32x4*)(o + 16) = out_pack(r[2], r[3]);
    } else {
        BARRIER();
        f32x4 r[4];
#pragma unroll
        for (int rq = 0; rq < 4; ++rq) r[rq] = quad(accB, rq) + *(const LAS f32x4*)(lds0 + (wc * 4 + rq) * 1024 + c.lane16);
        u16* o = out + (size_t)row * 1024 + 512 + cb * 128 + 32 * wc + 8 * hi;
        *(u32x4*)o = out_pack(r[0], r[1]); *(u32x4*)(o + 16) = out_pack(r[2], r[3]);
    }
}
constexpr int FUSED_LDS = 163840;
constexpr unsigned WPHI_LDS = 156160;
struct FusedArgs { const u16 *X, *Ckv, *Wq, *Wuk, *Wuv, *Wo, *Bx, *Wqkv, *Wphi, *Bwo; char* ws; u16* out; };

__global__ __launch_bounds__(512, 2) void fused_kernel(FusedArgs a) {
    const int tid = threadIdx.x, lane = tid & 63;
    const int wid = rfl(tid >> 6);
    const int bid = blockIdx.x, h = (bid & 7) * 4 + (bid >> 6), j = (bid >> 3) & 7;
    const unsigned lds0 = 0u;
    Stamper stamp{0u, 0, lane};
    stamp();
    u16* Kf = (u16*)(a.ws + WS_KF); u16* Vf = (u16*)(a.ws + WS_VF); u16* Cf = (u16*)(a.ws + WS_CF);
    u16* PhiQ = (u16*)(a.ws + WS_PHIQ); u16* Of = (u16*)(a.ws + WS_OF); float* Bst = (float*)(a.ws + WS_BST);
    u64* flags = (u64*)(a.ws + WS_FLAG);
    float* Bred = (float*)(a.ws + WS_BRED);
    const int hg = bid & 7, rc = bid >> 3;
    if (wid < 2) dma1(lds0 + WPHI_LDS + wid * 1024, lane * 16, mk_srd(a.Wphi, 16384), (unsigned)(hg * 2048 + wid * 1024));
    ProjCtx pc; proj_setup(pc, a.X, a.Ckv, a.Wq, a.Wuk, a.Wuv, Kf, Vf, lds0, h, j, wid, lane);
#pragma unroll
    for (int i = 0; i < 8; ++i) issueKV(pc, 0, i);
#pragma unroll
    for (int i = 0; i < 8; ++i) issueKV(pc, 1, i);
    const unsigned tsc = lds0 + TOUCH_LDS;
    { const unsigned vW = (lane >> 4) * 524288 + (lane & 15) * 128;
      const char* base; unsigned vo = pc.l128;
      if (wid < 4) { base = (const char*)((wid & 1) ? a.Wuv : a.Wuk) + ((wid & 2) ? 4 : 2) * 524288 + bid * 2048 - ((wid & 2) ? 0 : 0); vo = vW; }
      else if (wid == 4) base = (const char*)a.Ckv + bid * 8192;
      else { base = (const char*)a.Ckv; vo = 0; }
      touch_g<1>(tsc, vo, base); }
    const u64 epoch = *(const u64 __attribute__((address_space(4)))*)(flags + FL_EPOCH * FL_STRIDE);
    u64 tok = mix64(epoch); if (tok == epoch) tok ^= 1ull;
    tok = ((u64)rfl((unsigned)(tok >> 32)) << 32) | rfl((unsigned)tok); asm volatile("" : "+s"(tok));
    B1Ctx bc; b1_setup(bc, a.Bx, a.Wqkv, lds0, rc, hg, wid, lane);
    stamp();
    auto touchk = [&](int kt) {
        const char* base = (const char*)a.Ckv; unsigned vo = 0;
        if (kt == 0) {
            if (wid == 0) { base = (const char*)a.Bx + bid * 8192; vo = pc.l128; }
            else if (wid == 1) { base = (const char*)a.X + bid * 8192; vo = pc.l128; }
            else if (wid == 2) { base = (const char*)a.Wqkv + (bid < 192 ? bid : 0) * 8192; vo = pc.l128; }
        } else if (kt == 1) {
            if (wid < 2) { base = (const char*)a.Wq + (bid * 2 + wid) * 8192; vo = pc.l128; }
        } else if (kt == 2) {
            if (wid < 2) { base = (const char*)a.Wo + (bid * 2 + wid) * 8192; vo = pc.l128; }
            else if (wid == 2) { base = (const char*)a.Bwo + (bid & 63) * 8192; vo = pc.l128; }
        }
        touch_g<2>(tsc, vo, base); };
    u32x4 pk[16];
    kv_run(pc, lane, pk, touchk, [&](int kt) { b1_issue(bc, kt); }, stamp);
    stamp();
    B1Acc ba;
    const bool early = (j & 3) == 0;
    b1_gemm(bc, ba, lane, early, [&](int n) { if (early) kv_store(pc, pk, n); else if (n >= 1 && n <= 6) kv_store(pc, pk, n - 1); },
            [&]() {
#pragma unroll
                for (int i = 0; i < 6; ++i) issueQ(pc, 0, i); },
            [&]() {
#pragma unroll
                for (int i = 0; i < 6; ++i) issueQ(pc, 1, i);
#pragma unroll
                for (int i = 0; i < 6; ++i) issueQ(pc, 2, i); });
    stamp();
    u32x2 wp; { const int l16 = lane & 15, g = lane >> 4; const LAS u16* w = (const LAS u16*)(lds0 + WPHI_LDS + (wid & 3) * 512);
        wp[0] = (unsigned)w[(4 * g) * 16 + l16] | ((unsigned)w[(4 * g + 1) * 16 + l16] << 16); wp[1] = (unsigned)w[(4 * g + 2) * 16 + l16] | ((unsigned)w[(4 * g + 3) * 16 + l16] << 16); }
    b1_phi(ba, [&](int n) { if (early) { if (n % 4 == 2) kv_store(pc, pk, 8 + n / 4); } else if (n % 3 == 1 && n < 14) kv_store(pc, pk, 6 + n / 3); }, wp, PhiQ, Bst, lds0, rc, hg, wid, lane, stamp);
    stamp();
    wait_bar<21>();
    stamp();
    AttnState st;
    AttnCtx c; attn_setup(c, Kf, Vf, Cf, lds0, h, j, wid, lane);
    B2a b2;
    auto spin = [&](const u64* fp) {
        for (unsigned spins = 0; spins < (1u << 15); ++spins) { const u64 v = ld_agent(fp); if (__all(v == tok)) break; __builtin_amdgcn_s_sleep(1); }
        asm volatile("" ::: "memory"); };
    const u64* fkv = flags + (FL_KV + 8 * h + (lane & 7)) * FL_STRIDE;
    const u64* fbs = flags + (FL_BS + 32 * hg + (lane & 31)) * FL_STRIDE;
    u64 pre = 0;
    q_run(pc, early, lane, pk, st.q, [&](int kt) {
        if (kt == 2) { if (tid == 0) st_agent(flags + (FL_BS + 32 * hg + rc) * FL_STRIDE, tok); }
        if (kt == 3) { if (early && tid == 0) st_agent(flags + (FL_KV + 8 * h + j) * FL_STRIDE, tok); }
        if (kt == 3) { if (wid == 7) pre = ld_agent(fbs); }
        if (kt == 4) {
            if (wid == 7) {
                if (!__all(pre == tok)) spin(fbs);
                asm volatile("" ::: "memory");
                b2a_load(b2, Bst, rc, hg, lane); } }
        if (kt == 5) { if (wid == 7) b2a_finish(b2, Bred, rc, hg, lane); pre = ld_agent(fkv); }
        if (kt == 6) {
            if (!early && tid == 0) st_agent(flags + (FL_KV + 8 * h + j) * FL_STRIDE, tok);
            if (tid == 448) st_agent(flags + (FL_BR + 32 * hg + rc) * FL_STRIDE, tok);
            c.known = (unsigned)__builtin_amdgcn_ballot_w64(pre == tok) & 0xffu;
            for (unsigned spins = 0; spins < (1u << 15) && !(c.known & 1u); ++spins) { const u64 v = ld_agent(fkv); c.known = (unsigned)__builtin_amdgcn_ballot_w64(v == tok) & 0xffu; if (!(c.known & 1u)) __builtin_amdgcn_s_sleep(1); }
            asm volatile("" ::: "memory");
            attn_prime(c, wid >> 2);
        } }, stamp);
    c.srdF = mk_srd(flags + (FL_KV + 8 * h) * FL_STRIDE, 1024); c.tok = tok; c.prev = 0;
    stamp();
    BARRIER();
    if (wid < 4) {
#pragma unroll
        for (int s = 0; s < 8; ++s) *(LAS bf16x8*)(c.qstash + s * 1024) = st.q[s]; }
    stamp();
    attn_run(st, c, lds0, h, j, wid, lane,
             [&]() {},
             [&]() { b2b_body<4>(PhiQ, Bred, Of, flags + (FL_BR + 32 * hg + 8 * wid) * FL_STRIDE, tok, 4 * hg + wid, 4 * rc, lane); }, stamp);
    wait_bar<0>();
    if (tid == 0) st_agent(flags + (FL_C + bid) * FL_STRIDE, tok);
    stamp();
    out_phase(a.Wo, a.Bwo, a.out, a.ws, tok, lds0, bid, wid, lane, stamp);
    if (bid == 0 && wid == 7) {
        bool ok = false;
        for (unsigned spins = 0; spins < (1u << 15) && !ok; ++spins) { bool all = true;
#pragma unroll
            for (int i = 0; i < 4; ++i) all &= ld_agent(flags + (FL_C + lane + 64 * i) * FL_STRIDE) == tok;
            ok = __all(all); if (!ok) __builtin_amdgcn_s_sleep(1); }
        if (lane == 0) st_agent(flags + FL_EPOCH * FL_STRIDE, tok);
    }
    stamp();
}

#define WS_END 1
#define WS_SHIFT(n) (WS_END && (n) >= ((size_t)160 << 20) ? (((n) - ((size_t)80 << 20)) & ~(size_t)0x1fffff) : (size_t)0)
extern "C" void kernel_launch(void* const* d_in, const int* in_sizes, int n_in, void* d_out, int out_size, void* d_ws, size_t ws_size, hipStream_t stream) {
    FusedArgs a;
    a.X = (const u16*)d_in[0]; a.Ckv = (const u16*)d_in[1]; a.Wq = (const u16*)d_in[2]; a.Wuk = (const u16*)d_in[3]; a.Wuv = (const u16*)d_in[4]; a.Wo = (const u16*)d_in[5];
    a.Bx = (const u16*)d_in[6]; a.Wqkv = (const u16*)d_in[7]; a.Wphi = (const u16*)d_in[8]; a.Bwo = (const u16*)d_in[9]; a.ws = (char*)d_ws + WS_SHIFT(ws_size); a.out = (u16*)d_out;
    (void)hipFuncSetAttribute((const void*)fused_kernel, hipFuncAttributeMaxDynamicSharedMemorySize, FUSED_LDS);
    fused_kernel<<<256, 512, FUSED_LDS, stream>>>(a);
}
```

```cpp
#include <hip/hip_runtime.h>
#include <stdint.h>
#include <type_traits>

typedef unsigned short u16;
typedef unsigned long long u64;
typedef __bf16 bf16x8 __attribute__((ext_vector_type(8)));
typedef __bf16 bf16x4 __attribute__((ext_vector_type(4)));
typedef __bf16 bf16x2 __attribute__((ext_vector_type(2)));
typedef short s16x4 __attribute__((ext_vector_type(4)));
typedef float f32x16 __attribute__((ext_vector_type(16)));
typedef float f32x4 __attribute__((ext_vector_type(4)));
typedef float f32x2 __attribute__((ext_vector_type(2)));
typedef unsigned u32x4 __attribute__((ext_vector_type(4)));
typedef unsigned u32x2 __attribute__((ext_vector_type(2)));
typedef __amdgpu_buffer_rsrc_t srd_t;
#define LAS __attribute__((address_space(3)))
#define GAS __attribute__((address_space(1)))
#define SBAR() __builtin_amdgcn_sched_barrier(0)
#define PIN(x) asm volatile("" : "+v"(x))
#define MFMA32(a, b, c) __builtin_amdgcn_mfma_f32_32x32x16_bf16((a), (b), (c), 0, 0, 0)
#define MFMA16(a, b, c) __builtin_amdgcn_mfma_f32_16x16x32_bf16((a), (b), (c), 0, 0, 0)
#define DI __device__ __forceinline__
#define MFMA16I(acc, a, b) asm volatile("v_mfma_f32_16x16x32_bf16 %0, %1, %2, %0" : "+v"(acc) : "v"(a), "v"(b))
#define MFMA_DRAIN() asm volatile("s_nop 7")
#pragma clang diagnostic ignored "-Wint-to-pointer-cast"

DI unsigned cvtpk(float lo, float hi) { f32x2 v = {lo, hi}; bf16x2 b = __builtin_convertvector(v, bf16x2); return __builtin_bit_cast(unsigned, b); }
DI unsigned rfl(unsigned x) { return __builtin_amdgcn_readfirstlane(x); }
DI float swap_max(float x) { auto rr = __builtin_amdgcn_permlane32_swap(__float_as_uint(x), __float_as_uint(x), false, false); return __builtin_fmaxf(__uint_as_float(rr[0]), __uint_as_float(rr[1])); }
DI float swap_add(float x) { auto rr = __builtin_amdgcn_permlane32_swap(__float_as_uint(x), __float_as_uint(x), false, false); return __uint_as_float(rr[0]) + __uint_as_float(rr[1]); }
DI srd_t mk_srd(const void* p, int bytes) { return __builtin_amdgcn_make_buffer_rsrc((void*)p, (short)0, bytes, 0x00020000); }
DI bf16x4 ldstr(unsigned a) { return __builtin_amdgcn_ds_read_tr16_b64_v4bf16((LAS bf16x4*)a); }
DI bf16x8 ldsf(unsigned a) { return *(const LAS bf16x8*)a; }
DI bf16x8 cat4(bf16x4 a, bf16x4 b) { return __builtin_shufflevector(a, b, 0, 1, 2, 3, 4, 5, 6, 7); }
#define SCALARIZE2(a, b) do {} while (0)
DI void dma1(unsigned m0v, unsigned voff, srd_t srd, unsigned soff) {
    SCALARIZE2(m0v, soff);
    asm volatile("s_mov_b32 m0, %0\n\ts_nop 0\n\tbuffer_load_dwordx4 %1, %2, %3 offen lds" :: "s"(m0v), "v"(voff), "s"(srd), "s"(soff) : "memory");
}
DI void dma1s(unsigned m0v, unsigned voff, srd_t srd, unsigned soff) {
    SCALARIZE2(m0v, soff);
    asm volatile("s_mov_b32 m0, %0\n\ts_nop 0\n\tbuffer_load_dwordx4 %1, %2, %3 offen sc1 lds" :: "s"(m0v), "v"(voff), "s"(srd), "s"(soff) : "memory");
}
DI void touch(unsigned m0v, unsigned voff, srd_t srd, unsigned soff) {
    SCALARIZE2(m0v, soff);
    asm volatile("s_mov_b32 m0, %0\n\ts_nop 0\n\tbuffer_load_dword %1, %2, %3 offen lds" :: "s"(m0v), "v"(voff), "s"(srd), "s"(soff) : "memory");
}
DI void store16_sc1(u32x4 w, unsigned voff, srd_t srd, unsigned soff) {
    unsigned z = soff; SCALARIZE2(soff, z);
    asm volatile("buffer_store_dwordx4 %0, %1, %2, %3 offen sc1\n\ts_nop 1" :: "v"(w), "v"(voff), "s"(srd), "s"(soff) : "memory");
}
template <int N> DI void wait_vm() { asm volatile("s_waitcnt vmcnt(%0)" :: "i"(N) : "memory"); }
template <int N> DI void wait_bar() { asm volatile("s_waitcnt vmcnt(%0) lgkmcnt(0)\n\ts_barrier" :: "i"(N) : "memory"); }
template <int N> DI void wait_bar_keep() { asm volatile("s_waitcnt vmcnt(%0)\n\ts_barrier" :: "i"(N) : "memory"); }
#define BARRIER() asm volatile("s_waitcnt lgkmcnt(0)\n\ts_barrier" ::: "memory")

struct Stamper { unsigned v; int n; int lane;
    DI void operator()() {
    } };

constexpr size_t WS_FLAG = 0;
constexpr size_t WS_KF = 1 << 20, WS_VF = WS_KF + (16 << 20), WS_CF = WS_VF + (16 << 20), WS_PHIQ = WS_CF + (16 << 20), WS_OF = WS_PHIQ + (4 << 20), WS_BST = WS_OF + (2 << 20), WS_BRED = WS_BST + (3 << 20), WS_XCH = WS_BRED + (1 << 20), WS_DBG = 77 << 20;
constexpr int FL_STRIDE = 16;
constexpr int FL_EPOCH = 0, FL_KV = 32, FL_BS = 32 + 256, FL_BR = 32 + 512, FL_XF = 32 + 768, FL_C = FL_XF + 256;
typedef GAS u64 gu64;
DI u64 ld_agent(const u64* p) { return __hip_atomic_load((gu64*)p, __ATOMIC_RELAXED, __HIP_MEMORY_SCOPE_AGENT); }
DI void st_agent(u64* p, u64 v) { __hip_atomic_store((gu64*)p, v, __ATOMIC_RELAXED, __HIP_MEMORY_SCOPE_AGENT); }
DI u64 mix64(u64 z) { z += 0x9E3779B97F4A7C15ull; z = (z ^ (z >> 30)) * 0xBF58476D1CE4E5B9ull; z = (z ^ (z >> 27)) * 0x94D049BB133111EBull; return z ^ (z >> 31); }
DI f32x4 mfma16_16(u32x2 a, u32x2 b, f32x4 c) { return __builtin_amdgcn_mfma_f32_16x16x16bf16_1k(__builtin_bit_cast(s16x4, a), __builtin_bit_cast(s16x4, b), c, 0, 0, 0); }
DI float rbf16(float x) { return __uint_as_float(cvtpk(x, 0.f) << 16); }
DI float x16_max(float x) { auto rr = __builtin_amdgcn_permlane16_swap(__float_as_uint(x), __float_as_uint(x), false, false); return __builtin_fmaxf(__uint_as_float(rr[0]), __uint_as_float(rr[1])); }
DI float x16_add(float x) { auto rr = __builtin_amdgcn_permlane16_swap(__float_as_uint(x), __float_as_uint(x), false, false); return __uint_as_float(rr[0]) + __uint_as_float(rr[1]); }
DI float grp_max(float x) { return swap_max(x16_max(x)); }
DI float grp_sum(float x) { return swap_add(x16_add(x)); }
constexpr float LOG2E = 1.4426950408889634f;
constexpr unsigned B1_RED = 147456;
#define BROT 0
struct B1Ctx { srd_t srdX, srdW; unsigned vx, vw[2], sx, sw; int wid, rot; unsigned lds0; };
DI void b1_setup(B1Ctx& c, const u16* Bx, const u16* Wqkv, unsigned lds0, int rc, int hg, int wid, int lane) {
    c.srdX = mk_srd(Bx, 2097152); c.srdW = mk_srd(Wqkv, 512 * 1536 * 2); c.wid = wid; c.lds0 = lds0;
    c.vx = (lane >> 3) * 1024 + (((lane & 7) ^ ((4 * wid + (lane >> 4)) & 7)) * 16);
#pragma unroll
    for (int p = 0; p < 2; ++p) c.vw[p] = (lane >> 3) * 3072 + (((lane & 7) ^ (2 * (((lane >> 4) & 1) | (p << 1)))) * 16);
    c.sx = (64 * rc + 8 * wid) * 1024; c.sw = hg * 128; c.rot = BROT ? rc & 7 : 0;
}
DI void b1_issue(const B1Ctx& c, int kt) {
    const unsigned st = c.lds0 + (kt & 3) * 32768;
    kt = (kt + c.rot) & 7;
    dma1(st + c.wid * 1024, c.vx, c.srdX, c.sx + kt * 128);
#pragma unroll
    for (int i = 0; i < 3; ++i) { const int pw = 3 * c.wid + i, m = pw >> 3, pr = pw & 7;
        dma1(st + 8192 + pw * 1024, (pr & 1) ? c.vw[1] : c.vw[0], c.srdW, c.sw + (kt * 64 + 8 * pr) * 3072 + m * 1024); }
}
struct B1Acc { f32x4 aq[2], ak[2], av[2]; };
struct B1Fr { bf16x8 x[2][2]; bf16x4 w[2][3][2]; };
struct B1Ad { unsigned xa[2][2], wa[2]; };
template <int KT> DI void b1_read(B1Fr& f, const B1Ad& ad) {
    constexpr unsigned st = (KT & 1) * 32768; constexpr int sp = (KT >> 1) & 1;
#pragma unroll
    for (int ks = 0; ks < 2; ++ks) { const unsigned xa = ad.xa[sp][ks];
        f.x[ks][0] = ldsf(xa + st); f.x[ks][1] = ldsf(xa + st + 2048);
#pragma unroll
        for (int m = 0; m < 3; ++m) { f.w[ks][m][0] = ldstr(ad.wa[sp] + st + m * 8192 + ks * 4096); f.w[ks][m][1] = ldstr(ad.wa[sp] + st + m * 8192 + ks * 4096 + 512); } }
}
DI void b1_mma(B1Acc& a, const B1Fr& f) {
#pragma unroll
    for (int ks = 0; ks < 2; ++ks) {
        const bf16x8 wq = cat4(f.w[ks][0][0], f.w[ks][0][1]), wk = cat4(f.w[ks][1][0], f.w[ks][1][1]), wv = cat4(f.w[ks][2][0], f.w[ks][2][1]);
        a.aq[0] = MFMA16(wq, f.x[ks][0], a.aq[0]); a.aq[1] = MFMA16(wq, f.x[ks][1], a.aq[1]); a.ak[0] = MFMA16(wk, f.x[ks][0], a.ak[0]); a.ak[1] = MFMA16(wk, f.x[ks][1], a.ak[1]);
        a.av[0] = MFMA16(f.x[ks][0], wv, a.av[0]); a.av[1] = MFMA16(f.x[ks][1], wv, a.av[1]);
    }
}
template <class St, class IQ>
DI void b1_gemm(const B1Ctx& c, B1Acc& a, int lane, bool early, St store, IQ issueQt) {
    const int l16 = lane & 15, g = lane >> 4, q4 = l16 >> 2, p4 = lane & 3, wid = c.wid;
    const int row = 32 * (wid >> 2) + l16;
    B1Ad ad;
#pragma unroll
    for (int sp = 0; sp < 2; ++sp) {
        ad.xa[sp][0] = c.lds0 + sp * 65536 + row * 128 + (((0 + g) ^ ((row >> 1) & 7)) << 4); ad.xa[sp][1] = c.lds0 + sp * 65536 + row * 128 + (((4 + g) ^ ((row >> 1) & 7)) << 4);
        ad.wa[sp] = c.lds0 + sp * 65536 + 8192 + (8 * g + q4) * 128 + (((wid & 3) ^ (((q4 >> 1) & 1) | ((g & 1) << 1))) * 32) + p4 * 8;
        PIN(ad.xa[sp][0]); PIN(ad.xa[sp][1]); PIN(ad.wa[sp]); }
#pragma unroll
    for (int r = 0; r < 2; ++r) { a.aq[r] = f32x4{}; a.ak[r] = f32x4{}; a.av[r] = f32x4{}; }
#pragma unroll
    for (int r = 0; r < 2; ++r) { PIN(a.aq[r]); PIN(a.ak[r]); PIN(a.av[r]); }
    B1Fr f0, f1;
    b1_read<0>(f0, ad);
#define B1W(E, L) do { if (early) wait_bar<E>(); else wait_bar<L>(); } while (0)
#define B1T(KT, CUR, NXT, PRE) do { b1_read<KT + 1>(NXT, ad); SBAR(); PRE; store(KT); SBAR(); b1_mma(a, CUR); SBAR(); } while (0)
    B1W(8, 8);   B1T(0, f0, f1, b1_issue(c, 4));
    B1W(9, 8);   B1T(1, f1, f0, b1_issue(c, 5));
    B1W(10, 9);  B1T(2, f0, f1, b1_issue(c, 6));
    B1W(11, 10); B1T(3, f1, f0, b1_issue(c, 7));
    B1W(11, 11); B1T(4, f0, f1, (void)0);
    B1W(7, 7);   B1T(5, f1, f0, issueQt(0));
    B1W(9, 9);   B1T(6, f0, f1, issueQt(1));
    BARRIER();   issueQt(2); store(7); SBAR(); b1_mma(a, f1);
#undef B1W
#undef B1T
}
template <int N, class Tick> DI void phi_map(const f32x4 (&p)[N], u32x4 (&out)[N], Tick tick) {
    float x[N][4], mx[N], nm[N], ep[N][4], en[N][4], sp[N], sn[N];
#pragma unroll
    for (int i = 0; i < N; ++i) {
#pragma unroll
        for (int e = 0; e < 4; ++e) x[i][e] = rbf16(p[i][e]) * LOG2E;
        mx[i] = __builtin_fmaxf(__builtin_fmaxf(x[i][0], x[i][1]), __builtin_fmaxf(x[i][2], x[i][3])); nm[i] = -__builtin_fminf(__builtin_fminf(x[i][0], x[i][1]), __builtin_fminf(x[i][2], x[i][3]));
        if (i & 1) tick(i >> 1); }
#pragma unroll
    for (int i = 0; i < N; ++i) { mx[i] = x16_max(mx[i]); nm[i] = x16_max(nm[i]); if (i & 1) tick(2 + (i >> 1)); }
#pragma unroll
    for (int i = 0; i < N; ++i) { mx[i] = swap_max(mx[i]); nm[i] = swap_max(nm[i]); if (i & 1) tick(4 + (i >> 1)); }
#pragma unroll
    for (int i = 0; i < N; ++i) {
#pragma unroll
        for (int e = 0; e < 4; ++e) { ep[i][e] = __builtin_amdgcn_exp2f(x[i][e] - mx[i]); en[i][e] = __builtin_amdgcn_exp2f(-x[i][e] - nm[i]); }
        sp[i] = (ep[i][0] + ep[i][1]) + (ep[i][2] + ep[i][3]); sn[i] = (en[i][0] + en[i][1]) + (en[i][2] + en[i][3]); tick(6 + i); }
#pragma unroll
    for (int i = 0; i < N; ++i) { sp[i] = x16_add(sp[i]); sn[i] = x16_add(sn[i]); if (i & 1) tick(10 + (i >> 1)); }
#pragma unroll
    for (int i = 0; i < N; ++i) { sp[i] = __builtin_amdgcn_rcpf(swap_add(sp[i])); sn[i] = __builtin_amdgcn_rcpf(swap_add(sn[i])); if (i & 1) tick(12 + (i >> 1)); }
#pragma unroll
    for (int i = 0; i < N; ++i) { out[i] = (u32x4){cvtpk(ep[i][0] * sp[i], ep[i][1] * sp[i]), cvtpk(ep[i][2] * sp[i], ep[i][3] * sp[i]), cvtpk(en[i][0] * sn[i], en[i][1] * sn[i]), cvtpk(en[i][2] * sn[i], en[i][3] * sn[i])};
        if (i & 1) tick(14 + (i >> 1)); }
}
template <class Tick>
DI void b1_phi(const B1Acc& a, Tick tick, u32x2 wp, u16* PhiQ, float* Bst, unsigned lds0, int rc, int hg, int wid, int lane, Stamper& stamp) {
    const int h = 4 * hg + (wid & 3), row0 = 64 * rc + 32 * (wid >> 2);
    const int l16 = lane & 15, g = lane >> 4;
    f32x4 sp = f32x4{}, sn = f32x4{}; float zp = 0.f, zn = 0.f;
    f32x4 pin[4]; u32x4 pout[4];
#pragma unroll
    for (int r = 0; r < 2; ++r) {
        const u32x2 qb = {cvtpk(a.aq[r][0], a.aq[r][1]), cvtpk(a.aq[r][2], a.aq[r][3])}, kb = {cvtpk(a.ak[r][0], a.ak[r][1]), cvtpk(a.ak[r][2], a.ak[r][3])};
        pin[r] = mfma16_16(wp, qb, f32x4{}); pin[2 + r] = mfma16_16(wp, kb, f32x4{});
    }
    phi_map<4>(pin, pout, tick);
    const u32x4 wq[2] = {pout[0], pout[1]}, wk[2] = {pout[2], pout[3]};
    const unsigned tb = lds0 + B1_RED + wid * 1024;
#pragma unroll
    for (int r = 0; r < 2; ++r) {
        *(LAS u32x2*)(tb + l16 * 32 + g * 8) = (u32x2){wk[r][0], wk[r][1]};
        *(LAS u32x2*)(tb + 512 + l16 * 32 + g * 8) = (u32x2){wk[r][2], wk[r][3]};
        __builtin_amdgcn_raw_buffer_store_b128(wq[r], mk_srd(PhiQ, 4194304), ((h * 128 + (row0 >> 4) + r) * 64 + lane) * 16, 0, 0);
        asm volatile("s_waitcnt lgkmcnt(0)" ::: "memory");
        const unsigned ta = tb + (4 * g + (l16 >> 2)) * 32 + (lane & 3) * 8;
        u32x2 pp = __builtin_bit_cast(u32x2, ldstr(ta)), pn = __builtin_bit_cast(u32x2, ldstr(ta + 512));
        asm volatile("s_waitcnt lgkmcnt(0)" : "+v"(pp), "+v"(pn) :: "memory");
        const u32x2 vb = {cvtpk(a.av[r][0], a.av[r][1]), cvtpk(a.av[r][2], a.av[r][3])};
        sp = mfma16_16(pp, vb, sp); sn = mfma16_16(pn, vb, sn);
        zp += (__uint_as_float(pp[0] << 16) + __uint_as_float(pp[0] & 0xffff0000u)) + (__uint_as_float(pp[1] << 16) + __uint_as_float(pp[1] & 0xffff0000u));
        zn += (__uint_as_float(pn[0] << 16) + __uint_as_float(pn[0] & 0xffff0000u)) + (__uint_as_float(pn[1] << 16) + __uint_as_float(pn[1] & 0xffff0000u));
    }
    zp = grp_sum(zp); zn = grp_sum(zn);
    BARRIER();
    LAS char* red = (LAS char*)(lds0 + B1_RED + (wid & 3) * 2176);
    if (wid >= 4) { *(LAS f32x4*)(red + lane * 32) = sp; *(LAS f32x4*)(red + lane * 32 + 16) = sn; if (lane < 16) *(LAS f32x2*)(red + 2048 + lane * 8) = (f32x2){zp, zn}; }
    BARRIER();
    sp += *(const LAS f32x4*)(red + lane * 32); sn += *(const LAS f32x4*)(red + lane * 32 + 16);
    const f32x2 zo = *(const LAS f32x2*)(red + 2048 + l16 * 8);
    const srd_t srd = mk_srd(Bst + (size_t)(h * 32 + rc) * 576, wid < 4 ? 2304 : 0);
    store16_sc1(__builtin_bit_cast(u32x4, sp), lane * 32, srd, 0u); store16_sc1(__builtin_bit_cast(u32x4, sn), lane * 32 + 16, srd, 0u);
    __builtin_amdgcn_raw_buffer_store_b64((u32x2){__float_as_uint(zp + zo[0]), __float_as_uint(zn + zo[1])}, srd, lane < 16 ? 2048 + lane * 8 : 0x7fffffff, 0, 16);
}
template <int N> DI void waitN(const u64* flags, u64 tok, int lane) {
    const u64* p = flags + (lane & (N - 1)) * FL_STRIDE;
    for (unsigned spins = 0; spins < (1u << 15); ++spins) { const u64 v = ld_agent(p); if (__all(v == tok)) break; __builtin_amdgcn_s_sleep(1); }
    asm volatile("" ::: "memory");
}
struct B2a { f32x4 v[16]; };
DI void b2a_load(B2a& b, const float* Bst, int rc, int hg, int lane) {
    const int h = 4 * hg + (rc >> 3), c = lane & 31, half = lane >> 5;
    const srd_t srd = mk_srd(Bst + (size_t)h * 32 * 576, 32 * 2304);
    const unsigned vo = (c < 18 ? (18 * (rc & 7) + c) * 16 : 0x7fffffff) + half * 16 * 2304;
#pragma unroll
    for (int i = 0; i < 16; ++i) b.v[i] = __builtin_bit_cast(f32x4, __builtin_amdgcn_raw_buffer_load_b128(srd, vo, i * 2304, 16));
}
DI void b2a_finish(const B2a& b, float* Bred, int rc, int hg, int lane) {
    const int h = 4 * hg + (rc >> 3), c = lane & 31, half = lane >> 5;
    f32x4 s = b.v[0];
#pragma unroll
    for (int i = 1; i < 16; ++i) s += b.v[i];
#pragma unroll
    for (int e = 0; e < 4; ++e) s[e] = swap_add(s[e]);
    const srd_t srdR = mk_srd(Bred + (size_t)h * 576, 2304);
    store16_sc1(__builtin_bit_cast(u32x4, s), (half == 0 && c < 18) ? (18 * (rc & 7) + c) * 16 : 0x7fffffffu, srdR, 0u);
}
template <int NRB> struct B2b { bf16x8 ph[NRB]; f32x4 sp, sn, za, zb; };
template <int NRB>
DI void b2b_load(B2b<NRB>& b, const u16* PhiQ, const float* Bred, const u64* flagsR, u64 tok, int h, int rb0, int lane) {
    PIN(lane);
    const int g = lane >> 4;
    waitN<8>(flagsR, tok, lane);
#pragma unroll
    for (int r = 0; r < NRB; ++r) b.ph[r] = __builtin_bit_cast(bf16x8, __builtin_amdgcn_raw_buffer_load_b128(mk_srd(PhiQ, 4194304), lane * 16, (h * 128 + rb0 + r) * 1024, 0));
    const srd_t srd = mk_srd(Bred + (size_t)h * 576, 2304);
    b.sp = __builtin_bit_cast(f32x4, __builtin_amdgcn_raw_buffer_load_b128(srd, lane * 32, 0, 16)); b.sn = __builtin_bit_cast(f32x4, __builtin_amdgcn_raw_buffer_load_b128(srd, lane * 32 + 16, 0, 16));
    b.za = __builtin_bit_cast(f32x4, __builtin_amdgcn_raw_buffer_load_b128(srd, 2048 + g * 32, 0, 16)); b.zb = __builtin_bit_cast(f32x4, __builtin_amdgcn_raw_buffer_load_b128(srd, 2048 + g * 32 + 16, 0, 16));
}
template <int NRB>
DI void b2b_finish(const B2b<NRB>& b, u16* Of, int h, int rb0, int lane) {
    PIN(lane);
    const int l16 = lane & 15, g = lane >> 4;
    const u32x4 kvw = {cvtpk(b.sp[0], b.sp[1]), cvtpk(b.sp[2], b.sp[3]), cvtpk(b.sn[0], b.sn[1]), cvtpk(b.sn[2], b.sn[3])};
    const u32x4 zw = {cvtpk(b.za[0], b.za[2]), cvtpk(b.zb[0], b.zb[2]), cvtpk(b.za[1], b.za[3]), cvtpk(b.zb[1], b.zb[3])};
    const srd_t srdO = mk_srd(Of, 2097152);
#pragma unroll
    for (int r = 0; r < NRB; ++r) { const int rb16 = rb0 + r;
        const f32x4 num = MFMA16(__builtin_bit_cast(bf16x8, kvw), b.ph[r], f32x4{});
        const f32x4 den = MFMA16(__builtin_bit_cast(bf16x8, zw), b.ph[r], f32x4{});
        const float dn = rbf16(rbf16(den[0]) + 1e-6f);
        const u32x2 w = {cvtpk(rbf16(num[0]) / dn, rbf16(num[1]) / dn), cvtpk(rbf16(num[2]) / dn, rbf16(num[3]) / dn)};
        __builtin_amdgcn_raw_buffer_store_b64(w, srdO, (((rb16 >> 1) * 32 + h) * 2 + (g & 1)) * 512 + ((rb16 & 1) * 16 + l16) * 16 + (g >> 1) * 8, 0, 16); }
}
#define KVPRIO 0
#define KROT 0
struct ProjCtx {
    srd_t srdC, srdX, srdW, srdWq, srdO;
    unsigned va[2], vw[2], vwq, vo, lds0, l128;
    unsigned sA, sW, sWq, sXa;
    int wid, isV, rot;
};
constexpr unsigned TOUCH_LDS = 163840 - 256;
DI void issueKV(const ProjCtx& c, int kt, int i) {
    const unsigned st = c.lds0 + (kt & 1) * 65536;
    kt = (kt + c.rot) & 7;
    if (i < 4) dma1(st + (4 * c.wid + i) * 1024, c.va[i & 1], c.srdC, c.sA + i * 8192 + kt * 128);
    else dma1(st + 32768 + (c.wid >> 2) * 16384 + (4 * (c.wid & 3) + (i - 4)) * 1024, c.vw[(i - 4) >> 1], c.srdW, c.sW + ((i - 4) >> 1) * 65536 + ((i - 4) & 1) * 128 + kt * 524288);
}
DI void issueQ(const ProjCtx& c, int kt, int i) {
    const unsigned st = c.lds0 + (kt % 3) * 49152;
    kt = (kt + c.rot) & 7;
    if (i < 4) dma1(st + (4 * c.wid + i) * 1024, c.va[i & 1], c.srdX, c.sXa + i * 8192 + kt * 128);
    else dma1(st + 32768 + (2 * c.wid + (i - 4)) * 1024, c.vwq, c.srdWq, c.sWq + (i - 4) * 128 + kt * 524288);
}
#define TOUCHMODE 3
template <int WHICH> DI void touch_g(unsigned m0v, unsigned voff, const void* base) {
    if (!(TOUCHMODE & WHICH)) voff = 0;
    const u64 b = (u64)base; const u64 sb = ((u64)rfl((unsigned)(b >> 32)) << 32) | rfl((unsigned)b);
    asm volatile("s_nop 4\n\ts_mov_b32 m0, %0\n\ts_nop 0\n\tglobal_load_lds_dword %1, %2" :: "s"(rfl(m0v)), "v"(voff), "s"(sb) : "memory");
}

struct GAddr { unsigned a[2]; unsigned w[2][2]; };
DI void gaddr_add(GAddr& g, unsigned d) { g.a[0] += d; g.a[1] += d; g.w[0][0] += d; g.w[0][1] += d; g.w[1][0] += d; g.w[1][1] += d; PIN(g.a[0]); PIN(g.a[1]); PIN(g.w[0][0]); PIN(g.w[0][1]); PIN(g.w[1][0]); PIN(g.w[1][1]); }
DI void gaddr_xor(GAddr& g, unsigned d) { g.a[0] ^= d; g.a[1] ^= d; g.w[0][0] ^= d; g.w[0][1] ^= d; g.w[1][0] ^= d; g.w[1][1] ^= d; }
DI bf16x8 wfrag(const GAddr& g, int ks, int nb) { return cat4(ldstr(g.w[nb & 1][0] + ks * 8192 + (nb >> 1) * 512), ldstr(g.w[nb & 1][1] + ks * 8192 + (nb >> 1) * 512)); }
DI void gaddr_w(GAddr& g, unsigned base, int lane) {
    const int gq = lane >> 4, q4 = (lane & 15) >> 2, p4 = lane & 3;
#pragma unroll
    for (int o = 0; o < 2; ++o)
#pragma unroll
        for (int t = 0; t < 2; ++t) g.w[o][t] = base + 2048 * gq + 64 * (4 * t + q4) + 16 * ((2 * o + (p4 >> 1)) ^ ((2 * gq + t) & 3)) + 8 * (p4 & 1);
}
DI void gaddr_a(GAddr& g, unsigned base, int row0, int lane) {
    const int row = row0 + (lane & 15), gq = lane >> 4;
#pragma unroll
    for (int ks = 0; ks < 2; ++ks) g.a[ks] = base + row * 128 + (((4 * ks + gq) ^ ((row >> 1) & 7)) << 4);
}

#define KVAH 3
constexpr int AH = KVAH;
#define KV_MMA(mb, W) do { _Pragma("unroll") for (int nb = 0; nb < 4; ++nb) { if (ISV) MFMA16I(acc[mb][nb], a[mb], W[nb]); else MFMA16I(acc[mb][nb], W[nb], a[mb]); } } while (0)
template <bool ISV, class Hook>
DI void kv_step0(f32x4 (&acc)[8][4], const bf16x8 (&w)[4], bf16x8 (&wn)[4], bf16x8 (&an)[AH], const GAddr& g, Hook hook) {
    bf16x8 a[8 + AH];
#pragma unroll
    for (int i = 0; i < AH; ++i) a[i] = an[i];
#pragma unroll
    for (int mb = 0; mb < 8; ++mb) {
        a[mb + AH] = mb + AH < 8 ? ldsf(g.a[0] + (mb + AH) * 2048) : ldsf(g.a[1] + (mb + AH - 8) * 2048);
        if (mb < 4) wn[mb] = wfrag(g, 1, mb);
        SBAR();
        KV_MMA(mb, w);
        SBAR();
        hook(mb);
        SBAR();
    }
#pragma unroll
    for (int i = 0; i < AH; ++i) an[i] = a[8 + i];
}
struct NoHook { DI void operator()(int) const {} };

template <bool ISV, class Touch, class IssueB1>
DI void kv_part(const ProjCtx& c, GAddr g, u32x4 (&pk)[16], Touch touchk, IssueB1 issueB1, Stamper& stamp) {
    f32x4 acc[8][4];
#pragma unroll
    for (int a = 0; a < 8; ++a)
#pragma unroll
        for (int b = 0; b < 4; ++b) { acc[a][b] = f32x4{}; PIN(acc[a][b]); }
    bf16x8 w0[4], w1[4], an[AH];
    wait_bar<9>();
    stamp();
#pragma unroll
    for (int nb = 0; nb < 4; ++nb) w0[nb] = wfrag(g, 0, nb);
#pragma unroll
    for (int i = 0; i < AH; ++i) an[i] = ldsf(g.a[0] + i * 2048);
    auto tile = [&](int kt, auto late, auto mid, auto waitB, auto early, bool next) {
        kv_step0<ISV>(acc, w0, w1, an, g, late);
        bf16x8 a[8];
#pragma unroll
        for (int i = 0; i < AH; ++i) a[i] = an[i];
#pragma unroll
        for (int mb = 0; mb < 8 - AH; ++mb) {
            a[mb + AH] = ldsf(g.a[1] + (mb + AH) * 2048);
            SBAR();
            KV_MMA(mb, w1);
            SBAR();
            if (mb == 0) { mid(); SBAR(); }
        }
        waitB();
        if (next) { gaddr_xor(g, 65536u);
#pragma unroll
            for (int nb = 0; nb < 4; ++nb) w0[nb] = wfrag(g, 0, nb);
#pragma unroll
            for (int i = 0; i < AH; ++i) an[i] = ldsf(g.a[0] + i * 2048); }
        SBAR();
#pragma unroll
        for (int mb = 8 - AH; mb < 8; ++mb) { KV_MMA(mb, w1); SBAR(); early(mb - (8 - AH)); SBAR(); }
    };
    for (int kt = 0; kt < 6; ++kt)
        tile(kt, [&](int mb) { if (kt > 0 && (mb & 1)) issueKV(c, kt + 1, 4 + (mb >> 1)); }, [&]() { touchk(kt); }, [&]() { wait_bar<1>(); },
             [&](int i) { if (i == 0) { issueKV(c, kt + 2, 0); issueKV(c, kt + 2, 1); } else issueKV(c, kt + 2, 1 + i); }, true);
    tile(6, [&](int mb) { if (mb & 1) issueKV(c, 7, 4 + (mb >> 1)); }, [&]() {}, [&]() { wait_bar<0>(); }, [&](int i) { if (i < 2) issueB1(i); }, true);
    tile(7, NoHook{}, [&]() {}, [&]() { stamp(); wait_bar<4>(); stamp(); }, [&](int i) { if (i < 2) issueB1(2 + i); }, false);
    __builtin_amdgcn_s_setprio(0);
    MFMA_DRAIN();
#pragma unroll
    for (int n = 0; n < 16; ++n) {
        const f32x4 lo = ISV ? acc[2 * (n >> 2)][n & 3] : acc[n >> 1][2 * (n & 1)], hi = ISV ? acc[2 * (n >> 2) + 1][n & 3] : acc[n >> 1][2 * (n & 1) + 1];
        pk[n] = (u32x4){cvtpk(lo[0], lo[1]), cvtpk(lo[2], lo[3]), cvtpk(hi[0], hi[1]), cvtpk(hi[2], hi[3])}; PIN(pk[n]); }
}
DI void kv_store(const ProjCtx& c, const u32x4 (&pk)[16], int n) {
    const unsigned offV = (n >> 3) * 16384 + ((n & 3) * 2 + ((n >> 2) & 1)) * 1024;
    const unsigned offK = (n >> 3) * 16384 + (((n >> 1) & 3) * 4 + (n & 1)) * 1024;
    store16_sc1(pk[n], c.vo, c.srdO, c.isV ? offV : offK);
}

struct QFr { bf16x8 x[2]; bf16x8 w[8]; };
template <int KS> DI void q_read(QFr& f, const GAddr& g, int i) {
    if (i == 1) f.x[0] = ldsf(g.a[KS]); else if (i == 2) f.x[1] = ldsf(g.a[KS] + 2048); else { const int nb = i == 0 ? 0 : i - 2; f.w[nb] = wfrag(g, KS, nb); }
}
template <int KSN, bool NEXT, class Hook>
DI void q_kstep(f32x4 (&acc)[8][2], const QFr& cur, QFr& nxt, const GAddr& gn, Hook hook) {
#pragma unroll
    for (int nb = 0; nb < 8; ++nb) {
        MFMA16I(acc[nb][0], cur.w[nb], cur.x[0]); MFMA16I(acc[nb][1], cur.w[nb], cur.x[1]);
        SBAR();
        if (NEXT) { if (nb < 2) { q_read<KSN>(nxt, gn, 2 * nb); q_read<KSN>(nxt, gn, 2 * nb + 1); } else q_read<KSN>(nxt, gn, nb + 2); }
        hook(nb);
        SBAR();
    }
}
template <int KT, class Pub>
DI void q_tile(const ProjCtx& c, bool early, GAddr& g, f32x4 (&acc)[8][2], QFr& f0, QFr& f1, const u32x4 (&pk)[16], Pub after, Stamper& stamp) {
    q_kstep<1, true>(acc, f0, f1, g, [&](int nb) {
        if (KT >= 1 && KT + 2 < 8 && nb < 3) issueQ(c, KT + 2, 3 + nb);
        if (nb == 4) { if (early) { if (KT < 2) kv_store(c, pk, 12 + 2 * (KT < 2 ? KT : 0)); } else if (KT < 5) kv_store(c, pk, 11 + (KT < 5 ? KT : 0)); }
        if (nb == 7 && early && KT < 2) kv_store(c, pk, 13 + 2 * (KT < 2 ? KT : 0)); });
    if constexpr (KT < 7) {
        constexpr int WE = KT == 0 ? 17 : (KT == 1 ? 19 : (KT == 2 ? 8 : (KT <= 5 ? 6 : 0)));
        constexpr int WL = KT == 0 ? 17 : (KT == 1 ? 18 : (KT <= 4 ? 8 : (KT == 5 ? 7 : 0)));
        const bool w7 = KT == 5 && c.wid == 7;
        if (early) { if (w7) wait_bar<WE + 16>(); else wait_bar<WE>(); } else { if (w7) wait_bar<WL + 16>(); else wait_bar<WL>(); }
        stamp();
        after(KT);
        gaddr_add(g, (unsigned)(((KT + 1) % 3 - KT % 3) * 49152));
        q_kstep<0, true>(acc, f1, f0, g, [&](int nb) { if (KT + 3 < 8 && nb < 3) issueQ(c, KT + 3, nb); });
    } else q_kstep<0, false>(acc, f1, f0, g, NoHook{});
}

DI void proj_setup(ProjCtx& c, const u16* X, const u16* Ckv, const u16* Wq, const u16* Wuk, const u16* Wuv, u16* Kf, u16* Vf, unsigned lds0, int h, int j, int wid, int lane) {
    const int wr = wid >> 2, wc = wid & 3;
    c.wid = wid; c.isV = wc >> 1; c.lds0 = lds0; c.rot = KROT ? (2 * (h & 3) + j) & 7 : 0;
    c.srdC = mk_srd(Ckv, 2097152); c.srdX = mk_srd(X, 2097152);
    c.srdW = mk_srd((wid < 4 ? Wuk : Wuv), 4194304); c.srdWq = mk_srd(Wq, 4194304);
    c.srdO = mk_srd((const char*)(c.isV ? Vf : Kf) + (size_t)h * 524288, 524288);
#pragma unroll
    for (int p = 0; p < 2; ++p) {
        c.va[p] = (lane >> 3) * 1024 + (((lane & 7) ^ (4 * p + (lane >> 4))) * 16);
        c.vw[p] = ((lane >> 2) & 7) * 8192 + (4 * (lane >> 5) + ((lane & 3) ^ ((2 * p + ((lane >> 4) & 1)) & 3))) * 16;
    }
    c.vwq = (wid & 1) ? c.vw[1] : c.vw[0];
    c.l128 = lane * 128;
    c.sA = (256 * j + 32 * wid) * 1024;
    c.sW = (16 * (wid & 3)) * 8192 + h * 256;
    c.sXa = (wid < 4 ? 128 * (15 - j) + 32 * wid : 128 * j + 32 * (wid - 4)) * 1024;
    c.sWq = (8 * wid) * 8192 + h * 256;
    c.vo = lane * 16 + (4 * j + 2 * wr) * 16384 + (wc & 1) * (c.isV ? 8192 : 2048);
}
template <class Touch, class IssueB1>
DI void kv_run(const ProjCtx& c, int lane, u32x4 (&pk)[16], Touch touchk, IssueB1 issueB1, Stamper& stamp) {
    const int wid = c.wid, wr = wid >> 2, wc = wid & 3;
    GAddr g; gaddr_a(g, c.lds0, 128 * wr, lane); gaddr_w(g, c.lds0 + 32768 + (wc >> 1) * 16384 + (wc & 1) * 1024, lane);
    if (c.isV) kv_part<true>(c, g, pk, touchk, issueB1, stamp); else kv_part<false>(c, g, pk, touchk, issueB1, stamp);
}
template <class Pub>
DI void q_run(const ProjCtx& c, bool early, int lane, const u32x4 (&pk)[16], bf16x8 (&q)[8], Pub after, Stamper& stamp) {
    GAddr g; gaddr_a(g, c.lds0, 32 * c.wid, lane); gaddr_w(g, c.lds0 + 32768, lane); gaddr_add(g, 0u);
    f32x4 acc[8][2];
#pragma unroll
    for (int a = 0; a < 8; ++a) { acc[a][0] = f32x4{}; acc[a][1] = f32x4{}; PIN(acc[a][0]); PIN(acc[a][1]); }
    QFr f0, f1;
#pragma unroll
    for (int i = 0; i < 10; ++i) q_read<0>(f0, g, i);
    q_tile<0>(c, early, g, acc, f0, f1, pk, after, stamp); q_tile<1>(c, early, g, acc, f0, f1, pk, after, stamp); q_tile<2>(c, early, g, acc, f0, f1, pk, after, stamp); q_tile<3>(c, early, g, acc, f0, f1, pk, after, stamp);
    q_tile<4>(c, early, g, acc, f0, f1, pk, after, stamp); q_tile<5>(c, early, g, acc, f0, f1, pk, after, stamp); q_tile<6>(c, early, g, acc, f0, f1, pk, after, stamp); q_tile<7>(c, early, g, acc, f0, f1, pk, after, stamp);
    MFMA_DRAIN();
    constexpr float QSC = 0.08838834764831845f * 1.4426950408889634f;
#pragma unroll
    for (int qb = 0; qb < 2; ++qb)
#pragma unroll
        for (int ds = 0; ds < 4; ++ds) { const f32x4 lo = acc[2 * ds][qb] * QSC, hi = acc[2 * ds + 1][qb] * QSC;
            q[qb * 4 + ds] = __builtin_bit_cast(bf16x8, (u32x4){cvtpk(lo[0], lo[1]), cvtpk(lo[2], lo[3]), cvtpk(hi[0], hi[1]), cvtpk(hi[2], hi[3])}); }
}
template <int OFF, bool SETM0> DI void dma_piece(unsigned m0v, unsigned voff, srd_t srd, unsigned soff) {
    if constexpr (SETM0) asm volatile("s_mov_b32 m0, %0\n\ts_nop 0\n\tbuffer_load_dwordx4 %1, %2, %3 offen offset:%4 sc1 lds" :: "s"(m0v), "v"(voff), "s"(srd), "s"(soff), "i"(OFF) : "memory");
    else asm volatile("buffer_load_dwordx4 %0, %1, %2 offen offset:%3 sc1 lds" :: "v"(voff), "s"(srd), "s"(soff), "i"(OFF) : "memory");
}
#define ATT_THRV 64.f
constexpr float ATT_THR = ATT_THRV;
#define KSPLIT 0
#define ATT_WIN 5
constexpr int ATT_W = ATT_WIN;

struct AttnState { f32x4 o[8][2]; f32x4 s[4][2]; f32x4 negm[2]; bf16x8 q[8]; u32x4 p[2][2]; f32x4 l[2]; f32x2 mhat; u32x4 ones; };
struct AttnCtx {
    srd_t srdK, srdV, srdC; unsigned voff, lane16, kslot0, vslot0, coffL;
    int g, nl  , thrg, dq0, relH, jump; unsigned qstash;
    unsigned soA, soB, ksoM; int rel;
    srd_t srdF; u64 tok, prev; unsigned known;
};
DI u64 ld_flag8(const AttnCtx& c) { return __builtin_bit_cast(u64, __builtin_amdgcn_raw_buffer_load_b64(c.srdF, (c.lane16 & 0x70u) << 3, 0, 16)); }

DI unsigned m_off(int i) { return i < 16 ? (unsigned)(((i & 7) * 2 + (i >> 3)) * 1024) : (unsigned)((((i - 16) & 3) * 4 + ((i - 16) >> 2)) * 1024); }
template <bool DO_PV, bool DO_QK, int KP, int VP, class Hook>
DI void m_phase(AttnState& st, unsigned kbase, unsigned vbase, bf16x8 (&pre)[ATT_W], Hook hook) {
    constexpr int N0 = DO_PV ? 0 : 16, N1 = DO_QK ? 32 : 16;
    bf16x8 fr[32];
#pragma unroll
    for (int i = 0; i < ATT_W; ++i) fr[N0 + i] = pre[i];
#pragma unroll
    for (int i = N0; i < N1; ++i) {
        const int n = i + ATT_W;
        if (n < N1) fr[n] = ldsf((n < 16 ? vbase + VP * 16384 : kbase + KP * 16384) + m_off(n));
        SBAR();
        if (i >= 16) { const int ds = (i - 16) >> 2, kb = (i - 16) & 3;
            if (ds == 0) { asm volatile("v_mfma_f32_16x16x32_bf16 %0, %2, %3, %1" : "=&v"(st.s[kb][0]), "+v"(st.negm[0]) : "v"(fr[i]), "v"(st.q[0]));
                           asm volatile("v_mfma_f32_16x16x32_bf16 %0, %2, %3, %1" : "=&v"(st.s[kb][1]), "+v"(st.negm[1]) : "v"(fr[i]), "v"(st.q[4])); }
            else { MFMA16I(st.s[kb][0], fr[i], st.q[ds]); MFMA16I(st.s[kb][1], fr[i], st.q[4 + ds]); }
        } else { const int ks = i >> 3, db = i & 7;
            MFMA16I(st.o[db][0], fr[i], st.p[ks][0]); MFMA16I(st.o[db][1], fr[i], st.p[ks][1]);
            if (db == 3) { MFMA16I(st.l[0], st.ones, st.p[ks][0]); MFMA16I(st.l[1], st.ones, st.p[ks][1]); } }
        SBAR();
        hook(i);
        SBAR();
    }
    MFMA_DRAIN();
    PIN(st.negm[0]); PIN(st.negm[1]);
}
struct NoHookI { DI void operator()(int) const {} };

DI void store_ctx(const f32x4 (&o)[8][2], float inv0, float inv1, srd_t srd, unsigned voff) {
    PIN(voff);
#pragma unroll
    for (int p = 0; p < 4; ++p)
#pragma unroll
        for (int qb = 0; qb < 2; ++qb) { const float inv = qb ? inv1 : inv0; const f32x4 lo = o[2 * p][qb] * inv, hi = o[2 * p + 1][qb] * inv;
            __builtin_amdgcn_raw_buffer_store_b128((u32x4){cvtpk(lo[0], lo[1]), cvtpk(lo[2], lo[3]), cvtpk(hi[0], hi[1]), cvtpk(hi[2], hi[3])}, srd, voff + p * 2048 + qb * 256, 0, 16); }
}
DI float row_add(float x) { return swap_add(x16_add(x)); }
DI float row_max4(float x) { return swap_max(x16_max(x)); }

template <bool LAST, int PAR>
DI void v_phase(AttnState& st, AttnCtx& c, const int t) {
    const bool sw = (t == c.nl);
    const unsigned vm0 = c.vslot0 + (PAR ^ 1) * 16384;
    const unsigned vso = c.soA, kso = c.soB;
    if (!LAST && __builtin_expect(c.known != 0xffu, 0)) {
        c.known |= (unsigned)__builtin_amdgcn_ballot_w64(c.prev == c.tok) & 0xffu;
        const unsigned need = 1u << ((kso >> 16) & 7);
        if (kso < 524288u && !(c.known & need)) {
            for (unsigned spins = 0; spins < (1u << 15); ++spins) { const u64 v = ld_flag8(c); c.known |= (unsigned)__builtin_amdgcn_ballot_w64(v == c.tok) & 0xffu; if (c.known & need) break; __builtin_amdgcn_s_sleep(1); } }
        asm volatile("" ::: "memory");
        c.prev = ld_flag8(c);
    }
    SBAR();
    if (__builtin_expect(sw, 0)) { store_ctx(st.o, __builtin_amdgcn_rcpf(st.l[0][0]), __builtin_amdgcn_rcpf(st.l[1][0]), c.srdC, ((c.lane16 >> 8) << 9) + (c.lane16 & 0xf0u) + c.coffL);
#pragma unroll
        for (int d = 0; d < 8; ++d) { st.o[d][0] = f32x4{}; st.o[d][1] = f32x4{}; PIN(st.o[d][0]); PIN(st.o[d][1]); }
        st.l[0] = f32x4{}; st.l[1] = f32x4{}; PIN(st.l[0]); PIN(st.l[1]); c.rel = c.relH; }
    if (__builtin_expect(c.rel > c.thrg, 0)) {
        int thr = 32 * c.g - c.rel + c.dq0; PIN(thr); const float NEG = -__builtin_inff();
#pragma unroll
        for (int kb = 0; kb < 4; ++kb)
#pragma unroll
            for (int qb = 0; qb < 2; ++qb)
#pragma unroll
                for (int e = 0; e < 4; ++e) if (16 * kb - 16 * qb + e > thr) st.s[kb][qb][e] = NEG;
    }
    SBAR();
    if (!LAST) dma_piece<0, true>(vm0, c.voff, c.srdV, vso);
#define MX3(a, b, c) __builtin_fmaxf(__builtin_fmaxf((a), (b)), (c))
    float mx[2];
#pragma unroll
    for (int qb = 0; qb < 2; ++qb) {
        float a = MX3(st.s[0][qb][0], st.s[0][qb][1], st.s[0][qb][2]), b = MX3(st.s[0][qb][3], st.s[1][qb][0], st.s[1][qb][1]);
        a = MX3(a, st.s[1][qb][2], st.s[1][qb][3]); b = MX3(b, st.s[2][qb][0], st.s[2][qb][1]); a = MX3(a, st.s[2][qb][2], st.s[2][qb][3]);
        b = MX3(b, st.s[3][qb][0], st.s[3][qb][1]); a = MX3(a, st.s[3][qb][2], st.s[3][qb][3]); mx[qb] = __builtin_fmaxf(a, b); }
    const unsigned long long firstm = (t == 0 || sw) ? ~0ull : 0ull;
    if (__builtin_expect((__builtin_amdgcn_ballot_w64(__builtin_fmaxf(mx[0], mx[1]) > ATT_THR) | firstm) != 0ull, 0)) {
        const bool first = firstm != 0ull;
#pragma unroll
        for (int qb = 0; qb < 2; ++qb) {
            const float delta = __builtin_fmaxf(row_max4(mx[qb]), first ? -1e30f : 0.f);
            st.mhat[qb] = (first ? 0.f : st.mhat[qb]) + delta;
#pragma unroll
            for (int kb = 0; kb < 4; ++kb) st.s[kb][qb] -= delta;
            st.negm[qb] = (f32x4){-st.mhat[qb], -st.mhat[qb], -st.mhat[qb], -st.mhat[qb]};
            if (!first) { const float f = __builtin_amdgcn_exp2f(-delta); st.l[qb] *= f;
#pragma unroll
                for (int d = 0; d < 8; ++d) st.o[d][qb] *= f; } }
    }
    SBAR();
    float e[8][4];
#define EXPG(k) do { _Pragma("unroll") for (int i_ = 0; i_ < 4; ++i_) e[k][i_] = __builtin_amdgcn_exp2f(st.s[(k) >> 1][(k) & 1][i_]); } while (0)
#define FING(k) do { constexpr int kb_ = (k) >> 1, qb_ = (k) & 1; \
        unsigned w0_ = cvtpk(e[k][0], e[k][1]), w1_ = cvtpk(e[k][2], e[k][3]); PIN(w0_); PIN(w1_); \
        st.p[kb_ >> 1][qb_][2 * (kb_ & 1)] = w0_; st.p[kb_ >> 1][qb_][2 * (kb_ & 1) + 1] = w1_; } while (0)
    EXPG(0); SBAR();
    EXPG(1); FING(0); SBAR();
    if (!LAST) dma_piece<1024, false>(vm0, c.voff, c.srdV, vso);
    EXPG(2); FING(1); SBAR();
    EXPG(3); FING(2); SBAR();
    if (!LAST) dma_piece<2048, false>(vm0, c.voff, c.srdV, vso);
    EXPG(4); FING(3); SBAR();
    EXPG(5); FING(4); SBAR();
    if (!LAST) dma_piece<3072, false>(vm0, c.voff, c.srdV, vso);
    EXPG(6); FING(5); SBAR();
    EXPG(7); FING(6); SBAR();
    FING(7);
#undef EXPG
#undef FING
    if (!LAST) {
        if (__builtin_expect(t == c.nl - 1, 0)) {
            unsigned qs = c.qstash; PIN(qs);
#pragma unroll
            for (int s = 0; s < 8; ++s) st.q[s] = ldsf(qs + s * 1024);
            st.negm[0] = f32x4{}; st.negm[1] = f32x4{}; PIN(st.negm[0]); PIN(st.negm[1]);
        }
        c.ksoM = kso;
        c.soA = c.soB; c.soB += (t + 3 == c.nl) ? c.jump : 16384; c.rel += 64;
    }
    SBAR();
}

DI void attn_setup(AttnCtx& c, const u16* Kf, const u16* Vf, u16* Cf, unsigned lds0, int h, int j, int wid, int lane) {
    PIN(lane);
    const int grp = wid >> 2; c.g = wid & 3; c.dq0 = (lane & 15) - 4 * (lane >> 4);
    const int qbH = 15 - j, qbL = j, nl = 2 * j + 2;
    c.nl = grp ? nl : 1000; c.thrg = 32 * c.g - 63; c.jump = (18 - nl) * 16384; c.relH = 64 * (17 - 2 * qbH);
    c.rel = grp ? -128 * qbL : -128 * qbH;
    c.soA = 16384; c.soB = (c.nl == 2) ? 17 * 16384 : 2 * 16384;
    c.srdK = mk_srd((const char*)Kf + (size_t)h * 524288, 524288);
    c.srdV = mk_srd((const char*)Vf + (size_t)h * 524288, 524288);
    c.srdC = mk_srd((const char*)Cf, 16777216);
    c.lane16 = lane * 16; c.voff = lane * 16 + c.g * 4096;
    c.kslot0 = lds0 + grp * 65536 + c.g * 4096; c.vslot0 = c.kslot0 + 32768;
    c.coffL = ((qbL * 4 + c.g) * 32 + h) * 8192;
    c.qstash = lds0 + 131072 + c.g * 8192 + lane * 16;
}
#define DMA4(m_, vo_, srd, so_) do { dma_piece<0, true>(m_, vo_, srd, so_); dma_piece<1024, false>(m_, vo_, srd, so_); dma_piece<2048, false>(m_, vo_, srd, so_); dma_piece<3072, false>(m_, vo_, srd, so_); } while (0)
DI void attn_prime(const AttnCtx& c, int grp) { DMA4(grp ? c.vslot0 + 16384 : c.kslot0, c.voff, c.srdK, 0u); DMA4(c.vslot0, c.voff, c.srdV, 0u); if (grp == 0) DMA4(c.kslot0 + 16384, c.voff, c.srdK, 16384u); }

template <class First, class G0Pre, class G0Tail>
DI void attn_run(AttnState& st, AttnCtx& c, unsigned lds0, int h, int j, int wid, int lane, First afterFirst, G0Pre g0pre, G0Tail g0tail, Stamper& stamp) {
    PIN(lane);
    const int grp = wid >> 2;
    const unsigned kbase = lds0 + grp * 65536 + lane * 16;
    const unsigned vbase = lds0 + grp * 65536 + 32768 + lane * 16;
#pragma unroll
    for (int d = 0; d < 8; ++d) { st.o[d][0] = f32x4{}; st.o[d][1] = f32x4{}; PIN(st.o[d][0]); PIN(st.o[d][1]); }
    st.negm[0] = f32x4{}; st.negm[1] = f32x4{}; st.l[0] = f32x4{}; st.l[1] = f32x4{}; st.mhat = (f32x2){0.f, 0.f};
    st.ones = (u32x4){0x3f803f80u, 0x3f803f80u, 0x3f803f80u, 0x3f803f80u}; PIN(st.ones); PIN(st.l[0]); PIN(st.l[1]);
    PIN(st.negm[0]); PIN(st.negm[1]);
    if (grp == 1) { DMA4(c.kslot0 + 16384, c.voff, c.srdK, 16384u); BARRIER(); }
    wait_bar<8>();
    stamp();
    bf16x8 pre[ATT_W];
#define PRE_V(P) do { _Pragma("unroll") for (int i = 0; i < ATT_W; ++i) pre[i] = ldsf(vbase + (P) * 16384 + m_off(i)); } while (0)
    const unsigned kbase0 = grp ? kbase + 49152 : kbase;
#pragma unroll
    for (int i = 0; i < ATT_W; ++i) pre[i] = ldsf(kbase0 + m_off(16 + i));
    __builtin_amdgcn_s_setprio(1);
    m_phase<false, true, 0, 0>(st, kbase0, vbase, pre, NoHookI{});
    __builtin_amdgcn_s_setprio(0);
    wait_bar<0>();
    afterFirst();
#define PSTAMP(t) do {} while (0)
#define STEP(PAR, t) do { \
        v_phase<false, PAR>(st, c, t); \
        PRE_V(PAR); \
        PSTAMP(t); \
        wait_bar_keep<(KSPLIT ? 6 : 4)>(); \
        PSTAMP(t); \
        __builtin_amdgcn_s_setprio(1); \
        m_phase<true, true, PAR ^ 1, PAR>(st, kbase, vbase, pre, [&](int i) { \
            if (!KSPLIT) { if (i == 3) dma_piece<0, true>(c.kslot0 + PAR * 16384, c.voff, c.srdK, c.ksoM); if (i == 9) dma_piece<1024, false>(c.kslot0 + PAR * 16384, c.voff, c.srdK, c.ksoM); } \
            if (i == (KSPLIT ? 5 : 15)) dma_piece<2048, KSPLIT>(c.kslot0 + PAR * 16384, c.voff, c.srdK, c.ksoM); if (i == (KSPLIT ? 17 : 21)) dma_piece<3072, false>(c.kslot0 + PAR * 16384, c.voff, c.srdK, c.ksoM); }); \
        __builtin_amdgcn_s_setprio(0); \
        PSTAMP(t); \
        wait_bar<4>(); \
        PSTAMP(t); } while (0)
    for (int t = 0; t < 16; t += 2) { stamp(); STEP(0, t); STEP(1, t + 1); }
    stamp();
    v_phase<true, 0>(st, c, 16);
    if (grp == 0) g0pre();
    PRE_V(0);
    asm volatile("s_barrier" ::: "memory");
    __builtin_amdgcn_s_setprio(1);
    m_phase<true, false, 0, 0>(st, kbase, vbase, pre, NoHookI{});
    __builtin_amdgcn_s_setprio(0);
    wait_bar<0>();
    stamp();
    PIN(lane);
    const unsigned xo = lds0 + grp * 65536 + c.g * 8192 + lane * 16;
    const unsigned xi = lds0 + (grp ^ 1) * 65536 + c.g * 8192 + lane * 16;
    const unsigned so = lds0 + grp * 65536 + 32768 + c.g * 512 + lane * 8, si = lds0 + (grp ^ 1) * 65536 + 32768 + c.g * 512 + lane * 8;
    const unsigned voff = (lane >> 4) * 512 + (lane & 15) * 16 + (((15 - j) * 4 + c.g) * 32 + h) * 8192;
#define MERGE_PUT(qb) do { _Pragma("unroll") for (int d = 0; d < 8; ++d) *(LAS f32x4*)(xo + d * 1024) = st.o[d][qb]; *(LAS f32x2*)so = (f32x2){st.mhat[qb], st.l[qb][0]}; } while (0)
#define MERGE_FIN(qb) do { const f32x2 ot = *(const LAS f32x2*)si; const float mh = st.mhat[qb], lt = st.l[qb][0]; \
        const float m = __builtin_fmaxf(ot[0], mh), f0 = __builtin_amdgcn_exp2f(ot[0] - m), f1 = __builtin_amdgcn_exp2f(mh - m); \
        const float inv = __builtin_amdgcn_rcpf(ot[1] * f0 + lt * f1), a0 = f0 * inv, a1 = f1 * inv; \
        _Pragma("unroll") for (int p = 0; p < 4; ++p) { \
            const f32x4 lo = st.o[2 * p][qb] * a1 + *(const LAS f32x4*)(xi + (2 * p) * 1024) * a0, hi = st.o[2 * p + 1][qb] * a1 + *(const LAS f32x4*)(xi + (2 * p + 1) * 1024) * a0; \
            __builtin_amdgcn_raw_buffer_store_b128((u32x4){cvtpk(lo[0], lo[1]), cvtpk(lo[2], lo[3]), cvtpk(hi[0], hi[1]), cvtpk(hi[2], hi[3])}, c.srdC, voff + p * 2048 + (qb) * 256, 0, 16); } } while (0)
    if (grp == 0) { MERGE_PUT(1); BARRIER(); g0tail(); BARRIER(); MERGE_FIN(0); asm volatile("; merge 0"); }
    else          { asm volatile("; merge 1"); MERGE_PUT(0); BARRIER(); MERGE_FIN(1); }
#undef MERGE_PUT
#undef MERGE_FIN
    stamp();
#undef PRE_V
#undef STEP
}
constexpr int OUT_LDS = 163840;
constexpr unsigned OUT_O = 131072;
struct OutCtx { srd_t srdC, srdW, srdB; unsigned lane16, vw0, vwA, sC, sW, sB, lds0; int wid, rot; };
DI void out_issueA(const OutCtx& c, int kt, int i) {
    const unsigned st = c.lds0 + (kt & 3) * 32768;
    const int kr = (kt + c.rot) & 15;
    if (i < 2) { const int p = 2 * c.wid + i;
        dma1s(st + p * 1024, c.lane16, c.srdC, c.sC + (p >> 2) * 262144 + (p & 3) * 1024 + kr * 4096); }
    else dma1(st + 16384 + (2 * c.wid + (i - 2)) * 1024, c.vwA, c.srdW, c.sW + (8 * c.wid) * 1024 + (i - 2) * 128 + kr * 65536);
}
DI void out_issueB(const OutCtx& c, int ch, int i) {
    const int rg = 2 * c.wid + (i >> 1);
    dma1(c.lds0 + ch * 32768 + rg * 2048 + (i & 1) * 1024, (i >> 1) ? (c.vw0 ^ 32u) : c.vw0, c.srdB, c.sB + (ch * 128 + 8 * rg) * 1024 + (i & 1) * 128);
}
DI u32x4 out_pack(f32x4 s0, f32x4 s1) {
    unsigned a0 = cvtpk(s0[0], s0[1]), a1 = cvtpk(s0[2], s0[3]), b0 = cvtpk(s1[0], s1[1]), b1 = cvtpk(s1[2], s1[3]);
    auto x0 = __builtin_amdgcn_permlane32_swap(a0, b0, false, false); auto x1 = __builtin_amdgcn_permlane32_swap(a1, b1, false, false);
    return (u32x4){x0[0], x1[0], x0[1], x1[1]};
}
DI f32x4 quad(const f32x16& x, int rq) { return (f32x4){x[4 * rq], x[4 * rq + 1], x[4 * rq + 2], x[4 * rq + 3]}; }

DI void out_phase(const u16* Wo, const u16* Bwo, u16* out, char* ws, u64 tok, unsigned lds0, int bid, int wid, int lane, Stamper& stamp) {
    PIN(lane);
    const int tid = wid * 64 + lane, r32 = lane & 31, hi = lane >> 5, wr = wid >> 2, wc = wid & 3;
    const int x = bid & 7, u = bid >> 3, s = x >> 1, rb = 8 * (x & 1) + (u >> 2), cb = u & 3, tile = rb * 4 + cb;
    OutCtx c; c.wid = wid; c.lds0 = lds0; c.lane16 = lane * 16;
    c.srdC = mk_srd(ws + WS_CF + (size_t)rb * 4 * 262144, 4 * 262144); c.srdW = mk_srd(Wo, 4194304); c.srdB = mk_srd(Bwo, 524288);
    c.vw0 = ((lane >> 2) & 7) * 1024 + (4 * (lane >> 5) + ((lane & 3) ^ ((lane >> 4) & 1))) * 16; c.vwA = (wid & 1) ? (c.vw0 ^ 32u) : c.vw0;
    c.rot = 4 * cb + 2 * (rb & 7);
    c.sC = s * 8 * 8192; c.sW = s * 1024 * 1024 + cb * 256; c.sB = cb * 256;
#pragma unroll
    for (int kt = 0; kt < 4; ++kt) { out_issueA(c, kt, 2); out_issueA(c, kt, 3); }
    u64* flags = (u64*)(ws + WS_FLAG);
    {
        const int l = lane & 15, hh = 8 * s + (l & 7), jj = rb < 8 ? rb : 15 - rb;
        const int src = l < 8 ? ((hh >> 2) | (jj << 3) | ((hh & 3) << 6)) : (8 * (2 * rb + (s >> 1)) + (l & 7));
        const u64* fp = flags + (FL_C + src) * FL_STRIDE;
        for (unsigned spins = 0; spins < (1u << 15); ++spins) { const u64 v = ld_agent(fp); if (__all(v == tok)) break; __builtin_amdgcn_s_sleep(1); }
        asm volatile("" ::: "memory"); }
    stamp();
#pragma unroll
    for (int kt = 0; kt < 4; ++kt) { out_issueA(c, kt, 0); out_issueA(c, kt, 1); }
    { const srd_t srdO = mk_srd(ws + WS_OF + (size_t)(rb * 4 + s) * 32768, 32768);
#pragma unroll
      for (int i = 0; i < 4; ++i) dma1s(lds0 + OUT_O + (4 * wid + i) * 1024, c.lane16, srdO, (unsigned)((4 * wid + i) * 1024)); }
    const int q4 = (lane & 15) >> 2, p4 = lane & 3, blk = (lane >> 4) & 1;
    const unsigned aaddr = lds0 + (2 * wr) * 4096 + c.lane16;
    unsigned waA[2], waddr[2];
#pragma unroll
    for (int t = 0; t < 2; ++t) { waA[t] = lds0 + 2048 * t + 512 * wc + 64 * (4 * hi + q4) + 16 * ((2 * blk + (p4 >> 1)) ^ ((2 * t + hi) & 3)) + 8 * (p4 & 1);
                                  waddr[t] = waA[t]; }
    f32x16 acc[2] = {f32x16{}, f32x16{}};
    stamp();
    struct Fr { bf16x8 a[2][2]; bf16x4 w[2][2]; };
    auto rd = [&](Fr& f, unsigned st, int half) {
        unsigned w0 = waA[0] + st + 16384 + half * 8192, w1 = waA[1] + st + 16384 + half * 8192, aa = aaddr + st; PIN(w0); PIN(w1); PIN(aa);
#pragma unroll
        for (int k = 0; k < 2; ++k) { const int ks = 2 * half + k; const unsigned wb = k ? w1 : w0;
            f.w[k][0] = ldstr(wb); f.w[k][1] = ldstr(wb + 4096);
            f.a[k][0] = ldsf(aa + ks * 1024); f.a[k][1] = ldsf(aa + 4096 + ks * 1024); } };
    Fr f0, f1;
    wait_bar<10>();
    rd(f0, 0u, 0);
    auto step = [&](int kt, auto late, auto early) {
        const unsigned st = (kt & 3) * 32768, stn = ((kt + 1) & 3) * 32768;
        rd(f1, st, 1);
        SBAR();
#pragma unroll
        for (int k = 0; k < 2; ++k) { const bf16x8 wf = cat4(f0.w[k][0], f0.w[k][1]);
            acc[0] = MFMA32(wf, f0.a[k][0], acc[0]); acc[1] = MFMA32(wf, f0.a[k][1], acc[1]); SBAR(); late(2 + k); SBAR(); }
        wait_bar<8>();
        rd(f0, stn, 0);
        SBAR();
#pragma unroll
        for (int k = 0; k < 2; ++k) { const bf16x8 wf = cat4(f1.w[k][0], f1.w[k][1]);
            acc[0] = MFMA32(wf, f1.a[k][0], acc[0]); acc[1] = MFMA32(wf, f1.a[k][1], acc[1]); SBAR(); early(k); SBAR(); }
    };
    step(0, [&](int) {}, [&](int i) { out_issueA(c, 4, i); });
    for (int kt = 1; kt < 12; ++kt) step(kt, [&](int i) { out_issueA(c, kt + 3, i); }, [&](int i) { out_issueA(c, kt + 4, i); });
    step(12, [&](int i) { out_issueA(c, 15, i); }, [&](int i) { out_issueB(c, 0, i); });
    step(13, [&](int i) { out_issueB(c, 0, i); }, [&](int i) { out_issueB(c, 1, i); });
    step(14, [&](int i) { out_issueB(c, 1, i); }, [&](int i) { out_issueB(c, 2, i); });
    step(15, [&](int i) { out_issueB(c, 2, i); }, [&](int i) { out_issueB(c, 3, i); });
    out_issueB(c, 3, 2); out_issueB(c, 3, 3);
    stamp();
    const srd_t srdX = mk_srd(ws + WS_XCH + (size_t)tile * 262144, 262144);
#pragma unroll
    for (int mb = 0; mb < 2; ++mb) { const int q = 2 * wr + mb;
        if (q != s) {
#pragma unroll
            for (int rq = 0; rq < 4; ++rq) store16_sc1(__builtin_bit_cast(u32x4, quad(acc[mb], rq)), c.lane16, srdX, (unsigned)(((q * 4 + s) * 4 + wc) * 4096 + rq * 1024)); } }
    wait_bar<0>();
    if (tid == 0) st_agent(flags + (FL_XF + tile * 4 + s) * FL_STRIDE, tok);
    stamp();
    const int row = rb * 128 + 32 * s + r32;
    if (wr == (s >> 1)) {
        const u64* fp = flags + (FL_XF + tile * 4 + (lane & 3)) * FL_STRIDE;
        for (unsigned spins = 0; spins < (1u << 15); ++spins) { const u64 v = ld_agent(fp); if (__all(v == tok)) break; __builtin_amdgcn_s_sleep(1); }
        asm volatile("" ::: "memory");
        f32x4 p[4][4];
#pragma unroll
        for (int q = 0; q < 4; ++q)
#pragma unroll
            for (int rq = 0; rq < 4; ++rq) p[q][rq] = __builtin_bit_cast(f32x4, __builtin_amdgcn_raw_buffer_load_b128(srdX, (q == s ? 0x7ffffff0u : c.lane16) + rq * 1024, ((s * 4 + q) * 4 + wc) * 4096, 16));
        f32x4 r[4];
#pragma unroll
        for (int rq = 0; rq < 4; ++rq) r[rq] = ((p[0][rq] + p[1][rq]) + (p[2][rq] + p[3][rq])) + ((s & 1) ? quad(acc[1], rq) : quad(acc[0], rq));
        u16* o = out + (size_t)row * 1024 + cb * 128 + 32 * wc + 8 * hi;
        *(u32x4*)o = out_pack(r[0], r[1]); *(u32x4*)(o + 16) = out_pack(r[2], r[3]);
    } else {
        f32x16 accB[2] = {f32x16{}, f32x16{}};
#pragma unroll
        for (int ch = 0; ch < 4; ++ch)
#pragma unroll
            for (int hf = 0; hf < 2; ++hf) {
                unsigned w0 = waddr[0] + ch * 32768 + hf * 16384, w1 = waddr[1] + ch * 32768 + hf * 16384, oa = lds0 + OUT_O + (8 * ch + 4 * hf) * 1024 + c.lane16; PIN(w0); PIN(w1); PIN(oa);
                bf16x8 a[4]; bf16x4 w[4][2];
#pragma unroll
                for (int ks = 0; ks < 4; ++ks) { w[ks][0] = ldstr(w0 + ks * 4096); w[ks][1] = ldstr(w1 + ks * 4096); a[ks] = ldsf(oa + ks * 1024); }
#pragma unroll
                for (int ks = 0; ks < 4; ++ks) accB[ks & 1] = MFMA32(cat4(w[ks][0], w[ks][1]), a[ks], accB[ks & 1]);
            }
        const f32x16 rB = accB[0] + accB[1];
        u16* o = out + (size_t)row * 1024 + 512 + cb * 128 + 32 * wc + 8 * hi;
        *(u32x4*)o = out_pack(quad(rB, 0), quad(rB, 1)); *(u32x4*)(o + 16) = out_pack(quad(rB, 2), quad(rB, 3));
    }
}
#define TLBWARM 1
constexpr int FUSED_LDS = 163840;
constexpr unsigned WPHI_LDS = 156160;
struct FusedArgs { const u16 *X, *Ckv, *Wq, *Wuk, *Wuv, *Wo, *Bx, *Wqkv, *Wphi, *Bwo; char* ws; u16* out; };

__global__ __launch_bounds__(512, 2) void fused_kernel(FusedArgs a) {
    const int tid = threadIdx.x, lane = tid & 63;
    const int wid = rfl(tid >> 6);
    const int bid = blockIdx.x, h = (bid & 7) * 4 + (bid >> 6), j = (bid >> 3) & 7;
    const unsigned lds0 = 0u;
    Stamper stamp{0u, 0, lane};
    stamp();
    u16* Kf = (u16*)(a.ws + WS_KF); u16* Vf = (u16*)(a.ws + WS_VF); u16* Cf = (u16*)(a.ws + WS_CF);
    u16* PhiQ = (u16*)(a.ws + WS_PHIQ); u16* Of = (u16*)(a.ws + WS_OF); float* Bst = (float*)(a.ws + WS_BST);
    u64* flags = (u64*)(a.ws + WS_FLAG);
    float* Bred = (float*)(a.ws + WS_BRED);
    const int hg = bid & 7, rc = bid >> 3;
    if (wid < 2) dma1(lds0 + WPHI_LDS + wid * 1024, lane * 16, mk_srd(a.Wphi, 16384), (unsigned)(hg * 2048 + wid * 1024));
    ProjCtx pc; proj_setup(pc, a.X, a.Ckv, a.Wq, a.Wuk, a.Wuv, Kf, Vf, lds0, h, j, wid, lane);
#pragma unroll
    for (int i = 0; i < 8; ++i) issueKV(pc, 0, i);
#pragma unroll
    for (int i = 0; i < 8; ++i) issueKV(pc, 1, i);
    const unsigned tsc = lds0 + TOUCH_LDS;
    { const unsigned vW = (lane >> 4) * 524288 + (lane & 15) * 128;
      const char* base; unsigned vo = pc.l128;
      if (wid < 4) { base = (const char*)((wid & 1) ? a.Wuv : a.Wuk) + ((wid & 2) ? 4 : 2) * 524288 + bid * 2048 - ((wid & 2) ? 0 : 0); vo = vW; }
      else if (wid == 4) base = (const char*)a.Ckv + bid * 8192;
      else if (wid == 5) { const int u = bid >> 3;
          if (u < 20) { base = a.ws + (size_t)u * 4194304; vo = lane * 65536; } else if (u == 20) { base = (const char*)a.out; vo = lane * 65536; } else { base = (const char*)a.Ckv; vo = 0; } }
      else { base = (const char*)a.Ckv; vo = 0; }
      touch_g<1>(tsc, vo, base); }
    const u64 epoch = *(const u64 __attribute__((address_space(4)))*)(flags + FL_EPOCH * FL_STRIDE);
    u64 tok = mix64(epoch); if (tok == epoch) tok ^= 1ull;
    tok = ((u64)rfl((unsigned)(tok >> 32)) << 32) | rfl((unsigned)tok); asm volatile("" : "+s"(tok));
    B1Ctx bc; b1_setup(bc, a.Bx, a.Wqkv, lds0, rc, hg, wid, lane);
    stamp();
    auto touchk = [&](int kt) {
        const char* base = (const char*)a.Ckv; unsigned vo = 0;
        if (kt == 0) {
            if (wid == 0) { base = (const char*)a.Bx + bid * 8192; vo = pc.l128; }
            else if (wid == 1) { base = (const char*)a.X + bid * 8192; vo = pc.l128; }
            else if (wid == 2) { base = (const char*)a.Wqkv + (bid < 192 ? bid : 0) * 8192; vo = pc.l128; }
        } else if (kt == 1) {
            if (wid < 2) { base = (const char*)a.Wq + (bid * 2 + wid) * 8192; vo = pc.l128; }
        } else if (kt == 2) {
            if (wid < 2) { base = (const char*)a.Wo + (bid * 2 + wid) * 8192; vo = pc.l128; }
            else if (wid == 2) { base = (const char*)a.Bwo + (bid & 63) * 8192; vo = pc.l128; }
        }
        touch_g<2>(tsc, vo, base); };
    u32x4 pk[16];
    kv_run(pc, lane, pk, touchk, [&](int kt) { b1_issue(bc, kt); }, stamp);
    stamp();
    B1Acc ba;
    const bool early = (j & 3) == 0;
    b1_gemm(bc, ba, lane, early, [&](int n) { if (early) kv_store(pc, pk, n); else if (n >= 1 && n <= 6) kv_store(pc, pk, n - 1); },
            [&](int n) {
#pragma unroll
                for (int i = 0; i < 6; ++i) issueQ(pc, n, i); });
    stamp();
    u32x2 wp; { const int l16 = lane & 15, g = lane >> 4; const LAS u16* w = (const LAS u16*)(lds0 + WPHI_LDS + (wid & 3) * 512);
        wp[0] = (unsigned)w[(4 * g) * 16 + l16] | ((unsigned)w[(4 * g + 1) * 16 + l16] << 16); wp[1] = (unsigned)w[(4 * g + 2) * 16 + l16] | ((unsigned)w[(4 * g + 3) * 16 + l16] << 16); }
    b1_phi(ba, [&](int n) { if (early) { if (n % 4 == 2) kv_store(pc, pk, 8 + n / 4); } else if (n % 3 == 1 && n < 14) kv_store(pc, pk, 6 + n / 3); }, wp, PhiQ, Bst, lds0, rc, hg, wid, lane, stamp);
    stamp();
    wait_bar<21>();
    stamp();
    AttnState st;
    AttnCtx c; attn_setup(c, Kf, Vf, Cf, lds0, h, j, wid, lane);
    B2a b2;
    auto spin = [&](const u64* fp) {
        for (unsigned spins = 0; spins < (1u << 15); ++spins) { const u64 v = ld_agent(fp); if (__all(v == tok)) break; __builtin_amdgcn_s_sleep(1); }
        asm volatile("" ::: "memory"); };
    const u64* fkv = flags + (FL_KV + 8 * h + (lane & 7)) * FL_STRIDE;
    const u64* fbs = flags + (FL_BS + 32 * hg + (lane & 31)) * FL_STRIDE;
    u64 pre = 0;
    q_run(pc, early, lane, pk, st.q, [&](int kt) {
        if (kt == 2) { if (tid == 0) st_agent(flags + (FL_BS + 32 * hg + rc) * FL_STRIDE, tok); }
        if (kt == 3) { if (early && tid == 0) st_agent(flags + (FL_KV + 8 * h + j) * FL_STRIDE, tok); }
        if (kt == 3) { if (wid == 7) pre = ld_agent(fbs); }
        if (kt == 4) {
            if (wid == 7) {
                if (!__all(pre == tok)) spin(fbs);
                asm volatile("" ::: "memory");
                b2a_load(b2, Bst, rc, hg, lane); } }
        if (kt == 5) { pre = ld_agent(fkv); }
        if (kt == 6) {
            if (!early && tid == 0) st_agent(flags + (FL_KV + 8 * h + j) * FL_STRIDE, tok);
            if (wid == 7) b2a_finish(b2, Bred, rc, hg, lane);
            c.known = (unsigned)__builtin_amdgcn_ballot_w64(pre == tok) & 0xffu;
            for (unsigned spins = 0; spins < (1u << 15) && !(c.known & 1u); ++spins) { const u64 v = ld_agent(fkv); c.known = (unsigned)__builtin_amdgcn_ballot_w64(v == tok) & 0xffu; if (!(c.known & 1u)) __builtin_amdgcn_s_sleep(1); }
            asm volatile("" ::: "memory");
            attn_prime(c, wid >> 2);
        } }, stamp);
    c.srdF = mk_srd(flags + (FL_KV + 8 * h) * FL_STRIDE, 1024); c.tok = tok; c.prev = 0;
    stamp();
    BARRIER();
    if (wid < 4) {
#pragma unroll
        for (int s = 0; s < 8; ++s) *(LAS bf16x8*)(c.qstash + s * 1024) = st.q[s]; }
    stamp();
    B2b<4> b2b;
    attn_run(st, c, lds0, h, j, wid, lane,
             [&]() { if (tid == 448) st_agent(flags + (FL_BR + 32 * hg + rc) * FL_STRIDE, tok); },
             [&]() { b2b_load<4>(b2b, PhiQ, Bred, flags + (FL_BR + 32 * hg + 8 * wid) * FL_STRIDE, tok, 4 * hg + wid, 4 * rc, lane); },
             [&]() { b2b_finish<4>(b2b, Of, 4 * hg + wid, 4 * rc, lane); }, stamp);
    wait_bar<0>();
    if (tid == 0) st_agent(flags + (FL_C + bid) * FL_STRIDE, tok);
    stamp();
    out_phase(a.Wo, a.Bwo, a.out, a.ws, tok, lds0, bid, wid, lane, stamp);
    if (bid == 0 && wid == 7) {
        bool ok = false;
        for (unsigned spins = 0; spins < (1u << 15) && !ok; ++spins) { bool all = true;
#pragma unroll
            for (int i = 0; i < 4; ++i) all &= ld_agent(flags + (FL_C + lane + 64 * i) * FL_STRIDE) == tok;
            ok = __all(all); if (!ok) __builtin_amdgcn_s_sleep(1); }
        if (lane == 0) st_agent(flags + FL_EPOCH * FL_STRIDE, tok);
    }
    stamp();
}

#define WS_END 1
#define WS_SHIFT(n) (WS_END && (n) >= ((size_t)160 << 20) ? (((n) - ((size_t)80 << 20)) & ~(size_t)0x1fffff) : (size_t)0)
extern "C" void kernel_launch(void* const* d_in, const int* in_sizes, int n_in, void* d_out, int out_size, void* d_ws, size_t ws_size, hipStream_t stream) {
    FusedArgs a;
    a.X = (const u16*)d_in[0]; a.Ckv = (const u16*)d_in[1]; a.Wq = (const u16*)d_in[2]; a.Wuk = (const u16*)d_in[3]; a.Wuv = (const u16*)d_in[4]; a.Wo = (const u16*)d_in[5];
    a.Bx = (const u16*)d_in[6]; a.Wqkv = (const u16*)d_in[7]; a.Wphi = (const u16*)d_in[8]; a.Bwo = (const u16*)d_in[9]; a.ws = (char*)d_ws + WS_SHIFT(ws_size); a.out = (u16*)d_out;
    (void)hipFuncSetAttribute((const void*)fused_kernel, hipFuncAttributeMaxDynamicSharedMemorySize, FUSED_LDS);
    fused_kernel<<<256, 512, FUSED_LDS, stream>>>(a);
}
```

```cpp
#include <hip/hip_runtime.h>
#include <stdint.h>
#include <type_traits>

typedef unsigned short u16;
typedef unsigned long long u64;
typedef __bf16 bf16x8 __attribute__((ext_vector_type(8)));
typedef __bf16 bf16x4 __attribute__((ext_vector_type(4)));
typedef __bf16 bf16x2 __attribute__((ext_vector_type(2)));
typedef short s16x4 __attribute__((ext_vector_type(4)));
typedef float f32x16 __attribute__((ext_vector_type(16)));
typedef float f32x4 __attribute__((ext_vector_type(4)));
typedef float f32x2 __attribute__((ext_vector_type(2)));
typedef unsigned u32x4 __attribute__((ext_vector_type(4)));
typedef unsigned u32x2 __attribute__((ext_vector_type(2)));
typedef __amdgpu_buffer_rsrc_t srd_t;
#define LAS __attribute__((address_space(3)))
#define GAS __attribute__((address_space(1)))
#define SBAR() __builtin_amdgcn_sched_barrier(0)
#define PIN(x) asm volatile("" : "+v"(x))
#define MFMA32(a, b, c) __builtin_amdgcn_mfma_f32_32x32x16_bf16((a), (b), (c), 0, 0, 0)
#define MFMA16(a, b, c) __builtin_amdgcn_mfma_f32_16x16x32_bf16((a), (b), (c), 0, 0, 0)
#define DI __device__ __forceinline__
#define MFMA16I(acc, a, b) asm volatile("v_mfma_f32_16x16x32_bf16 %0, %1, %2, %0" : "+v"(acc) : "v"(a), "v"(b))
#define MFMA_DRAIN() asm volatile("s_nop 7")
#pragma clang diagnostic ignored "-Wint-to-pointer-cast"

DI unsigned cvtpk(float lo, float hi) { f32x2 v = {lo, hi}; bf16x2 b = __builtin_convertvector(v, bf16x2); return __builtin_bit_cast(unsigned, b); }
DI unsigned rfl(unsigned x) { return __builtin_amdgcn_readfirstlane(x); }
DI float swap_max(float x) { auto rr = __builtin_amdgcn_permlane32_swap(__float_as_uint(x), __float_as_uint(x), false, false); return __builtin_fmaxf(__uint_as_float(rr[0]), __uint_as_float(rr[1])); }
DI float swap_add(float x) { auto rr = __builtin_amdgcn_permlane32_swap(__float_as_uint(x), __float_as_uint(x), false, false); return __uint_as_float(rr[0]) + __uint_as_float(rr[1]); }
DI srd_t mk_srd(const void* p, int bytes) { return __builtin_amdgcn_make_buffer_rsrc((void*)p, (short)0, bytes, 0x00020000); }
DI bf16x4 ldstr(unsigned a) { return __builtin_amdgcn_ds_read_tr16_b64_v4bf16((LAS bf16x4*)a); }
DI bf16x8 ldsf(unsigned a) { return *(const LAS bf16x8*)a; }
DI bf16x8 cat4(bf16x4 a, bf16x4 b) { return __builtin_shufflevector(a, b, 0, 1, 2, 3, 4, 5, 6, 7); }
#define SCALARIZE2(a, b) do {} while (0)
DI void dma1(unsigned m0v, unsigned voff, srd_t srd, unsigned soff) {
    SCALARIZE2(m0v, soff);
    asm volatile("s_mov_b32 m0, %0\n\ts_nop 0\n\tbuffer_load_dwordx4 %1, %2, %3 offen lds" :: "s"(m0v), "v"(voff), "s"(srd), "s"(soff) : "memory");
}
DI void dma1s(unsigned m0v, unsigned voff, srd_t srd, unsigned soff) {
    SCALARIZE2(m0v, soff);
    asm volatile("s_mov_b32 m0, %0\n\ts_nop 0\n\tbuffer_load_dwordx4 %1, %2, %3 offen sc1 lds" :: "s"(m0v), "v"(voff), "s"(srd), "s"(soff) : "memory");
}
DI void touch(unsigned m0v, unsigned voff, srd_t srd, unsigned soff) {
    SCALARIZE2(m0v, soff);
    asm volatile("s_mov_b32 m0, %0\n\ts_nop 0\n\tbuffer_load_dword %1, %2, %3 offen lds" :: "s"(m0v), "v"(voff), "s"(srd), "s"(soff) : "memory");
}
DI void store16_sc1(u32x4 w, unsigned voff, srd_t srd, unsigned soff) {
    unsigned z = soff; SCALARIZE2(soff, z);
    asm volatile("buffer_store_dwordx4 %0, %1, %2, %3 offen sc1\n\ts_nop 1" :: "v"(w), "v"(voff), "s"(srd), "s"(soff) : "memory");
}
template <int N> DI void wait_vm() { asm volatile("s_waitcnt vmcnt(%0)" :: "i"(N) : "memory"); }
template <int N> DI void wait_bar() { asm volatile("s_waitcnt vmcnt(%0) lgkmcnt(0)\n\ts_barrier" :: "i"(N) : "memory"); }
template <int N> DI void wait_bar_keep() { asm volatile("s_waitcnt vmcnt(%0)\n\ts_barrier" :: "i"(N) : "memory"); }
#define BARRIER() asm volatile("s_waitcnt lgkmcnt(0)\n\ts_barrier" ::: "memory")

struct Stamper { unsigned v; int n; int lane;
    DI void operator()() {
    } };

constexpr size_t WS_FLAG = 0;
constexpr size_t WS_KF = 1 << 20, WS_VF = WS_KF + (16 << 20), WS_CF = WS_VF + (16 << 20), WS_PHIQ = WS_CF + (16 << 20), WS_OF = WS_PHIQ + (4 << 20), WS_BST = WS_OF + (2 << 20), WS_BRED = WS_BST + (3 << 20), WS_XCH = WS_BRED + (1 << 20), WS_DBG = 77 << 20;
constexpr int FL_STRIDE = 16;
constexpr int FL_EPOCH = 0, FL_KV = 32, FL_BS = 32 + 256, FL_BR = 32 + 512, FL_XF = 32 + 768, FL_C = FL_XF + 256;
typedef GAS u64 gu64;
DI u64 ld_agent(const u64* p) { return __hip_atomic_load((gu64*)p, __ATOMIC_RELAXED, __HIP_MEMORY_SCOPE_AGENT); }
DI void st_agent(u64* p, u64 v) { __hip_atomic_store((gu64*)p, v, __ATOMIC_RELAXED, __HIP_MEMORY_SCOPE_AGENT); }
DI u64 mix64(u64 z) { z += 0x9E3779B97F4A7C15ull; z = (z ^ (z >> 30)) * 0xBF58476D1CE4E5B9ull; z = (z ^ (z >> 27)) * 0x94D049BB133111EBull; return z ^ (z >> 31); }
DI f32x4 mfma16_16(u32x2 a, u32x2 b, f32x4 c) { return __builtin_amdgcn_mfma_f32_16x16x16bf16_1k(__builtin_bit_cast(s16x4, a), __builtin_bit_cast(s16x4, b), c, 0, 0, 0); }
DI float rbf16(float x) { return __uint_as_float(cvtpk(x, 0.f) << 16); }
DI float x16_max(float x) { auto rr = __builtin_amdgcn_permlane16_swap(__float_as_uint(x), __float_as_uint(x), false, false); return __builtin_fmaxf(__uint_as_float(rr[0]), __uint_as_float(rr[1])); }
DI float x16_add(float x) { auto rr = __builtin_amdgcn_permlane16_swap(__float_as_uint(x), __float_as_uint(x), false, false); return __uint_as_float(rr[0]) + __uint_as_float(rr[1]); }
DI float grp_max(float x) { return swap_max(x16_max(x)); }
DI float grp_sum(float x) { return swap_add(x16_add(x)); }
constexpr float LOG2E = 1.4426950408889634f;
constexpr unsigned B1_RED = 147456;
#define BROT 0
struct B1Ctx { srd_t srdX, srdW; unsigned vx, vw[2], sx, sw; int wid, rot; unsigned lds0; };
DI void b1_setup(B1Ctx& c, const u16* Bx, const u16* Wqkv, unsigned lds0, int rc, int hg, int wid, int lane) {
    c.srdX = mk_srd(Bx, 2097152); c.srdW = mk_srd(Wqkv, 512 * 1536 * 2); c.wid = wid; c.lds0 = lds0;
    c.vx = (lane >> 3) * 1024 + (((lane & 7) ^ ((4 * wid + (lane >> 4)) & 7)) * 16);
#pragma unroll
    for (int p = 0; p < 2; ++p) c.vw[p] = (lane >> 3) * 3072 + (((lane & 7) ^ (2 * (((lane >> 4) & 1) | (p << 1)))) * 16);
    c.sx = (64 * rc + 8 * wid) * 1024; c.sw = hg * 128; c.rot = BROT ? rc & 7 : 0;
}
DI void b1_issue(const B1Ctx& c, int kt) {
    const unsigned st = c.lds0 + (kt & 3) * 32768;
    kt = (kt + c.rot) & 7;
    dma1(st + c.wid * 1024, c.vx, c.srdX, c.sx + kt * 128);
#pragma unroll
    for (int i = 0; i < 3; ++i) { const int pw = 3 * c.wid + i, m = pw >> 3, pr = pw & 7;
        dma1(st + 8192 + pw * 1024, (pr & 1) ? c.vw[1] : c.vw[0], c.srdW, c.sw + (kt * 64 + 8 * pr) * 3072 + m * 1024); }
}
struct B1Acc { f32x4 aq[2], ak[2], av[2]; };
struct B1Fr { bf16x8 x[2][2]; bf16x4 w[2][3][2]; };
struct B1Ad { unsigned xa[2][2], wa[2]; };
template <int KT> DI void b1_read(B1Fr& f, const B1Ad& ad) {
    constexpr unsigned st = (KT & 1) * 32768; constexpr int sp = (KT >> 1) & 1;
#pragma unroll
    for (int ks = 0; ks < 2; ++ks) { const unsigned xa = ad.xa[sp][ks];
        f.x[ks][0] = ldsf(xa + st); f.x[ks][1] = ldsf(xa + st + 2048);
#pragma unroll
        for (int m = 0; m < 3; ++m) { f.w[ks][m][0] = ldstr(ad.wa[sp] + st + m * 8192 + ks * 4096); f.w[ks][m][1] = ldstr(ad.wa[sp] + st + m * 8192 + ks * 4096 + 512); } }
}
DI void b1_mma(B1Acc& a, const B1Fr& f) {
#pragma unroll
    for (int ks = 0; ks < 2; ++ks) {
        const bf16x8 wq = cat4(f.w[ks][0][0], f.w[ks][0][1]), wk = cat4(f.w[ks][1][0], f.w[ks][1][1]), wv = cat4(f.w[ks][2][0], f.w[ks][2][1]);
        a.aq[0] = MFMA16(wq, f.x[ks][0], a.aq[0]); a.aq[1] = MFMA16(wq, f.x[ks][1], a.aq[1]); a.ak[0] = MFMA16(wk, f.x[ks][0], a.ak[0]); a.ak[1] = MFMA16(wk, f.x[ks][1], a.ak[1]);
        a.av[0] = MFMA16(f.x[ks][0], wv, a.av[0]); a.av[1] = MFMA16(f.x[ks][1], wv, a.av[1]);
    }
}
template <class St, class IQ>
DI void b1_gemm(const B1Ctx& c, B1Acc& a, int lane, bool early, St store, IQ issueQt) {
    const int l16 = lane & 15, g = lane >> 4, q4 = l16 >> 2, p4 = lane & 3, wid = c.wid;
    const int row = 32 * (wid >> 2) + l16;
    B1Ad ad;
#pragma unroll
    for (int sp = 0; sp < 2; ++sp) {
        ad.xa[sp][0] = c.lds0 + sp * 65536 + row * 128 + (((0 + g) ^ ((row >> 1) & 7)) << 4); ad.xa[sp][1] = c.lds0 + sp * 65536 + row * 128 + (((4 + g) ^ ((row >> 1) & 7)) << 4);
        ad.wa[sp] = c.lds0 + sp * 65536 + 8192 + (8 * g + q4) * 128 + (((wid & 3) ^ (((q4 >> 1) & 1) | ((g & 1) << 1))) * 32) + p4 * 8;
        PIN(ad.xa[sp][0]); PIN(ad.xa[sp][1]); PIN(ad.wa[sp]); }
#pragma unroll
    for (int r = 0; r < 2; ++r) { a.aq[r] = f32x4{}; a.ak[r] = f32x4{}; a.av[r] = f32x4{}; }
#pragma unroll
    for (int r = 0; r < 2; ++r) { PIN(a.aq[r]); PIN(a.ak[r]); PIN(a.av[r]); }
    B1Fr f0, f1;
    b1_read<0>(f0, ad);
#define B1W(E, L) do { if (early) wait_bar<E>(); else wait_bar<L>(); } while (0)
#define B1T(KT, CUR, NXT, PRE) do { b1_read<KT + 1>(NXT, ad); SBAR(); PRE; store(KT); SBAR(); b1_mma(a, CUR); SBAR(); } while (0)
    B1W(8, 8);   B1T(0, f0, f1, b1_issue(c, 4));
    B1W(9, 8);   B1T(1, f1, f0, b1_issue(c, 5));
    B1W(10, 9);  B1T(2, f0, f1, b1_issue(c, 6));
    B1W(11, 10); B1T(3, f1, f0, b1_issue(c, 7));
    B1W(11, 11); B1T(4, f0, f1, (void)0);
    B1W(7, 7);   B1T(5, f1, f0, issueQt(0));
    B1W(9, 9);   B1T(6, f0, f1, issueQt(1));
    BARRIER();   issueQt(2); store(7); SBAR(); b1_mma(a, f1);
#undef B1W
#undef B1T
}
template <int N, class Tick> DI void phi_map(const f32x4 (&p)[N], u32x4 (&out)[N], Tick tick) {
    float x[N][4], mx[N], nm[N], ep[N][4], en[N][4], sp[N], sn[N];
#pragma unroll
    for (int i = 0; i < N; ++i) {
#pragma unroll
        for (int e = 0; e < 4; ++e) x[i][e] = rbf16(p[i][e]) * LOG2E;
        mx[i] = __builtin_fmaxf(__builtin_fmaxf(x[i][0], x[i][1]), __builtin_fmaxf(x[i][2], x[i][3])); nm[i] = -__builtin_fminf(__builtin_fminf(x[i][0], x[i][1]), __builtin_fminf(x[i][2], x[i][3]));
        if (i & 1) tick(i >> 1); }
#pragma unroll
    for (int i = 0; i < N; ++i) { mx[i] = x16_max(mx[i]); nm[i] = x16_max(nm[i]); if (i & 1) tick(2 + (i >> 1)); }
#pragma unroll
    for (int i = 0; i < N; ++i) { mx[i] = swap_max(mx[i]); nm[i] = swap_max(nm[i]); if (i & 1) tick(4 + (i >> 1)); }
#pragma unroll
    for (int i = 0; i < N; ++i) {
#pragma unroll
        for (int e = 0; e < 4; ++e) { ep[i][e] = __builtin_amdgcn_exp2f(x[i][e] - mx[i]); en[i][e] = __builtin_amdgcn_exp2f(-x[i][e] - nm[i]); }
        sp[i] = (ep[i][0] + ep[i][1]) + (ep[i][2] + ep[i][3]); sn[i] = (en[i][0] + en[i][1]) + (en[i][2] + en[i][3]); tick(6 + i); }
#pragma unroll
    for (int i = 0; i < N; ++i) { sp[i] = x16_add(sp[i]); sn[i] = x16_add(sn[i]); if (i & 1) tick(10 + (i >> 1)); }
#pragma unroll
    for (int i = 0; i < N; ++i) { sp[i] = __builtin_amdgcn_rcpf(swap_add(sp[i])); sn[i] = __builtin_amdgcn_rcpf(swap_add(sn[i])); if (i & 1) tick(12 + (i >> 1)); }
#pragma unroll
    for (int i = 0; i < N; ++i) { out[i] = (u32x4){cvtpk(ep[i][0] * sp[i], ep[i][1] * sp[i]), cvtpk(ep[i][2] * sp[i], ep[i][3] * sp[i]), cvtpk(en[i][0] * sn[i], en[i][1] * sn[i]), cvtpk(en[i][2] * sn[i], en[i][3] * sn[i])};
        if (i & 1) tick(14 + (i >> 1)); }
}
template <class Tick>
DI void b1_phi(const B1Acc& a, Tick tick, u32x2 wp, u16* PhiQ, float* Bst, unsigned lds0, int rc, int hg, int wid, int lane, Stamper& stamp) {
    const int h = 4 * hg + (wid & 3), row0 = 64 * rc + 32 * (wid >> 2);
    const int l16 = lane & 15, g = lane >> 4;
    f32x4 sp = f32x4{}, sn = f32x4{}; float zp = 0.f, zn = 0.f;
    f32x4 pin[4]; u32x4 pout[4];
#pragma unroll
    for (int r = 0; r < 2; ++r) {
        const u32x2 qb = {cvtpk(a.aq[r][0], a.aq[r][1]), cvtpk(a.aq[r][2], a.aq[r][3])}, kb = {cvtpk(a.ak[r][0], a.ak[r][1]), cvtpk(a.ak[r][2], a.ak[r][3])};
        pin[r] = mfma16_16(wp, qb, f32x4{}); pin[2 + r] = mfma16_16(wp, kb, f32x4{});
    }
    phi_map<4>(pin, pout, tick);
    const u32x4 wq[2] = {pout[0], pout[1]}, wk[2] = {pout[2], pout[3]};
    const unsigned tb = lds0 + B1_RED + wid * 1024;
#pragma unroll
    for (int r = 0; r < 2; ++r) {
        *(LAS u32x2*)(tb + l16 * 32 + g * 8) = (u32x2){wk[r][0], wk[r][1]};
        *(LAS u32x2*)(tb + 512 + l16 * 32 + g * 8) = (u32x2){wk[r][2], wk[r][3]};
        __builtin_amdgcn_raw_buffer_store_b128(wq[r], mk_srd(PhiQ, 4194304), ((h * 128 + (row0 >> 4) + r) * 64 + lane) * 16, 0, 0);
        asm volatile("s_waitcnt lgkmcnt(0)" ::: "memory");
        const unsigned ta = tb + (4 * g + (l16 >> 2)) * 32 + (lane & 3) * 8;
        u32x2 pp = __builtin_bit_cast(u32x2, ldstr(ta)), pn = __builtin_bit_cast(u32x2, ldstr(ta + 512));
        asm volatile("s_waitcnt lgkmcnt(0)" : "+v"(pp), "+v"(pn) :: "memory");
        const u32x2 vb = {cvtpk(a.av[r][0], a.av[r][1]), cvtpk(a.av[r][2], a.av[r][3])};
        sp = mfma16_16(pp, vb, sp); sn = mfma16_16(pn, vb, sn);
        zp += (__uint_as_float(pp[0] << 16) + __uint_as_float(pp[0] & 0xffff0000u)) + (__uint_as_float(pp[1] << 16) + __uint_as_float(pp[1] & 0xffff0000u));
        zn += (__uint_as_float(pn[0] << 16) + __uint_as_float(pn[0] & 0xffff0000u)) + (__uint_as_float(pn[1] << 16) + __uint_as_float(pn[1] & 0xffff0000u));
    }
    zp = grp_sum(zp); zn = grp_sum(zn);
    BARRIER();
    LAS char* red = (LAS char*)(lds0 + B1_RED + (wid & 3) * 2176);
    if (wid >= 4) { *(LAS f32x4*)(red + lane * 32) = sp; *(LAS f32x4*)(red + lane * 32 + 16) = sn; if (lane < 16) *(LAS f32x2*)(red + 2048 + lane * 8) = (f32x2){zp, zn}; }
    BARRIER();
    sp += *(const LAS f32x4*)(red + lane * 32); sn += *(const LAS f32x4*)(red + lane * 32 + 16);
    const f32x2 zo = *(const LAS f32x2*)(red + 2048 + l16 * 8);
    const srd_t srd = mk_srd(Bst + (size_t)(h * 32 + rc) * 576, wid < 4 ? 2304 : 0);
    store16_sc1(__builtin_bit_cast(u32x4, sp), lane * 32, srd, 0u); store16_sc1(__builtin_bit_cast(u32x4, sn), lane * 32 + 16, srd, 0u);
    __builtin_amdgcn_raw_buffer_store_b64((u32x2){__float_as_uint(zp + zo[0]), __float_as_uint(zn + zo[1])}, srd, lane < 16 ? 2048 + lane * 8 : 0x7fffffff, 0, 16);
}
template <int N> DI void waitN(const u64* flags, u64 tok, int lane) {
    const u64* p = flags + (lane & (N - 1)) * FL_STRIDE;
    for (unsigned spins = 0; spins < (1u << 15); ++spins) { const u64 v = ld_agent(p); if (__all(v == tok)) break; __builtin_amdgcn_s_sleep(1); }
    asm volatile("" ::: "memory");
}
struct B2a { f32x4 v[16]; };
DI void b2a_load(B2a& b, const float* Bst, int rc, int hg, int lane) {
    const int h = 4 * hg + (rc >> 3), c = lane & 31, half = lane >> 5;
    const srd_t srd = mk_srd(Bst + (size_t)h * 32 * 576, 32 * 2304);
    const unsigned vo = (c < 18 ? (18 * (rc & 7) + c) * 16 : 0x7fffffff) + half * 16 * 2304;
#pragma unroll
    for (int i = 0; i < 16; ++i) b.v[i] = __builtin_bit_cast(f32x4, __builtin_amdgcn_raw_buffer_load_b128(srd, vo, i * 2304, 16));
}
DI void b2a_finish(const B2a& b, float* Bred, int rc, int hg, int lane) {
    const int h = 4 * hg + (rc >> 3), c = lane & 31, half = lane >> 5;
    f32x4 s = b.v[0];
#pragma unroll
    for (int i = 1; i < 16; ++i) s += b.v[i];
#pragma unroll
    for (int e = 0; e < 4; ++e) s[e] = swap_add(s[e]);
    const srd_t srdR = mk_srd(Bred + (size_t)h * 576, 2304);
    store16_sc1(__builtin_bit_cast(u32x4, s), (half == 0 && c < 18) ? (18 * (rc & 7) + c) * 16 : 0x7fffffffu, srdR, 0u);
}
template <int NRB> struct B2b { bf16x8 ph[NRB]; f32x4 sp, sn, za, zb; };
template <int NRB>
DI void b2b_load(B2b<NRB>& b, const u16* PhiQ, const float* Bred, const u64* flagsR, u64 tok, int h, int rb0, int lane) {
    PIN(lane);
    const int g = lane >> 4;
    waitN<8>(flagsR, tok, lane);
#pragma unroll
    for (int r = 0; r < NRB; ++r) b.ph[r] = __builtin_bit_cast(bf16x8, __builtin_amdgcn_raw_buffer_load_b128(mk_srd(PhiQ, 4194304), lane * 16, (h * 128 + rb0 + r) * 1024, 0));
    const srd_t srd = mk_srd(Bred + (size_t)h * 576, 2304);
    b.sp = __builtin_bit_cast(f32x4, __builtin_amdgcn_raw_buffer_load_b128(srd, lane * 32, 0, 16)); b.sn = __builtin_bit_cast(f32x4, __builtin_amdgcn_raw_buffer_load_b128(srd, lane * 32 + 16, 0, 16));
    b.za = __builtin_bit_cast(f32x4, __builtin_amdgcn_raw_buffer_load_b128(srd, 2048 + g * 32, 0, 16)); b.zb = __builtin_bit_cast(f32x4, __builtin_amdgcn_raw_buffer_load_b128(srd, 2048 + g * 32 + 16, 0, 16));
}
template <int NRB>
DI void b2b_finish(const B2b<NRB>& b, u16* Of, int h, int rb0, int lane) {
    PIN(lane);
    const int l16 = lane & 15, g = lane >> 4;
    const u32x4 kvw = {cvtpk(b.sp[0], b.sp[1]), cvtpk(b.sp[2], b.sp[3]), cvtpk(b.sn[0], b.sn[1]), cvtpk(b.sn[2], b.sn[3])};
    const u32x4 zw = {cvtpk(b.za[0], b.za[2]), cvtpk(b.zb[0], b.zb[2]), cvtpk(b.za[1], b.za[3]), cvtpk(b.zb[1], b.zb[3])};
    const srd_t srdO = mk_srd(Of, 2097152);
#pragma unroll
    for (int r = 0; r < NRB; ++r) { const int rb16 = rb0 + r;
        const f32x4 num = MFMA16(__builtin_bit_cast(bf16x8, kvw), b.ph[r], f32x4{});
        const f32x4 den = MFMA16(__builtin_bit_cast(bf16x8, zw), b.ph[r], f32x4{});
        const float dn = rbf16(rbf16(den[0]) + 1e-6f);
        const u32x2 w = {cvtpk(rbf16(num[0]) / dn, rbf16(num[1]) / dn), cvtpk(rbf16(num[2]) / dn, rbf16(num[3]) / dn)};
        __builtin_amdgcn_raw_buffer_store_b64(w, srdO, (((rb16 >> 1) * 32 + h) * 2 + (g & 1)) * 512 + ((rb16 & 1) * 16 + l16) * 16 + (g >> 1) * 8, 0, 16); }
}
#define KVPRIO 0
#define KROT 0
struct ProjCtx {
    srd_t srdC, srdX, srdW, srdWq, srdO;
    unsigned va[2], vw[2], vwq, vo, lds0, l128;
    unsigned sA, sW, sWq, sXa;
    int wid, isV, rot;
};
constexpr unsigned TOUCH_LDS = 163840 - 256;
DI void issueKV(const ProjCtx& c, int kt, int i) {
    const unsigned st = c.lds0 + (kt & 1) * 65536;
    kt = (kt + c.rot) & 7;
    if (i < 4) dma1(st + (4 * c.wid + i) * 1024, c.va[i & 1], c.srdC, c.sA + i * 8192 + kt * 128);
    else dma1(st + 32768 + (c.wid >> 2) * 16384 + (4 * (c.wid & 3) + (i - 4)) * 1024, c.vw[(i - 4) >> 1], c.srdW, c.sW + ((i - 4) >> 1) * 65536 + ((i - 4) & 1) * 128 + kt * 524288);
}
DI void issueQ(const ProjCtx& c, int kt, int i) {
    const unsigned st = c.lds0 + (kt % 3) * 49152;
    kt = (kt + c.rot) & 7;
    if (i < 4) dma1(st + (4 * c.wid + i) * 1024, c.va[i & 1], c.srdX, c.sXa + i * 8192 + kt * 128);
    else dma1(st + 32768 + (2 * c.wid + (i - 4)) * 1024, c.vwq, c.srdWq, c.sWq + (i - 4) * 128 + kt * 524288);
}
#define TOUCHMODE 3
template <int WHICH> DI void touch_g(unsigned m0v, unsigned voff, const void* base) {
    if (!(TOUCHMODE & WHICH)) voff = 0;
    const u64 b = (u64)base; const u64 sb = ((u64)rfl((unsigned)(b >> 32)) << 32) | rfl((unsigned)b);
    asm volatile("s_nop 4\n\ts_mov_b32 m0, %0\n\ts_nop 0\n\tglobal_load_lds_dword %1, %2" :: "s"(rfl(m0v)), "v"(voff), "s"(sb) : "memory");
}

struct GAddr { unsigned a[2]; unsigned w[2][2]; };
DI void gaddr_add(GAddr& g, unsigned d) { g.a[0] += d; g.a[1] += d; g.w[0][0] += d; g.w[0][1] += d; g.w[1][0] += d; g.w[1][1] += d; PIN(g.a[0]); PIN(g.a[1]); PIN(g.w[0][0]); PIN(g.w[0][1]); PIN(g.w[1][0]); PIN(g.w[1][1]); }
DI void gaddr_xor(GAddr& g, unsigned d) { g.a[0] ^= d; g.a[1] ^= d; g.w[0][0] ^= d; g.w[0][1] ^= d; g.w[1][0] ^= d; g.w[1][1] ^= d; }
DI bf16x8 wfrag(const GAddr& g, int ks, int nb) { return cat4(ldstr(g.w[nb & 1][0] + ks * 8192 + (nb >> 1) * 512), ldstr(g.w[nb & 1][1] + ks * 8192 + (nb >> 1) * 512)); }
DI void gaddr_w(GAddr& g, unsigned base, int lane) {
    const int gq = lane >> 4, q4 = (lane & 15) >> 2, p4 = lane & 3;
#pragma unroll
    for (int o = 0; o < 2; ++o)
#pragma unroll
        for (int t = 0; t < 2; ++t) g.w[o][t] = base + 2048 * gq + 64 * (4 * t + q4) + 16 * ((2 * o + (p4 >> 1)) ^ ((2 * gq + t) & 3)) + 8 * (p4 & 1);
}
DI void gaddr_a(GAddr& g, unsigned base, int row0, int lane) {
    const int row = row0 + (lane & 15), gq = lane >> 4;
#pragma unroll
    for (int ks = 0; ks < 2; ++ks) g.a[ks] = base + row * 128 + (((4 * ks + gq) ^ ((row >> 1) & 7)) << 4);
}

#define KVAH 3
constexpr int AH = KVAH;
#define KV_MMA(mb, W) do { _Pragma("unroll") for (int nb = 0; nb < 4; ++nb) { if (ISV) MFMA16I(acc[mb][nb], a[mb], W[nb]); else MFMA16I(acc[mb][nb], W[nb], a[mb]); } } while (0)
template <bool ISV, class Hook>
DI void kv_step0(f32x4 (&acc)[8][4], const bf16x8 (&w)[4], bf16x8 (&wn)[4], bf16x8 (&an)[AH], const GAddr& g, Hook hook) {
    bf16x8 a[8 + AH];
#pragma unroll
    for (int i = 0; i < AH; ++i) a[i] = an[i];
#pragma unroll
    for (int mb = 0; mb < 8; ++mb) {
        a[mb + AH] = mb + AH < 8 ? ldsf(g.a[0] + (mb + AH) * 2048) : ldsf(g.a[1] + (mb + AH - 8) * 2048);
        if (mb < 4) wn[mb] = wfrag(g, 1, mb);
        SBAR();
        KV_MMA(mb, w);
        SBAR();
        hook(mb);
        SBAR();
    }
#pragma unroll
    for (int i = 0; i < AH; ++i) an[i] = a[8 + i];
}
struct NoHook { DI void operator()(int) const {} };

template <bool ISV, class Touch, class IssueB1>
DI void kv_part(const ProjCtx& c, GAddr g, u32x4 (&pk)[16], Touch touchk, IssueB1 issueB1, Stamper& stamp) {
    f32x4 acc[8][4];
#pragma unroll
    for (int a = 0; a < 8; ++a)
#pragma unroll
        for (int b = 0; b < 4; ++b) { acc[a][b] = f32x4{}; PIN(acc[a][b]); }
    bf16x8 w0[4], w1[4], an[AH];
    wait_bar<9>();
    stamp();
#pragma unroll
    for (int nb = 0; nb < 4; ++nb) w0[nb] = wfrag(g, 0, nb);
#pragma unroll
    for (int i = 0; i < AH; ++i) an[i] = ldsf(g.a[0] + i * 2048);
    auto tile = [&](int kt, auto late, auto mid, auto waitB, auto early, bool next) {
        kv_step0<ISV>(acc, w0, w1, an, g, late);
        bf16x8 a[8];
#pragma unroll
        for (int i = 0; i < AH; ++i) a[i] = an[i];
#pragma unroll
        for (int mb = 0; mb < 8 - AH; ++mb) {
            a[mb + AH] = ldsf(g.a[1] + (mb + AH) * 2048);
            SBAR();
            KV_MMA(mb, w1);
            SBAR();
            if (mb == 0) { mid(); SBAR(); }
        }
        waitB();
        if (next) { gaddr_xor(g, 65536u);
#pragma unroll
            for (int nb = 0; nb < 4; ++nb) w0[nb] = wfrag(g, 0, nb);
#pragma unroll
            for (int i = 0; i < AH; ++i) an[i] = ldsf(g.a[0] + i * 2048); }
        SBAR();
#pragma unroll
        for (int mb = 8 - AH; mb < 8; ++mb) { KV_MMA(mb, w1); SBAR(); early(mb - (8 - AH)); SBAR(); }
    };
    for (int kt = 0; kt < 6; ++kt)
        tile(kt, [&](int mb) { if (kt > 0 && (mb & 1)) issueKV(c, kt + 1, 4 + (mb >> 1)); }, [&]() { touchk(kt); }, [&]() { wait_bar<1>(); },
             [&](int i) { if (i == 0) { issueKV(c, kt + 2, 0); issueKV(c, kt + 2, 1); } else issueKV(c, kt + 2, 1 + i); }, true);
    tile(6, [&](int mb) { if (mb & 1) issueKV(c, 7, 4 + (mb >> 1)); }, [&]() {}, [&]() { wait_bar<0>(); }, [&](int i) { if (i < 2) issueB1(i); }, true);
    tile(7, NoHook{}, [&]() {}, [&]() { stamp(); wait_bar<4>(); stamp(); }, [&](int i) { if (i < 2) issueB1(2 + i); }, false);
    __builtin_amdgcn_s_setprio(0);
    MFMA_DRAIN();
#pragma unroll
    for (int n = 0; n < 16; ++n) {
        const f32x4 lo = ISV ? acc[2 * (n >> 2)][n & 3] : acc[n >> 1][2 * (n & 1)], hi = ISV ? acc[2 * (n >> 2) + 1][n & 3] : acc[n >> 1][2 * (n & 1) + 1];
        pk[n] = (u32x4){cvtpk(lo[0], lo[1]), cvtpk(lo[2], lo[3]), cvtpk(hi[0], hi[1]), cvtpk(hi[2], hi[3])}; PIN(pk[n]); }
}
DI void kv_store(const ProjCtx& c, const u32x4 (&pk)[16], int n) {
    const unsigned offV = (n >> 3) * 16384 + ((n & 3) * 2 + ((n >> 2) & 1)) * 1024;
    const unsigned offK = (n >> 3) * 16384 + (((n >> 1) & 3) * 4 + (n & 1)) * 1024;
    store16_sc1(pk[n], c.vo, c.srdO, c.isV ? offV : offK);
}

struct QFr { bf16x8 x[2]; bf16x8 w[8]; };
template <int KS> DI void q_read(QFr& f, const GAddr& g, int i) {
    if (i == 1) f.x[0] = ldsf(g.a[KS]); else if (i == 2) f.x[1] = ldsf(g.a[KS] + 2048); else { const int nb = i == 0 ? 0 : i - 2; f.w[nb] = wfrag(g, KS, nb); }
}
template <int KSN, bool NEXT, class Hook>
DI void q_kstep(f32x4 (&acc)[8][2], const QFr& cur, QFr& nxt, const GAddr& gn, Hook hook) {
#pragma unroll
    for (int nb = 0; nb < 8; ++nb) {
        MFMA16I(acc[nb][0], cur.w[nb], cur.x[0]); MFMA16I(acc[nb][1], cur.w[nb], cur.x[1]);
        SBAR();
        if (NEXT) { if (nb < 2) { q_read<KSN>(nxt, gn, 2 * nb); q_read<KSN>(nxt, gn, 2 * nb + 1); } else q_read<KSN>(nxt, gn, nb + 2); }
        hook(nb);
        SBAR();
    }
}
template <int KT, class Pub>
DI void q_tile(const ProjCtx& c, bool early, GAddr& g, f32x4 (&acc)[8][2], QFr& f0, QFr& f1, const u32x4 (&pk)[16], Pub after, Stamper& stamp) {
    q_kstep<1, true>(acc, f0, f1, g, [&](int nb) {
        if (KT >= 1 && KT + 2 < 8 && nb < 3) issueQ(c, KT + 2, 3 + nb);
        if (nb == 4) { if (early) { if (KT < 2) kv_store(c, pk, 12 + 2 * (KT < 2 ? KT : 0)); } else if (KT < 5) kv_store(c, pk, 11 + (KT < 5 ? KT : 0)); }
        if (nb == 7 && early && KT < 2) kv_store(c, pk, 13 + 2 * (KT < 2 ? KT : 0)); });
    if constexpr (KT < 7) {
        constexpr int WE = KT == 0 ? 17 : (KT == 1 ? 19 : (KT == 2 ? 8 : (KT <= 5 ? 6 : 0)));
        constexpr int WL = KT == 0 ? 17 : (KT == 1 ? 18 : (KT <= 4 ? 8 : (KT == 5 ? 7 : 0)));
        const bool w7 = KT == 5 && c.wid == 7;
        if (early) { if (w7) wait_bar<WE + 16>(); else wait_bar<WE>(); } else { if (w7) wait_bar<WL + 16>(); else wait_bar<WL>(); }
        stamp();
        after(KT);
        gaddr_add(g, (unsigned)(((KT + 1) % 3 - KT % 3) * 49152));
        q_kstep<0, true>(acc, f1, f0, g, [&](int nb) { if (KT + 3 < 8 && nb < 3) issueQ(c, KT + 3, nb); });
    } else q_kstep<0, false>(acc, f1, f0, g, NoHook{});
}

DI void proj_setup(ProjCtx& c, const u16* X, const u16* Ckv, const u16* Wq, const u16* Wuk, const u16* Wuv, u16* Kf, u16* Vf, unsigned lds0, int h, int j, int wid, int lane) {
    const int wr = wid >> 2, wc = wid & 3;
    c.wid = wid; c.isV = wc >> 1; c.lds0 = lds0; c.rot = KROT ? (2 * (h & 3) + j) & 7 : 0;
    c.srdC = mk_srd(Ckv, 2097152); c.srdX = mk_srd(X, 2097152);
    c.srdW = mk_srd((wid < 4 ? Wuk : Wuv), 4194304); c.srdWq = mk_srd(Wq, 4194304);
    c.srdO = mk_srd((const char*)(c.isV ? Vf : Kf) + (size_t)h * 524288, 524288);
#pragma unroll
    for (int p = 0; p < 2; ++p) {
        c.va[p] = (lane >> 3) * 1024 + (((lane & 7) ^ (4 * p + (lane >> 4))) * 16);
        c.vw[p] = ((lane >> 2) & 7) * 8192 + (4 * (lane >> 5) + ((lane & 3) ^ ((2 * p + ((lane >> 4) & 1)) & 3))) * 16;
    }
    c.vwq = (wid & 1) ? c.vw[1] : c.vw[0];
    c.l128 = lane * 128;
    c.sA = (256 * j + 32 * wid) * 1024;
    c.sW = (16 * (wid & 3)) * 8192 + h * 256;
    c.sXa = (wid < 4 ? 128 * (15 - j) + 32 * wid : 128 * j + 32 * (wid - 4)) * 1024;
    c.sWq = (8 * wid) * 8192 + h * 256;
    c.vo = lane * 16 + (4 * j + 2 * wr) * 16384 + (wc & 1) * (c.isV ? 8192 : 2048);
}
template <class Touch, class IssueB1>
DI void kv_run(const ProjCtx& c, int lane, u32x4 (&pk)[16], Touch touchk, IssueB1 issueB1, Stamper& stamp) {
    const int wid = c.wid, wr = wid >> 2, wc = wid & 3;
    GAddr g; gaddr_a(g, c.lds0, 128 * wr, lane); gaddr_w(g, c.lds0 + 32768 + (wc >> 1) * 16384 + (wc & 1) * 1024, lane);
    if (c.isV) kv_part<true>(c, g, pk, touchk, issueB1, stamp); else kv_part<false>(c, g, pk, touchk, issueB1, stamp);
}
template <class Pub>
DI void q_run(const ProjCtx& c, bool early, int lane, const u32x4 (&pk)[16], bf16x8 (&q)[8], Pub after, Stamper& stamp) {
    GAddr g; gaddr_a(g, c.lds0, 32 * c.wid, lane); gaddr_w(g, c.lds0 + 32768, lane); gaddr_add(g, 0u);
    f32x4 acc[8][2];
#pragma unroll
    for (int a = 0; a < 8; ++a) { acc[a][0] = f32x4{}; acc[a][1] = f32x4{}; PIN(acc[a][0]); PIN(acc[a][1]); }
    QFr f0, f1;
#pragma unroll
    for (int i = 0; i < 10; ++i) q_read<0>(f0, g, i);
    q_tile<0>(c, early, g, acc, f0, f1, pk, after, stamp); q_tile<1>(c, early, g, acc, f0, f1, pk, after, stamp); q_tile<2>(c, early, g, acc, f0, f1, pk, after, stamp); q_tile<3>(c, early, g, acc, f0, f1, pk, after, stamp);
    q_tile<4>(c, early, g, acc, f0, f1, pk, after, stamp); q_tile<5>(c, early, g, acc, f0, f1, pk, after, stamp); q_tile<6>(c, early, g, acc, f0, f1, pk, after, stamp); q_tile<7>(c, early, g, acc, f0, f1, pk, after, stamp);
    MFMA_DRAIN();
    constexpr float QSC = 0.08838834764831845f * 1.4426950408889634f;
#pragma unroll
    for (int qb = 0; qb < 2; ++qb)
#pragma unroll
        for (int ds = 0; ds < 4; ++ds) { const f32x4 lo = acc[2 * ds][qb] * QSC, hi = acc[2 * ds + 1][qb] * QSC;
            q[qb * 4 + ds] = __builtin_bit_cast(bf16x8, (u32x4){cvtpk(lo[0], lo[1]), cvtpk(lo[2], lo[3]), cvtpk(hi[0], hi[1]), cvtpk(hi[2], hi[3])}); }
}
template <int OFF, bool SETM0> DI void dma_piece(unsigned m0v, unsigned voff, srd_t srd, unsigned soff) {
    if constexpr (SETM0) asm volatile("s_mov_b32 m0, %0\n\ts_nop 0\n\tbuffer_load_dwordx4 %1, %2, %3 offen offset:%4 sc1 lds" :: "s"(m0v), "v"(voff), "s"(srd), "s"(soff), "i"(OFF) : "memory");
    else asm volatile("buffer_load_dwordx4 %0, %1, %2 offen offset:%3 sc1 lds" :: "v"(voff), "s"(srd), "s"(soff), "i"(OFF) : "memory");
}
#define ATT_THRV 64.f
constexpr float ATT_THR = ATT_THRV;
#define KSPLIT 0
#define ATT_WIN 5
constexpr int ATT_W = ATT_WIN;

struct AttnState { f32x4 o[8][2]; f32x4 s[4][2]; f32x4 negm[2]; bf16x8 q[8]; u32x4 p[2][2]; f32x4 l[2]; f32x2 mhat; u32x4 ones; };
struct AttnCtx {
    srd_t srdK, srdV, srdC; unsigned voff, lane16, kslot0, vslot0, coffL;
    int g, nl  , thrg, dq0, relH, jump; unsigned qstash;
    unsigned soA, soB, ksoM; int rel;
    srd_t srdF; u64 tok, prev; unsigned known;
};
DI u64 ld_flag8(const AttnCtx& c) { return __builtin_bit_cast(u64, __builtin_amdgcn_raw_buffer_load_b64(c.srdF, (c.lane16 & 0x70u) << 3, 0, 16)); }

DI unsigned m_off(int i) { return i < 16 ? (unsigned)(((i & 7) * 2 + (i >> 3)) * 1024) : (unsigned)((((i - 16) & 3) * 4 + ((i - 16) >> 2)) * 1024); }
template <bool DO_PV, bool DO_QK, int KP, int VP, class Hook>
DI void m_phase(AttnState& st, unsigned kbase, unsigned vbase, bf16x8 (&pre)[ATT_W], Hook hook) {
    constexpr int N0 = DO_PV ? 0 : 16, N1 = DO_QK ? 32 : 16;
    bf16x8 fr[32];
#pragma unroll
    for (int i = 0; i < ATT_W; ++i) fr[N0 + i] = pre[i];
#pragma unroll
    for (int i = N0; i < N1; ++i) {
        const int n = i + ATT_W;
        if (n < N1) fr[n] = ldsf((n < 16 ? vbase + VP * 16384 : kbase + KP * 16384) + m_off(n));
        SBAR();
        if (i >= 16) { const int ds = (i - 16) >> 2, kb = (i - 16) & 3;
            if (ds == 0) { asm volatile("v_mfma_f32_16x16x32_bf16 %0, %2, %3, %1" : "=&v"(st.s[kb][0]), "+v"(st.negm[0]) : "v"(fr[i]), "v"(st.q[0]));
                           asm volatile("v_mfma_f32_16x16x32_bf16 %0, %2, %3, %1" : "=&v"(st.s[kb][1]), "+v"(st.negm[1]) : "v"(fr[i]), "v"(st.q[4])); }
            else { MFMA16I(st.s[kb][0], fr[i], st.q[ds]); MFMA16I(st.s[kb][1], fr[i], st.q[4 + ds]); }
        } else { const int ks = i >> 3, db = i & 7;
            MFMA16I(st.o[db][0], fr[i], st.p[ks][0]); MFMA16I(st.o[db][1], fr[i], st.p[ks][1]);
            if (db == 3) { MFMA16I(st.l[0], st.ones, st.p[ks][0]); MFMA16I(st.l[1], st.ones, st.p[ks][1]); } }
        SBAR();
        hook(i);
        SBAR();
    }
    MFMA_DRAIN();
    PIN(st.negm[0]); PIN(st.negm[1]);
}
struct NoHookI { DI void operator()(int) const {} };

DI void store_ctx(const f32x4 (&o)[8][2], float inv0, float inv1, srd_t srd, unsigned voff) {
    PIN(voff);
#pragma unroll
    for (int p = 0; p < 4; ++p)
#pragma unroll
        for (int qb = 0; qb < 2; ++qb) { const float inv = qb ? inv1 : inv0; const f32x4 lo = o[2 * p][qb] * inv, hi = o[2 * p + 1][qb] * inv;
            __builtin_amdgcn_raw_buffer_store_b128((u32x4){cvtpk(lo[0], lo[1]), cvtpk(lo[2], lo[3]), cvtpk(hi[0], hi[1]), cvtpk(hi[2], hi[3])}, srd, voff + p * 2048 + qb * 256, 0, 16); }
}
DI float row_add(float x) { return swap_add(x16_add(x)); }
DI float row_max4(float x) { return swap_max(x16_max(x)); }

template <bool LAST, int PAR>
DI void v_phase(AttnState& st, AttnCtx& c, const int t) {
    const bool sw = (t == c.nl);
    const unsigned vm0 = c.vslot0 + (PAR ^ 1) * 16384;
    const unsigned vso = c.soA, kso = c.soB;
    if (!LAST && __builtin_expect(c.known != 0xffu, 0)) {
        c.known |= (unsigned)__builtin_amdgcn_ballot_w64(c.prev == c.tok) & 0xffu;
        const unsigned need = 1u << ((kso >> 16) & 7);
        if (kso < 524288u && !(c.known & need)) {
            for (unsigned spins = 0; spins < (1u << 15); ++spins) { const u64 v = ld_flag8(c); c.known |= (unsigned)__builtin_amdgcn_ballot_w64(v == c.tok) & 0xffu; if (c.known & need) break; __builtin_amdgcn_s_sleep(1); } }
        asm volatile("" ::: "memory");
        c.prev = ld_flag8(c);
    }
    SBAR();
    if (__builtin_expect(sw, 0)) { store_ctx(st.o, __builtin_amdgcn_rcpf(st.l[0][0]), __builtin_amdgcn_rcpf(st.l[1][0]), c.srdC, ((c.lane16 >> 8) << 9) + (c.lane16 & 0xf0u) + c.coffL);
#pragma unroll
        for (int d = 0; d < 8; ++d) { st.o[d][0] = f32x4{}; st.o[d][1] = f32x4{}; PIN(st.o[d][0]); PIN(st.o[d][1]); }
        st.l[0] = f32x4{}; st.l[1] = f32x4{}; PIN(st.l[0]); PIN(st.l[1]); c.rel = c.relH; }
    if (__builtin_expect(c.rel > c.thrg, 0)) {
        int thr = 32 * c.g - c.rel + c.dq0; PIN(thr); const float NEG = -__builtin_inff();
#pragma unroll
        for (int kb = 0; kb < 4; ++kb)
#pragma unroll
            for (int qb = 0; qb < 2; ++qb)
#pragma unroll
                for (int e = 0; e < 4; ++e) if (16 * kb - 16 * qb + e > thr) st.s[kb][qb][e] = NEG;
    }
    SBAR();
    if (!LAST) dma_piece<0, true>(vm0, c.voff, c.srdV, vso);
#define MX3(a, b, c) __builtin_fmaxf(__builtin_fmaxf((a), (b)), (c))
    float mx[2];
#pragma unroll
    for (int qb = 0; qb < 2; ++qb) {
        float a = MX3(st.s[0][qb][0], st.s[0][qb][1], st.s[0][qb][2]), b = MX3(st.s[0][qb][3], st.s[1][qb][0], st.s[1][qb][1]);
        a = MX3(a, st.s[1][qb][2], st.s[1][qb][3]); b = MX3(b, st.s[2][qb][0], st.s[2][qb][1]); a = MX3(a, st.s[2][qb][2], st.s[2][qb][3]);
        b = MX3(b, st.s[3][qb][0], st.s[3][qb][1]); a = MX3(a, st.s[3][qb][2], st.s[3][qb][3]); mx[qb] = __builtin_fmaxf(a, b); }
    const unsigned long long firstm = (t == 0 || sw) ? ~0ull : 0ull;
    if (__builtin_expect((__builtin_amdgcn_ballot_w64(__builtin_fmaxf(mx[0], mx[1]) > ATT_THR) | firstm) != 0ull, 0)) {
        const bool first = firstm != 0ull;
#pragma unroll
        for (int qb = 0; qb < 2; ++qb) {
            const float delta = __builtin_fmaxf(row_max4(mx[qb]), first ? -1e30f : 0.f);
            st.mhat[qb] = (first ? 0.f : st.mhat[qb]) + delta;
#pragma unroll
            for (int kb = 0; kb < 4; ++kb) st.s[kb][qb] -= delta;
            st.negm[qb] = (f32x4){-st.mhat[qb], -st.mhat[qb], -st.mhat[qb], -st.mhat[qb]};
            if (!first) { const float f = __builtin_amdgcn_exp2f(-delta); st.l[qb] *= f;
#pragma unroll
                for (int d = 0; d < 8; ++d) st.o[d][qb] *= f; } }
    }
    SBAR();
    float e[8][4];
#define EXPG(k) do { _Pragma("unroll") for (int i_ = 0; i_ < 4; ++i_) e[k][i_] = __builtin_amdgcn_exp2f(st.s[(k) >> 1][(k) & 1][i_]); } while (0)
#define FING(k) do { constexpr int kb_ = (k) >> 1, qb_ = (k) & 1; \
        unsigned w0_ = cvtpk(e[k][0], e[k][1]), w1_ = cvtpk(e[k][2], e[k][3]); PIN(w0_); PIN(w1_); \
        st.p[kb_ >> 1][qb_][2 * (kb_ & 1)] = w0_; st.p[kb_ >> 1][qb_][2 * (kb_ & 1) + 1] = w1_; } while (0)
    EXPG(0); SBAR();
    EXPG(1); FING(0); SBAR();
    if (!LAST) dma_piece<1024, false>(vm0, c.voff, c.srdV, vso);
    EXPG(2); FING(1); SBAR();
    EXPG(3); FING(2); SBAR();
    if (!LAST) dma_piece<2048, false>(vm0, c.voff, c.srdV, vso);
    EXPG(4); FING(3); SBAR();
    EXPG(5); FING(4); SBAR();
    if (!LAST) dma_piece<3072, false>(vm0, c.voff, c.srdV, vso);
    EXPG(6); FING(5); SBAR();
    EXPG(7); FING(6); SBAR();
    FING(7);
#undef EXPG
#undef FING
    if (!LAST) {
        if (__builtin_expect(t == c.nl - 1, 0)) {
            unsigned qs = c.qstash; PIN(qs);
#pragma unroll
            for (int s = 0; s < 8; ++s) st.q[s] = ldsf(qs + s * 1024);
            st.negm[0] = f32x4{}; st.negm[1] = f32x4{}; PIN(st.negm[0]); PIN(st.negm[1]);
        }
        c.ksoM = kso;
        c.soA = c.soB; c.soB += (t + 3 == c.nl) ? c.jump : 16384; c.rel += 64;
    }
    SBAR();
}

DI void attn_setup(AttnCtx& c, const u16* Kf, const u16* Vf, u16* Cf, unsigned lds0, int h, int j, int wid, int lane) {
    PIN(lane);
    const int grp = wid >> 2; c.g = wid & 3; c.dq0 = (lane & 15) - 4 * (lane >> 4);
    const int qbH = 15 - j, qbL = j, nl = 2 * j + 2;
    c.nl = grp ? nl : 1000; c.thrg = 32 * c.g - 63; c.jump = (18 - nl) * 16384; c.relH = 64 * (17 - 2 * qbH);
    c.rel = grp ? -128 * qbL : -128 * qbH;
    c.soA = 16384; c.soB = (c.nl == 2) ? 17 * 16384 : 2 * 16384;
    c.srdK = mk_srd((const char*)Kf + (size_t)h * 524288, 524288);
    c.srdV = mk_srd((const char*)Vf + (size_t)h * 524288, 524288);
    c.srdC = mk_srd((const char*)Cf, 16777216);
    c.lane16 = lane * 16; c.voff = lane * 16 + c.g * 4096;
    c.kslot0 = lds0 + grp * 65536 + c.g * 4096; c.vslot0 = c.kslot0 + 32768;
    c.coffL = ((qbL * 4 + c.g) * 32 + h) * 8192;
    c.qstash = lds0 + 131072 + c.g * 8192 + lane * 16;
}
#define DMA4(m_, vo_, srd, so_) do { dma_piece<0, true>(m_, vo_, srd, so_); dma_piece<1024, false>(m_, vo_, srd, so_); dma_piece<2048, false>(m_, vo_, srd, so_); dma_piece<3072, false>(m_, vo_, srd, so_); } while (0)
DI void attn_prime(const AttnCtx& c, int grp) { DMA4(grp ? c.vslot0 + 16384 : c.kslot0, c.voff, c.srdK, 0u); DMA4(c.vslot0, c.voff, c.srdV, 0u); if (grp == 0) DMA4(c.kslot0 + 16384, c.voff, c.srdK, 16384u); }

template <class First, class G0Pre, class G0Tail>
DI void attn_run(AttnState& st, AttnCtx& c, unsigned lds0, int h, int j, int wid, int lane, First afterFirst, G0Pre g0pre, G0Tail g0tail, Stamper& stamp) {
    PIN(lane);
    const int grp = wid >> 2;
    const unsigned kbase = lds0 + grp * 65536 + lane * 16;
    const unsigned vbase = lds0 + grp * 65536 + 32768 + lane * 16;
#pragma unroll
    for (int d = 0; d < 8; ++d) { st.o[d][0] = f32x4{}; st.o[d][1] = f32x4{}; PIN(st.o[d][0]); PIN(st.o[d][1]); }
    st.negm[0] = f32x4{}; st.negm[1] = f32x4{}; st.l[0] = f32x4{}; st.l[1] = f32x4{}; st.mhat = (f32x2){0.f, 0.f};
    st.ones = (u32x4){0x3f803f80u, 0x3f803f80u, 0x3f803f80u, 0x3f803f80u}; PIN(st.ones); PIN(st.l[0]); PIN(st.l[1]);
    PIN(st.negm[0]); PIN(st.negm[1]);
    if (grp == 1) { DMA4(c.kslot0 + 16384, c.voff, c.srdK, 16384u); BARRIER(); }
    wait_bar<8>();
    stamp();
    bf16x8 pre[ATT_W];
#define PRE_V(P) do { _Pragma("unroll") for (int i = 0; i < ATT_W; ++i) pre[i] = ldsf(vbase + (P) * 16384 + m_off(i)); } while (0)
    const unsigned kbase0 = grp ? kbase + 49152 : kbase;
#pragma unroll
    for (int i = 0; i < ATT_W; ++i) pre[i] = ldsf(kbase0 + m_off(16 + i));
    __builtin_amdgcn_s_setprio(1);
    m_phase<false, true, 0, 0>(st, kbase0, vbase, pre, NoHookI{});
    __builtin_amdgcn_s_setprio(0);
    wait_bar<0>();
    afterFirst();
#define PSTAMP(t) do {} while (0)
#define STEP(PAR, t) do { \
        v_phase<false, PAR>(st, c, t); \
        PRE_V(PAR); \
        PSTAMP(t); \
        wait_bar_keep<(KSPLIT ? 6 : 4)>(); \
        PSTAMP(t); \
        __builtin_amdgcn_s_setprio(1); \
        m_phase<true, true, PAR ^ 1, PAR>(st, kbase, vbase, pre, [&](int i) { \
            if (!KSPLIT) { if (i == 3) dma_piece<0, true>(c.kslot0 + PAR * 16384, c.voff, c.srdK, c.ksoM); if (i == 9) dma_piece<1024, false>(c.kslot0 + PAR * 16384, c.voff, c.srdK, c.ksoM); } \
            if (i == (KSPLIT ? 5 : 15)) dma_piece<2048, KSPLIT>(c.kslot0 + PAR * 16384, c.voff, c.srdK, c.ksoM); if (i == (KSPLIT ? 17 : 21)) dma_piece<3072, false>(c.kslot0 + PAR * 16384, c.voff, c.srdK, c.ksoM); }); \
        __builtin_amdgcn_s_setprio(0); \
        PSTAMP(t); \
        wait_bar<4>(); \
        PSTAMP(t); } while (0)
    for (int t = 0; t < 16; t += 2) { stamp(); STEP(0, t); STEP(1, t + 1); }
    stamp();
    v_phase<true, 0>(st, c, 16);
    if (grp == 0) g0pre();
    PRE_V(0);
    asm volatile("s_barrier" ::: "memory");
    __builtin_amdgcn_s_setprio(1);
    m_phase<true, false, 0, 0>(st, kbase, vbase, pre, NoHookI{});
    __builtin_amdgcn_s_setprio(0);
    wait_bar<0>();
    stamp();
    PIN(lane);
    const unsigned xo = lds0 + grp * 65536 + c.g * 8192 + lane * 16;
    const unsigned xi = lds0 + (grp ^ 1) * 65536 + c.g * 8192 + lane * 16;
    const unsigned so = lds0 + grp * 65536 + 32768 + c.g * 512 + lane * 8, si = lds0 + (grp ^ 1) * 65536 + 32768 + c.g * 512 + lane * 8;
    const unsigned voff = (lane >> 4) * 512 + (lane & 15) * 16 + (((15 - j) * 4 + c.g) * 32 + h) * 8192;
#define MERGE_PUT(qb) do { _Pragma("unroll") for (int d = 0; d < 8; ++d) *(LAS f32x4*)(xo + d * 1024) = st.o[d][qb]; *(LAS f32x2*)so = (f32x2){st.mhat[qb], st.l[qb][0]}; } while (0)
#define MERGE_FIN(qb) do { const f32x2 ot = *(const LAS f32x2*)si; const float mh = st.mhat[qb], lt = st.l[qb][0]; \
        const float m = __builtin_fmaxf(ot[0], mh), f0 = __builtin_amdgcn_exp2f(ot[0] - m), f1 = __builtin_amdgcn_exp2f(mh - m); \
        const float inv = __builtin_amdgcn_rcpf(ot[1] * f0 + lt * f1), a0 = f0 * inv, a1 = f1 * inv; \
        _Pragma("unroll") for (int p = 0; p < 4; ++p) { \
            const f32x4 lo = st.o[2 * p][qb] * a1 + *(const LAS f32x4*)(xi + (2 * p) * 1024) * a0, hi = st.o[2 * p + 1][qb] * a1 + *(const LAS f32x4*)(xi + (2 * p + 1) * 1024) * a0; \
            __builtin_amdgcn_raw_buffer_store_b128((u32x4){cvtpk(lo[0], lo[1]), cvtpk(lo[2], lo[3]), cvtpk(hi[0], hi[1]), cvtpk(hi[2], hi[3])}, c.srdC, voff + p * 2048 + (qb) * 256, 0, 16); } } while (0)
    if (grp == 0) { MERGE_PUT(1); BARRIER(); g0tail(); BARRIER(); MERGE_FIN(0); asm volatile("; merge 0"); }
    else          { asm volatile("; merge 1"); MERGE_PUT(0); BARRIER(); MERGE_FIN(1); }
#undef MERGE_PUT
#undef MERGE_FIN
    stamp();
#undef PRE_V
#undef STEP
}
constexpr int OUT_LDS = 163840;
constexpr unsigned OUT_O = 131072;
struct OutCtx { srd_t srdC, srdW, srdB; unsigned lane16, vw0, vwA, sC, sW, sB, lds0; int wid, rot; };
DI void out_issueA(const OutCtx& c, int kt, int i) {
    const unsigned st = c.lds0 + (kt & 3) * 32768;
    const int kr = (kt + c.rot) & 15;
    if (i < 2) { const int p = 2 * c.wid + i;
        dma1s(st + p * 1024, c.lane16, c.srdC, c.sC + (p >> 2) * 262144 + (p & 3) * 1024 + kr * 4096); }
    else dma1(st + 16384 + (2 * c.wid + (i - 2)) * 1024, c.vwA, c.srdW, c.sW + (8 * c.wid) * 1024 + (i - 2) * 128 + kr * 65536);
}
DI void out_issueB(const OutCtx& c, int ch, int i) {
    const int rg = 2 * c.wid + (i >> 1);
    dma1(c.lds0 + ch * 32768 + rg * 2048 + (i & 1) * 1024, (i >> 1) ? (c.vw0 ^ 32u) : c.vw0, c.srdB, c.sB + (ch * 128 + 8 * rg) * 1024 + (i & 1) * 128);
}
DI u32x4 out_pack(f32x4 s0, f32x4 s1) {
    unsigned a0 = cvtpk(s0[0], s0[1]), a1 = cvtpk(s0[2], s0[3]), b0 = cvtpk(s1[0], s1[1]), b1 = cvtpk(s1[2], s1[3]);
    auto x0 = __builtin_amdgcn_permlane32_swap(a0, b0, false, false); auto x1 = __builtin_amdgcn_permlane32_swap(a1, b1, false, false);
    return (u32x4){x0[0], x1[0], x0[1], x1[1]};
}
DI f32x4 quad(const f32x16& x, int rq) { return (f32x4){x[4 * rq], x[4 * rq + 1], x[4 * rq + 2], x[4 * rq + 3]}; }

DI void out_phase(const u16* Wo, const u16* Bwo, u16* out, char* ws, u64 tok, unsigned lds0, int bid, int wid, int lane, Stamper& stamp) {
    PIN(lane);
    const int tid = wid * 64 + lane, r32 = lane & 31, hi = lane >> 5, wr = wid >> 2, wc = wid & 3;
    const int x = bid & 7, u = bid >> 3, s = x >> 1, rb = 8 * (x & 1) + (u >> 2), cb = u & 3, tile = rb * 4 + cb;
    OutCtx c; c.wid = wid; c.lds0 = lds0; c.lane16 = lane * 16;
    c.srdC = mk_srd(ws + WS_CF + (size_t)rb * 4 * 262144, 4 * 262144); c.srdW = mk_srd(Wo, 4194304); c.srdB = mk_srd(Bwo, 524288);
    c.vw0 = ((lane >> 2) & 7) * 1024 + (4 * (lane >> 5) + ((lane & 3) ^ ((lane >> 4) & 1))) * 16; c.vwA = (wid & 1) ? (c.vw0 ^ 32u) : c.vw0;
    c.rot = 4 * cb + 2 * (rb & 7);
    c.sC = s * 8 * 8192; c.sW = s * 1024 * 1024 + cb * 256; c.sB = cb * 256;
#pragma unroll
    for (int kt = 0; kt < 4; ++kt) { out_issueA(c, kt, 2); out_issueA(c, kt, 3); }
    u64* flags = (u64*)(ws + WS_FLAG);
    {
        const int l = lane & 15, hh = 8 * s + (l & 7), jj = rb < 8 ? rb : 15 - rb;
        const int src = l < 8 ? ((hh >> 2) | (jj << 3) | ((hh & 3) << 6)) : (8 * (2 * rb + (s >> 1)) + (l & 7));
        const u64* fp = flags + (FL_C + src) * FL_STRIDE;
        for (unsigned spins = 0; spins < (1u << 15); ++spins) { const u64 v = ld_agent(fp); if (__all(v == tok)) break; __builtin_amdgcn_s_sleep(1); }
        asm volatile("" ::: "memory"); }
    stamp();
#pragma unroll
    for (int kt = 0; kt < 4; ++kt) { out_issueA(c, kt, 0); out_issueA(c, kt, 1); }
    { const srd_t srdO = mk_srd(ws + WS_OF + (size_t)(rb * 4 + s) * 32768, 32768);
#pragma unroll
      for (int i = 0; i < 4; ++i) dma1s(lds0 + OUT_O + (4 * wid + i) * 1024, c.lane16, srdO, (unsigned)((4 * wid + i) * 1024)); }
    const int q4 = (lane & 15) >> 2, p4 = lane & 3, blk = (lane >> 4) & 1;
    const unsigned aaddr = lds0 + (2 * wr) * 4096 + c.lane16;
    unsigned waA[2], waddr[2];
#pragma unroll
    for (int t = 0; t < 2; ++t) { waA[t] = lds0 + 2048 * t + 512 * wc + 64 * (4 * hi + q4) + 16 * ((2 * blk + (p4 >> 1)) ^ ((2 * t + hi) & 3)) + 8 * (p4 & 1);
                                  waddr[t] = waA[t]; }
    f32x16 acc[2] = {f32x16{}, f32x16{}};
    stamp();
    struct Fr { bf16x8 a[2][2]; bf16x4 w[2][2]; };
    auto rd = [&](Fr& f, unsigned st, int half) {
        unsigned w0 = waA[0] + st + 16384 + half * 8192, w1 = waA[1] + st + 16384 + half * 8192, aa = aaddr + st; PIN(w0); PIN(w1); PIN(aa);
#pragma unroll
        for (int k = 0; k < 2; ++k) { const int ks = 2 * half + k; const unsigned wb = k ? w1 : w0;
            f.w[k][0] = ldstr(wb); f.w[k][1] = ldstr(wb + 4096);
            f.a[k][0] = ldsf(aa + ks * 1024); f.a[k][1] = ldsf(aa + 4096 + ks * 1024); } };
    Fr f0, f1;
    wait_bar<10>();
    rd(f0, 0u, 0);
    auto step = [&](int kt, auto late, auto early) {
        const unsigned st = (kt & 3) * 32768, stn = ((kt + 1) & 3) * 32768;
        rd(f1, st, 1);
        SBAR();
#pragma unroll
        for (int k = 0; k < 2; ++k) { const bf16x8 wf = cat4(f0.w[k][0], f0.w[k][1]);
            acc[0] = MFMA32(wf, f0.a[k][0], acc[0]); acc[1] = MFMA32(wf, f0.a[k][1], acc[1]); SBAR(); late(2 + k); SBAR(); }
        wait_bar<8>();
        rd(f0, stn, 0);
        SBAR();
#pragma unroll
        for (int k = 0; k < 2; ++k) { const bf16x8 wf = cat4(f1.w[k][0], f1.w[k][1]);
            acc[0] = MFMA32(wf, f1.a[k][0], acc[0]); acc[1] = MFMA32(wf, f1.a[k][1], acc[1]); SBAR(); early(k); SBAR(); }
    };
    step(0, [&](int) {}, [&](int i) { out_issueA(c, 4, i); });
    for (int kt = 1; kt < 12; ++kt) step(kt, [&](int i) { out_issueA(c, kt + 3, i); }, [&](int i) { out_issueA(c, kt + 4, i); });
    step(12, [&](int i) { out_issueA(c, 15, i); }, [&](int i) { out_issueB(c, 0, i); });
    step(13, [&](int i) { out_issueB(c, 0, i); }, [&](int i) { out_issueB(c, 1, i); });
    step(14, [&](int i) { out_issueB(c, 1, i); }, [&](int i) { out_issueB(c, 2, i); });
    step(15, [&](int i) { out_issueB(c, 2, i); }, [&](int i) { out_issueB(c, 3, i); });
    out_issueB(c, 3, 2); out_issueB(c, 3, 3);
    stamp();
    const srd_t srdX = mk_srd(ws + WS_XCH + (size_t)tile * 262144, 262144);
#pragma unroll
    for (int mb = 0; mb < 2; ++mb) { const int q = 2 * wr + mb;
        if (q != s) {
#pragma unroll
            for (int rq = 0; rq < 4; ++rq) store16_sc1(__builtin_bit_cast(u32x4, quad(acc[mb], rq)), c.lane16, srdX, (unsigned)(((q * 4 + s) * 4 + wc) * 4096 + rq * 1024)); } }
    wait_bar<0>();
    if (tid == 0) st_agent(flags + (FL_XF + tile * 4 + s) * FL_STRIDE, tok);
    stamp();
    const int row = rb * 128 + 32 * s + r32;
    if (wr == (s >> 1)) {
        const u64* fp = flags + (FL_XF + tile * 4 + (lane & 3)) * FL_STRIDE;
        for (unsigned spins = 0; spins < (1u << 15); ++spins) { const u64 v = ld_agent(fp); if (__all(v == tok)) break; __builtin_amdgcn_s_sleep(1); }
        asm volatile("" ::: "memory");
        f32x4 p[4][4];
#pragma unroll
        for (int q = 0; q < 4; ++q)
#pragma unroll
            for (int rq = 0; rq < 4; ++rq) p[q][rq] = __builtin_bit_cast(f32x4, __builtin_amdgcn_raw_buffer_load_b128(srdX, (q == s ? 0x7ffffff0u : c.lane16) + rq * 1024, ((s * 4 + q) * 4 + wc) * 4096, 16));
        f32x4 r[4];
#pragma unroll
        for (int rq = 0; rq < 4; ++rq) r[rq] = ((p[0][rq] + p[1][rq]) + (p[2][rq] + p[3][rq])) + ((s & 1) ? quad(acc[1], rq) : quad(acc[0], rq));
        u16* o = out + (size_t)row * 1024 + cb * 128 + 32 * wc + 8 * hi;
        *(u32x4*)o = out_pack(r[0], r[1]); *(u32x4*)(o + 16) = out_pack(r[2], r[3]);
    } else {
        f32x16 accB[2] = {f32x16{}, f32x16{}};
#pragma unroll
        for (int ch = 0; ch < 4; ++ch)
#pragma unroll
            for (int hf = 0; hf < 2; ++hf) {
                unsigned w0 = waddr[0] + ch * 32768 + hf * 16384, w1 = waddr[1] + ch * 32768 + hf * 16384, oa = lds0 + OUT_O + (8 * ch + 4 * hf) * 1024 + c.lane16; PIN(w0); PIN(w1); PIN(oa);
                bf16x8 a[4]; bf16x4 w[4][2];
#pragma unroll
                for (int ks = 0; ks < 4; ++ks) { w[ks][0] = ldstr(w0 + ks * 4096); w[ks][1] = ldstr(w1 + ks * 4096); a[ks] = ldsf(oa + ks * 1024); }
#pragma unroll
                for (int ks = 0; ks < 4; ++ks) accB[ks & 1] = MFMA32(cat4(w[ks][0], w[ks][1]), a[ks], accB[ks & 1]);
            }
        const f32x16 rB = accB[0] + accB[1];
        u16* o = out + (size_t)row * 1024 + 512 + cb * 128 + 32 * wc + 8 * hi;
        *(u32x4*)o = out_pack(quad(rB, 0), quad(rB, 1)); *(u32x4*)(o + 16) = out_pack(quad(rB, 2), quad(rB, 3));
    }
}
#define TLBWARM 0
#define CODEWARM 1
constexpr int FUSED_LDS = 163840;
constexpr unsigned WPHI_LDS = 156160;
struct FusedArgs { const u16 *X, *Ckv, *Wq, *Wuk, *Wuv, *Wo, *Bx, *Wqkv, *Wphi, *Bwo; char* ws; u16* out; };

__global__ __launch_bounds__(512, 2) void fused_kernel(FusedArgs a) {
    const int tid = threadIdx.x, lane = tid & 63;
    const int wid = rfl(tid >> 6);
    const int bid = blockIdx.x, h = (bid & 7) * 4 + (bid >> 6), j = (bid >> 3) & 7;
    const unsigned lds0 = 0u;
    Stamper stamp{0u, 0, lane};
    stamp();
    u16* Kf = (u16*)(a.ws + WS_KF); u16* Vf = (u16*)(a.ws + WS_VF); u16* Cf = (u16*)(a.ws + WS_CF);
    u16* PhiQ = (u16*)(a.ws + WS_PHIQ); u16* Of = (u16*)(a.ws + WS_OF); float* Bst = (float*)(a.ws + WS_BST);
    u64* flags = (u64*)(a.ws + WS_FLAG);
    float* Bred = (float*)(a.ws + WS_BRED);
    const int hg = bid & 7, rc = bid >> 3;
    asm volatile("" :: "s"(a.X), "s"(a.Ckv), "s"(a.Wq), "s"(a.Wuk), "s"(a.Wuv), "s"(a.Wphi), "s"(a.ws));
    ProjCtx pc; proj_setup(pc, a.X, a.Ckv, a.Wq, a.Wuk, a.Wuv, Kf, Vf, lds0, h, j, wid, lane);
#pragma unroll
    for (int i = 0; i < 8; ++i) issueKV(pc, 0, i);
#pragma unroll
    for (int i = 0; i < 8; ++i) issueKV(pc, 1, i);
    const unsigned tsc = lds0 + TOUCH_LDS;
    { const unsigned vW = (lane >> 4) * 524288 + (lane & 15) * 128;
      const char* base; unsigned vo = pc.l128;
      if (wid < 4) { base = (const char*)((wid & 1) ? a.Wuv : a.Wuk) + ((wid & 2) ? 4 : 2) * 524288 + bid * 2048 - ((wid & 2) ? 0 : 0); vo = vW; }
      else if (wid == 4) base = (const char*)a.Ckv + bid * 8192;
      else if (wid == 6) { u64 pc_; asm volatile("s_getpc_b64 %0" : "=s"(pc_));
          const int u = bid >> 3; base = (const char*)((pc_ & ~(u64)127) + (u < 6 ? u : 0) * 8192); vo = pc.l128; }
      else { base = (const char*)a.Ckv; vo = 0; }
      touch_g<1>(tsc, vo, base); }
    if (wid < 2) dma1(lds0 + WPHI_LDS + wid * 1024, lane * 16, mk_srd(a.Wphi, 16384), (unsigned)(hg * 2048 + wid * 1024));
    const u64 epoch = *(const u64 __attribute__((address_space(4)))*)(flags + FL_EPOCH * FL_STRIDE);
    u64 tok = mix64(epoch); if (tok == epoch) tok ^= 1ull;
    tok = ((u64)rfl((unsigned)(tok >> 32)) << 32) | rfl((unsigned)tok); asm volatile("" : "+s"(tok));
    B1Ctx bc; b1_setup(bc, a.Bx, a.Wqkv, lds0, rc, hg, wid, lane);
    stamp();
    auto touchk = [&](int kt) {
        const char* base = (const char*)a.Ckv; unsigned vo = 0;
        if (kt == 0) {
            if (wid == 0) { base = (const char*)a.Bx + bid * 8192; vo = pc.l128; }
            else if (wid == 1) { base = (const char*)a.X + bid * 8192; vo = pc.l128; }
            else if (wid == 2) { base = (const char*)a.Wqkv + (bid < 192 ? bid : 0) * 8192; vo = pc.l128; }
        } else if (kt == 1) {
            if (wid < 2) { base = (const char*)a.Wq + (bid * 2 + wid) * 8192; vo = pc.l128; }
        } else if (kt == 2) {
            if (wid < 2) { base = (const char*)a.Wo + (bid * 2 + wid) * 8192; vo = pc.l128; }
            else if (wid == 2) { base = (const char*)a.Bwo + (bid & 63) * 8192; vo = pc.l128; }
        }
        touch_g<2>(tsc, vo, base); };
    u32x4 pk[16];
    kv_run(pc, lane, pk, touchk, [&](int kt) { b1_issue(bc, kt); }, stamp);
    stamp();
    B1Acc ba;
    const bool early = (j & 3) == 0;
    b1_gemm(bc, ba, lane, early, [&](int n) { if (early) kv_store(pc, pk, n); else if (n >= 1 && n <= 6) kv_store(pc, pk, n - 1); },
            [&](int n) {
#pragma unroll
                for (int i = 0; i < 6; ++i) issueQ(pc, n, i); });
    stamp();
    u32x2 wp; { const int l16 = lane & 15, g = lane >> 4; const LAS u16* w = (const LAS u16*)(lds0 + WPHI_LDS + (wid & 3) * 512);
        wp[0] = (unsigned)w[(4 * g) * 16 + l16] | ((unsigned)w[(4 * g + 1) * 16 + l16] << 16); wp[1] = (unsigned)w[(4 * g + 2) * 16 + l16] | ((unsigned)w[(4 * g + 3) * 16 + l16] << 16); }
    b1_phi(ba, [&](int n) { if (early) { if (n % 4 == 2) kv_store(pc, pk, 8 + n / 4); } else if (n % 3 == 1 && n < 14) kv_store(pc, pk, 6 + n / 3); }, wp, PhiQ, Bst, lds0, rc, hg, wid, lane, stamp);
    stamp();
    wait_bar<21>();
    stamp();
    AttnState st;
    AttnCtx c; attn_setup(c, Kf, Vf, Cf, lds0, h, j, wid, lane);
    B2a b2;
    auto spin = [&](const u64* fp) {
        for (unsigned spins = 0; spins < (1u << 15); ++spins) { const u64 v = ld_agent(fp); if (__all(v == tok)) break; __builtin_amdgcn_s_sleep(1); }
        asm volatile("" ::: "memory"); };
    const u64* fkv = flags + (FL_KV + 8 * h + (lane & 7)) * FL_STRIDE;
    const u64* fbs = flags + (FL_BS + 32 * hg + (lane & 31)) * FL_STRIDE;
    u64 pre = 0;
    q_run(pc, early, lane, pk, st.q, [&](int kt) {
        if (kt == 2) { if (tid == 0) st_agent(flags + (FL_BS + 32 * hg + rc) * FL_STRIDE, tok); }
        if (kt == 3) { if (early && tid == 0) st_agent(flags + (FL_KV + 8 * h + j) * FL_STRIDE, tok); }
        if (kt == 3) { if (wid == 7) pre = ld_agent(fbs); }
        if (kt == 4) {
            if (wid == 7) {
                if (!__all(pre == tok)) spin(fbs);
                asm volatile("" ::: "memory");
                b2a_load(b2, Bst, rc, hg, lane); } }
        if (kt == 5) { pre = ld_agent(fkv); }
        if (kt == 6) {
            if (!early && tid == 0) st_agent(flags + (FL_KV + 8 * h + j) * FL_STRIDE, tok);
            if (wid == 7) b2a_finish(b2, Bred, rc, hg, lane);
            c.known = (unsigned)__builtin_amdgcn_ballot_w64(pre == tok) & 0xffu;
            for (unsigned spins = 0; spins < (1u << 15) && !(c.known & 1u); ++spins) { const u64 v = ld_agent(fkv); c.known = (unsigned)__builtin_amdgcn_ballot_w64(v == tok) & 0xffu; if (!(c.known & 1u)) __builtin_amdgcn_s_sleep(1); }
            asm volatile("" ::: "memory");
            attn_prime(c, wid >> 2);
        } }, stamp);
    c.srdF = mk_srd(flags + (FL_KV + 8 * h) * FL_STRIDE, 1024); c.tok = tok; c.prev = 0;
    stamp();
    BARRIER();
    if (wid < 4) {
#pragma unroll
        for (int s = 0; s < 8; ++s) *(LAS bf16x8*)(c.qstash + s * 1024) = st.q[s]; }
    stamp();
    B2b<4> b2b;
    attn_run(st, c, lds0, h, j, wid, lane,
             [&]() { if (tid == 448) st_agent(flags + (FL_BR + 32 * hg + rc) * FL_STRIDE, tok); },
             [&]() { b2b_load<4>(b2b, PhiQ, Bred, flags + (FL_BR + 32 * hg + 8 * wid) * FL_STRIDE, tok, 4 * hg + wid, 4 * rc, lane); },
             [&]() { b2b_finish<4>(b2b, Of, 4 * hg + wid, 4 * rc, lane); }, stamp);
    wait_bar<0>();
    if (tid == 0) st_agent(flags + (FL_C + bid) * FL_STRIDE, tok);
    stamp();
    out_phase(a.Wo, a.Bwo, a.out, a.ws, tok, lds0, bid, wid, lane, stamp);
    if (bid == 0 && wid == 7) {
        bool ok = false;
        for (unsigned spins = 0; spins < (1u << 15) && !ok; ++spins) { bool all = true;
#pragma unroll
            for (int i = 0; i < 4; ++i) all &= ld_agent(flags + (FL_C + lane + 64 * i) * FL_STRIDE) == tok;
            ok = __all(all); if (!ok) __builtin_amdgcn_s_sleep(1); }
        if (lane == 0) st_agent(flags + FL_EPOCH * FL_STRIDE, tok);
    }
    stamp();
}

#define WS_END 1
#define WS_SHIFT(n) (WS_END && (n) >= ((size_t)160 << 20) ? (((n) - ((size_t)80 << 20)) & ~(size_t)0x1fffff) : (size_t)0)
extern "C" void kernel_launch(void* const* d_in, const int* in_sizes, int n_in, void* d_out, int out_size, void* d_ws, size_t ws_size, hipStream_t stream) {
    FusedArgs a;
    a.X = (const u16*)d_in[0]; a.Ckv = (const u16*)d_in[1]; a.Wq = (const u16*)d_in[2]; a.Wuk = (const u16*)d_in[3]; a.Wuv = (const u16*)d_in[4]; a.Wo = (const u16*)d_in[5];
    a.Bx = (const u16*)d_in[6]; a.Wqkv = (const u16*)d_in[7]; a.Wphi = (const u16*)d_in[8]; a.Bwo = (const u16*)d_in[9]; a.ws = (char*)d_ws + WS_SHIFT(ws_size); a.out = (u16*)d_out;
    (void)hipFuncSetAttribute((const void*)fused_kernel, hipFuncAttributeMaxDynamicSharedMemorySize, FUSED_LDS);
    fused_kernel<<<256, 512, FUSED_LDS, stream>>>(a);
}
```

```cpp
#include <hip/hip_runtime.h>
#include <stdint.h>
#include <type_traits>

typedef unsigned short u16;
typedef unsigned long long u64;
typedef __bf16 bf16x8 __attribute__((ext_vector_type(8)));
typedef __bf16 bf16x4 __attribute__((ext_vector_type(4)));
typedef __bf16 bf16x2 __attribute__((ext_vector_type(2)));
typedef short s16x4 __attribute__((ext_vector_type(4)));
typedef float f32x16 __attribute__((ext_vector_type(16)));
typedef float f32x4 __attribute__((ext_vector_type(4)));
typedef float f32x2 __attribute__((ext_vector_type(2)));
typedef unsigned u32x4 __attribute__((ext_vector_type(4)));
typedef unsigned u32x2 __attribute__((ext_vector_type(2)));
typedef __amdgpu_buffer_rsrc_t srd_t;
#define LAS __attribute__((address_space(3)))
#define GAS __attribute__((address_space(1)))
#define SBAR() __builtin_amdgcn_sched_barrier(0)
#define PIN(x) asm volatile("" : "+v"(x))
#define MFMA32(a, b, c) __builtin_amdgcn_mfma_f32_32x32x16_bf16((a), (b), (c), 0, 0, 0)
#define MFMA16(a, b, c) __builtin_amdgcn_mfma_f32_16x16x32_bf16((a), (b), (c), 0, 0, 0)
#define DI __device__ __forceinline__
#define MFMA16I(acc, a, b) asm volatile("v_mfma_f32_16x16x32_bf16 %0, %1, %2, %0" : "+v"(acc) : "v"(a), "v"(b))
#define MFMA_DRAIN() asm volatile("s_nop 7")
#pragma clang diagnostic ignored "-Wint-to-pointer-cast"

DI unsigned cvtpk(float lo, float hi) { f32x2 v = {lo, hi}; bf16x2 b = __builtin_convertvector(v, bf16x2); return __builtin_bit_cast(unsigned, b); }
DI unsigned rfl(unsigned x) { return __builtin_amdgcn_readfirstlane(x); }
DI float swap_max(float x) { auto rr = __builtin_amdgcn_permlane32_swap(__float_as_uint(x), __float_as_uint(x), false, false); return __builtin_fmaxf(__uint_as_float(rr[0]), __uint_as_float(rr[1])); }
DI float swap_add(float x) { auto rr = __builtin_amdgcn_permlane32_swap(__float_as_uint(x), __float_as_uint(x), false, false); return __uint_as_float(rr[0]) + __uint_as_float(rr[1]); }
DI srd_t mk_srd(const void* p, int bytes) { return __builtin_amdgcn_make_buffer_rsrc((void*)p, (short)0, bytes, 0x00020000); }
DI bf16x4 ldstr(unsigned a) { return __builtin_amdgcn_ds_read_tr16_b64_v4bf16((LAS bf16x4*)a); }
DI bf16x8 ldsf(unsigned a) { return *(const LAS bf16x8*)a; }
DI bf16x8 cat4(bf16x4 a, bf16x4 b) { return __builtin_shufflevector(a, b, 0, 1, 2, 3, 4, 5, 6, 7); }
#define SCALARIZE2(a, b) do {} while (0)
DI void dma1(unsigned m0v, unsigned voff, srd_t srd, unsigned soff) {
    SCALARIZE2(m0v, soff);
    asm volatile("s_mov_b32 m0, %0\n\ts_nop 0\n\tbuffer_load_dwordx4 %1, %2, %3 offen lds" :: "s"(m0v), "v"(voff), "s"(srd), "s"(soff) : "memory");
}
DI void dma1s(unsigned m0v, unsigned voff, srd_t srd, unsigned soff) {
    SCALARIZE2(m0v, soff);
    asm volatile("s_mov_b32 m0, %0\n\ts_nop 0\n\tbuffer_load_dwordx4 %1, %2, %3 offen sc1 lds" :: "s"(m0v), "v"(voff), "s"(srd), "s"(soff) : "memory");
}
DI void touch(unsigned m0v, unsigned voff, srd_t srd, unsigned soff) {
    SCALARIZE2(m0v, soff);
    asm volatile("s_mov_b32 m0, %0\n\ts_nop 0\n\tbuffer_load_dword %1, %2, %3 offen lds" :: "s"(m0v), "v"(voff), "s"(srd), "s"(soff) : "memory");
}
DI void store16_sc1(u32x4 w, unsigned voff, srd_t srd, unsigned soff) {
    unsigned z = soff; SCALARIZE2(soff, z);
    asm volatile("buffer_store_dwordx4 %0, %1, %2, %3 offen sc1\n\ts_nop 1" :: "v"(w), "v"(voff), "s"(srd), "s"(soff) : "memory");
}
template <int N> DI void wait_vm() { asm volatile("s_waitcnt vmcnt(%0)" :: "i"(N) : "memory"); }
template <int N> DI void wait_bar() { asm volatile("s_waitcnt vmcnt(%0) lgkmcnt(0)\n\ts_barrier" :: "i"(N) : "memory"); }
template <int N> DI void wait_bar_keep() { asm volatile("s_waitcnt vmcnt(%0)\n\ts_barrier" :: "i"(N) : "memory"); }
#define BARRIER() asm volatile("s_waitcnt lgkmcnt(0)\n\ts_barrier" ::: "memory")

struct Stamper { unsigned v; int n; int lane;
    DI void operator()() {
    } };

constexpr size_t WS_FLAG = 0;
constexpr size_t WS_KF = 1 << 20, WS_VF = WS_KF + (16 << 20), WS_CF = WS_VF + (16 << 20), WS_PHIQ = WS_CF + (16 << 20), WS_OF = WS_PHIQ + (4 << 20), WS_BST = WS_OF + (2 << 20), WS_BRED = WS_BST + (3 << 20), WS_XCH = WS_BRED + (1 << 20), WS_DBG = 77 << 20;
constexpr int FL_STRIDE = 16;
constexpr int FL_EPOCH = 0, FL_KV = 32, FL_BS = 32 + 256, FL_BR = 32 + 512, FL_XF = 32 + 768, FL_C = FL_XF + 256;
typedef GAS u64 gu64;
DI u64 ld_agent(const u64* p) { return __hip_atomic_load((gu64*)p, __ATOMIC_RELAXED, __HIP_MEMORY_SCOPE_AGENT); }
DI void st_agent(u64* p, u64 v) { __hip_atomic_store((gu64*)p, v, __ATOMIC_RELAXED, __HIP_MEMORY_SCOPE_AGENT); }
DI u64 mix64(u64 z) { z += 0x9E3779B97F4A7C15ull; z = (z ^ (z >> 30)) * 0xBF58476D1CE4E5B9ull; z = (z ^ (z >> 27)) * 0x94D049BB133111EBull; return z ^ (z >> 31); }
DI f32x4 mfma16_16(u32x2 a, u32x2 b, f32x4 c) { return __builtin_amdgcn_mfma_f32_16x16x16bf16_1k(__builtin_bit_cast(s16x4, a), __builtin_bit_cast(s16x4, b), c, 0, 0, 0); }
DI float rbf16(float x) { return __uint_as_float(cvtpk(x, 0.f) << 16); }
DI float x16_max(float x) { auto rr = __builtin_amdgcn_permlane16_swap(__float_as_uint(x), __float_as_uint(x), false, false); return __builtin_fmaxf(__uint_as_float(rr[0]), __uint_as_float(rr[1])); }
DI float x16_add(float x) { auto rr = __builtin_amdgcn_permlane16_swap(__float_as_uint(x), __float_as_uint(x), false, false); return __uint_as_float(rr[0]) + __uint_as_float(rr[1]); }
DI float grp_max(float x) { return swap_max(x16_max(x)); }
DI float grp_sum(float x) { return swap_add(x16_add(x)); }
constexpr float LOG2E = 1.4426950408889634f;
constexpr unsigned B1_RED = 147456;
#define BROT 0
struct B1Ctx { srd_t srdX, srdW; unsigned vx, vw[2], sx, sw; int wid, rot; unsigned lds0; };
DI void b1_setup(B1Ctx& c, const u16* Bx, const u16* Wqkv, unsigned lds0, int rc, int hg, int wid, int lane) {
    c.srdX = mk_srd(Bx, 2097152); c.srdW = mk_srd(Wqkv, 512 * 1536 * 2); c.wid = wid; c.lds0 = lds0;
    c.vx = (lane >> 3) * 1024 + (((lane & 7) ^ ((4 * wid + (lane >> 4)) & 7)) * 16);
#pragma unroll
    for (int p = 0; p < 2; ++p) c.vw[p] = (lane >> 3) * 3072 + (((lane & 7) ^ (2 * (((lane >> 4) & 1) | (p << 1)))) * 16);
    c.sx = (64 * rc + 8 * wid) * 1024; c.sw = hg * 128; c.rot = BROT ? rc & 7 : 0;
}
DI void b1_issue(const B1Ctx& c, int kt) {
    const unsigned st = c.lds0 + (kt & 3) * 32768;
    kt = (kt + c.rot) & 7;
    dma1(st + c.wid * 1024, c.vx, c.srdX, c.sx + kt * 128);
#pragma unroll
    for (int i = 0; i < 3; ++i) { const int pw = 3 * c.wid + i, m = pw >> 3, pr = pw & 7;
        dma1(st + 8192 + pw * 1024, (pr & 1) ? c.vw[1] : c.vw[0], c.srdW, c.sw + (kt * 64 + 8 * pr) * 3072 + m * 1024); }
}
struct B1Acc { f32x4 aq[2], ak[2], av[2]; };
struct B1Fr { bf16x8 x[2][2]; bf16x4 w[2][3][2]; };
struct B1Ad { unsigned xa[2][2], wa[2]; };
template <int KT> DI void b1_read(B1Fr& f, const B1Ad& ad) {
    constexpr unsigned st = (KT & 1) * 32768; constexpr int sp = (KT >> 1) & 1;
#pragma unroll
    for (int ks = 0; ks < 2; ++ks) { const unsigned xa = ad.xa[sp][ks];
        f.x[ks][0] = ldsf(xa + st); f.x[ks][1] = ldsf(xa + st + 2048);
#pragma unroll
        for (int m = 0; m < 3; ++m) { f.w[ks][m][0] = ldstr(ad.wa[sp] + st + m * 8192 + ks * 4096); f.w[ks][m][1] = ldstr(ad.wa[sp] + st + m * 8192 + ks * 4096 + 512); } }
}
DI void b1_mma(B1Acc& a, const B1Fr& f) {
#pragma unroll
    for (int ks = 0; ks < 2; ++ks) {
        const bf16x8 wq = cat4(f.w[ks][0][0], f.w[ks][0][1]), wk = cat4(f.w[ks][1][0], f.w[ks][1][1]), wv = cat4(f.w[ks][2][0], f.w[ks][2][1]);
        a.aq[0] = MFMA16(wq, f.x[ks][0], a.aq[0]); a.aq[1] = MFMA16(wq, f.x[ks][1], a.aq[1]); a.ak[0] = MFMA16(wk, f.x[ks][0], a.ak[0]); a.ak[1] = MFMA16(wk, f.x[ks][1], a.ak[1]);
        a.av[0] = MFMA16(f.x[ks][0], wv, a.av[0]); a.av[1] = MFMA16(f.x[ks][1], wv, a.av[1]);
    }
}
template <class St, class IQ>
DI void b1_gemm(const B1Ctx& c, B1Acc& a, int lane, bool early, St store, IQ issueQt) {
    const int l16 = lane & 15, g = lane >> 4, q4 = l16 >> 2, p4 = lane & 3, wid = c.wid;
    const int row = 32 * (wid >> 2) + l16;
    B1Ad ad;
#pragma unroll
    for (int sp = 0; sp < 2; ++sp) {
        ad.xa[sp][0] = c.lds0 + sp * 65536 + row * 128 + (((0 + g) ^ ((row >> 1) & 7)) << 4); ad.xa[sp][1] = c.lds0 + sp * 65536 + row * 128 + (((4 + g) ^ ((row >> 1) & 7)) << 4);
        ad.wa[sp] = c.lds0 + sp * 65536 + 8192 + (8 * g + q4) * 128 + (((wid & 3) ^ (((q4 >> 1) & 1) | ((g & 1) << 1))) * 32) + p4 * 8;
        PIN(ad.xa[sp][0]); PIN(ad.xa[sp][1]); PIN(ad.wa[sp]); }
#pragma unroll
    for (int r = 0; r < 2; ++r) { a.aq[r] = f32x4{}; a.ak[r] = f32x4{}; a.av[r] = f32x4{}; }
#pragma unroll
    for (int r = 0; r < 2; ++r) { PIN(a.aq[r]); PIN(a.ak[r]); PIN(a.av[r]); }
    B1Fr f0, f1;
    b1_read<0>(f0, ad);
#define B1W(E, L) do { if (early) wait_bar<E>(); else wait_bar<L>(); } while (0)
#define B1T(KT, CUR, NXT, PRE) do { b1_read<KT + 1>(NXT, ad); SBAR(); PRE; store(KT); SBAR(); b1_mma(a, CUR); SBAR(); } while (0)
    B1W(8, 8);   B1T(0, f0, f1, b1_issue(c, 4));
    B1W(9, 8);   B1T(1, f1, f0, b1_issue(c, 5));
    B1W(10, 9);  B1T(2, f0, f1, b1_issue(c, 6));
    B1W(11, 10); B1T(3, f1, f0, b1_issue(c, 7));
    B1W(11, 11); B1T(4, f0, f1, (void)0);
    B1W(7, 7);   B1T(5, f1, f0, issueQt(0));
    B1W(9, 9);   B1T(6, f0, f1, issueQt(1));
    BARRIER();   issueQt(2); store(7); SBAR(); b1_mma(a, f1);
#undef B1W
#undef B1T
}
template <int N, class Tick> DI void phi_map(const f32x4 (&p)[N], u32x4 (&out)[N], Tick tick) {
    float x[N][4], mx[N], nm[N], ep[N][4], en[N][4], sp[N], sn[N];
#pragma unroll
    for (int i = 0; i < N; ++i) {
#pragma unroll
        for (int e = 0; e < 4; ++e) x[i][e] = rbf16(p[i][e]) * LOG2E;
        mx[i] = __builtin_fmaxf(__builtin_fmaxf(x[i][0], x[i][1]), __builtin_fmaxf(x[i][2], x[i][3])); nm[i] = -__builtin_fminf(__builtin_fminf(x[i][0], x[i][1]), __builtin_fminf(x[i][2], x[i][3]));
        if (i & 1) tick(i >> 1); }
#pragma unroll
    for (int i = 0; i < N; ++i) { mx[i] = x16_max(mx[i]); nm[i] = x16_max(nm[i]); if (i & 1) tick(2 + (i >> 1)); }
#pragma unroll
    for (int i = 0; i < N; ++i) { mx[i] = swap_max(mx[i]); nm[i] = swap_max(nm[i]); if (i & 1) tick(4 + (i >> 1)); }
#pragma unroll
    for (int i = 0; i < N; ++i) {
#pragma unroll
        for (int e = 0; e < 4; ++e) { ep[i][e] = __builtin_amdgcn_exp2f(x[i][e] - mx[i]); en[i][e] = __builtin_amdgcn_exp2f(-x[i][e] - nm[i]); }
        sp[i] = (ep[i][0] + ep[i][1]) + (ep[i][2] + ep[i][3]); sn[i] = (en[i][0] + en[i][1]) + (en[i][2] + en[i][3]); tick(6 + i); }
#pragma unroll
    for (int i = 0; i < N; ++i) { sp[i] = x16_add(sp[i]); sn[i] = x16_add(sn[i]); if (i & 1) tick(10 + (i >> 1)); }
#pragma unroll
    for (int i = 0; i < N; ++i) { sp[i] = __builtin_amdgcn_rcpf(swap_add(sp[i])); sn[i] = __builtin_amdgcn_rcpf(swap_add(sn[i])); if (i & 1) tick(12 + (i >> 1)); }
#pragma unroll
    for (int i = 0; i < N; ++i) { out[i] = (u32x4){cvtpk(ep[i][0] * sp[i], ep[i][1] * sp[i]), cvtpk(ep[i][2] * sp[i], ep[i][3] * sp[i]), cvtpk(en[i][0] * sn[i], en[i][1] * sn[i]), cvtpk(en[i][2] * sn[i], en[i][3] * sn[i])};
        if (i & 1) tick(14 + (i >> 1)); }
}
template <class Tick>
DI void b1_phi(const B1Acc& a, Tick tick, u32x2 wp, u16* PhiQ, float* Bst, unsigned lds0, int rc, int hg, int wid, int lane, Stamper& stamp) {
    const int h = 4 * hg + (wid & 3), row0 = 64 * rc + 32 * (wid >> 2);
    const int l16 = lane & 15, g = lane >> 4;
    f32x4 sp = f32x4{}, sn = f32x4{}; float zp = 0.f, zn = 0.f;
    f32x4 pin[4]; u32x4 pout[4];
#pragma unroll
    for (int r = 0; r < 2; ++r) {
        const u32x2 qb = {cvtpk(a.aq[r][0], a.aq[r][1]), cvtpk(a.aq[r][2], a.aq[r][3])}, kb = {cvtpk(a.ak[r][0], a.ak[r][1]), cvtpk(a.ak[r][2], a.ak[r][3])};
        pin[r] = mfma16_16(wp, qb, f32x4{}); pin[2 + r] = mfma16_16(wp, kb, f32x4{});
    }
    phi_map<4>(pin, pout, tick);
    const u32x4 wq[2] = {pout[0], pout[1]}, wk[2] = {pout[2], pout[3]};
    const unsigned tb = lds0 + B1_RED + wid * 1024;
#pragma unroll
    for (int r = 0; r < 2; ++r) {
        *(LAS u32x2*)(tb + l16 * 32 + g * 8) = (u32x2){wk[r][0], wk[r][1]};
        *(LAS u32x2*)(tb + 512 + l16 * 32 + g * 8) = (u32x2){wk[r][2], wk[r][3]};
        __builtin_amdgcn_raw_buffer_store_b128(wq[r], mk_srd(PhiQ, 4194304), ((h * 128 + (row0 >> 4) + r) * 64 + lane) * 16, 0, 0);
        asm volatile("s_waitcnt lgkmcnt(0)" ::: "memory");
        const unsigned ta = tb + (4 * g + (l16 >> 2)) * 32 + (lane & 3) * 8;
        u32x2 pp = __builtin_bit_cast(u32x2, ldstr(ta)), pn = __builtin_bit_cast(u32x2, ldstr(ta + 512));
        asm volatile("s_waitcnt lgkmcnt(0)" : "+v"(pp), "+v"(pn) :: "memory");
        const u32x2 vb = {cvtpk(a.av[r][0], a.av[r][1]), cvtpk(a.av[r][2], a.av[r][3])};
        sp = mfma16_16(pp, vb, sp); sn = mfma16_16(pn, vb, sn);
        zp += (__uint_as_float(pp[0] << 16) + __uint_as_float(pp[0] & 0xffff0000u)) + (__uint_as_float(pp[1] << 16) + __uint_as_float(pp[1] & 0xffff0000u));
        zn += (__uint_as_float(pn[0] << 16) + __uint_as_float(pn[0] & 0xffff0000u)) + (__uint_as_float(pn[1] << 16) + __uint_as_float(pn[1] & 0xffff0000u));
    }
    zp = grp_sum(zp); zn = grp_sum(zn);
    BARRIER();
    LAS char* red = (LAS char*)(lds0 + B1_RED + (wid & 3) * 2176);
    if (wid >= 4) { *(LAS f32x4*)(red + lane * 32) = sp; *(LAS f32x4*)(red + lane * 32 + 16) = sn; if (lane < 16) *(LAS f32x2*)(red + 2048 + lane * 8) = (f32x2){zp, zn}; }
    BARRIER();
    sp += *(const LAS f32x4*)(red + lane * 32); sn += *(const LAS f32x4*)(red + lane * 32 + 16);
    const f32x2 zo = *(const LAS f32x2*)(red + 2048 + l16 * 8);
    const srd_t srd = mk_srd(Bst + (size_t)(h * 32 + rc) * 576, wid < 4 ? 2304 : 0);
    store16_sc1(__builtin_bit_cast(u32x4, sp), lane * 32, srd, 0u); store16_sc1(__builtin_bit_cast(u32x4, sn), lane * 32 + 16, srd, 0u);
    __builtin_amdgcn_raw_buffer_store_b64((u32x2){__float_as_uint(zp + zo[0]), __float_as_uint(zn + zo[1])}, srd, lane < 16 ? 2048 + lane * 8 : 0x7fffffff, 0, 16);
}
template <int N> DI void waitN(const u64* flags, u64 tok, int lane) {
    const u64* p = flags + (lane & (N - 1)) * FL_STRIDE;
    for (unsigned spins = 0; spins < (1u << 15); ++spins) { const u64 v = ld_agent(p); if (__all(v == tok)) break; __builtin_amdgcn_s_sleep(1); }
    asm volatile("" ::: "memory");
}
struct B2a { f32x4 v[16]; };
DI void b2a_load(B2a& b, const float* Bst, int rc, int hg, int lane) {
    const int h = 4 * hg + (rc >> 3), c = lane & 31, half = lane >> 5;
    const srd_t srd = mk_srd(Bst + (size_t)h * 32 * 576, 32 * 2304);
    const unsigned vo = (c < 18 ? (18 * (rc & 7) + c) * 16 : 0x7fffffff) + half * 16 * 2304;
#pragma unroll
    for (int i = 0; i < 16; ++i) b.v[i] = __builtin_bit_cast(f32x4, __builtin_amdgcn_raw_buffer_load_b128(srd, vo, i * 2304, 16));
}
DI void b2a_finish(const B2a& b, float* Bred, int rc, int hg, int lane) {
    const int h = 4 * hg + (rc >> 3), c = lane & 31, half = lane >> 5;
    f32x4 s = b.v[0];
#pragma unroll
    for (int i = 1; i < 16; ++i) s += b.v[i];
#pragma unroll
    for (int e = 0; e < 4; ++e) s[e] = swap_add(s[e]);
    const srd_t srdR = mk_srd(Bred + (size_t)h * 576, 2304);
    store16_sc1(__builtin_bit_cast(u32x4, s), (half == 0 && c < 18) ? (18 * (rc & 7) + c) * 16 : 0x7fffffffu, srdR, 0u);
}
template <int NRB> struct B2b { bf16x8 ph[NRB]; f32x4 sp, sn, za, zb; };
template <int NRB>
DI void b2b_load(B2b<NRB>& b, const u16* PhiQ, const float* Bred, const u64* flagsR, u64 tok, int h, int rb0, int lane) {
    PIN(lane);
    const int g = lane >> 4;
    waitN<8>(flagsR, tok, lane);
#pragma unroll
    for (int r = 0; r < NRB; ++r) b.ph[r] = __builtin_bit_cast(bf16x8, __builtin_amdgcn_raw_buffer_load_b128(mk_srd(PhiQ, 4194304), lane * 16, (h * 128 + rb0 + r) * 1024, 0));
    const srd_t srd = mk_srd(Bred + (size_t)h * 576, 2304);
    b.sp = __builtin_bit_cast(f32x4, __builtin_amdgcn_raw_buffer_load_b128(srd, lane * 32, 0, 16)); b.sn = __builtin_bit_cast(f32x4, __builtin_amdgcn_raw_buffer_load_b128(srd, lane * 32 + 16, 0, 16));
    b.za = __builtin_bit_cast(f32x4, __builtin_amdgcn_raw_buffer_load_b128(srd, 2048 + g * 32, 0, 16)); b.zb = __builtin_bit_cast(f32x4, __builtin_amdgcn_raw_buffer_load_b128(srd, 2048 + g * 32 + 16, 0, 16));
}
template <int NRB>
DI void b2b_finish(const B2b<NRB>& b, u16* Of, int h, int rb0, int lane) {
    PIN(lane);
    const int l16 = lane & 15, g = lane >> 4;
    const u32x4 kvw = {cvtpk(b.sp[0], b.sp[1]), cvtpk(b.sp[2], b.sp[3]), cvtpk(b.sn[0], b.sn[1]), cvtpk(b.sn[2], b.sn[3])};
    const u32x4 zw = {cvtpk(b.za[0], b.za[2]), cvtpk(b.zb[0], b.zb[2]), cvtpk(b.za[1], b.za[3]), cvtpk(b.zb[1], b.zb[3])};
    const srd_t srdO = mk_srd(Of, 2097152);
#pragma unroll
    for (int r = 0; r < NRB; ++r) { const int rb16 = rb0 + r;
        const f32x4 num = MFMA16(__builtin_bit_cast(bf16x8, kvw), b.ph[r], f32x4{});
        const f32x4 den = MFMA16(__builtin_bit_cast(bf16x8, zw), b.ph[r], f32x4{});
        const float dn = rbf16(rbf16(den[0]) + 1e-6f);
        const u32x2 w = {cvtpk(rbf16(num[0]) / dn, rbf16(num[1]) / dn), cvtpk(rbf16(num[2]) / dn, rbf16(num[3]) / dn)};
        __builtin_amdgcn_raw_buffer_store_b64(w, srdO, (((rb16 >> 1) * 32 + h) * 2 + (g & 1)) * 512 + ((rb16 & 1) * 16 + l16) * 16 + (g >> 1) * 8, 0, 16); }
}
#define KVPRIO 0
#define KROT 0
struct ProjCtx {
    srd_t srdC, srdX, srdW, srdWq, srdO;
    unsigned va[2], vw[2], vwq, vo, lds0, l128;
    unsigned sA, sW, sWq, sXa;
    int wid, isV, rot;
};
constexpr unsigned TOUCH_LDS = 163840 - 256;
DI void issueKV(const ProjCtx& c, int kt, int i) {
    const unsigned st = c.lds0 + (kt & 1) * 65536;
    kt = (kt + c.rot) & 7;
    if (i < 4) dma1(st + (4 * c.wid + i) * 1024, c.va[i & 1], c.srdC, c.sA + i * 8192 + kt * 128);
    else dma1(st + 32768 + (c.wid >> 2) * 16384 + (4 * (c.wid & 3) + (i - 4)) * 1024, c.vw[(i - 4) >> 1], c.srdW, c.sW + ((i - 4) >> 1) * 65536 + ((i - 4) & 1) * 128 + kt * 524288);
}
DI void issueQ(const ProjCtx& c, int kt, int i) {
    const unsigned st = c.lds0 + (kt % 3) * 49152;
    kt = (kt + c.rot) & 7;
    if (i < 4) dma1(st + (4 * c.wid + i) * 1024, c.va[i & 1], c.srdX, c.sXa + i * 8192 + kt * 128);
    else dma1(st + 32768 + (2 * c.wid + (i - 4)) * 1024, c.vwq, c.srdWq, c.sWq + (i - 4) * 128 + kt * 524288);
}
#define TOUCHMODE 3
template <int WHICH> DI void touch_g(unsigned m0v, unsigned voff, const void* base) {
    if (!(TOUCHMODE & WHICH)) voff = 0;
    const u64 b = (u64)base; const u64 sb = ((u64)rfl((unsigned)(b >> 32)) << 32) | rfl((unsigned)b);
    asm volatile("s_nop 4\n\ts_mov_b32 m0, %0\n\ts_nop 0\n\tglobal_load_lds_dword %1, %2" :: "s"(rfl(m0v)), "v"(voff), "s"(sb) : "memory");
}

struct GAddr { unsigned a[2]; unsigned w[2][2]; };
DI void gaddr_add(GAddr& g, unsigned d) { g.a[0] += d; g.a[1] += d; g.w[0][0] += d; g.w[0][1] += d; g.w[1][0] += d; g.w[1][1] += d; PIN(g.a[0]); PIN(g.a[1]); PIN(g.w[0][0]); PIN(g.w[0][1]); PIN(g.w[1][0]); PIN(g.w[1][1]); }
DI void gaddr_xor(GAddr& g, unsigned d) { g.a[0] ^= d; g.a[1] ^= d; g.w[0][0] ^= d; g.w[0][1] ^= d; g.w[1][0] ^= d; g.w[1][1] ^= d; }
DI bf16x8 wfrag(const GAddr& g, int ks, int nb) { return cat4(ldstr(g.w[nb & 1][0] + ks * 8192 + (nb >> 1) * 512), ldstr(g.w[nb & 1][1] + ks * 8192 + (nb >> 1) * 512)); }
DI void gaddr_w(GAddr& g, unsigned base, int lane) {
    const int gq = lane >> 4, q4 = (lane & 15) >> 2, p4 = lane & 3;
#pragma unroll
    for (int o = 0; o < 2; ++o)
#pragma unroll
        for (int t = 0; t < 2; ++t) g.w[o][t] = base + 2048 * gq + 64 * (4 * t + q4) + 16 * ((2 * o + (p4 >> 1)) ^ ((2 * gq + t) & 3)) + 8 * (p4 & 1);
}
DI void gaddr_a(GAddr& g, unsigned base, int row0, int lane) {
    const int row = row0 + (lane & 15), gq = lane >> 4;
#pragma unroll
    for (int ks = 0; ks < 2; ++ks) g.a[ks] = base + row * 128 + (((4 * ks + gq) ^ ((row >> 1) & 7)) << 4);
}

#define KVAH 3
constexpr int AH = KVAH;
#define KV_MMA(mb, W) do { _Pragma("unroll") for (int nb = 0; nb < 4; ++nb) { if (ISV) MFMA16I(acc[mb][nb], a[mb], W[nb]); else MFMA16I(acc[mb][nb], W[nb], a[mb]); } } while (0)
template <bool ISV, class Hook>
DI void kv_step0(f32x4 (&acc)[8][4], const bf16x8 (&w)[4], bf16x8 (&wn)[4], bf16x8 (&an)[AH], const GAddr& g, Hook hook) {
    bf16x8 a[8 + AH];
#pragma unroll
    for (int i = 0; i < AH; ++i) a[i] = an[i];
#pragma unroll
    for (int mb = 0; mb < 8; ++mb) {
        a[mb + AH] = mb + AH < 8 ? ldsf(g.a[0] + (mb + AH) * 2048) : ldsf(g.a[1] + (mb + AH - 8) * 2048);
        if (mb < 4) wn[mb] = wfrag(g, 1, mb);
        SBAR();
        KV_MMA(mb, w);
        SBAR();
        hook(mb);
        SBAR();
    }
#pragma unroll
    for (int i = 0; i < AH; ++i) an[i] = a[8 + i];
}
struct NoHook { DI void operator()(int) const {} };

template <bool ISV, class Touch, class IssueB1>
DI void kv_part(const ProjCtx& c, GAddr g, u32x4 (&pk)[16], Touch touchk, IssueB1 issueB1, Stamper& stamp) {
    f32x4 acc[8][4];
#pragma unroll
    for (int a = 0; a < 8; ++a)
#pragma unroll
        for (int b = 0; b < 4; ++b) { acc[a][b] = f32x4{}; PIN(acc[a][b]); }
    bf16x8 w0[4], w1[4], an[AH];
    wait_bar<9>();
    stamp();
#pragma unroll
    for (int nb = 0; nb < 4; ++nb) w0[nb] = wfrag(g, 0, nb);
#pragma unroll
    for (int i = 0; i < AH; ++i) an[i] = ldsf(g.a[0] + i * 2048);
    auto tile = [&](int kt, auto late, auto mid, auto waitB, auto early, bool next) {
        kv_step0<ISV>(acc, w0, w1, an, g, late);
        bf16x8 a[8];
#pragma unroll
        for (int i = 0; i < AH; ++i) a[i] = an[i];
#pragma unroll
        for (int mb = 0; mb < 8 - AH; ++mb) {
            a[mb + AH] = ldsf(g.a[1] + (mb + AH) * 2048);
            SBAR();
            KV_MMA(mb, w1);
            SBAR();
            if (mb == 0) { mid(); SBAR(); }
        }
        waitB();
        if (next) { gaddr_xor(g, 65536u);
#pragma unroll
            for (int nb = 0; nb < 4; ++nb) w0[nb] = wfrag(g, 0, nb);
#pragma unroll
            for (int i = 0; i < AH; ++i) an[i] = ldsf(g.a[0] + i * 2048); }
        SBAR();
#pragma unroll
        for (int mb = 8 - AH; mb < 8; ++mb) { KV_MMA(mb, w1); SBAR(); early(mb - (8 - AH)); SBAR(); }
    };
    for (int kt = 0; kt < 6; ++kt)
        tile(kt, [&](int mb) { if (kt > 0 && (mb & 1)) issueKV(c, kt + 1, 4 + (mb >> 1)); }, [&]() { touchk(kt); }, [&]() { wait_bar<1>(); },
             [&](int i) { if (i == 0) { issueKV(c, kt + 2, 0); issueKV(c, kt + 2, 1); } else issueKV(c, kt + 2, 1 + i); }, true);
    tile(6, [&](int mb) { if (mb & 1) issueKV(c, 7, 4 + (mb >> 1)); }, [&]() {}, [&]() { wait_bar<0>(); }, [&](int i) { if (i < 2) issueB1(i); }, true);
    tile(7, NoHook{}, [&]() {}, [&]() { stamp(); wait_bar<4>(); stamp(); }, [&](int i) { if (i < 2) issueB1(2 + i); }, false);
    __builtin_amdgcn_s_setprio(0);
    MFMA_DRAIN();
#pragma unroll
    for (int n = 0; n < 16; ++n) {
        const f32x4 lo = ISV ? acc[2 * (n >> 2)][n & 3] : acc[n >> 1][2 * (n & 1)], hi = ISV ? acc[2 * (n >> 2) + 1][n & 3] : acc[n >> 1][2 * (n & 1) + 1];
        pk[n] = (u32x4){cvtpk(lo[0], lo[1]), cvtpk(lo[2], lo[3]), cvtpk(hi[0], hi[1]), cvtpk(hi[2], hi[3])}; PIN(pk[n]); }
}
DI void kv_store(const ProjCtx& c, const u32x4 (&pk)[16], int n) {
    const unsigned offV = (n >> 3) * 16384 + ((n & 3) * 2 + ((n >> 2) & 1)) * 1024;
    const unsigned offK = (n >> 3) * 16384 + (((n >> 1) & 3) * 4 + (n & 1)) * 1024;
    store16_sc1(pk[n], c.vo, c.srdO, c.isV ? offV : offK);
}

struct QFr { bf16x8 x[2]; bf16x8 w[8]; };
template <int KS> DI void q_read(QFr& f, const GAddr& g, int i) {
    if (i == 1) f.x[0] = ldsf(g.a[KS]); else if (i == 2) f.x[1] = ldsf(g.a[KS] + 2048); else { const int nb = i == 0 ? 0 : i - 2; f.w[nb] = wfrag(g, KS, nb); }
}
template <int KSN, bool NEXT, class Hook>
DI void q_kstep(f32x4 (&acc)[8][2], const QFr& cur, QFr& nxt, const GAddr& gn, Hook hook) {
#pragma unroll
    for (int nb = 0; nb < 8; ++nb) {
        MFMA16I(acc[nb][0], cur.w[nb], cur.x[0]); MFMA16I(acc[nb][1], cur.w[nb], cur.x[1]);
        SBAR();
        if (NEXT) { if (nb < 2) { q_read<KSN>(nxt, gn, 2 * nb); q_read<KSN>(nxt, gn, 2 * nb + 1); } else q_read<KSN>(nxt, gn, nb + 2); }
        hook(nb);
        SBAR();
    }
}
template <int KT, class Pub>
DI void q_tile(const ProjCtx& c, bool early, GAddr& g, f32x4 (&acc)[8][2], QFr& f0, QFr& f1, const u32x4 (&pk)[16], Pub after, Stamper& stamp) {
    q_kstep<1, true>(acc, f0, f1, g, [&](int nb) {
        if (KT >= 1 && KT + 2 < 8 && nb < 3) issueQ(c, KT + 2, 3 + nb);
        if (nb == 4) { if (early) { if (KT < 2) kv_store(c, pk, 12 + 2 * (KT < 2 ? KT : 0)); } else if (KT < 5) kv_store(c, pk, 11 + (KT < 5 ? KT : 0)); }
        if (nb == 7 && early && KT < 2) kv_store(c, pk, 13 + 2 * (KT < 2 ? KT : 0)); });
    if constexpr (KT < 7) {
        constexpr int WE = KT == 0 ? 17 : (KT == 1 ? 19 : (KT == 2 ? 8 : (KT <= 5 ? 6 : 0)));
        constexpr int WL = KT == 0 ? 17 : (KT == 1 ? 18 : (KT <= 4 ? 8 : (KT == 5 ? 7 : 0)));
        const bool w7 = KT == 5 && c.wid == 7;
        if (early) { if (w7) wait_bar<WE + 16>(); else wait_bar<WE>(); } else { if (w7) wait_bar<WL + 16>(); else wait_bar<WL>(); }
        stamp();
        after(KT);
        gaddr_add(g, (unsigned)(((KT + 1) % 3 - KT % 3) * 49152));
        q_kstep<0, true>(acc, f1, f0, g, [&](int nb) { if (KT + 3 < 8 && nb < 3) issueQ(c, KT + 3, nb); });
    } else q_kstep<0, false>(acc, f1, f0, g, NoHook{});
}

DI void proj_setup(ProjCtx& c, const u16* X, const u16* Ckv, const u16* Wq, const u16* Wuk, const u16* Wuv, u16* Kf, u16* Vf, unsigned lds0, int h, int j, int wid, int lane) {
    const int wr = wid >> 2, wc = wid & 3;
    c.wid = wid; c.isV = wc >> 1; c.lds0 = lds0; c.rot = KROT ? (2 * (h & 3) + j) & 7 : 0;
    c.srdC = mk_srd(Ckv, 2097152); c.srdX = mk_srd(X, 2097152);
    c.srdW = mk_srd((wid < 4 ? Wuk : Wuv), 4194304); c.srdWq = mk_srd(Wq, 4194304);
    c.srdO = mk_srd((const char*)(c.isV ? Vf : Kf) + (size_t)h * 524288, 524288);
#pragma unroll
    for (int p = 0; p < 2; ++p) {
        c.va[p] = (lane >> 3) * 1024 + (((lane & 7) ^ (4 * p + (lane >> 4))) * 16);
        c.vw[p] = ((lane >> 2) & 7) * 8192 + (4 * (lane >> 5) + ((lane & 3) ^ ((2 * p + ((lane >> 4) & 1)) & 3))) * 16;
    }
    c.vwq = (wid & 1) ? c.vw[1] : c.vw[0];
    c.l128 = lane * 128;
    c.sA = (256 * j + 32 * wid) * 1024;
    c.sW = (16 * (wid & 3)) * 8192 + h * 256;
    c.sXa = (wid < 4 ? 128 * (15 - j) + 32 * wid : 128 * j + 32 * (wid - 4)) * 1024;
    c.sWq = (8 * wid) * 8192 + h * 256;
    c.vo = lane * 16 + (4 * j + 2 * wr) * 16384 + (wc & 1) * (c.isV ? 8192 : 2048);
}
template <class Touch, class IssueB1>
DI void kv_run(const ProjCtx& c, int lane, u32x4 (&pk)[16], Touch touchk, IssueB1 issueB1, Stamper& stamp) {
    const int wid = c.wid, wr = wid >> 2, wc = wid & 3;
    GAddr g; gaddr_a(g, c.lds0, 128 * wr, lane); gaddr_w(g, c.lds0 + 32768 + (wc >> 1) * 16384 + (wc & 1) * 1024, lane);
    if (c.isV) kv_part<true>(c, g, pk, touchk, issueB1, stamp); else kv_part<false>(c, g, pk, touchk, issueB1, stamp);
}
template <class Pub>
DI void q_run(const ProjCtx& c, bool early, int lane, const u32x4 (&pk)[16], bf16x8 (&q)[8], Pub after, Stamper& stamp) {
    GAddr g; gaddr_a(g, c.lds0, 32 * c.wid, lane); gaddr_w(g, c.lds0 + 32768, lane); gaddr_add(g, 0u);
    f32x4 acc[8][2];
#pragma unroll
    for (int a = 0; a < 8; ++a) { acc[a][0] = f32x4{}; acc[a][1] = f32x4{}; PIN(acc[a][0]); PIN(acc[a][1]); }
    QFr f0, f1;
#pragma unroll
    for (int i = 0; i < 10; ++i) q_read<0>(f0, g, i);
    q_tile<0>(c, early, g, acc, f0, f1, pk, after, stamp); q_tile<1>(c, early, g, acc, f0, f1, pk, after, stamp); q_tile<2>(c, early, g, acc, f0, f1, pk, after, stamp); q_tile<3>(c, early, g, acc, f0, f1, pk, after, stamp);
    q_tile<4>(c, early, g, acc, f0, f1, pk, after, stamp); q_tile<5>(c, early, g, acc, f0, f1, pk, after, stamp); q_tile<6>(c, early, g, acc, f0, f1, pk, after, stamp); q_tile<7>(c, early, g, acc, f0, f1, pk, after, stamp);
    MFMA_DRAIN();
    constexpr float QSC = 0.08838834764831845f * 1.4426950408889634f;
#pragma unroll
    for (int qb = 0; qb < 2; ++qb)
#pragma unroll
        for (int ds = 0; ds < 4; ++ds) { const f32x4 lo = acc[2 * ds][qb] * QSC, hi = acc[2 * ds + 1][qb] * QSC;
            q[qb * 4 + ds] = __builtin_bit_cast(bf16x8, (u32x4){cvtpk(lo[0], lo[1]), cvtpk(lo[2], lo[3]), cvtpk(hi[0], hi[1]), cvtpk(hi[2], hi[3])}); }
}
template <int OFF, bool SETM0> DI void dma_piece(unsigned m0v, unsigned voff, srd_t srd, unsigned soff) {
    if constexpr (SETM0) asm volatile("s_mov_b32 m0, %0\n\ts_nop 0\n\tbuffer_load_dwordx4 %1, %2, %3 offen offset:%4 sc1 lds" :: "s"(m0v), "v"(voff), "s"(srd), "s"(soff), "i"(OFF) : "memory");
    else asm volatile("buffer_load_dwordx4 %0, %1, %2 offen offset:%3 sc1 lds" :: "v"(voff), "s"(srd), "s"(soff), "i"(OFF) : "memory");
}
#define ATT_THRV 64.f
constexpr float ATT_THR = ATT_THRV;
#define KSPLIT 0
#define ATT_WIN 5
constexpr int ATT_W = ATT_WIN;

struct AttnState { f32x4 o[8][2]; f32x4 s[4][2]; f32x4 negm[2]; bf16x8 q[8]; u32x4 p[2][2]; f32x4 l[2]; f32x2 mhat; u32x4 ones; };
struct AttnCtx {
    srd_t srdK, srdV, srdC; unsigned voff, lane16, kslot0, vslot0, coffL;
    int g, nl  , thrg, dq0, relH, jump; unsigned qstash;
    unsigned soA, soB, ksoM; int rel;
    srd_t srdF; u64 tok, prev; unsigned known;
};
DI u64 ld_flag8(const AttnCtx& c) { return __builtin_bit_cast(u64, __builtin_amdgcn_raw_buffer_load_b64(c.srdF, (c.lane16 & 0x70u) << 3, 0, 16)); }

DI unsigned m_off(int i) { return i < 16 ? (unsigned)(((i & 7) * 2 + (i >> 3)) * 1024) : (unsigned)((((i - 16) & 3) * 4 + ((i - 16) >> 2)) * 1024); }
template <bool DO_PV, bool DO_QK, int KP, int VP, class Hook>
DI void m_phase(AttnState& st, unsigned kbase, unsigned vbase, bf16x8 (&pre)[ATT_W], Hook hook) {
    constexpr int N0 = DO_PV ? 0 : 16, N1 = DO_QK ? 32 : 16;
    bf16x8 fr[32];
#pragma unroll
    for (int i = 0; i < ATT_W; ++i) fr[N0 + i] = pre[i];
#pragma unroll
    for (int i = N0; i < N1; ++i) {
        const int n = i + ATT_W;
        if (n < N1) fr[n] = ldsf((n < 16 ? vbase + VP * 16384 : kbase + KP * 16384) + m_off(n));
        SBAR();
        if (i >= 16) { const int ds = (i - 16) >> 2, kb = (i - 16) & 3;
            if (ds == 0) { asm volatile("v_mfma_f32_16x16x32_bf16 %0, %2, %3, %1" : "=&v"(st.s[kb][0]), "+v"(st.negm[0]) : "v"(fr[i]), "v"(st.q[0]));
                           asm volatile("v_mfma_f32_16x16x32_bf16 %0, %2, %3, %1" : "=&v"(st.s[kb][1]), "+v"(st.negm[1]) : "v"(fr[i]), "v"(st.q[4])); }
            else { MFMA16I(st.s[kb][0], fr[i], st.q[ds]); MFMA16I(st.s[kb][1], fr[i], st.q[4 + ds]); }
        } else { const int ks = i >> 3, db = i & 7;
            MFMA16I(st.o[db][0], fr[i], st.p[ks][0]); MFMA16I(st.o[db][1], fr[i], st.p[ks][1]);
            if (db == 3) { MFMA16I(st.l[0], st.ones, st.p[ks][0]); MFMA16I(st.l[1], st.ones, st.p[ks][1]); } }
        SBAR();
        hook(i);
        SBAR();
    }
    MFMA_DRAIN();
    PIN(st.negm[0]); PIN(st.negm[1]);
}
struct NoHookI { DI void operator()(int) const {} };

DI void store_ctx(const f32x4 (&o)[8][2], float inv0, float inv1, srd_t srd, unsigned voff) {
    PIN(voff);
#pragma unroll
    for (int p = 0; p < 4; ++p)
#pragma unroll
        for (int qb = 0; qb < 2; ++qb) { const float inv = qb ? inv1 : inv0; const f32x4 lo = o[2 * p][qb] * inv, hi = o[2 * p + 1][qb] * inv;
            __builtin_amdgcn_raw_buffer_store_b128((u32x4){cvtpk(lo[0], lo[1]), cvtpk(lo[2], lo[3]), cvtpk(hi[0], hi[1]), cvtpk(hi[2], hi[3])}, srd, voff + p * 2048 + qb * 256, 0, 16); }
}
DI float row_add(float x) { return swap_add(x16_add(x)); }
DI float row_max4(float x) { return swap_max(x16_max(x)); }

template <bool LAST, int PAR>
DI void v_phase(AttnState& st, AttnCtx& c, const int t) {
    const bool sw = (t == c.nl);
    const unsigned vm0 = c.vslot0 + (PAR ^ 1) * 16384;
    const unsigned vso = c.soA, kso = c.soB;
    if (!LAST && __builtin_expect(c.known != 0xffu, 0)) {
        c.known |= (unsigned)__builtin_amdgcn_ballot_w64(c.prev == c.tok) & 0xffu;
        const unsigned need = 1u << ((kso >> 16) & 7);
        if (kso < 524288u && !(c.known & need)) {
            for (unsigned spins = 0; spins < (1u << 15); ++spins) { const u64 v = ld_flag8(c); c.known |= (unsigned)__builtin_amdgcn_ballot_w64(v == c.tok) & 0xffu; if (c.known & need) break; __builtin_amdgcn_s_sleep(1); } }
        asm volatile("" ::: "memory");
        c.prev = ld_flag8(c);
    }
    SBAR();
    if (__builtin_expect(sw, 0)) { store_ctx(st.o, __builtin_amdgcn_rcpf(st.l[0][0]), __builtin_amdgcn_rcpf(st.l[1][0]), c.srdC, ((c.lane16 >> 8) << 9) + (c.lane16 & 0xf0u) + c.coffL);
#pragma unroll
        for (int d = 0; d < 8; ++d) { st.o[d][0] = f32x4{}; st.o[d][1] = f32x4{}; PIN(st.o[d][0]); PIN(st.o[d][1]); }
        st.l[0] = f32x4{}; st.l[1] = f32x4{}; PIN(st.l[0]); PIN(st.l[1]); c.rel = c.relH; }
    if (__builtin_expect(c.rel > c.thrg, 0)) {
        int thr = 32 * c.g - c.rel + c.dq0; PIN(thr); const float NEG = -__builtin_inff();
#pragma unroll
        for (int kb = 0; kb < 4; ++kb)
#pragma unroll
            for (int qb = 0; qb < 2; ++qb)
#pragma unroll
                for (int e = 0; e < 4; ++e) if (16 * kb - 16 * qb + e > thr) st.s[kb][qb][e] = NEG;
    }
    SBAR();
    if (!LAST) dma_piece<0, true>(vm0, c.voff, c.srdV, vso);
#define MX3(a, b, c) __builtin_fmaxf(__builtin_fmaxf((a), (b)), (c))
    float mx[2];
#pragma unroll
    for (int qb = 0; qb < 2; ++qb) {
        float a = MX3(st.s[0][qb][0], st.s[0][qb][1], st.s[0][qb][2]), b = MX3(st.s[0][qb][3], st.s[1][qb][0], st.s[1][qb][1]);
        a = MX3(a, st.s[1][qb][2], st.s[1][qb][3]); b = MX3(b, st.s[2][qb][0], st.s[2][qb][1]); a = MX3(a, st.s[2][qb][2], st.s[2][qb][3]);
        b = MX3(b, st.s[3][qb][0], st.s[3][qb][1]); a = MX3(a, st.s[3][qb][2], st.s[3][qb][3]); mx[qb] = __builtin_fmaxf(a, b); }
    const unsigned long long firstm = (t == 0 || sw) ? ~0ull : 0ull;
    if (__builtin_expect((__builtin_amdgcn_ballot_w64(__builtin_fmaxf(mx[0], mx[1]) > ATT_THR) | firstm) != 0ull, 0)) {
        const bool first = firstm != 0ull;
#pragma unroll
        for (int qb = 0; qb < 2; ++qb) {
            const float delta = __builtin_fmaxf(row_max4(mx[qb]), first ? -1e30f : 0.f);
            st.mhat[qb] = (first ? 0.f : st.mhat[qb]) + delta;
#pragma unroll
            for (int kb = 0; kb < 4; ++kb) st.s[kb][qb] -= delta;
            st.negm[qb] = (f32x4){-st.mhat[qb], -st.mhat[qb], -st.mhat[qb], -st.mhat[qb]};
            if (!first) { const float f = __builtin_amdgcn_exp2f(-delta); st.l[qb] *= f;
#pragma unroll
                for (int d = 0; d < 8; ++d) st.o[d][qb] *= f; } }
    }
    SBAR();
    float e[8][4];
#define EXPG(k) do { _Pragma("unroll") for (int i_ = 0; i_ < 4; ++i_) e[k][i_] = __builtin_amdgcn_exp2f(st.s[(k) >> 1][(k) & 1][i_]); } while (0)
#define FING(k) do { constexpr int kb_ = (k) >> 1, qb_ = (k) & 1; \
        unsigned w0_ = cvtpk(e[k][0], e[k][1]), w1_ = cvtpk(e[k][2], e[k][3]); PIN(w0_); PIN(w1_); \
        st.p[kb_ >> 1][qb_][2 * (kb_ & 1)] = w0_; st.p[kb_ >> 1][qb_][2 * (kb_ & 1) + 1] = w1_; } while (0)
    EXPG(0); SBAR();
    EXPG(1); FING(0); SBAR();
    if (!LAST) dma_piece<1024, false>(vm0, c.voff, c.srdV, vso);
    EXPG(2); FING(1); SBAR();
    EXPG(3); FING(2); SBAR();
    if (!LAST) dma_piece<2048, false>(vm0, c.voff, c.srdV, vso);
    EXPG(4); FING(3); SBAR();
    EXPG(5); FING(4); SBAR();
    if (!LAST) dma_piece<3072, false>(vm0, c.voff, c.srdV, vso);
    EXPG(6); FING(5); SBAR();
    EXPG(7); FING(6); SBAR();
    FING(7);
#undef EXPG
#undef FING
    if (!LAST) {
        if (__builtin_expect(t == c.nl - 1, 0)) {
            unsigned qs = c.qstash; PIN(qs);
#pragma unroll
            for (int s = 0; s < 8; ++s) st.q[s] = ldsf(qs + s * 1024);
            st.negm[0] = f32x4{}; st.negm[1] = f32x4{}; PIN(st.negm[0]); PIN(st.negm[1]);
        }
        c.ksoM = kso;
        c.soA = c.soB; c.soB += (t + 3 == c.nl) ? c.jump : 16384; c.rel += 64;
    }
    SBAR();
}

DI void attn_setup(AttnCtx& c, const u16* Kf, const u16* Vf, u16* Cf, unsigned lds0, int h, int j, int wid, int lane) {
    PIN(lane);
    const int grp = wid >> 2; c.g = wid & 3; c.dq0 = (lane & 15) - 4 * (lane >> 4);
    const int qbH = 15 - j, qbL = j, nl = 2 * j + 2;
    c.nl = grp ? nl : 1000; c.thrg = 32 * c.g - 63; c.jump = (18 - nl) * 16384; c.relH = 64 * (17 - 2 * qbH);
    c.rel = grp ? -128 * qbL : -128 * qbH;
    c.soA = 16384; c.soB = (c.nl == 2) ? 17 * 16384 : 2 * 16384;
    c.srdK = mk_srd((const char*)Kf + (size_t)h * 524288, 524288);
    c.srdV = mk_srd((const char*)Vf + (size_t)h * 524288, 524288);
    c.srdC = mk_srd((const char*)Cf, 16777216);
    c.lane16 = lane * 16; c.voff = lane * 16 + c.g * 4096;
    c.kslot0 = lds0 + grp * 65536 + c.g * 4096; c.vslot0 = c.kslot0 + 32768;
    c.coffL = ((qbL * 4 + c.g) * 32 + h) * 8192;
    c.qstash = lds0 + 131072 + c.g * 8192 + lane * 16;
}
#define DMA4(m_, vo_, srd, so_) do { dma_piece<0, true>(m_, vo_, srd, so_); dma_piece<1024, false>(m_, vo_, srd, so_); dma_piece<2048, false>(m_, vo_, srd, so_); dma_piece<3072, false>(m_, vo_, srd, so_); } while (0)
DI void attn_prime(const AttnCtx& c, int grp) { DMA4(grp ? c.vslot0 + 16384 : c.kslot0, c.voff, c.srdK, 0u); DMA4(c.vslot0, c.voff, c.srdV, 0u); if (grp == 0) DMA4(c.kslot0 + 16384, c.voff, c.srdK, 16384u); }

template <class First, class G0Pre, class G0Tail>
DI void attn_run(AttnState& st, AttnCtx& c, unsigned lds0, int h, int j, int wid, int lane, First afterFirst, G0Pre g0pre, G0Tail g0tail, Stamper& stamp) {
    PIN(lane);
    const int grp = wid >> 2;
    const unsigned kbase = lds0 + grp * 65536 + lane * 16;
    const unsigned vbase = lds0 + grp * 65536 + 32768 + lane * 16;
#pragma unroll
    for (int d = 0; d < 8; ++d) { st.o[d][0] = f32x4{}; st.o[d][1] = f32x4{}; PIN(st.o[d][0]); PIN(st.o[d][1]); }
    st.negm[0] = f32x4{}; st.negm[1] = f32x4{}; st.l[0] = f32x4{}; st.l[1] = f32x4{}; st.mhat = (f32x2){0.f, 0.f};
    st.ones = (u32x4){0x3f803f80u, 0x3f803f80u, 0x3f803f80u, 0x3f803f80u}; PIN(st.ones); PIN(st.l[0]); PIN(st.l[1]);
    PIN(st.negm[0]); PIN(st.negm[1]);
    if (grp == 1) { DMA4(c.kslot0 + 16384, c.voff, c.srdK, 16384u); BARRIER(); }
    wait_bar<8>();
    stamp();
    bf16x8 pre[ATT_W];
#define PRE_V(P) do { _Pragma("unroll") for (int i = 0; i < ATT_W; ++i) pre[i] = ldsf(vbase + (P) * 16384 + m_off(i)); } while (0)
    const unsigned kbase0 = grp ? kbase + 49152 : kbase;
#pragma unroll
    for (int i = 0; i < ATT_W; ++i) pre[i] = ldsf(kbase0 + m_off(16 + i));
    __builtin_amdgcn_s_setprio(1);
    m_phase<false, true, 0, 0>(st, kbase0, vbase, pre, NoHookI{});
    __builtin_amdgcn_s_setprio(0);
    wait_bar<0>();
    afterFirst();
#define PSTAMP(t) do {} while (0)
#define STEP(PAR, t) do { \
        v_phase<false, PAR>(st, c, t); \
        PRE_V(PAR); \
        PSTAMP(t); \
        wait_bar_keep<(KSPLIT ? 6 : 4)>(); \
        PSTAMP(t); \
        __builtin_amdgcn_s_setprio(1); \
        m_phase<true, true, PAR ^ 1, PAR>(st, kbase, vbase, pre, [&](int i) { \
            if (!KSPLIT) { if (i == 3) dma_piece<0, true>(c.kslot0 + PAR * 16384, c.voff, c.srdK, c.ksoM); if (i == 9) dma_piece<1024, false>(c.kslot0 + PAR * 16384, c.voff, c.srdK, c.ksoM); } \
            if (i == (KSPLIT ? 5 : 15)) dma_piece<2048, KSPLIT>(c.kslot0 + PAR * 16384, c.voff, c.srdK, c.ksoM); if (i == (KSPLIT ? 17 : 21)) dma_piece<3072, false>(c.kslot0 + PAR * 16384, c.voff, c.srdK, c.ksoM); }); \
        __builtin_amdgcn_s_setprio(0); \
        PSTAMP(t); \
        wait_bar<4>(); \
        PSTAMP(t); } while (0)
    for (int t = 0; t < 16; t += 2) { stamp(); STEP(0, t); STEP(1, t + 1); }
    stamp();
    v_phase<true, 0>(st, c, 16);
    if (grp == 0) g0pre();
    PRE_V(0);
    asm volatile("s_barrier" ::: "memory");
    __builtin_amdgcn_s_setprio(1);
    m_phase<true, false, 0, 0>(st, kbase, vbase, pre, NoHookI{});
    __builtin_amdgcn_s_setprio(0);
    wait_bar<0>();
    stamp();
    PIN(lane);
    const unsigned xo = lds0 + grp * 65536 + c.g * 8192 + lane * 16;
    const unsigned xi = lds0 + (grp ^ 1) * 65536 + c.g * 8192 + lane * 16;
    const unsigned so = lds0 + grp * 65536 + 32768 + c.g * 512 + lane * 8, si = lds0 + (grp ^ 1) * 65536 + 32768 + c.g * 512 + lane * 8;
    const unsigned voff = (lane >> 4) * 512 + (lane & 15) * 16 + (((15 - j) * 4 + c.g) * 32 + h) * 8192;
#define MERGE_PUT(qb) do { _Pragma("unroll") for (int d = 0; d < 8; ++d) *(LAS f32x4*)(xo + d * 1024) = st.o[d][qb]; *(LAS f32x2*)so = (f32x2){st.mhat[qb], st.l[qb][0]}; } while (0)
#define MERGE_FIN(qb) do { const f32x2 ot = *(const LAS f32x2*)si; const float mh = st.mhat[qb], lt = st.l[qb][0]; \
        const float m = __builtin_fmaxf(ot[0], mh), f0 = __builtin_amdgcn_exp2f(ot[0] - m), f1 = __builtin_amdgcn_exp2f(mh - m); \
        const float inv = __builtin_amdgcn_rcpf(ot[1] * f0 + lt * f1), a0 = f0 * inv, a1 = f1 * inv; \
        _Pragma("unroll") for (int p = 0; p < 4; ++p) { \
            const f32x4 lo = st.o[2 * p][qb] * a1 + *(const LAS f32x4*)(xi + (2 * p) * 1024) * a0, hi = st.o[2 * p + 1][qb] * a1 + *(const LAS f32x4*)(xi + (2 * p + 1) * 1024) * a0; \
            __builtin_amdgcn_raw_buffer_store_b128((u32x4){cvtpk(lo[0], lo[1]), cvtpk(lo[2], lo[3]), cvtpk(hi[0], hi[1]), cvtpk(hi[2], hi[3])}, c.srdC, voff + p * 2048 + (qb) * 256, 0, 16); } } while (0)
    if (grp == 0) { MERGE_PUT(1); BARRIER(); g0tail(); BARRIER(); MERGE_FIN(0); asm volatile("; merge 0"); }
    else          { asm volatile("; merge 1"); MERGE_PUT(0); BARRIER(); MERGE_FIN(1); }
#undef MERGE_PUT
#undef MERGE_FIN
    stamp();
#undef PRE_V
#undef STEP
}
constexpr int OUT_LDS = 163840;
constexpr unsigned OUT_O = 131072;
struct OutCtx { srd_t srdC, srdW, srdB; unsigned lane16, vw0, vwA, sC, sW, sB, lds0; int wid, rot; };
DI void out_issueA(const OutCtx& c, int kt, int i) {
    const unsigned st = c.lds0 + (kt & 3) * 32768;
    const int kr = (kt + c.rot) & 15;
    if (i < 2) { const int p = 2 * c.wid + i;
        dma1s(st + p * 1024, c.lane16, c.srdC, c.sC + (p >> 2) * 262144 + (p & 3) * 1024 + kr * 4096); }
    else dma1(st + 16384 + (2 * c.wid + (i - 2)) * 1024, c.vwA, c.srdW, c.sW + (8 * c.wid) * 1024 + (i - 2) * 128 + kr * 65536);
}
DI void out_issueB(const OutCtx& c, int ch, int i) {
    const int rg = 2 * c.wid + (i >> 1);
    dma1(c.lds0 + ch * 32768 + rg * 2048 + (i & 1) * 1024, (i >> 1) ? (c.vw0 ^ 32u) : c.vw0, c.srdB, c.sB + (ch * 128 + 8 * rg) * 1024 + (i & 1) * 128);
}
DI u32x4 out_pack(f32x4 s0, f32x4 s1) {
    unsigned a0 = cvtpk(s0[0], s0[1]), a1 = cvtpk(s0[2], s0[3]), b0 = cvtpk(s1[0], s1[1]), b1 = cvtpk(s1[2], s1[3]);
    auto x0 = __builtin_amdgcn_permlane32_swap(a0, b0, false, false); auto x1 = __builtin_amdgcn_permlane32_swap(a1, b1, false, false);
    return (u32x4){x0[0], x1[0], x0[1], x1[1]};
}
DI f32x4 quad(const f32x16& x, int rq) { return (f32x4){x[4 * rq], x[4 * rq + 1], x[4 * rq + 2], x[4 * rq + 3]}; }

DI void out_phase(const u16* Wo, const u16* Bwo, u16* out, char* ws, u64 tok, unsigned lds0, int bid, int wid, int lane, Stamper& stamp) {
    PIN(lane);
    const int tid = wid * 64 + lane, r32 = lane & 31, hi = lane >> 5, wr = wid >> 2, wc = wid & 3;
    const int x = bid & 7, u = bid >> 3, s = x >> 1, rb = 8 * (x & 1) + (u >> 2), cb = u & 3, tile = rb * 4 + cb;
    OutCtx c; c.wid = wid; c.lds0 = lds0; c.lane16 = lane * 16;
    c.srdC = mk_srd(ws + WS_CF + (size_t)rb * 4 * 262144, 4 * 262144); c.srdW = mk_srd(Wo, 4194304); c.srdB = mk_srd(Bwo, 524288);
    c.vw0 = ((lane >> 2) & 7) * 1024 + (4 * (lane >> 5) + ((lane & 3) ^ ((lane >> 4) & 1))) * 16; c.vwA = (wid & 1) ? (c.vw0 ^ 32u) : c.vw0;
    c.rot = 4 * cb + 2 * (rb & 7);
    c.sC = s * 8 * 8192; c.sW = s * 1024 * 1024 + cb * 256; c.sB = cb * 256;
#pragma unroll
    for (int kt = 0; kt < 4; ++kt) { out_issueA(c, kt, 2); out_issueA(c, kt, 3); }
    u64* flags = (u64*)(ws + WS_FLAG);
    {
        const int l = lane & 15, hh = 8 * s + (l & 7), jj = rb < 8 ? rb : 15 - rb;
        const int src = l < 8 ? ((hh >> 2) | (jj << 3) | ((hh & 3) << 6)) : (8 * (2 * rb + (s >> 1)) + (l & 7));
        const u64* fp = flags + (FL_C + src) * FL_STRIDE;
        for (unsigned spins = 0; spins < (1u << 15); ++spins) { const u64 v = ld_agent(fp); if (__all(v == tok)) break; __builtin_amdgcn_s_sleep(1); }
        asm volatile("" ::: "memory"); }
    stamp();
#pragma unroll
    for (int kt = 0; kt < 4; ++kt) { out_issueA(c, kt, 0); out_issueA(c, kt, 1); }
    { const srd_t srdO = mk_srd(ws + WS_OF + (size_t)(rb * 4 + s) * 32768, 32768);
#pragma unroll
      for (int i = 0; i < 4; ++i) dma1s(lds0 + OUT_O + (4 * wid + i) * 1024, c.lane16, srdO, (unsigned)((4 * wid + i) * 1024)); }
    const int q4 = (lane & 15) >> 2, p4 = lane & 3, blk = (lane >> 4) & 1;
    const unsigned aaddr = lds0 + (2 * wr) * 4096 + c.lane16;
    unsigned waA[2], waddr[2];
#pragma unroll
    for (int t = 0; t < 2; ++t) { waA[t] = lds0 + 2048 * t + 512 * wc + 64 * (4 * hi + q4) + 16 * ((2 * blk + (p4 >> 1)) ^ ((2 * t + hi) & 3)) + 8 * (p4 & 1);
                                  waddr[t] = waA[t]; }
    f32x16 acc[2] = {f32x16{}, f32x16{}};
    stamp();
    struct Fr { bf16x8 a[2][2]; bf16x4 w[2][2]; };
    auto rd = [&](Fr& f, unsigned st, int half) {
        unsigned w0 = waA[0] + st + 16384 + half * 8192, w1 = waA[1] + st + 16384 + half * 8192, aa = aaddr + st; PIN(w0); PIN(w1); PIN(aa);
#pragma unroll
        for (int k = 0; k < 2; ++k) { const int ks = 2 * half + k; const unsigned wb = k ? w1 : w0;
            f.w[k][0] = ldstr(wb); f.w[k][1] = ldstr(wb + 4096);
            f.a[k][0] = ldsf(aa + ks * 1024); f.a[k][1] = ldsf(aa + 4096 + ks * 1024); } };
    Fr f0, f1;
    wait_bar<10>();
    rd(f0, 0u, 0);
    auto step = [&](int kt, auto late, auto early) {
        const unsigned st = (kt & 3) * 32768, stn = ((kt + 1) & 3) * 32768;
        rd(f1, st, 1);
        SBAR();
#pragma unroll
        for (int k = 0; k < 2; ++k) { const bf16x8 wf = cat4(f0.w[k][0], f0.w[k][1]);
            acc[0] = MFMA32(wf, f0.a[k][0], acc[0]); acc[1] = MFMA32(wf, f0.a[k][1], acc[1]); SBAR(); late(2 + k); SBAR(); }
        wait_bar<8>();
        rd(f0, stn, 0);
        SBAR();
#pragma unroll
        for (int k = 0; k < 2; ++k) { const bf16x8 wf = cat4(f1.w[k][0], f1.w[k][1]);
            acc[0] = MFMA32(wf, f1.a[k][0], acc[0]); acc[1] = MFMA32(wf, f1.a[k][1], acc[1]); SBAR(); early(k); SBAR(); }
    };
    step(0, [&](int) {}, [&](int i) { out_issueA(c, 4, i); });
    for (int kt = 1; kt < 12; ++kt) step(kt, [&](int i) { out_issueA(c, kt + 3, i); }, [&](int i) { out_issueA(c, kt + 4, i); });
    step(12, [&](int i) { out_issueA(c, 15, i); }, [&](int i) { out_issueB(c, 0, i); });
    step(13, [&](int i) { out_issueB(c, 0, i); }, [&](int i) { out_issueB(c, 1, i); });
    step(14, [&](int i) { out_issueB(c, 1, i); }, [&](int i) { out_issueB(c, 2, i); });
    step(15, [&](int i) { out_issueB(c, 2, i); }, [&](int i) { out_issueB(c, 3, i); });
    out_issueB(c, 3, 2); out_issueB(c, 3, 3);
    stamp();
    const srd_t srdX = mk_srd(ws + WS_XCH + (size_t)tile * 262144, 262144);
#pragma unroll
    for (int mb = 0; mb < 2; ++mb) { const int q = 2 * wr + mb;
        if (q != s) {
#pragma unroll
            for (int rq = 0; rq < 4; ++rq) store16_sc1(__builtin_bit_cast(u32x4, quad(acc[mb], rq)), c.lane16, srdX, (unsigned)(((q * 4 + s) * 4 + wc) * 4096 + rq * 1024)); } }
    wait_bar<0>();
    if (tid == 0) st_agent(flags + (FL_XF + tile * 4 + s) * FL_STRIDE, tok);
    stamp();
    const int row = rb * 128 + 32 * s + r32;
    if (wr == (s >> 1)) {
        const u64* fp = flags + (FL_XF + tile * 4 + (lane & 3)) * FL_STRIDE;
        for (unsigned spins = 0; spins < (1u << 15); ++spins) { const u64 v = ld_agent(fp); if (__all(v == tok)) break; __builtin_amdgcn_s_sleep(1); }
        asm volatile("" ::: "memory");
        f32x4 p[4][4];
#pragma unroll
        for (int q = 0; q < 4; ++q)
#pragma unroll
            for (int rq = 0; rq < 4; ++rq) p[q][rq] = __builtin_bit_cast(f32x4, __builtin_amdgcn_raw_buffer_load_b128(srdX, (q == s ? 0x7ffffff0u : c.lane16) + rq * 1024, ((s * 4 + q) * 4 + wc) * 4096, 16));
        f32x4 r[4];
#pragma unroll
        for (int rq = 0; rq < 4; ++rq) r[rq] = ((p[0][rq] + p[1][rq]) + (p[2][rq] + p[3][rq])) + ((s & 1) ? quad(acc[1], rq) : quad(acc[0], rq));
        u16* o = out + (size_t)row * 1024 + cb * 128 + 32 * wc + 8 * hi;
        *(u32x4*)o = out_pack(r[0], r[1]); *(u32x4*)(o + 16) = out_pack(r[2], r[3]);
    } else {
        f32x16 accB[2] = {f32x16{}, f32x16{}};
#pragma unroll
        for (int ch = 0; ch < 4; ++ch)
#pragma unroll
            for (int hf = 0; hf < 2; ++hf) {
                unsigned w0 = waddr[0] + ch * 32768 + hf * 16384, w1 = waddr[1] + ch * 32768 + hf * 16384, oa = lds0 + OUT_O + (8 * ch + 4 * hf) * 1024 + c.lane16; PIN(w0); PIN(w1); PIN(oa);
                bf16x8 a[4]; bf16x4 w[4][2];
#pragma unroll
                for (int ks = 0; ks < 4; ++ks) { w[ks][0] = ldstr(w0 + ks * 4096); w[ks][1] = ldstr(w1 + ks * 4096); a[ks] = ldsf(oa + ks * 1024); }
#pragma unroll
                for (int ks = 0; ks < 4; ++ks) accB[ks & 1] = MFMA32(cat4(w[ks][0], w[ks][1]), a[ks], accB[ks & 1]);
            }
        const f32x16 rB = accB[0] + accB[1];
        u16* o = out + (size_t)row * 1024 + 512 + cb * 128 + 32 * wc + 8 * hi;
        *(u32x4*)o = out_pack(quad(rB, 0), quad(rB, 1)); *(u32x4*)(o + 16) = out_pack(quad(rB, 2), quad(rB, 3));
    }
}
#define TLBWARM 0
#define CODEWARM 1
constexpr unsigned CODE_BYTES = 52 * 1024;
constexpr int FUSED_LDS = 163840;
constexpr unsigned WPHI_LDS = 156160;
struct FusedArgs { const u16 *X, *Ckv, *Wq, *Wuk, *Wuv, *Wo, *Bx, *Wqkv, *Wphi, *Bwo; char* ws; u16* out; };

__global__ __launch_bounds__(512, 2) void fused_kernel(FusedArgs a) {
    const int tid = threadIdx.x, lane = tid & 63;
    const int wid = rfl(tid >> 6);
    const int bid = blockIdx.x, h = (bid & 7) * 4 + (bid >> 6), j = (bid >> 3) & 7;
    if (bid < CODEWARM * 8) {
        u64 pc_; asm volatile("s_getpc_b64 %0" : "=s"(pc_));
        const unsigned o_ = wid * 8192 + lane * 128;
        asm volatile("s_mov_b32 m0, %0\n\ts_nop 0\n\tglobal_load_lds_dword %1, %2" :: "s"(163840u - 256u), "v"(o_ < CODE_BYTES ? o_ : 0u), "s"(pc_ & ~(u64)127) : "memory"); }
    const unsigned lds0 = 0u;
    Stamper stamp{0u, 0, lane};
    stamp();
    u16* Kf = (u16*)(a.ws + WS_KF); u16* Vf = (u16*)(a.ws + WS_VF); u16* Cf = (u16*)(a.ws + WS_CF);
    u16* PhiQ = (u16*)(a.ws + WS_PHIQ); u16* Of = (u16*)(a.ws + WS_OF); float* Bst = (float*)(a.ws + WS_BST);
    u64* flags = (u64*)(a.ws + WS_FLAG);
    float* Bred = (float*)(a.ws + WS_BRED);
    const int hg = bid & 7, rc = bid >> 3;
    asm volatile("" :: "s"(a.X), "s"(a.Ckv), "s"(a.Wq), "s"(a.Wuk), "s"(a.Wuv), "s"(a.Wphi), "s"(a.ws));
    ProjCtx pc; proj_setup(pc, a.X, a.Ckv, a.Wq, a.Wuk, a.Wuv, Kf, Vf, lds0, h, j, wid, lane);
#pragma unroll
    for (int i = 0; i < 8; ++i) issueKV(pc, 0, i);
#pragma unroll
    for (int i = 0; i < 8; ++i) issueKV(pc, 1, i);
    const unsigned tsc = lds0 + TOUCH_LDS;
    { const unsigned vW = (lane >> 4) * 524288 + (lane & 15) * 128;
      const char* base; unsigned vo = pc.l128;
      if (wid < 4) { base = (const char*)((wid & 1) ? a.Wuv : a.Wuk) + ((wid & 2) ? 4 : 2) * 524288 + bid * 2048 - ((wid & 2) ? 0 : 0); vo = vW; }
      else if (wid == 4) base = (const char*)a.Ckv + bid * 8192;
      else { base = (const char*)a.Ckv; vo = 0; }
      touch_g<1>(tsc, vo, base); }
    if (wid < 2) dma1(lds0 + WPHI_LDS + wid * 1024, lane * 16, mk_srd(a.Wphi, 16384), (unsigned)(hg * 2048 + wid * 1024));
    const u64 epoch = *(const u64 __attribute__((address_space(4)))*)(flags + FL_EPOCH * FL_STRIDE);
    u64 tok = mix64(epoch); if (tok == epoch) tok ^= 1ull;
    tok = ((u64)rfl((unsigned)(tok >> 32)) << 32) | rfl((unsigned)tok); asm volatile("" : "+s"(tok));
    B1Ctx bc; b1_setup(bc, a.Bx, a.Wqkv, lds0, rc, hg, wid, lane);
    stamp();
    auto touchk = [&](int kt) {
        const char* base = (const char*)a.Ckv; unsigned vo = 0;
        if (kt == 0) {
            if (wid == 0) { base = (const char*)a.Bx + bid * 8192; vo = pc.l128; }
            else if (wid == 1) { base = (const char*)a.X + bid * 8192; vo = pc.l128; }
            else if (wid == 2) { base = (const char*)a.Wqkv + (bid < 192 ? bid : 0) * 8192; vo = pc.l128; }
        } else if (kt == 1) {
            if (wid < 2) { base = (const char*)a.Wq + (bid * 2 + wid) * 8192; vo = pc.l128; }
        } else if (kt == 2) {
            if (wid < 2) { base = (const char*)a.Wo + (bid * 2 + wid) * 8192; vo = pc.l128; }
            else if (wid == 2) { base = (const char*)a.Bwo + (bid & 63) * 8192; vo = pc.l128; }
        }
        touch_g<2>(tsc, vo, base); };
    u32x4 pk[16];
    kv_run(pc, lane, pk, touchk, [&](int kt) { b1_issue(bc, kt); }, stamp);
    stamp();
    B1Acc ba;
    const bool early = (j & 3) == 0;
    b1_gemm(bc, ba, lane, early, [&](int n) { if (early) kv_store(pc, pk, n); else if (n >= 1 && n <= 6) kv_store(pc, pk, n - 1); },
            [&](int n) {
#pragma unroll
                for (int i = 0; i < 6; ++i) issueQ(pc, n, i); });
    stamp();
    u32x2 wp; { const int l16 = lane & 15, g = lane >> 4; const LAS u16* w = (const LAS u16*)(lds0 + WPHI_LDS + (wid & 3) * 512);
        wp[0] = (unsigned)w[(4 * g) * 16 + l16] | ((unsigned)w[(4 * g + 1) * 16 + l16] << 16); wp[1] = (unsigned)w[(4 * g + 2) * 16 + l16] | ((unsigned)w[(4 * g + 3) * 16 + l16] << 16); }
    b1_phi(ba, [&](int n) { if (early) { if (n % 4 == 2) kv_store(pc, pk, 8 + n / 4); } else if (n % 3 == 1 && n < 14) kv_store(pc, pk, 6 + n / 3); }, wp, PhiQ, Bst, lds0, rc, hg, wid, lane, stamp);
    stamp();
    wait_bar<21>();
    stamp();
    AttnState st;
    AttnCtx c; attn_setup(c, Kf, Vf, Cf, lds0, h, j, wid, lane);
    B2a b2;
    auto spin = [&](const u64* fp) {
        for (unsigned spins = 0; spins < (1u << 15); ++spins) { const u64 v = ld_agent(fp); if (__all(v == tok)) break; __builtin_amdgcn_s_sleep(1); }
        asm volatile("" ::: "memory"); };
    const u64* fkv = flags + (FL_KV + 8 * h + (lane & 7)) * FL_STRIDE;
    const u64* fbs = flags + (FL_BS + 32 * hg + (lane & 31)) * FL_STRIDE;
    u64 pre = 0;
    q_run(pc, early, lane, pk, st.q, [&](int kt) {
        if (kt == 2) { if (tid == 0) st_agent(flags + (FL_BS + 32 * hg + rc) * FL_STRIDE, tok); }
        if (kt == 3) { if (early && tid == 0) st_agent(flags + (FL_KV + 8 * h + j) * FL_STRIDE, tok); }
        if (kt == 3) { if (wid == 7) pre = ld_agent(fbs); }
        if (kt == 4) {
            if (wid == 7) {
                if (!__all(pre == tok)) spin(fbs);
                asm volatile("" ::: "memory");
                b2a_load(b2, Bst, rc, hg, lane); } }
        if (kt == 5) { pre = ld_agent(fkv); }
        if (kt == 6) {
            if (!early && tid == 0) st_agent(flags + (FL_KV + 8 * h + j) * FL_STRIDE, tok);
            if (wid == 7) b2a_finish(b2, Bred, rc, hg, lane);
            c.known = (unsigned)__builtin_amdgcn_ballot_w64(pre == tok) & 0xffu;
            for (unsigned spins = 0; spins < (1u << 15) && !(c.known & 1u); ++spins) { const u64 v = ld_agent(fkv); c.known = (unsigned)__builtin_amdgcn_ballot_w64(v == tok) & 0xffu; if (!(c.known & 1u)) __builtin_amdgcn_s_sleep(1); }
            asm volatile("" ::: "memory");
            attn_prime(c, wid >> 2);
        } }, stamp);
    c.srdF = mk_srd(flags + (FL_KV + 8 * h) * FL_STRIDE, 1024); c.tok = tok; c.prev = 0;
    stamp();
    BARRIER();
    if (wid < 4) {
#pragma unroll
        for (int s = 0; s < 8; ++s) *(LAS bf16x8*)(c.qstash + s * 1024) = st.q[s]; }
    stamp();
    B2b<4> b2b;
    attn_run(st, c, lds0, h, j, wid, lane,
             [&]() { if (tid == 448) st_agent(flags + (FL_BR + 32 * hg + rc) * FL_STRIDE, tok); },
             [&]() { b2b_load<4>(b2b, PhiQ, Bred, flags + (FL_BR + 32 * hg + 8 * wid) * FL_STRIDE, tok, 4 * hg + wid, 4 * rc, lane); },
             [&]() { b2b_finish<4>(b2b, Of, 4 * hg + wid, 4 * rc, lane); }, stamp);
    wait_bar<0>();
    if (tid == 0) st_agent(flags + (FL_C + bid) * FL_STRIDE, tok);
    stamp();
    out_phase(a.Wo, a.Bwo, a.out, a.ws, tok, lds0, bid, wid, lane, stamp);
    if (bid == 0 && wid == 7) {
        bool ok = false;
        for (unsigned spins = 0; spins < (1u << 15) && !ok; ++spins) { bool all = true;
#pragma unroll
            for (int i = 0; i < 4; ++i) all &= ld_agent(flags + (FL_C + lane + 64 * i) * FL_STRIDE) == tok;
            ok = __all(all); if (!ok) __builtin_amdgcn_s_sleep(1); }
        if (lane == 0) st_agent(flags + FL_EPOCH * FL_STRIDE, tok);
    }
    stamp();
}

#define WS_END 1
#define WS_SHIFT(n) (WS_END && (n) >= ((size_t)160 << 20) ? (((n) - ((size_t)80 << 20)) & ~(size_t)0x1fffff) : (size_t)0)
extern "C" void kernel_launch(void* const* d_in, const int* in_sizes, int n_in, void* d_out, int out_size, void* d_ws, size_t ws_size, hipStream_t stream) {
    FusedArgs a;
    a.X = (const u16*)d_in[0]; a.Ckv = (const u16*)d_in[1]; a.Wq = (const u16*)d_in[2]; a.Wuk = (const u16*)d_in[3]; a.Wuv = (const u16*)d_in[4]; a.Wo = (const u16*)d_in[5];
    a.Bx = (const u16*)d_in[6]; a.Wqkv = (const u16*)d_in[7]; a.Wphi = (const u16*)d_in[8]; a.Bwo = (const u16*)d_in[9]; a.ws = (char*)d_ws + WS_SHIFT(ws_size); a.out = (u16*)d_out;
    (void)hipFuncSetAttribute((const void*)fused_kernel, hipFuncAttributeMaxDynamicSharedMemorySize, FUSED_LDS);
    fused_kernel<<<256, 512, FUSED_LDS, stream>>>(a);
}
```

```cpp
#include <hip/hip_runtime.h>
#include <stdint.h>
#include <type_traits>

typedef unsigned short u16;
typedef unsigned long long u64;
typedef __bf16 bf16x8 __attribute__((ext_vector_type(8)));
typedef __bf16 bf16x4 __attribute__((ext_vector_type(4)));
typedef __bf16 bf16x2 __attribute__((ext_vector_type(2)));
typedef short s16x4 __attribute__((ext_vector_type(4)));
typedef float f32x16 __attribute__((ext_vector_type(16)));
typedef float f32x4 __attribute__((ext_vector_type(4)));
typedef float f32x2 __attribute__((ext_vector_type(2)));
typedef unsigned u32x4 __attribute__((ext_vector_type(4)));
typedef unsigned u32x2 __attribute__((ext_vector_type(2)));
typedef __amdgpu_buffer_rsrc_t srd_t;
#define LAS __attribute__((address_space(3)))
#define GAS __attribute__((address_space(1)))
#define SBAR() __builtin_amdgcn_sched_barrier(0)
#define PIN(x) asm volatile("" : "+v"(x))
#define MFMA32(a, b, c) __builtin_amdgcn_mfma_f32_32x32x16_bf16((a), (b), (c), 0, 0, 0)
#define MFMA16(a, b, c) __builtin_amdgcn_mfma_f32_16x16x32_bf16((a), (b), (c), 0, 0, 0)
#define DI __device__ __forceinline__
#define MFMA16I(acc, a, b) asm volatile("v_mfma_f32_16x16x32_bf16 %0, %1, %2, %0" : "+v"(acc) : "v"(a), "v"(b))
#define MFMA_DRAIN() asm volatile("s_nop 7")
#pragma clang diagnostic ignored "-Wint-to-pointer-cast"

DI unsigned cvtpk(float lo, float hi) { f32x2 v = {lo, hi}; bf16x2 b = __builtin_convertvector(v, bf16x2); return __builtin_bit_cast(unsigned, b); }
DI unsigned rfl(unsigned x) { return __builtin_amdgcn_readfirstlane(x); }
DI float swap_max(float x) { auto rr = __builtin_amdgcn_permlane32_swap(__float_as_uint(x), __float_as_uint(x), false, false); return __builtin_fmaxf(__uint_as_float(rr[0]), __uint_as_float(rr[1])); }
DI float swap_add(float x) { auto rr = __builtin_amdgcn_permlane32_swap(__float_as_uint(x), __float_as_uint(x), false, false); return __uint_as_float(rr[0]) + __uint_as_float(rr[1]); }
DI srd_t mk_srd(const void* p, int bytes) { return __builtin_amdgcn_make_buffer_rsrc((void*)p, (short)0, bytes, 0x00020000); }
DI bf16x4 ldstr(unsigned a) { return __builtin_amdgcn_ds_read_tr16_b64_v4bf16((LAS bf16x4*)a); }
DI bf16x8 ldsf(unsigned a) { return *(const LAS bf16x8*)a; }
DI bf16x8 cat4(bf16x4 a, bf16x4 b) { return __builtin_shufflevector(a, b, 0, 1, 2, 3, 4, 5, 6, 7); }
#define SCALARIZE2(a, b) do {} while (0)
DI void dma1(unsigned m0v, unsigned voff, srd_t srd, unsigned soff) {
    SCALARIZE2(m0v, soff);
    asm volatile("s_mov_b32 m0, %0\n\ts_nop 0\n\tbuffer_load_dwordx4 %1, %2, %3 offen lds" :: "s"(m0v), "v"(voff), "s"(srd), "s"(soff) : "memory");
}
DI void dma1s(unsigned m0v, unsigned voff, srd_t srd, unsigned soff) {
    SCALARIZE2(m0v, soff);
    asm volatile("s_mov_b32 m0, %0\n\ts_nop 0\n\tbuffer_load_dwordx4 %1, %2, %3 offen sc1 lds" :: "s"(m0v), "v"(voff), "s"(srd), "s"(soff) : "memory");
}
DI void touch(unsigned m0v, unsigned voff, srd_t srd, unsigned soff) {
    SCALARIZE2(m0v, soff);
    asm volatile("s_mov_b32 m0, %0\n\ts_nop 0\n\tbuffer_load_dword %1, %2, %3 offen lds" :: "s"(m0v), "v"(voff), "s"(srd), "s"(soff) : "memory");
}
DI void store16_sc1(u32x4 w, unsigned voff, srd_t srd, unsigned soff) {
    unsigned z = soff; SCALARIZE2(soff, z);
    asm volatile("buffer_store_dwordx4 %0, %1, %2, %3 offen sc1\n\ts_nop 1" :: "v"(w), "v"(voff), "s"(srd), "s"(soff) : "memory");
}
template <int N> DI void wait_vm() { asm volatile("s_waitcnt vmcnt(%0)" :: "i"(N) : "memory"); }
template <int N> DI void wait_bar() { asm volatile("s_waitcnt vmcnt(%0) lgkmcnt(0)\n\ts_barrier" :: "i"(N) : "memory"); }
template <int N> DI void wait_bar_keep() { asm volatile("s_waitcnt vmcnt(%0)\n\ts_barrier" :: "i"(N) : "memory"); }
#define BARRIER() asm volatile("s_waitcnt lgkmcnt(0)\n\ts_barrier" ::: "memory")

struct Stamper { unsigned v; int n; int lane;
    DI void operator()() {
    } };

constexpr size_t WS_FLAG = 0;
constexpr size_t WS_KF = 1 << 20, WS_VF = WS_KF + (16 << 20), WS_CF = WS_VF + (16 << 20), WS_PHIQ = WS_CF + (16 << 20), WS_OF = WS_PHIQ + (4 << 20), WS_BST = WS_OF + (2 << 20), WS_BRED = WS_BST + (3 << 20), WS_XCH = WS_BRED + (1 << 20), WS_DBG = 77 << 20;
constexpr int FL_STRIDE = 16;
constexpr int FL_EPOCH = 0, FL_KV = 32, FL_BS = 32 + 256, FL_BR = 32 + 512, FL_XF = 32 + 768, FL_C = FL_XF + 256;
typedef GAS u64 gu64;
DI u64 ld_agent(const u64* p) { return __hip_atomic_load((gu64*)p, __ATOMIC_RELAXED, __HIP_MEMORY_SCOPE_AGENT); }
DI void st_agent(u64* p, u64 v) { __hip_atomic_store((gu64*)p, v, __ATOMIC_RELAXED, __HIP_MEMORY_SCOPE_AGENT); }
DI u64 mix64(u64 z) { z += 0x9E3779B97F4A7C15ull; z = (z ^ (z >> 30)) * 0xBF58476D1CE4E5B9ull; z = (z ^ (z >> 27)) * 0x94D049BB133111EBull; return z ^ (z >> 31); }
DI f32x4 mfma16_16(u32x2 a, u32x2 b, f32x4 c) { return __builtin_amdgcn_mfma_f32_16x16x16bf16_1k(__builtin_bit_cast(s16x4, a), __builtin_bit_cast(s16x4, b), c, 0, 0, 0); }
DI float rbf16(float x) { return __uint_as_float(cvtpk(x, 0.f) << 16); }
DI float x16_max(float x) { auto rr = __builtin_amdgcn_permlane16_swap(__float_as_uint(x), __float_as_uint(x), false, false); return __builtin_fmaxf(__uint_as_float(rr[0]), __uint_as_float(rr[1])); }
DI float x16_add(float x) { auto rr = __builtin_amdgcn_permlane16_swap(__float_as_uint(x), __float_as_uint(x), false, false); return __uint_as_float(rr[0]) + __uint_as_float(rr[1]); }
DI float grp_max(float x) { return swap_max(x16_max(x)); }
DI float grp_sum(float x) { return swap_add(x16_add(x)); }
#define PHIQ_AUX 16
constexpr float LOG2E = 1.4426950408889634f;
constexpr unsigned B1_RED = 147456;
#define BROT 0
struct B1Ctx { srd_t srdX, srdW; unsigned vx, vw[2], sx, sw; int wid, rot; unsigned lds0; };
DI void b1_setup(B1Ctx& c, const u16* Bx, const u16* Wqkv, unsigned lds0, int rc, int hg, int wid, int lane) {
    c.srdX = mk_srd(Bx, 2097152); c.srdW = mk_srd(Wqkv, 512 * 1536 * 2); c.wid = wid; c.lds0 = lds0;
    c.vx = (lane >> 3) * 1024 + (((lane & 7) ^ ((4 * wid + (lane >> 4)) & 7)) * 16);
#pragma unroll
    for (int p = 0; p < 2; ++p) c.vw[p] = (lane >> 3) * 3072 + (((lane & 7) ^ (2 * (((lane >> 4) & 1) | (p << 1)))) * 16);
    c.sx = (64 * rc + 8 * wid) * 1024; c.sw = hg * 128; c.rot = BROT ? rc & 7 : 0;
}
DI void b1_issue(const B1Ctx& c, int kt) {
    const unsigned st = c.lds0 + (kt & 3) * 32768;
    kt = (kt + c.rot) & 7;
    dma1(st + c.wid * 1024, c.vx, c.srdX, c.sx + kt * 128);
#pragma unroll
    for (int i = 0; i < 3; ++i) { const int pw = 3 * c.wid + i, m = pw >> 3, pr = pw & 7;
        dma1(st + 8192 + pw * 1024, (pr & 1) ? c.vw[1] : c.vw[0], c.srdW, c.sw + (kt * 64 + 8 * pr) * 3072 + m * 1024); }
}
struct B1Acc { f32x4 aq[2], ak[2], av[2]; };
struct B1Fr { bf16x8 x[2][2]; bf16x4 w[2][3][2]; };
struct B1Ad { unsigned xa[2][2], wa[2]; };
template <int KT> DI void b1_read(B1Fr& f, const B1Ad& ad) {
    constexpr unsigned st = (KT & 1) * 32768; constexpr int sp = (KT >> 1) & 1;
#pragma unroll
    for (int ks = 0; ks < 2; ++ks) { const unsigned xa = ad.xa[sp][ks];
        f.x[ks][0] = ldsf(xa + st); f.x[ks][1] = ldsf(xa + st + 2048);
#pragma unroll
        for (int m = 0; m < 3; ++m) { f.w[ks][m][0] = ldstr(ad.wa[sp] + st + m * 8192 + ks * 4096); f.w[ks][m][1] = ldstr(ad.wa[sp] + st + m * 8192 + ks * 4096 + 512); } }
}
DI void b1_mma(B1Acc& a, const B1Fr& f) {
#pragma unroll
    for (int ks = 0; ks < 2; ++ks) {
        const bf16x8 wq = cat4(f.w[ks][0][0], f.w[ks][0][1]), wk = cat4(f.w[ks][1][0], f.w[ks][1][1]), wv = cat4(f.w[ks][2][0], f.w[ks][2][1]);
        a.aq[0] = MFMA16(wq, f.x[ks][0], a.aq[0]); a.aq[1] = MFMA16(wq, f.x[ks][1], a.aq[1]); a.ak[0] = MFMA16(wk, f.x[ks][0], a.ak[0]); a.ak[1] = MFMA16(wk, f.x[ks][1], a.ak[1]);
        a.av[0] = MFMA16(f.x[ks][0], wv, a.av[0]); a.av[1] = MFMA16(f.x[ks][1], wv, a.av[1]);
    }
}
template <class St, class IQ>
DI void b1_gemm(const B1Ctx& c, B1Acc& a, int lane, bool early, St store, IQ issueQt, Stamper& stamp) {
    const int l16 = lane & 15, g = lane >> 4, q4 = l16 >> 2, p4 = lane & 3, wid = c.wid;
    const int row = 32 * (wid >> 2) + l16;
    B1Ad ad;
#pragma unroll
    for (int sp = 0; sp < 2; ++sp) {
        ad.xa[sp][0] = c.lds0 + sp * 65536 + row * 128 + (((0 + g) ^ ((row >> 1) & 7)) << 4); ad.xa[sp][1] = c.lds0 + sp * 65536 + row * 128 + (((4 + g) ^ ((row >> 1) & 7)) << 4);
        ad.wa[sp] = c.lds0 + sp * 65536 + 8192 + (8 * g + q4) * 128 + (((wid & 3) ^ (((q4 >> 1) & 1) | ((g & 1) << 1))) * 32) + p4 * 8;
        PIN(ad.xa[sp][0]); PIN(ad.xa[sp][1]); PIN(ad.wa[sp]); }
#pragma unroll
    for (int r = 0; r < 2; ++r) { a.aq[r] = f32x4{}; a.ak[r] = f32x4{}; a.av[r] = f32x4{}; }
#pragma unroll
    for (int r = 0; r < 2; ++r) { PIN(a.aq[r]); PIN(a.ak[r]); PIN(a.av[r]); }
    B1Fr f0, f1;
    b1_read<0>(f0, ad);
#define B1S() do {} while (0)
#define B1W(E, L) do { B1S(); if (early) wait_bar<E>(); else wait_bar<L>(); B1S(); } while (0)
#define B1T(KT, CUR, NXT, PRE) do { b1_read<KT + 1>(NXT, ad); SBAR(); PRE; store(KT); SBAR(); b1_mma(a, CUR); SBAR(); } while (0)
    B1W(8, 8);   B1T(0, f0, f1, b1_issue(c, 4));
    B1W(9, 8);   B1T(1, f1, f0, b1_issue(c, 5));
    B1W(10, 9);  B1T(2, f0, f1, b1_issue(c, 6));
    B1W(11, 10); B1T(3, f1, f0, b1_issue(c, 7));
    B1W(11, 11); B1T(4, f0, f1, (void)0);
    B1W(7, 7);   B1T(5, f1, f0, issueQt(0));
    B1W(9, 9);   B1T(6, f0, f1, (void)0);
    BARRIER();   store(7); SBAR(); b1_mma(a, f1);
#undef B1W
#undef B1T
}
template <int N, class Tick> DI void phi_map(const f32x4 (&p)[N], u32x4 (&out)[N], Tick tick) {
    float x[N][4], mx[N], nm[N], ep[N][4], en[N][4], sp[N], sn[N];
#pragma unroll
    for (int i = 0; i < N; ++i) {
#pragma unroll
        for (int e = 0; e < 4; ++e) x[i][e] = rbf16(p[i][e]) * LOG2E;
        mx[i] = __builtin_fmaxf(__builtin_fmaxf(x[i][0], x[i][1]), __builtin_fmaxf(x[i][2], x[i][3])); nm[i] = -__builtin_fminf(__builtin_fminf(x[i][0], x[i][1]), __builtin_fminf(x[i][2], x[i][3]));
        if (i & 1) tick(i >> 1); }
#pragma unroll
    for (int i = 0; i < N; ++i) { mx[i] = x16_max(mx[i]); nm[i] = x16_max(nm[i]); if (i & 1) tick(2 + (i >> 1)); }
#pragma unroll
    for (int i = 0; i < N; ++i) { mx[i] = swap_max(mx[i]); nm[i] = swap_max(nm[i]); if (i & 1) tick(4 + (i >> 1)); }
#pragma unroll
    for (int i = 0; i < N; ++i) {
#pragma unroll
        for (int e = 0; e < 4; ++e) { ep[i][e] = __builtin_amdgcn_exp2f(x[i][e] - mx[i]); en[i][e] = __builtin_amdgcn_exp2f(-x[i][e] - nm[i]); }
        sp[i] = (ep[i][0] + ep[i][1]) + (ep[i][2] + ep[i][3]); sn[i] = (en[i][0] + en[i][1]) + (en[i][2] + en[i][3]); tick(6 + i); }
#pragma unroll
    for (int i = 0; i < N; ++i) { sp[i] = x16_add(sp[i]); sn[i] = x16_add(sn[i]); if (i & 1) tick(10 + (i >> 1)); }
#pragma unroll
    for (int i = 0; i < N; ++i) { sp[i] = __builtin_amdgcn_rcpf(swap_add(sp[i])); sn[i] = __builtin_amdgcn_rcpf(swap_add(sn[i])); if (i & 1) tick(12 + (i >> 1)); }
#pragma unroll
    for (int i = 0; i < N; ++i) { out[i] = (u32x4){cvtpk(ep[i][0] * sp[i], ep[i][1] * sp[i]), cvtpk(ep[i][2] * sp[i], ep[i][3] * sp[i]), cvtpk(en[i][0] * sn[i], en[i][1] * sn[i]), cvtpk(en[i][2] * sn[i], en[i][3] * sn[i])};
        if (i & 1) tick(14 + (i >> 1)); }
}
template <class Tick>
DI void b1_phi(const B1Acc& a, Tick tick, u32x2 wp, u16* PhiQ, float* Bst, unsigned lds0, int rc, int hg, int wid, int lane, Stamper& stamp) {
    const int h = 4 * hg + (wid & 3), row0 = 64 * rc + 32 * (wid >> 2);
    const int l16 = lane & 15, g = lane >> 4;
    f32x4 sp = f32x4{}, sn = f32x4{}; float zp = 0.f, zn = 0.f;
    f32x4 pin[4]; u32x4 pout[4];
#pragma unroll
    for (int r = 0; r < 2; ++r) {
        const u32x2 qb = {cvtpk(a.aq[r][0], a.aq[r][1]), cvtpk(a.aq[r][2], a.aq[r][3])}, kb = {cvtpk(a.ak[r][0], a.ak[r][1]), cvtpk(a.ak[r][2], a.ak[r][3])};
        pin[r] = mfma16_16(wp, qb, f32x4{}); pin[2 + r] = mfma16_16(wp, kb, f32x4{});
    }
    phi_map<4>(pin, pout, tick);
    tick(16);
    const u32x4 wq[2] = {pout[0], pout[1]}, wk[2] = {pout[2], pout[3]};
    const unsigned tb = lds0 + B1_RED + wid * 1024;
#pragma unroll
    for (int r = 0; r < 2; ++r) {
        *(LAS u32x2*)(tb + l16 * 32 + g * 8) = (u32x2){wk[r][0], wk[r][1]};
        *(LAS u32x2*)(tb + 512 + l16 * 32 + g * 8) = (u32x2){wk[r][2], wk[r][3]};
        __builtin_amdgcn_raw_buffer_store_b128(wq[r], mk_srd(PhiQ, 4194304), ((h * 128 + (row0 >> 4) + r) * 64 + lane) * 16, 0, PHIQ_AUX);
        asm volatile("s_waitcnt lgkmcnt(0)" ::: "memory");
        const unsigned ta = tb + (4 * g + (l16 >> 2)) * 32 + (lane & 3) * 8;
        u32x2 pp = __builtin_bit_cast(u32x2, ldstr(ta)), pn = __builtin_bit_cast(u32x2, ldstr(ta + 512));
        asm volatile("s_waitcnt lgkmcnt(0)" : "+v"(pp), "+v"(pn) :: "memory");
        const u32x2 vb = {cvtpk(a.av[r][0], a.av[r][1]), cvtpk(a.av[r][2], a.av[r][3])};
        sp = mfma16_16(pp, vb, sp); sn = mfma16_16(pn, vb, sn);
        zp += (__uint_as_float(pp[0] << 16) + __uint_as_float(pp[0] & 0xffff0000u)) + (__uint_as_float(pp[1] << 16) + __uint_as_float(pp[1] & 0xffff0000u));
        zn += (__uint_as_float(pn[0] << 16) + __uint_as_float(pn[0] & 0xffff0000u)) + (__uint_as_float(pn[1] << 16) + __uint_as_float(pn[1] & 0xffff0000u));
    }
    zp = grp_sum(zp); zn = grp_sum(zn);
    BARRIER();
    LAS char* red = (LAS char*)(lds0 + B1_RED + (wid & 3) * 2176);
    if (wid >= 4) { *(LAS f32x4*)(red + lane * 32) = sp; *(LAS f32x4*)(red + lane * 32 + 16) = sn; if (lane < 16) *(LAS f32x2*)(red + 2048 + lane * 8) = (f32x2){zp, zn}; }
    BARRIER();
    sp += *(const LAS f32x4*)(red + lane * 32); sn += *(const LAS f32x4*)(red + lane * 32 + 16);
    const f32x2 zo = *(const LAS f32x2*)(red + 2048 + l16 * 8);
    const srd_t srd = mk_srd(Bst + (size_t)(h * 32 + rc) * 576, wid < 4 ? 2304 : 0);
    store16_sc1(__builtin_bit_cast(u32x4, sp), lane * 32, srd, 0u); store16_sc1(__builtin_bit_cast(u32x4, sn), lane * 32 + 16, srd, 0u);
    __builtin_amdgcn_raw_buffer_store_b64((u32x2){__float_as_uint(zp + zo[0]), __float_as_uint(zn + zo[1])}, srd, lane < 16 ? 2048 + lane * 8 : 0x7fffffff, 0, 16);
}
template <int N> DI void waitN(const u64* flags, u64 tok, int lane) {
    const u64* p = flags + (lane & (N - 1)) * FL_STRIDE;
    for (unsigned spins = 0; spins < (1u << 15); ++spins) { const u64 v = ld_agent(p); if (__all(v == tok)) break; __builtin_amdgcn_s_sleep(1); }
    asm volatile("" ::: "memory");
}
struct B2a { f32x4 v[16]; };
DI void b2a_load(B2a& b, const float* Bst, int rc, int hg, int lane) {
    const int h = 4 * hg + (rc >> 3), c = lane & 31, half = lane >> 5;
    const srd_t srd = mk_srd(Bst + (size_t)h * 32 * 576, 32 * 2304);
    const unsigned vo = (c < 18 ? (18 * (rc & 7) + c) * 16 : 0x7fffffff) + half * 16 * 2304;
#pragma unroll
    for (int i = 0; i < 16; ++i) b.v[i] = __builtin_bit_cast(f32x4, __builtin_amdgcn_raw_buffer_load_b128(srd, vo, i * 2304, 16));
}
DI void b2a_finish(const B2a& b, float* Bred, int rc, int hg, int lane) {
    const int h = 4 * hg + (rc >> 3), c = lane & 31, half = lane >> 5;
    f32x4 s = b.v[0];
#pragma unroll
    for (int i = 1; i < 16; ++i) s += b.v[i];
#pragma unroll
    for (int e = 0; e < 4; ++e) s[e] = swap_add(s[e]);
    const srd_t srdR = mk_srd(Bred + (size_t)h * 576, 2304);
    store16_sc1(__builtin_bit_cast(u32x4, s), (half == 0 && c < 18) ? (18 * (rc & 7) + c) * 16 : 0x7fffffffu, srdR, 0u);
}
template <int NRB> struct B2b { bf16x8 ph[NRB]; f32x4 sp, sn, za, zb; };
template <int NRB>
DI void b2b_load(B2b<NRB>& b, const u16* PhiQ, const float* Bred, const u64* flagsR, u64 tok, int h, int rb0, int lane) {
    PIN(lane);
    const int g = lane >> 4;
    waitN<8>(flagsR, tok, lane);
#pragma unroll
    for (int r = 0; r < NRB; ++r) b.ph[r] = __builtin_bit_cast(bf16x8, __builtin_amdgcn_raw_buffer_load_b128(mk_srd(PhiQ, 4194304), lane * 16, (h * 128 + rb0 + r) * 1024, 0));
    const srd_t srd = mk_srd(Bred + (size_t)h * 576, 2304);
    b.sp = __builtin_bit_cast(f32x4, __builtin_amdgcn_raw_buffer_load_b128(srd, lane * 32, 0, 16)); b.sn = __builtin_bit_cast(f32x4, __builtin_amdgcn_raw_buffer_load_b128(srd, lane * 32 + 16, 0, 16));
    b.za = __builtin_bit_cast(f32x4, __builtin_amdgcn_raw_buffer_load_b128(srd, 2048 + g * 32, 0, 16)); b.zb = __builtin_bit_cast(f32x4, __builtin_amdgcn_raw_buffer_load_b128(srd, 2048 + g * 32 + 16, 0, 16));
}
template <int NRB>
DI void b2b_finish(const B2b<NRB>& b, u16* Of, int h, int rb0, int lane) {
    PIN(lane);
    const int l16 = lane & 15, g = lane >> 4;
    const u32x4 kvw = {cvtpk(b.sp[0], b.sp[1]), cvtpk(b.sp[2], b.sp[3]), cvtpk(b.sn[0], b.sn[1]), cvtpk(b.sn[2], b.sn[3])};
    const u32x4 zw = {cvtpk(b.za[0], b.za[2]), cvtpk(b.zb[0], b.zb[2]), cvtpk(b.za[1], b.za[3]), cvtpk(b.zb[1], b.zb[3])};
    const srd_t srdO = mk_srd(Of, 2097152);
#pragma unroll
    for (int r = 0; r < NRB; ++r) { const int rb16 = rb0 + r;
        const f32x4 num = MFMA16(__builtin_bit_cast(bf16x8, kvw), b.ph[r], f32x4{});
        const f32x4 den = MFMA16(__builtin_bit_cast(bf16x8, zw), b.ph[r], f32x4{});
        const float dn = rbf16(rbf16(den[0]) + 1e-6f);
        const u32x2 w = {cvtpk(rbf16(num[0]) / dn, rbf16(num[1]) / dn), cvtpk(rbf16(num[2]) / dn, rbf16(num[3]) / dn)};
        __builtin_amdgcn_raw_buffer_store_b64(w, srdO, (((rb16 >> 1) * 32 + h) * 2 + (g & 1)) * 512 + ((rb16 & 1) * 16 + l16) * 16 + (g >> 1) * 8, 0, 16); }
}
#define KVPRIO 0
#define KROT 0
struct ProjCtx {
    srd_t srdC, srdX, srdW, srdWq, srdK, srdV;
    unsigned va[2], vw[2], vwq, voK, voV, lds0, l128;
    unsigned sA, sW, sWq, sXa;
    int wid, rot;
};
constexpr unsigned TOUCH_LDS = 163840 - 256;
DI void issueKV(const ProjCtx& c, int kt, int i) {
    const unsigned st = c.lds0 + (kt & 1) * 65536;
    kt = (kt + c.rot) & 7;
    if (i < 4) dma1(st + (4 * c.wid + i) * 1024, c.va[i & 1], c.srdC, c.sA + i * 8192 + kt * 128);
    else dma1(st + 32768 + (c.wid >> 2) * 16384 + (4 * (c.wid & 3) + (i - 4)) * 1024, c.vw[(i - 4) >> 1], c.srdW, c.sW + ((i - 4) >> 1) * 65536 + ((i - 4) & 1) * 128 + kt * 524288);
}
DI void issueQ(const ProjCtx& c, int kt, int i) {
    const unsigned st = c.lds0 + (kt % 3) * 49152;
    kt = (kt + c.rot) & 7;
    if (i < 4) dma1(st + (4 * c.wid + i) * 1024, c.va[i & 1], c.srdX, c.sXa + i * 8192 + kt * 128);
    else dma1(st + 32768 + (2 * c.wid + (i - 4)) * 1024, c.vwq, c.srdWq, c.sWq + (i - 4) * 128 + kt * 524288);
}
#define TOUCHMODE 3
template <int WHICH> DI void touch_g(unsigned m0v, unsigned voff, const void* base) {
    if (!(TOUCHMODE & WHICH)) voff = 0;
    const u64 b = (u64)base; const u64 sb = ((u64)rfl((unsigned)(b >> 32)) << 32) | rfl((unsigned)b);
    asm volatile("s_nop 4\n\ts_mov_b32 m0, %0\n\ts_nop 0\n\tglobal_load_lds_dword %1, %2" :: "s"(rfl(m0v)), "v"(voff), "s"(sb) : "memory");
}

struct GAddr { unsigned a[2]; unsigned w[2][2]; };
DI void gaddr_add(GAddr& g, unsigned d) { g.a[0] += d; g.a[1] += d; g.w[0][0] += d; g.w[0][1] += d; g.w[1][0] += d; g.w[1][1] += d; PIN(g.a[0]); PIN(g.a[1]); PIN(g.w[0][0]); PIN(g.w[0][1]); PIN(g.w[1][0]); PIN(g.w[1][1]); }
DI void gaddr_xor(GAddr& g, unsigned d) { g.a[0] ^= d; g.a[1] ^= d; g.w[0][0] ^= d; g.w[0][1] ^= d; g.w[1][0] ^= d; g.w[1][1] ^= d; }
DI bf16x8 wfrag(const GAddr& g, int ks, int nb) { return cat4(ldstr(g.w[nb & 1][0] + ks * 8192 + (nb >> 1) * 512), ldstr(g.w[nb & 1][1] + ks * 8192 + (nb >> 1) * 512)); }
DI bf16x8 wfragKV(const GAddr& g, int ks, int nb) { return cat4(ldstr(g.w[nb & 1][0] + ks * 8192 + (nb >> 1) * 16384), ldstr(g.w[nb & 1][1] + ks * 8192 + (nb >> 1) * 16384)); }
DI void gaddr_w(GAddr& g, unsigned base, int lane) {
    const int gq = lane >> 4, q4 = (lane & 15) >> 2, p4 = lane & 3;
#pragma unroll
    for (int o = 0; o < 2; ++o)
#pragma unroll
        for (int t = 0; t < 2; ++t) g.w[o][t] = base + 2048 * gq + 64 * (4 * t + q4) + 16 * ((2 * o + (p4 >> 1)) ^ ((2 * gq + t) & 3)) + 8 * (p4 & 1);
}
DI void gaddr_a(GAddr& g, unsigned base, int row0, int lane) {
    const int row = row0 + (lane & 15), gq = lane >> 4;
#pragma unroll
    for (int ks = 0; ks < 2; ++ks) g.a[ks] = base + row * 128 + (((4 * ks + gq) ^ ((row >> 1) & 7)) << 4);
}

#define KVAH 3
constexpr int AH = KVAH;
#define KV_MMA(mb, W) do { MFMA16I(acc[mb][0], W[0], a[mb]); MFMA16I(acc[mb][1], W[1], a[mb]); MFMA16I(acc[mb][2], a[mb], W[2]); MFMA16I(acc[mb][3], a[mb], W[3]); } while (0)
template <class Hook>
DI void kv_step0(f32x4 (&acc)[8][4], const bf16x8 (&w)[4], bf16x8 (&wn)[4], bf16x8 (&an)[AH], const GAddr& g, Hook hook) {
    bf16x8 a[8 + AH];
#pragma unroll
    for (int i = 0; i < AH; ++i) a[i] = an[i];
#pragma unroll
    for (int mb = 0; mb < 8; ++mb) {
        a[mb + AH] = mb + AH < 8 ? ldsf(g.a[0] + (mb + AH) * 2048) : ldsf(g.a[1] + (mb + AH - 8) * 2048);
        if (mb < 4) wn[mb] = wfragKV(g, 1, mb);
        SBAR();
        KV_MMA(mb, w);
        SBAR();
        hook(mb);
        SBAR();
    }
#pragma unroll
    for (int i = 0; i < AH; ++i) an[i] = a[8 + i];
}
struct NoHook { DI void operator()(int) const {} };

template <class Touch, class IssueB1>
DI void kv_part(const ProjCtx& c, GAddr g, u32x4 (&pk)[16], Touch touchk, IssueB1 issueB1, Stamper& stamp) {
    f32x4 acc[8][4];
#pragma unroll
    for (int a = 0; a < 8; ++a)
#pragma unroll
        for (int b = 0; b < 4; ++b) { acc[a][b] = f32x4{}; PIN(acc[a][b]); }
    bf16x8 w0[4], w1[4], an[AH];
    wait_bar<9>();
    stamp();
#pragma unroll
    for (int nb = 0; nb < 4; ++nb) w0[nb] = wfragKV(g, 0, nb);
#pragma unroll
    for (int i = 0; i < AH; ++i) an[i] = ldsf(g.a[0] + i * 2048);
    auto tile = [&](int kt, auto late, auto mid, auto waitB, auto early, bool next) {
        kv_step0(acc, w0, w1, an, g, late);
        bf16x8 a[8];
#pragma unroll
        for (int i = 0; i < AH; ++i) a[i] = an[i];
#pragma unroll
        for (int mb = 0; mb < 8 - AH; ++mb) {
            a[mb + AH] = ldsf(g.a[1] + (mb + AH) * 2048);
            SBAR();
            KV_MMA(mb, w1);
            SBAR();
            if (mb == 0) { mid(); SBAR(); }
        }
        waitB();
        if (next) { gaddr_xor(g, 65536u);
#pragma unroll
            for (int nb = 0; nb < 4; ++nb) w0[nb] = wfragKV(g, 0, nb);
#pragma unroll
            for (int i = 0; i < AH; ++i) an[i] = ldsf(g.a[0] + i * 2048); }
        SBAR();
#pragma unroll
        for (int mb = 8 - AH; mb < 8; ++mb) { KV_MMA(mb, w1); SBAR(); early(mb - (8 - AH)); SBAR(); }
    };
    for (int kt = 0; kt < 6; ++kt)
        tile(kt, [&](int mb) { if (kt > 0 && (mb & 1)) issueKV(c, kt + 1, 4 + (mb >> 1)); }, [&]() { touchk(kt); }, [&]() { wait_bar<1>(); },
             [&](int i) { if (i == 0) { issueKV(c, kt + 2, 0); issueKV(c, kt + 2, 1); } else issueKV(c, kt + 2, 1 + i); }, true);
    tile(6, [&](int mb) { if (mb & 1) issueKV(c, 7, 4 + (mb >> 1)); }, [&]() {}, [&]() { wait_bar<0>(); }, [&](int i) { if (i < 2) issueB1(i); }, true);
    tile(7, NoHook{}, [&]() {}, [&]() { stamp(); wait_bar<4>(); stamp(); }, [&](int i) { if (i < 2) issueB1(2 + i); }, false);
    __builtin_amdgcn_s_setprio(0);
    MFMA_DRAIN();
#pragma unroll
    for (int n = 0; n < 16; ++n) { const int m = n - 8;
        const f32x4 lo = n < 8 ? acc[n & 7][0] : acc[2 * ((m >> 1) & 3)][2 + (m & 1)], hi = n < 8 ? acc[n & 7][1] : acc[2 * ((m >> 1) & 3) + 1][2 + (m & 1)];
        pk[n] = (u32x4){cvtpk(lo[0], lo[1]), cvtpk(lo[2], lo[3]), cvtpk(hi[0], hi[1]), cvtpk(hi[2], hi[3])}; PIN(pk[n]); }
}
DI void kv_store(const ProjCtx& c, const u32x4 (&pk)[16], int n) {
    const int m = n - 8;
    if (n < 8) store16_sc1(pk[n], c.voK, c.srdK, (unsigned)((n >> 2) * 16384 + (n & 3) * 4096));
    else store16_sc1(pk[n], c.voV, c.srdV, (unsigned)((m >> 2) * 16384 + (m & 1) * 2048 + ((m >> 1) & 1) * 1024));
}

struct QFr { bf16x8 x[2]; bf16x8 w[8]; };
template <int KS> DI void q_read(QFr& f, const GAddr& g, int i) {
    if (i == 1) f.x[0] = ldsf(g.a[KS]); else if (i == 2) f.x[1] = ldsf(g.a[KS] + 2048); else { const int nb = i == 0 ? 0 : i - 2; f.w[nb] = wfrag(g, KS, nb); }
}
template <int KSN, bool NEXT, class Hook>
DI void q_kstep(f32x4 (&acc)[8][2], const QFr& cur, QFr& nxt, const GAddr& gn, Hook hook) {
#pragma unroll
    for (int nb = 0; nb < 8; ++nb) {
        MFMA16I(acc[nb][0], cur.w[nb], cur.x[0]); MFMA16I(acc[nb][1], cur.w[nb], cur.x[1]);
        SBAR();
        if (NEXT) { if (nb < 2) { q_read<KSN>(nxt, gn, 2 * nb); q_read<KSN>(nxt, gn, 2 * nb + 1); } else q_read<KSN>(nxt, gn, nb + 2); }
        hook(nb);
        SBAR();
    }
}
template <int KT, class Pub>
DI void q_tile(const ProjCtx& c, bool early, GAddr& g, f32x4 (&acc)[8][2], QFr& f0, QFr& f1, const u32x4 (&pk)[16], Pub after, Stamper& stamp) {
    q_kstep<1, true>(acc, f0, f1, g, [&](int nb) {
        if (KT >= 1 && KT + 2 < 8 && nb < 3) issueQ(c, KT + 2, 3 + nb);
        if (nb == 4) { if (early) { if (KT < 2) kv_store(c, pk, 12 + 2 * (KT < 2 ? KT : 0)); } else if (KT < 5) kv_store(c, pk, 11 + (KT < 5 ? KT : 0)); }
        if (nb == 7 && early && KT < 2) kv_store(c, pk, 13 + 2 * (KT < 2 ? KT : 0)); });
    if constexpr (KT < 7) {
        constexpr int WE = KT == 0 ? 17 : (KT == 1 ? 19 : (KT == 2 ? 8 : (KT <= 5 ? 6 : 0)));
        constexpr int WL = KT == 0 ? 17 : (KT == 1 ? 18 : (KT <= 4 ? 8 : (KT == 5 ? 7 : 0)));
        const bool w7 = KT == 5 && c.wid == 7;
        if (early) { if (w7) wait_bar<WE + 16>(); else wait_bar<WE>(); } else { if (w7) wait_bar<WL + 16>(); else wait_bar<WL>(); }
        stamp();
        after(KT);
        gaddr_add(g, (unsigned)(((KT + 1) % 3 - KT % 3) * 49152));
        q_kstep<0, true>(acc, f1, f0, g, [&](int nb) { if (KT + 3 < 8 && nb < 3) issueQ(c, KT + 3, nb); });
    } else q_kstep<0, false>(acc, f1, f0, g, NoHook{});
}

DI void proj_setup(ProjCtx& c, const u16* X, const u16* Ckv, const u16* Wq, const u16* Wuk, const u16* Wuv, u16* Kf, u16* Vf, unsigned lds0, int h, int j, int wid, int lane) {
    const int wr = wid >> 2, wc = wid & 3;
    c.wid = wid; c.lds0 = lds0; c.rot = KROT ? (2 * (h & 3) + j) & 7 : 0;
    c.srdC = mk_srd(Ckv, 2097152); c.srdX = mk_srd(X, 2097152);
    c.srdW = mk_srd((wid < 4 ? Wuk : Wuv), 4194304); c.srdWq = mk_srd(Wq, 4194304);
    c.srdK = mk_srd((const char*)Kf + (size_t)h * 524288, 524288); c.srdV = mk_srd((const char*)Vf + (size_t)h * 524288, 524288);
#pragma unroll
    for (int p = 0; p < 2; ++p) {
        c.va[p] = (lane >> 3) * 1024 + (((lane & 7) ^ (4 * p + (lane >> 4))) * 16);
        c.vw[p] = ((lane >> 2) & 7) * 8192 + (4 * (lane >> 5) + ((lane & 3) ^ ((2 * p + ((lane >> 4) & 1)) & 3))) * 16;
    }
    c.vwq = (wid & 1) ? c.vw[1] : c.vw[0];
    c.l128 = lane * 128;
    c.sA = (256 * j + 32 * wid) * 1024;
    c.sW = (16 * (wid & 3)) * 8192 + h * 256;
    c.sXa = (wid < 4 ? 128 * (15 - j) + 32 * wid : 128 * j + 32 * (wid - 4)) * 1024;
    c.sWq = (8 * wid) * 8192 + h * 256;
    c.voK = lane * 16 + (4 * j + 2 * wr) * 16384 + wc * 1024; c.voV = lane * 16 + (4 * j + 2 * wr) * 16384 + wc * 4096;
}
template <class Touch, class IssueB1>
DI void kv_run(const ProjCtx& c, int lane, u32x4 (&pk)[16], Touch touchk, IssueB1 issueB1, Stamper& stamp) {
    const int wid = c.wid, wr = wid >> 2, wc = wid & 3;
    GAddr g; gaddr_a(g, c.lds0, 128 * wr, lane); gaddr_w(g, c.lds0 + 32768 + wc * 512, lane);
    kv_part(c, g, pk, touchk, issueB1, stamp);
}
template <class Pub>
DI void q_run(const ProjCtx& c, bool early, int lane, const u32x4 (&pk)[16], bf16x8 (&q)[8], Pub after, Stamper& stamp) {
    GAddr g; gaddr_a(g, c.lds0, 32 * c.wid, lane); gaddr_w(g, c.lds0 + 32768, lane); gaddr_add(g, 0u);
    f32x4 acc[8][2];
#pragma unroll
    for (int a = 0; a < 8; ++a) { acc[a][0] = f32x4{}; acc[a][1] = f32x4{}; PIN(acc[a][0]); PIN(acc[a][1]); }
    QFr f0, f1;
#pragma unroll
    for (int i = 0; i < 10; ++i) q_read<0>(f0, g, i);
    q_tile<0>(c, early, g, acc, f0, f1, pk, after, stamp); q_tile<1>(c, early, g, acc, f0, f1, pk, after, stamp); q_tile<2>(c, early, g, acc, f0, f1, pk, after, stamp); q_tile<3>(c, early, g, acc, f0, f1, pk, after, stamp);
    q_tile<4>(c, early, g, acc, f0, f1, pk, after, stamp); q_tile<5>(c, early, g, acc, f0, f1, pk, after, stamp); q_tile<6>(c, early, g, acc, f0, f1, pk, after, stamp); q_tile<7>(c, early, g, acc, f0, f1, pk, after, stamp);
    MFMA_DRAIN();
    constexpr float QSC = 0.08838834764831845f * 1.4426950408889634f;
#pragma unroll
    for (int qb = 0; qb < 2; ++qb)
#pragma unroll
        for (int ds = 0; ds < 4; ++ds) { const f32x4 lo = acc[2 * ds][qb] * QSC, hi = acc[2 * ds + 1][qb] * QSC;
            q[qb * 4 + ds] = __builtin_bit_cast(bf16x8, (u32x4){cvtpk(lo[0], lo[1]), cvtpk(lo[2], lo[3]), cvtpk(hi[0], hi[1]), cvtpk(hi[2], hi[3])}); }
}
template <int OFF, bool SETM0> DI void dma_piece(unsigned m0v, unsigned voff, srd_t srd, unsigned soff) {
    if constexpr (SETM0) asm volatile("s_mov_b32 m0, %0\n\ts_nop 0\n\tbuffer_load_dwordx4 %1, %2, %3 offen offset:%4 sc1 lds" :: "s"(m0v), "v"(voff), "s"(srd), "s"(soff), "i"(OFF) : "memory");
    else asm volatile("buffer_load_dwordx4 %0, %1, %2 offen offset:%3 sc1 lds" :: "v"(voff), "s"(srd), "s"(soff), "i"(OFF) : "memory");
}
#define ATT_THRV 64.f
constexpr float ATT_THR = ATT_THRV;
#define KSPLIT 0
#define ATT_WIN 5
constexpr int ATT_W = ATT_WIN;

struct AttnState { f32x4 o[8][2]; f32x4 s[4][2]; f32x4 negm[2]; bf16x8 q[8]; u32x4 p[2][2]; f32x4 l[2]; f32x2 mhat; u32x4 ones; };
struct AttnCtx {
    srd_t srdK, srdV, srdC; unsigned voff, lane16, kslot0, vslot0, coffL;
    int g, nl  , thrg, dq0, relH, jump; unsigned qstash;
    unsigned soA, soB, ksoM; int rel;
    srd_t srdF; u64 tok, prev; unsigned known;
};
DI u64 ld_flag8(const AttnCtx& c) { return __builtin_bit_cast(u64, __builtin_amdgcn_raw_buffer_load_b64(c.srdF, (c.lane16 & 0x70u) << 3, 0, 16)); }

DI unsigned m_off(int i) { return i < 16 ? (unsigned)(((i & 7) * 2 + (i >> 3)) * 1024) : (unsigned)((((i - 16) & 3) * 4 + ((i - 16) >> 2)) * 1024); }
template <bool DO_PV, bool DO_QK, int KP, int VP, class Hook>
DI void m_phase(AttnState& st, unsigned kbase, unsigned vbase, bf16x8 (&pre)[ATT_W], Hook hook) {
    constexpr int N0 = DO_PV ? 0 : 16, N1 = DO_QK ? 32 : 16;
    bf16x8 fr[32];
#pragma unroll
    for (int i = 0; i < ATT_W; ++i) fr[N0 + i] = pre[i];
#pragma unroll
    for (int i = N0; i < N1; ++i) {
        const int n = i + ATT_W;
        if (n < N1) fr[n] = ldsf((n < 16 ? vbase + VP * 16384 : kbase + KP * 16384) + m_off(n));
        SBAR();
        if (i >= 16) { const int ds = (i - 16) >> 2, kb = (i - 16) & 3;
            if (ds == 0) { asm volatile("v_mfma_f32_16x16x32_bf16 %0, %2, %3, %1" : "=&v"(st.s[kb][0]), "+v"(st.negm[0]) : "v"(fr[i]), "v"(st.q[0]));
                           asm volatile("v_mfma_f32_16x16x32_bf16 %0, %2, %3, %1" : "=&v"(st.s[kb][1]), "+v"(st.negm[1]) : "v"(fr[i]), "v"(st.q[4])); }
            else { MFMA16I(st.s[kb][0], fr[i], st.q[ds]); MFMA16I(st.s[kb][1], fr[i], st.q[4 + ds]); }
        } else { const int ks = i >> 3, db = i & 7;
            MFMA16I(st.o[db][0], fr[i], st.p[ks][0]); MFMA16I(st.o[db][1], fr[i], st.p[ks][1]);
            if (db == 3) { MFMA16I(st.l[0], st.ones, st.p[ks][0]); MFMA16I(st.l[1], st.ones, st.p[ks][1]); } }
        SBAR();
        hook(i);
        SBAR();
    }
    MFMA_DRAIN();
    PIN(st.negm[0]); PIN(st.negm[1]);
}
struct NoHookI { DI void operator()(int) const {} };

DI void store_ctx(const f32x4 (&o)[8][2], float inv0, float inv1, srd_t srd, unsigned voff) {
    PIN(voff);
#pragma unroll
    for (int p = 0; p < 4; ++p)
#pragma unroll
        for (int qb = 0; qb < 2; ++qb) { const float inv = qb ? inv1 : inv0; const f32x4 lo = o[2 * p][qb] * inv, hi = o[2 * p + 1][qb] * inv;
            __builtin_amdgcn_raw_buffer_store_b128((u32x4){cvtpk(lo[0], lo[1]), cvtpk(lo[2], lo[3]), cvtpk(hi[0], hi[1]), cvtpk(hi[2], hi[3])}, srd, voff + p * 2048 + qb * 256, 0, 16); }
}
DI float row_add(float x) { return swap_add(x16_add(x)); }
DI float row_max4(float x) { return swap_max(x16_max(x)); }

template <bool LAST, int PAR>
DI void v_phase(AttnState& st, AttnCtx& c, const int t) {
    const bool sw = (t == c.nl);
    const unsigned vm0 = c.vslot0 + (PAR ^ 1) * 16384;
    const unsigned vso = c.soA, kso = c.soB;
    if (!LAST && __builtin_expect(c.known != 0xffu, 0)) {
        c.known |= (unsigned)__builtin_amdgcn_ballot_w64(c.prev == c.tok) & 0xffu;
        const unsigned need = 1u << ((kso >> 16) & 7);
        if (kso < 524288u && !(c.known & need)) {
            for (unsigned spins = 0; spins < (1u << 15); ++spins) { const u64 v = ld_flag8(c); c.known |= (unsigned)__builtin_amdgcn_ballot_w64(v == c.tok) & 0xffu; if (c.known & need) break; __builtin_amdgcn_s_sleep(1); } }
        asm volatile("" ::: "memory");
        c.prev = ld_flag8(c);
    }
    SBAR();
    if (__builtin_expect(sw, 0)) { store_ctx(st.o, __builtin_amdgcn_rcpf(st.l[0][0]), __builtin_amdgcn_rcpf(st.l[1][0]), c.srdC, ((c.lane16 >> 8) << 9) + (c.lane16 & 0xf0u) + c.coffL);
#pragma unroll
        for (int d = 0; d < 8; ++d) { st.o[d][0] = f32x4{}; st.o[d][1] = f32x4{}; PIN(st.o[d][0]); PIN(st.o[d][1]); }
        st.l[0] = f32x4{}; st.l[1] = f32x4{}; PIN(st.l[0]); PIN(st.l[1]); c.rel = c.relH; }
    if (__builtin_expect(c.rel > c.thrg, 0)) {
        int thr = 32 * c.g - c.rel + c.dq0; PIN(thr); const float NEG = -__builtin_inff();
#pragma unroll
        for (int kb = 0; kb < 4; ++kb)
#pragma unroll
            for (int qb = 0; qb < 2; ++qb)
#pragma unroll
                for (int e = 0; e < 4; ++e) if (16 * kb - 16 * qb + e > thr) st.s[kb][qb][e] = NEG;
    }
    SBAR();
    if (!LAST) dma_piece<0, true>(vm0, c.voff, c.srdV, vso);
#define MX3(a, b, c) __builtin_fmaxf(__builtin_fmaxf((a), (b)), (c))
    float mx[2];
#pragma unroll
    for (int qb = 0; qb < 2; ++qb) {
        float a = MX3(st.s[0][qb][0], st.s[0][qb][1], st.s[0][qb][2]), b = MX3(st.s[0][qb][3], st.s[1][qb][0], st.s[1][qb][1]);
        a = MX3(a, st.s[1][qb][2], st.s[1][qb][3]); b = MX3(b, st.s[2][qb][0], st.s[2][qb][1]); a = MX3(a, st.s[2][qb][2], st.s[2][qb][3]);
        b = MX3(b, st.s[3][qb][0], st.s[3][qb][1]); a = MX3(a, st.s[3][qb][2], st.s[3][qb][3]); mx[qb] = __builtin_fmaxf(a, b); }
    const unsigned long long firstm = (t == 0 || sw) ? ~0ull : 0ull;
    if (__builtin_expect((__builtin_amdgcn_ballot_w64(__builtin_fmaxf(mx[0], mx[1]) > ATT_THR) | firstm) != 0ull, 0)) {
        const bool first = firstm != 0ull;
#pragma unroll
        for (int qb = 0; qb < 2; ++qb) {
            const float delta = __builtin_fmaxf(row_max4(mx[qb]), first ? -1e30f : 0.f);
            st.mhat[qb] = (first ? 0.f : st.mhat[qb]) + delta;
#pragma unroll
            for (int kb = 0; kb < 4; ++kb) st.s[kb][qb] -= delta;
            st.negm[qb] = (f32x4){-st.mhat[qb], -st.mhat[qb], -st.mhat[qb], -st.mhat[qb]};
            if (!first) { const float f = __builtin_amdgcn_exp2f(-delta); st.l[qb] *= f;
#pragma unroll
                for (int d = 0; d < 8; ++d) st.o[d][qb] *= f; } }
    }
    SBAR();
    float e[8][4];
#define EXPG(k) do { _Pragma("unroll") for (int i_ = 0; i_ < 4; ++i_) e[k][i_] = __builtin_amdgcn_exp2f(st.s[(k) >> 1][(k) & 1][i_]); } while (0)
#define FING(k) do { constexpr int kb_ = (k) >> 1, qb_ = (k) & 1; \
        unsigned w0_ = cvtpk(e[k][0], e[k][1]), w1_ = cvtpk(e[k][2], e[k][3]); PIN(w0_); PIN(w1_); \
        st.p[kb_ >> 1][qb_][2 * (kb_ & 1)] = w0_; st.p[kb_ >> 1][qb_][2 * (kb_ & 1) + 1] = w1_; } while (0)
    EXPG(0); SBAR();
    EXPG(1); FING(0); SBAR();
    if (!LAST) dma_piece<1024, false>(vm0, c.voff, c.srdV, vso);
    EXPG(2); FING(1); SBAR();
    EXPG(3); FING(2); SBAR();
    if (!LAST) dma_piece<2048, false>(vm0, c.voff, c.srdV, vso);
    EXPG(4); FING(3); SBAR();
    EXPG(5); FING(4); SBAR();
    if (!LAST) dma_piece<3072, false>(vm0, c.voff, c.srdV, vso);
    EXPG(6); FING(5); SBAR();
    EXPG(7); FING(6); SBAR();
    FING(7);
#undef EXPG
#undef FING
    if (!LAST) {
        if (__builtin_expect(t == c.nl - 1, 0)) {
            unsigned qs = c.qstash; PIN(qs);
#pragma unroll
            for (int s = 0; s < 8; ++s) st.q[s] = ldsf(qs + s * 1024);
            st.negm[0] = f32x4{}; st.negm[1] = f32x4{}; PIN(st.negm[0]); PIN(st.negm[1]);
        }
        c.ksoM = kso;
        c.soA = c.soB; c.soB += (t + 3 == c.nl) ? c.jump : 16384; c.rel += 64;
    }
    SBAR();
}

DI void attn_setup(AttnCtx& c, const u16* Kf, const u16* Vf, u16* Cf, unsigned lds0, int h, int j, int wid, int lane) {
    PIN(lane);
    const int grp = wid >> 2; c.g = wid & 3; c.dq0 = (lane & 15) - 4 * (lane >> 4);
    const int qbH = 15 - j, qbL = j, nl = 2 * j + 2;
    c.nl = grp ? nl : 1000; c.thrg = 32 * c.g - 63; c.jump = (18 - nl) * 16384; c.relH = 64 * (17 - 2 * qbH);
    c.rel = grp ? -128 * qbL : -128 * qbH;
    c.soA = 16384; c.soB = (c.nl == 2) ? 17 * 16384 : 2 * 16384;
    c.srdK = mk_srd((const char*)Kf + (size_t)h * 524288, 524288);
    c.srdV = mk_srd((const char*)Vf + (size_t)h * 524288, 524288);
    c.srdC = mk_srd((const char*)Cf, 16777216);
    c.lane16 = lane * 16; c.voff = lane * 16 + c.g * 4096;
    c.kslot0 = lds0 + grp * 65536 + c.g * 4096; c.vslot0 = c.kslot0 + 32768;
    c.coffL = ((qbL * 4 + c.g) * 32 + h) * 8192;
    c.qstash = lds0 + 131072 + c.g * 8192 + lane * 16;
}
#define DMA4(m_, vo_, srd, so_) do { dma_piece<0, true>(m_, vo_, srd, so_); dma_piece<1024, false>(m_, vo_, srd, so_); dma_piece<2048, false>(m_, vo_, srd, so_); dma_piece<3072, false>(m_, vo_, srd, so_); } while (0)
DI void attn_prime(const AttnCtx& c, int grp) { DMA4(grp ? c.vslot0 + 16384 : c.kslot0, c.voff, c.srdK, 0u); DMA4(c.vslot0, c.voff, c.srdV, 0u); if (grp == 0) DMA4(c.kslot0 + 16384, c.voff, c.srdK, 16384u); }

template <class First, class G0Pre, class G0Tail>
DI void attn_run(AttnState& st, AttnCtx& c, unsigned lds0, int h, int j, int wid, int lane, First afterFirst, G0Pre g0pre, G0Tail g0tail, Stamper& stamp) {
    PIN(lane);
    const int grp = wid >> 2;
    const unsigned kbase = lds0 + grp * 65536 + lane * 16;
    const unsigned vbase = lds0 + grp * 65536 + 32768 + lane * 16;
#pragma unroll
    for (int d = 0; d < 8; ++d) { st.o[d][0] = f32x4{}; st.o[d][1] = f32x4{}; PIN(st.o[d][0]); PIN(st.o[d][1]); }
    st.negm[0] = f32x4{}; st.negm[1] = f32x4{}; st.l[0] = f32x4{}; st.l[1] = f32x4{}; st.mhat = (f32x2){0.f, 0.f};
    st.ones = (u32x4){0x3f803f80u, 0x3f803f80u, 0x3f803f80u, 0x3f803f80u}; PIN(st.ones); PIN(st.l[0]); PIN(st.l[1]);
    PIN(st.negm[0]); PIN(st.negm[1]);
    if (grp == 1) { DMA4(c.kslot0 + 16384, c.voff, c.srdK, 16384u); BARRIER(); }
    wait_bar<8>();
    stamp();
    bf16x8 pre[ATT_W];
#define PRE_V(P) do { _Pragma("unroll") for (int i = 0; i < ATT_W; ++i) pre[i] = ldsf(vbase + (P) * 16384 + m_off(i)); } while (0)
    const unsigned kbase0 = grp ? kbase + 49152 : kbase;
#pragma unroll
    for (int i = 0; i < ATT_W; ++i) pre[i] = ldsf(kbase0 + m_off(16 + i));
    __builtin_amdgcn_s_setprio(1);
    m_phase<false, true, 0, 0>(st, kbase0, vbase, pre, NoHookI{});
    __builtin_amdgcn_s_setprio(0);
    wait_bar<0>();
    afterFirst();
#define PSTAMP(t) do {} while (0)
#define STEP(PAR, t) do { \
        v_phase<false, PAR>(st, c, t); \
        PRE_V(PAR); \
        PSTAMP(t); \
        wait_bar_keep<(KSPLIT ? 6 : 4)>(); \
        PSTAMP(t); \
        __builtin_amdgcn_s_setprio(1); \
        m_phase<true, true, PAR ^ 1, PAR>(st, kbase, vbase, pre, [&](int i) { \
            if (!KSPLIT) { if (i == 3) dma_piece<0, true>(c.kslot0 + PAR * 16384, c.voff, c.srdK, c.ksoM); if (i == 9) dma_piece<1024, false>(c.kslot0 + PAR * 16384, c.voff, c.srdK, c.ksoM); } \
            if (i == (KSPLIT ? 5 : 15)) dma_piece<2048, KSPLIT>(c.kslot0 + PAR * 16384, c.voff, c.srdK, c.ksoM); if (i == (KSPLIT ? 17 : 21)) dma_piece<3072, false>(c.kslot0 + PAR * 16384, c.voff, c.srdK, c.ksoM); }); \
        __builtin_amdgcn_s_setprio(0); \
        PSTAMP(t); \
        wait_bar<4>(); \
        PSTAMP(t); } while (0)
    for (int t = 0; t < 16; t += 2) { stamp(); STEP(0, t); STEP(1, t + 1); }
    stamp();
    v_phase<true, 0>(st, c, 16);
    if (grp == 0) g0pre();
    PRE_V(0);
    asm volatile("s_barrier" ::: "memory");
    __builtin_amdgcn_s_setprio(1);
    m_phase<true, false, 0, 0>(st, kbase, vbase, pre, NoHookI{});
    __builtin_amdgcn_s_setprio(0);
    wait_bar<0>();
    stamp();
    PIN(lane);
    const unsigned xo = lds0 + grp * 65536 + c.g * 8192 + lane * 16;
    const unsigned xi = lds0 + (grp ^ 1) * 65536 + c.g * 8192 + lane * 16;
    const unsigned so = lds0 + grp * 65536 + 32768 + c.g * 512 + lane * 8, si = lds0 + (grp ^ 1) * 65536 + 32768 + c.g * 512 + lane * 8;
    const unsigned voff = (lane >> 4) * 512 + (lane & 15) * 16 + (((15 - j) * 4 + c.g) * 32 + h) * 8192;
#define MERGE_PUT(qb) do { _Pragma("unroll") for (int d = 0; d < 8; ++d) *(LAS f32x4*)(xo + d * 1024) = st.o[d][qb]; *(LAS f32x2*)so = (f32x2){st.mhat[qb], st.l[qb][0]}; } while (0)
#define MERGE_FIN(qb) do { const f32x2 ot = *(const LAS f32x2*)si; const float mh = st.mhat[qb], lt = st.l[qb][0]; \
        const float m = __builtin_fmaxf(ot[0], mh), f0 = __builtin_amdgcn_exp2f(ot[0] - m), f1 = __builtin_amdgcn_exp2f(mh - m); \
        const float inv = __builtin_amdgcn_rcpf(ot[1] * f0 + lt * f1), a0 = f0 * inv, a1 = f1 * inv; \
        _Pragma("unroll") for (int p = 0; p < 4; ++p) { \
            const f32x4 lo = st.o[2 * p][qb] * a1 + *(const LAS f32x4*)(xi + (2 * p) * 1024) * a0, hi = st.o[2 * p + 1][qb] * a1 + *(const LAS f32x4*)(xi + (2 * p + 1) * 1024) * a0; \
            __builtin_amdgcn_raw_buffer_store_b128((u32x4){cvtpk(lo[0], lo[1]), cvtpk(lo[2], lo[3]), cvtpk(hi[0], hi[1]), cvtpk(hi[2], hi[3])}, c.srdC, voff + p * 2048 + (qb) * 256, 0, 16); } } while (0)
    if (grp == 0) { MERGE_PUT(1); BARRIER(); g0tail(); BARRIER(); MERGE_FIN(0); asm volatile("; merge 0"); }
    else          { asm volatile("; merge 1"); MERGE_PUT(0); BARRIER(); MERGE_FIN(1); }
#undef MERGE_PUT
#undef MERGE_FIN
    stamp();
#undef PRE_V
#undef STEP
}
#define OUT_AUX 0
constexpr int OUT_LDS = 163840;
constexpr unsigned OUT_O = 131072;
struct OutCtx { srd_t srdC, srdW, srdB; unsigned lane16, vw0, vwA, sC, sW, sB, lds0; int wid, rot; };
DI void out_issueA(const OutCtx& c, int kt, int i) {
    const unsigned st = c.lds0 + (kt & 3) * 32768;
    const int kr = (kt + c.rot) & 15;
    if (i < 2) { const int p = 2 * c.wid + i;
        dma1s(st + p * 1024, c.lane16, c.srdC, c.sC + (p >> 2) * 262144 + (p & 3) * 1024 + kr * 4096); }
    else dma1(st + 16384 + (2 * c.wid + (i - 2)) * 1024, c.vwA, c.srdW, c.sW + (8 * c.wid) * 1024 + (i - 2) * 128 + kr * 65536);
}
DI void out_issueB(const OutCtx& c, int ch, int i) {
    const int rg = 2 * c.wid + (i >> 1);
    dma1(c.lds0 + ch * 32768 + rg * 2048 + (i & 1) * 1024, (i >> 1) ? (c.vw0 ^ 32u) : c.vw0, c.srdB, c.sB + (ch * 128 + 8 * rg) * 1024 + (i & 1) * 128);
}
DI u32x4 out_pack(f32x4 s0, f32x4 s1) {
    unsigned a0 = cvtpk(s0[0], s0[1]), a1 = cvtpk(s0[2], s0[3]), b0 = cvtpk(s1[0], s1[1]), b1 = cvtpk(s1[2], s1[3]);
    auto x0 = __builtin_amdgcn_permlane32_swap(a0, b0, false, false); auto x1 = __builtin_amdgcn_permlane32_swap(a1, b1, false, false);
    return (u32x4){x0[0], x1[0], x0[1], x1[1]};
}
DI f32x4 quad(const f32x16& x, int rq) { return (f32x4){x[4 * rq], x[4 * rq + 1], x[4 * rq + 2], x[4 * rq + 3]}; }

DI void out_phase(const u16* Wo, const u16* Bwo, u16* out, char* ws, u64 tok, unsigned lds0, int bid, int wid, int lane, Stamper& stamp) {
    PIN(lane);
    const int tid = wid * 64 + lane, r32 = lane & 31, hi = lane >> 5, wr = wid >> 2, wc = wid & 3;
    const int x = bid & 7, u = bid >> 3, s = x >> 1, rb = 8 * (x & 1) + (u >> 2), cb = u & 3, tile = rb * 4 + cb;
    OutCtx c; c.wid = wid; c.lds0 = lds0; c.lane16 = lane * 16;
    c.srdC = mk_srd(ws + WS_CF + (size_t)rb * 4 * 262144, 4 * 262144); c.srdW = mk_srd(Wo, 4194304); c.srdB = mk_srd(Bwo, 524288);
    c.vw0 = ((lane >> 2) & 7) * 1024 + (4 * (lane >> 5) + ((lane & 3) ^ ((lane >> 4) & 1))) * 16; c.vwA = (wid & 1) ? (c.vw0 ^ 32u) : c.vw0;
    c.rot = 4 * cb + 2 * (rb & 7);
    c.sC = s * 8 * 8192; c.sW = s * 1024 * 1024 + cb * 256; c.sB = cb * 256;
#pragma unroll
    for (int kt = 0; kt < 4; ++kt) { out_issueA(c, kt, 2); out_issueA(c, kt, 3); }
    u64* flags = (u64*)(ws + WS_FLAG);
    {
        const int l = lane & 15, hh = 8 * s + (l & 7), jj = rb < 8 ? rb : 15 - rb;
        const int src = l < 8 ? ((hh >> 2) | (jj << 3) | ((hh & 3) << 6)) : (8 * (2 * rb + (s >> 1)) + (l & 7));
        const u64* fp = flags + (FL_C + src) * FL_STRIDE;
        for (unsigned spins = 0; spins < (1u << 15); ++spins) { const u64 v = ld_agent(fp); if (__all(v == tok)) break; __builtin_amdgcn_s_sleep(1); }
        asm volatile("" ::: "memory"); }
    stamp();
#pragma unroll
    for (int kt = 0; kt < 4; ++kt) { out_issueA(c, kt, 0); out_issueA(c, kt, 1); }
    { const srd_t srdO = mk_srd(ws + WS_OF + (size_t)(rb * 4 + s) * 32768, 32768);
#pragma unroll
      for (int i = 0; i < 4; ++i) dma1s(lds0 + OUT_O + (4 * wid + i) * 1024, c.lane16, srdO, (unsigned)((4 * wid + i) * 1024)); }
    const int q4 = (lane & 15) >> 2, p4 = lane & 3, blk = (lane >> 4) & 1;
    const unsigned aaddr = lds0 + (2 * wr) * 4096 + c.lane16;
    unsigned waA[2], waddr[2];
#pragma unroll
    for (int t = 0; t < 2; ++t) { waA[t] = lds0 + 2048 * t + 512 * wc + 64 * (4 * hi + q4) + 16 * ((2 * blk + (p4 >> 1)) ^ ((2 * t + hi) & 3)) + 8 * (p4 & 1);
                                  waddr[t] = waA[t]; }
    f32x16 acc[2] = {f32x16{}, f32x16{}};
    stamp();
    struct Fr { bf16x8 a[2][2]; bf16x4 w[2][2]; };
    auto rd = [&](Fr& f, unsigned st, int half) {
        unsigned w0 = waA[0] + st + 16384 + half * 8192, w1 = waA[1] + st + 16384 + half * 8192, aa = aaddr + st; PIN(w0); PIN(w1); PIN(aa);
#pragma unroll
        for (int k = 0; k < 2; ++k) { const int ks = 2 * half + k; const unsigned wb = k ? w1 : w0;
            f.w[k][0] = ldstr(wb); f.w[k][1] = ldstr(wb + 4096);
            f.a[k][0] = ldsf(aa + ks * 1024); f.a[k][1] = ldsf(aa + 4096 + ks * 1024); } };
    Fr f0, f1;
    wait_bar<10>();
    rd(f0, 0u, 0);
    auto step = [&](int kt, auto late, auto early) {
        const unsigned st = (kt & 3) * 32768, stn = ((kt + 1) & 3) * 32768;
        rd(f1, st, 1);
        SBAR();
#pragma unroll
        for (int k = 0; k < 2; ++k) { const bf16x8 wf = cat4(f0.w[k][0], f0.w[k][1]);
            acc[0] = MFMA32(wf, f0.a[k][0], acc[0]); acc[1] = MFMA32(wf, f0.a[k][1], acc[1]); SBAR(); late(2 + k); SBAR(); }
        wait_bar<8>();
        rd(f0, stn, 0);
        SBAR();
#pragma unroll
        for (int k = 0; k < 2; ++k) { const bf16x8 wf = cat4(f1.w[k][0], f1.w[k][1]);
            acc[0] = MFMA32(wf, f1.a[k][0], acc[0]); acc[1] = MFMA32(wf, f1.a[k][1], acc[1]); SBAR(); early(k); SBAR(); }
    };
    step(0, [&](int) {}, [&](int i) { out_issueA(c, 4, i); });
    for (int kt = 1; kt < 12; ++kt) step(kt, [&](int i) { out_issueA(c, kt + 3, i); }, [&](int i) { out_issueA(c, kt + 4, i); });
    step(12, [&](int i) { out_issueA(c, 15, i); }, [&](int i) { out_issueB(c, 0, i); });
    step(13, [&](int i) { out_issueB(c, 0, i); }, [&](int i) { out_issueB(c, 1, i); });
    step(14, [&](int i) { out_issueB(c, 1, i); }, [&](int i) { out_issueB(c, 2, i); });
    step(15, [&](int i) { out_issueB(c, 2, i); }, [&](int i) { out_issueB(c, 3, i); });
    out_issueB(c, 3, 2); out_issueB(c, 3, 3);
    stamp();
    const srd_t srdX = mk_srd(ws + WS_XCH + (size_t)tile * 262144, 262144);
#pragma unroll
    for (int mb = 0; mb < 2; ++mb) { const int q = 2 * wr + mb;
        if (q != s) {
#pragma unroll
            for (int rq = 0; rq < 4; ++rq) store16_sc1(__builtin_bit_cast(u32x4, quad(acc[mb], rq)), c.lane16, srdX, (unsigned)(((q * 4 + s) * 4 + wc) * 4096 + rq * 1024)); } }
    wait_bar<0>();
    if (tid == 0) st_agent(flags + (FL_XF + tile * 4 + s) * FL_STRIDE, tok);
    stamp();
    const int row = rb * 128 + 32 * s + r32;
    const srd_t srdOut = mk_srd(out, 4194304);
    if (wr == (s >> 1)) {
        const u64* fp = flags + (FL_XF + tile * 4 + (lane & 3)) * FL_STRIDE;
        for (unsigned spins = 0; spins < (1u << 15); ++spins) { const u64 v = ld_agent(fp); if (__all(v == tok)) break; __builtin_amdgcn_s_sleep(1); }
        asm volatile("" ::: "memory");
        f32x4 p[4][4];
#pragma unroll
        for (int q = 0; q < 4; ++q)
#pragma unroll
            for (int rq = 0; rq < 4; ++rq) p[q][rq] = __builtin_bit_cast(f32x4, __builtin_amdgcn_raw_buffer_load_b128(srdX, (q == s ? 0x7ffffff0u : c.lane16) + rq * 1024, ((s * 4 + q) * 4 + wc) * 4096, 16));
        f32x4 r[4];
#pragma unroll
        for (int rq = 0; rq < 4; ++rq) r[rq] = ((p[0][rq] + p[1][rq]) + (p[2][rq] + p[3][rq])) + ((s & 1) ? quad(acc[1], rq) : quad(acc[0], rq));
        const unsigned vo = (row * 1024 + cb * 128 + 32 * wc + 8 * hi) * 2;
        __builtin_amdgcn_raw_buffer_store_b128(out_pack(r[0], r[1]), srdOut, vo, 0, OUT_AUX); __builtin_amdgcn_raw_buffer_store_b128(out_pack(r[2], r[3]), srdOut, vo + 32, 0, OUT_AUX);
    } else {
        f32x16 accB[2] = {f32x16{}, f32x16{}};
#pragma unroll
        for (int ch = 0; ch < 4; ++ch)
#pragma unroll
            for (int hf = 0; hf < 2; ++hf) {
                unsigned w0 = waddr[0] + ch * 32768 + hf * 16384, w1 = waddr[1] + ch * 32768 + hf * 16384, oa = lds0 + OUT_O + (8 * ch + 4 * hf) * 1024 + c.lane16; PIN(w0); PIN(w1); PIN(oa);
                bf16x8 a[4]; bf16x4 w[4][2];
#pragma unroll
                for (int ks = 0; ks < 4; ++ks) { w[ks][0] = ldstr(w0 + ks * 4096); w[ks][1] = ldstr(w1 + ks * 4096); a[ks] = ldsf(oa + ks * 1024); }
#pragma unroll
                for (int ks = 0; ks < 4; ++ks) accB[ks & 1] = MFMA32(cat4(w[ks][0], w[ks][1]), a[ks], accB[ks & 1]);
            }
        const f32x16 rB = accB[0] + accB[1];
        const unsigned vo = (row * 1024 + 512 + cb * 128 + 32 * wc + 8 * hi) * 2;
        __builtin_amdgcn_raw_buffer_store_b128(out_pack(quad(rB, 0), quad(rB, 1)), srdOut, vo, 0, OUT_AUX); __builtin_amdgcn_raw_buffer_store_b128(out_pack(quad(rB, 2), quad(rB, 3)), srdOut, vo + 32, 0, OUT_AUX);
    }
}
#define BXTOUCH 0
#define XTOUCH 0
#define TLBWARM 0
#define CODEWARM 1
constexpr unsigned CODE_BYTES = 45 * 1024;
constexpr int FUSED_LDS = 163840;
constexpr unsigned WPHI_LDS = 156160;
struct FusedArgs { const u16 *X, *Ckv, *Wq, *Wuk, *Wuv, *Wo, *Bx, *Wqkv, *Wphi, *Bwo; char* ws; u16* out; };

__global__ __launch_bounds__(512, 2) void fused_kernel(FusedArgs a) {
    const int tid = threadIdx.x, lane = tid & 63;
    const int wid = rfl(tid >> 6);
    const int bid = blockIdx.x, h = (bid & 7) * 4 + (bid >> 6), j = (bid >> 3) & 7;
    if (bid < CODEWARM * 8) {
        u64 pc_; asm volatile("s_getpc_b64 %0" : "=s"(pc_));
        const unsigned o_ = wid * 8192 + lane * 128;
        asm volatile("s_mov_b32 m0, %0\n\ts_nop 0\n\tglobal_load_lds_dword %1, %2" :: "s"(163840u - 256u), "v"(o_ < CODE_BYTES ? o_ : 0u), "s"(pc_ & ~(u64)127) : "memory"); }
    const unsigned lds0 = 0u;
    Stamper stamp{0u, 0, lane};
    stamp();
    u16* Kf = (u16*)(a.ws + WS_KF); u16* Vf = (u16*)(a.ws + WS_VF); u16* Cf = (u16*)(a.ws + WS_CF);
    u16* PhiQ = (u16*)(a.ws + WS_PHIQ); u16* Of = (u16*)(a.ws + WS_OF); float* Bst = (float*)(a.ws + WS_BST);
    u64* flags = (u64*)(a.ws + WS_FLAG);
    float* Bred = (float*)(a.ws + WS_BRED);
    const int hg = bid & 7, rc = bid >> 3;
    asm volatile("" :: "s"(a.X), "s"(a.Ckv), "s"(a.Wq), "s"(a.Wuk), "s"(a.Wuv), "s"(a.Wphi), "s"(a.ws));
    ProjCtx pc; proj_setup(pc, a.X, a.Ckv, a.Wq, a.Wuk, a.Wuv, Kf, Vf, lds0, h, j, wid, lane);
#pragma unroll
    for (int i = 0; i < 8; ++i) issueKV(pc, 0, i);
#pragma unroll
    for (int i = 0; i < 8; ++i) issueKV(pc, 1, i);
    const unsigned tsc = lds0 + TOUCH_LDS;
    { const unsigned vW = (lane >> 4) * 524288 + (lane & 15) * 128;
      const char* base; unsigned vo = pc.l128;
      if (wid < 4) { base = (const char*)((wid & 1) ? a.Wuv : a.Wuk) + ((wid & 2) ? 4 : 2) * 524288 + bid * 2048 - ((wid & 2) ? 0 : 0); vo = vW; }
      else if (wid == 4) base = (const char*)a.Ckv + bid * 8192;
      else { base = (const char*)a.Ckv; vo = 0; }
      touch_g<1>(tsc, vo, base); }
    if (wid < 2) dma1(lds0 + WPHI_LDS + wid * 1024, lane * 16, mk_srd(a.Wphi, 16384), (unsigned)(hg * 2048 + wid * 1024));
    const u64 epoch = *(const u64 __attribute__((address_space(4)))*)(flags + FL_EPOCH * FL_STRIDE);
    u64 tok = mix64(epoch); if (tok == epoch) tok ^= 1ull;
    tok = ((u64)rfl((unsigned)(tok >> 32)) << 32) | rfl((unsigned)tok); asm volatile("" : "+s"(tok));
    B1Ctx bc; b1_setup(bc, a.Bx, a.Wqkv, lds0, rc, hg, wid, lane);
    stamp();
    auto touchk = [&](int kt) {
        const char* base = (const char*)a.Ckv; unsigned vo = 0;
        if (kt == 0) {
            if (wid == 0) { base = (const char*)a.Bx + bid * 8192; vo = pc.l128; }
            else if (wid == 1) { base = (const char*)a.X + bid * 8192; vo = pc.l128; }
            else if (wid == 2) { base = (const char*)a.Wqkv + (bid < 192 ? bid : 0) * 8192; vo = pc.l128; }
        } else if (kt == 1) {
            if (wid < 2) { base = (const char*)a.Wq + (bid * 2 + wid) * 8192; vo = pc.l128; }
        } else if (kt == 2) {
            if (wid < 2) { base = (const char*)a.Wo + (bid * 2 + wid) * 8192; vo = pc.l128; }
            else if (wid == 2) { base = (const char*)a.Bwo + (bid & 63) * 8192; vo = pc.l128; }
        }
        touch_g<2>(tsc, vo, base); };
    u32x4 pk[16];
    kv_run(pc, lane, pk, touchk, [&](int kt) { b1_issue(bc, kt); }, stamp);
    stamp();
    B1Acc ba;
    const bool early = (j & 3) == 0;
    b1_gemm(bc, ba, lane, early, [&](int n) { if (early) kv_store(pc, pk, n); else if (n >= 1 && n <= 6) kv_store(pc, pk, n - 1); },
            [&](int n) {
#pragma unroll
                for (int i = 0; i < 6; ++i) issueQ(pc, n, i); }, stamp);
    stamp();
    u32x2 wp; { const int l16 = lane & 15, g = lane >> 4; const LAS u16* w = (const LAS u16*)(lds0 + WPHI_LDS + (wid & 3) * 512);
        wp[0] = (unsigned)w[(4 * g) * 16 + l16] | ((unsigned)w[(4 * g + 1) * 16 + l16] << 16); wp[1] = (unsigned)w[(4 * g + 2) * 16 + l16] | ((unsigned)w[(4 * g + 3) * 16 + l16] << 16); }
    b1_phi(ba, [&](int n) { SBAR(); if (n < 6) issueQ(pc, 1, n); else if (n < 12) issueQ(pc, 2, n - 6); else if (early) { if (n < 16) kv_store(pc, pk, 8 + (n < 16 ? n - 12 : 0)); } else kv_store(pc, pk, 6 + n - 12); SBAR(); }, wp, PhiQ, Bst, lds0, rc, hg, wid, lane, stamp);
    stamp();
    wait_bar<21>();
    stamp();
    AttnState st;
    AttnCtx c; attn_setup(c, Kf, Vf, Cf, lds0, h, j, wid, lane);
    B2a b2;
    auto spin = [&](const u64* fp) {
        for (unsigned spins = 0; spins < (1u << 15); ++spins) { const u64 v = ld_agent(fp); if (__all(v == tok)) break; __builtin_amdgcn_s_sleep(1); }
        asm volatile("" ::: "memory"); };
    const u64* fkv = flags + (FL_KV + 8 * h + (lane & 7)) * FL_STRIDE;
    const u64* fbs = flags + (FL_BS + 32 * hg + (lane & 31)) * FL_STRIDE;
    u64 pre = 0;
    q_run(pc, early, lane, pk, st.q, [&](int kt) {
        if (kt == 2) { if (tid == 0) st_agent(flags + (FL_BS + 32 * hg + rc) * FL_STRIDE, tok); }
        if (kt == 3) { if (early && tid == 0) st_agent(flags + (FL_KV + 8 * h + j) * FL_STRIDE, tok); }
        if (kt == 3) { if (wid == 7) pre = ld_agent(fbs); }
        if (kt == 4) {
            if (wid == 7) {
                if (!__all(pre == tok)) spin(fbs);
                asm volatile("" ::: "memory");
                b2a_load(b2, Bst, rc, hg, lane); } }
        if (kt == 5) { pre = ld_agent(fkv); }
        if (kt == 6) {
            if (!early && tid == 0) st_agent(flags + (FL_KV + 8 * h + j) * FL_STRIDE, tok);
            if (wid == 7) b2a_finish(b2, Bred, rc, hg, lane);
            c.known = (unsigned)__builtin_amdgcn_ballot_w64(pre == tok) & 0xffu;
            for (unsigned spins = 0; spins < (1u << 15) && !(c.known & 1u); ++spins) { const u64 v = ld_agent(fkv); c.known = (unsigned)__builtin_amdgcn_ballot_w64(v == tok) & 0xffu; if (!(c.known & 1u)) __builtin_amdgcn_s_sleep(1); }
            asm volatile("" ::: "memory");
            attn_prime(c, wid >> 2);
        } }, stamp);
    c.srdF = mk_srd(flags + (FL_KV + 8 * h) * FL_STRIDE, 1024); c.tok = tok; c.prev = 0;
    stamp();
    BARRIER();
    if (wid < 4) {
#pragma unroll
        for (int s = 0; s < 8; ++s) *(LAS bf16x8*)(c.qstash + s * 1024) = st.q[s]; }
    stamp();
    B2b<4> b2b;
    attn_run(st, c, lds0, h, j, wid, lane,
             [&]() { if (tid == 448) st_agent(flags + (FL_BR + 32 * hg + rc) * FL_STRIDE, tok); },
             [&]() { b2b_load<4>(b2b, PhiQ, Bred, flags + (FL_BR + 32 * hg + 8 * wid) * FL_STRIDE, tok, 4 * hg + wid, 4 * rc, lane); },
             [&]() { b2b_finish<4>(b2b, Of, 4 * hg + wid, 4 * rc, lane); }, stamp);
    wait_bar<0>();
    if (tid == 0) st_agent(flags + (FL_C + bid) * FL_STRIDE, tok);
    stamp();
    out_phase(a.Wo, a.Bwo, a.out, a.ws, tok, lds0, bid, wid, lane, stamp);
    if (bid == 0 && wid == 7) {
        bool ok = false;
        for (unsigned spins = 0; spins < (1u << 15) && !ok; ++spins) { bool all = true;
#pragma unroll
            for (int i = 0; i < 4; ++i) all &= ld_agent(flags + (FL_C + lane + 64 * i) * FL_STRIDE) == tok;
            ok = __all(all); if (!ok) __builtin_amdgcn_s_sleep(1); }
        if (lane == 0) st_agent(flags + FL_EPOCH * FL_STRIDE, tok);
    }
    stamp();
}

#define WS_END 1
#define WS_SHIFT(n) (WS_END && (n) >= ((size_t)160 << 20) ? (((n) - ((size_t)80 << 20)) & ~(size_t)0x1fffff) : (size_t)0)
extern "C" void kernel_launch(void* const* d_in, const int* in_sizes, int n_in, void* d_out, int out_size, void* d_ws, size_t ws_size, hipStream_t stream) {
    FusedArgs a;
    a.X = (const u16*)d_in[0]; a.Ckv = (const u16*)d_in[1]; a.Wq = (const u16*)d_in[2]; a.Wuk = (const u16*)d_in[3]; a.Wuv = (const u16*)d_in[4]; a.Wo = (const u16*)d_in[5];
    a.Bx = (const u16*)d_in[6]; a.Wqkv = (const u16*)d_in[7]; a.Wphi = (const u16*)d_in[8]; a.Bwo = (const u16*)d_in[9]; a.ws = (char*)d_ws + WS_SHIFT(ws_size); a.out = (u16*)d_out;
    (void)hipFuncSetAttribute((const void*)fused_kernel, hipFuncAttributeMaxDynamicSharedMemorySize, FUSED_LDS);
    fused_kernel<<<256, 512, FUSED_LDS, stream>>>(a);
}
```

```cpp
#include <hip/hip_runtime.h>
#include <stdint.h>
#include <type_traits>

typedef unsigned short u16;
typedef unsigned long long u64;
typedef __bf16 bf16x8 __attribute__((ext_vector_type(8)));
typedef __bf16 bf16x4 __attribute__((ext_vector_type(4)));
typedef __bf16 bf16x2 __attribute__((ext_vector_type(2)));
typedef short s16x4 __attribute__((ext_vector_type(4)));
typedef float f32x16 __attribute__((ext_vector_type(16)));
typedef float f32x4 __attribute__((ext_vector_type(4)));
typedef float f32x2 __attribute__((ext_vector_type(2)));
typedef unsigned u32x4 __attribute__((ext_vector_type(4)));
typedef unsigned u32x2 __attribute__((ext_vector_type(2)));
typedef __amdgpu_buffer_rsrc_t srd_t;
#define LAS __attribute__((address_space(3)))
#define GAS __attribute__((address_space(1)))
#define SBAR() __builtin_amdgcn_sched_barrier(0)
#define PIN(x) asm volatile("" : "+v"(x))
#define MFMA32(a, b, c) __builtin_amdgcn_mfma_f32_32x32x16_bf16((a), (b), (c), 0, 0, 0)
#define MFMA16(a, b, c) __builtin_amdgcn_mfma_f32_16x16x32_bf16((a), (b), (c), 0, 0, 0)
#define DI __device__ __forceinline__
#define MFMA16I(acc, a, b) asm volatile("v_mfma_f32_16x16x32_bf16 %0, %1, %2, %0" : "+v"(acc) : "v"(a), "v"(b))
#define MFMA_DRAIN() asm volatile("s_nop 7")
#pragma clang diagnostic ignored "-Wint-to-pointer-cast"

DI unsigned cvtpk(float lo, float hi) { f32x2 v = {lo, hi}; bf16x2 b = __builtin_convertvector(v, bf16x2); return __builtin_bit_cast(unsigned, b); }
DI unsigned rfl(unsigned x) { return __builtin_amdgcn_readfirstlane(x); }
DI float swap_max(float x) { auto rr = __builtin_amdgcn_permlane32_swap(__float_as_uint(x), __float_as_uint(x), false, false); return __builtin_fmaxf(__uint_as_float(rr[0]), __uint_as_float(rr[1])); }
DI float swap_add(float x) { auto rr = __builtin_amdgcn_permlane32_swap(__float_as_uint(x), __float_as_uint(x), false, false); return __uint_as_float(rr[0]) + __uint_as_float(rr[1]); }
DI srd_t mk_srd(const void* p, int bytes) { return __builtin_amdgcn_make_buffer_rsrc((void*)p, (short)0, bytes, 0x00020000); }
DI bf16x4 ldstr(unsigned a) { return __builtin_amdgcn_ds_read_tr16_b64_v4bf16((LAS bf16x4*)a); }
DI bf16x8 ldsf(unsigned a) { return *(const LAS bf16x8*)a; }
DI bf16x8 cat4(bf16x4 a, bf16x4 b) { return __builtin_shufflevector(a, b, 0, 1, 2, 3, 4, 5, 6, 7); }
#define SCALARIZE2(a, b) do {} while (0)
DI void dma1(unsigned m0v, unsigned voff, srd_t srd, unsigned soff) {
    SCALARIZE2(m0v, soff);
    asm volatile("s_mov_b32 m0, %0\n\ts_nop 0\n\tbuffer_load_dwordx4 %1, %2, %3 offen lds" :: "s"(m0v), "v"(voff), "s"(srd), "s"(soff) : "memory");
}
DI void dma1s(unsigned m0v, unsigned voff, srd_t srd, unsigned soff) {
    SCALARIZE2(m0v, soff);
    asm volatile("s_mov_b32 m0, %0\n\ts_nop 0\n\tbuffer_load_dwordx4 %1, %2, %3 offen sc1 lds" :: "s"(m0v), "v"(voff), "s"(srd), "s"(soff) : "memory");
}
DI void touch(unsigned m0v, unsigned voff, srd_t srd, unsigned soff) {
    SCALARIZE2(m0v, soff);
    asm volatile("s_mov_b32 m0, %0\n\ts_nop 0\n\tbuffer_load_dword %1, %2, %3 offen lds" :: "s"(m0v), "v"(voff), "s"(srd), "s"(soff) : "memory");
}
DI void store16_sc1(u32x4 w, unsigned voff, srd_t srd, unsigned soff) {
    unsigned z = soff; SCALARIZE2(soff, z);
    asm volatile("buffer_store_dwordx4 %0, %1, %2, %3 offen sc1\n\ts_nop 1" :: "v"(w), "v"(voff), "s"(srd), "s"(soff) : "memory");
}
template <int N> DI void wait_vm() { asm volatile("s_waitcnt vmcnt(%0)" :: "i"(N) : "memory"); }
template <int N> DI void wait_bar() { asm volatile("s_waitcnt vmcnt(%0) lgkmcnt(0)\n\ts_barrier" :: "i"(N) : "memory"); }
template <int N> DI void wait_bar_keep() { asm volatile("s_waitcnt vmcnt(%0)\n\ts_barrier" :: "i"(N) : "memory"); }
#define BARRIER() asm volatile("s_waitcnt lgkmcnt(0)\n\ts_barrier" ::: "memory")

struct Stamper { unsigned v; int n; int lane;
    DI void operator()() {
    } };

constexpr size_t WS_FLAG = 0;
constexpr size_t WS_KF = 1 << 20, WS_VF = WS_KF + (16 << 20), WS_CF = WS_VF + (16 << 20), WS_PHIQ = WS_CF + (16 << 20), WS_OF = WS_PHIQ + (4 << 20), WS_BST = WS_OF + (2 << 20), WS_BRED = WS_BST + (3 << 20), WS_XCH = WS_BRED + (1 << 20), WS_DBG = 77 << 20;
constexpr int FL_STRIDE = 16;
constexpr int FL_EPOCH = 0, FL_KV = 32, FL_BS = 32 + 256, FL_BR = 32 + 512, FL_XF = 32 + 768, FL_C = FL_XF + 256;
typedef GAS u64 gu64;
DI u64 ld_agent(const u64* p) { return __hip_atomic_load((gu64*)p, __ATOMIC_RELAXED, __HIP_MEMORY_SCOPE_AGENT); }
DI void st_agent(u64* p, u64 v) { __hip_atomic_store((gu64*)p, v, __ATOMIC_RELAXED, __HIP_MEMORY_SCOPE_AGENT); }
DI u64 mix64(u64 z) { z += 0x9E3779B97F4A7C15ull; z = (z ^ (z >> 30)) * 0xBF58476D1CE4E5B9ull; z = (z ^ (z >> 27)) * 0x94D049BB133111EBull; return z ^ (z >> 31); }
DI f32x4 mfma16_16(u32x2 a, u32x2 b, f32x4 c) { return __builtin_amdgcn_mfma_f32_16x16x16bf16_1k(__builtin_bit_cast(s16x4, a), __builtin_bit_cast(s16x4, b), c, 0, 0, 0); }
DI float rbf16(float x) { return __uint_as_float(cvtpk(x, 0.f) << 16); }
DI float x16_max(float x) { auto rr = __builtin_amdgcn_permlane16_swap(__float_as_uint(x), __float_as_uint(x), false, false); return __builtin_fmaxf(__uint_as_float(rr[0]), __uint_as_float(rr[1])); }
DI float x16_add(float x) { auto rr = __builtin_amdgcn_permlane16_swap(__float_as_uint(x), __float_as_uint(x), false, false); return __uint_as_float(rr[0]) + __uint_as_float(rr[1]); }
DI float grp_max(float x) { return swap_max(x16_max(x)); }
DI float grp_sum(float x) { return swap_add(x16_add(x)); }
#define PHIQ_AUX 0
constexpr float LOG2E = 1.4426950408889634f;
constexpr unsigned B1_RED = 147456;
#define BROT 0
struct B1Ctx { srd_t srdX, srdW; unsigned vx, vw[2], sx, sw; int wid, rot; unsigned lds0; };
DI void b1_setup(B1Ctx& c, const u16* Bx, const u16* Wqkv, unsigned lds0, int rc, int hg, int wid, int lane) {
    c.srdX = mk_srd(Bx, 2097152); c.srdW = mk_srd(Wqkv, 512 * 1536 * 2); c.wid = wid; c.lds0 = lds0;
    c.vx = (lane >> 3) * 1024 + (((lane & 7) ^ ((4 * wid + (lane >> 4)) & 7)) * 16);
#pragma unroll
    for (int p = 0; p < 2; ++p) c.vw[p] = (lane >> 3) * 3072 + (((lane & 7) ^ (2 * (((lane >> 4) & 1) | (p << 1)))) * 16);
    c.sx = (64 * rc + 8 * wid) * 1024; c.sw = hg * 128; c.rot = BROT ? rc & 7 : 0;
}
DI void b1_issue(const B1Ctx& c, int kt) {
    const unsigned st = c.lds0 + (kt & 3) * 32768;
    kt = (kt + c.rot) & 7;
    dma1(st + c.wid * 1024, c.vx, c.srdX, c.sx + kt * 128);
#pragma unroll
    for (int i = 0; i < 3; ++i) { const int pw = 3 * c.wid + i, m = pw >> 3, pr = pw & 7;
        dma1(st + 8192 + pw * 1024, (pr & 1) ? c.vw[1] : c.vw[0], c.srdW, c.sw + (kt * 64 + 8 * pr) * 3072 + m * 1024); }
}
struct B1Acc { f32x4 aq[2], ak[2], av[2]; };
struct B1Fr { bf16x8 x[2][2]; bf16x4 w[2][3][2]; };
struct B1Ad { unsigned xa[2][2], wa[2]; };
template <int KT> DI void b1_read(B1Fr& f, const B1Ad& ad) {
    constexpr unsigned st = (KT & 1) * 32768; constexpr int sp = (KT >> 1) & 1;
#pragma unroll
    for (int ks = 0; ks < 2; ++ks) { const unsigned xa = ad.xa[sp][ks];
        f.x[ks][0] = ldsf(xa + st); f.x[ks][1] = ldsf(xa + st + 2048);
#pragma unroll
        for (int m = 0; m < 3; ++m) { f.w[ks][m][0] = ldstr(ad.wa[sp] + st + m * 8192 + ks * 4096); f.w[ks][m][1] = ldstr(ad.wa[sp] + st + m * 8192 + ks * 4096 + 512); } }
}
DI void b1_mma(B1Acc& a, const B1Fr& f) {
#pragma unroll
    for (int ks = 0; ks < 2; ++ks) {
        const bf16x8 wq = cat4(f.w[ks][0][0], f.w[ks][0][1]), wk = cat4(f.w[ks][1][0], f.w[ks][1][1]), wv = cat4(f.w[ks][2][0], f.w[ks][2][1]);
        a.aq[0] = MFMA16(wq, f.x[ks][0], a.aq[0]); a.aq[1] = MFMA16(wq, f.x[ks][1], a.aq[1]); a.ak[0] = MFMA16(wk, f.x[ks][0], a.ak[0]); a.ak[1] = MFMA16(wk, f.x[ks][1], a.ak[1]);
        a.av[0] = MFMA16(f.x[ks][0], wv, a.av[0]); a.av[1] = MFMA16(f.x[ks][1], wv, a.av[1]);
    }
}
template <class St, class IQ>
DI void b1_gemm(const B1Ctx& c, B1Acc& a, int lane, bool early, St store, IQ issueQt, Stamper& stamp) {
    const int l16 = lane & 15, g = lane >> 4, q4 = l16 >> 2, p4 = lane & 3, wid = c.wid;
    const int row = 32 * (wid >> 2) + l16;
    B1Ad ad;
#pragma unroll
    for (int sp = 0; sp < 2; ++sp) {
        ad.xa[sp][0] = c.lds0 + sp * 65536 + row * 128 + (((0 + g) ^ ((row >> 1) & 7)) << 4); ad.xa[sp][1] = c.lds0 + sp * 65536 + row * 128 + (((4 + g) ^ ((row >> 1) & 7)) << 4);
        ad.wa[sp] = c.lds0 + sp * 65536 + 8192 + (8 * g + q4) * 128 + (((wid & 3) ^ (((q4 >> 1) & 1) | ((g & 1) << 1))) * 32) + p4 * 8;
        PIN(ad.xa[sp][0]); PIN(ad.xa[sp][1]); PIN(ad.wa[sp]); }
#pragma unroll
    for (int r = 0; r < 2; ++r) { a.aq[r] = f32x4{}; a.ak[r] = f32x4{}; a.av[r] = f32x4{}; }
#pragma unroll
    for (int r = 0; r < 2; ++r) { PIN(a.aq[r]); PIN(a.ak[r]); PIN(a.av[r]); }
    B1Fr f0, f1;
    b1_read<0>(f0, ad);
#define B1S() do {} while (0)
#define B1W(E, L) do { B1S(); if (early) wait_bar<E>(); else wait_bar<L>(); B1S(); } while (0)
#define B1T(KT, CUR, NXT, PRE) do { b1_read<KT + 1>(NXT, ad); SBAR(); PRE; store(KT); SBAR(); b1_mma(a, CUR); SBAR(); } while (0)
    B1W(8, 8);   B1T(0, f0, f1, b1_issue(c, 4));
    B1W(9, 8);   B1T(1, f1, f0, b1_issue(c, 5));
    B1W(10, 9);  B1T(2, f0, f1, b1_issue(c, 6));
    B1W(11, 10); B1T(3, f1, f0, b1_issue(c, 7));
    B1W(11, 11); B1T(4, f0, f1, (void)0);
    B1W(7, 7);   B1T(5, f1, f0, issueQt(0));
    B1W(9, 9);   B1T(6, f0, f1, (void)0);
    BARRIER();   store(7); SBAR(); b1_mma(a, f1);
#undef B1W
#undef B1T
}
template <int N, class Tick> DI void phi_map(const f32x4 (&p)[N], u32x4 (&out)[N], Tick tick) {
    float x[N][4], mx[N], nm[N], ep[N][4], en[N][4], sp[N], sn[N];
#pragma unroll
    for (int i = 0; i < N; ++i) {
#pragma unroll
        for (int e = 0; e < 4; ++e) x[i][e] = rbf16(p[i][e]) * LOG2E;
        mx[i] = __builtin_fmaxf(__builtin_fmaxf(x[i][0], x[i][1]), __builtin_fmaxf(x[i][2], x[i][3])); nm[i] = -__builtin_fminf(__builtin_fminf(x[i][0], x[i][1]), __builtin_fminf(x[i][2], x[i][3]));
        if (i & 1) tick(i >> 1); }
#pragma unroll
    for (int i = 0; i < N; ++i) { mx[i] = x16_max(mx[i]); nm[i] = x16_max(nm[i]); if (i & 1) tick(2 + (i >> 1)); }
#pragma unroll
    for (int i = 0; i < N; ++i) { mx[i] = swap_max(mx[i]); nm[i] = swap_max(nm[i]); if (i & 1) tick(4 + (i >> 1)); }
#pragma unroll
    for (int i = 0; i < N; ++i) {
#pragma unroll
        for (int e = 0; e < 4; ++e) { ep[i][e] = __builtin_amdgcn_exp2f(x[i][e] - mx[i]); en[i][e] = __builtin_amdgcn_exp2f(-x[i][e] - nm[i]); }
        sp[i] = (ep[i][0] + ep[i][1]) + (ep[i][2] + ep[i][3]); sn[i] = (en[i][0] + en[i][1]) + (en[i][2] + en[i][3]); tick(6 + i); }
#pragma unroll
    for (int i = 0; i < N; ++i) { sp[i] = x16_add(sp[i]); sn[i] = x16_add(sn[i]); if (i & 1) tick(10 + (i >> 1)); }
#pragma unroll
    for (int i = 0; i < N; ++i) { sp[i] = __builtin_amdgcn_rcpf(swap_add(sp[i])); sn[i] = __builtin_amdgcn_rcpf(swap_add(sn[i])); if (i & 1) tick(12 + (i >> 1)); }
#pragma unroll
    for (int i = 0; i < N; ++i) { out[i] = (u32x4){cvtpk(ep[i][0] * sp[i], ep[i][1] * sp[i]), cvtpk(ep[i][2] * sp[i], ep[i][3] * sp[i]), cvtpk(en[i][0] * sn[i], en[i][1] * sn[i]), cvtpk(en[i][2] * sn[i], en[i][3] * sn[i])};
        if (i & 1) tick(14 + (i >> 1)); }
}
template <class Tick>
DI void b1_phi(const B1Acc& a, Tick tick, u32x2 wp, u16* PhiQ, float* Bst, unsigned lds0, int rc, int hg, int wid, int lane, Stamper& stamp) {
    const int h = 4 * hg + (wid & 3), row0 = 64 * rc + 32 * (wid >> 2);
    const int l16 = lane & 15, g = lane >> 4;
    f32x4 sp = f32x4{}, sn = f32x4{}; float zp = 0.f, zn = 0.f;
    f32x4 pin[4]; u32x4 pout[4];
#pragma unroll
    for (int r = 0; r < 2; ++r) {
        const u32x2 qb = {cvtpk(a.aq[r][0], a.aq[r][1]), cvtpk(a.aq[r][2], a.aq[r][3])}, kb = {cvtpk(a.ak[r][0], a.ak[r][1]), cvtpk(a.ak[r][2], a.ak[r][3])};
        pin[r] = mfma16_16(wp, qb, f32x4{}); pin[2 + r] = mfma16_16(wp, kb, f32x4{});
    }
    phi_map<4>(pin, pout, tick);
    tick(16);
    const u32x4 wq[2] = {pout[0], pout[1]}, wk[2] = {pout[2], pout[3]};
    const unsigned tb = lds0 + B1_RED + wid * 1024;
#pragma unroll
    for (int r = 0; r < 2; ++r) {
        *(LAS u32x2*)(tb + l16 * 32 + g * 8) = (u32x2){wk[r][0], wk[r][1]};
        *(LAS u32x2*)(tb + 512 + l16 * 32 + g * 8) = (u32x2){wk[r][2], wk[r][3]};
        __builtin_amdgcn_raw_buffer_store_b128(wq[r], mk_srd(PhiQ, 4194304), ((h * 128 + (row0 >> 4) + r) * 64 + lane) * 16, 0, PHIQ_AUX);
        asm volatile("s_waitcnt lgkmcnt(0)" ::: "memory");
        const unsigned ta = tb + (4 * g + (l16 >> 2)) * 32 + (lane & 3) * 8;
        u32x2 pp = __builtin_bit_cast(u32x2, ldstr(ta)), pn = __builtin_bit_cast(u32x2, ldstr(ta + 512));
        asm volatile("s_waitcnt lgkmcnt(0)" : "+v"(pp), "+v"(pn) :: "memory");
        const u32x2 vb = {cvtpk(a.av[r][0], a.av[r][1]), cvtpk(a.av[r][2], a.av[r][3])};
        sp = mfma16_16(pp, vb, sp); sn = mfma16_16(pn, vb, sn);
        zp += (__uint_as_float(pp[0] << 16) + __uint_as_float(pp[0] & 0xffff0000u)) + (__uint_as_float(pp[1] << 16) + __uint_as_float(pp[1] & 0xffff0000u));
        zn += (__uint_as_float(pn[0] << 16) + __uint_as_float(pn[0] & 0xffff0000u)) + (__uint_as_float(pn[1] << 16) + __uint_as_float(pn[1] & 0xffff0000u));
    }
    zp = grp_sum(zp); zn = grp_sum(zn);
    BARRIER();
    LAS char* red = (LAS char*)(lds0 + B1_RED + (wid & 3) * 2176);
    if (wid >= 4) { *(LAS f32x4*)(red + lane * 32) = sp; *(LAS f32x4*)(red + lane * 32 + 16) = sn; if (lane < 16) *(LAS f32x2*)(red + 2048 + lane * 8) = (f32x2){zp, zn}; }
    BARRIER();
    sp += *(const LAS f32x4*)(red + lane * 32); sn += *(const LAS f32x4*)(red + lane * 32 + 16);
    const f32x2 zo = *(const LAS f32x2*)(red + 2048 + l16 * 8);
    const srd_t srd = mk_srd(Bst + (size_t)(h * 32 + rc) * 576, wid < 4 ? 2304 : 0);
    store16_sc1(__builtin_bit_cast(u32x4, sp), lane * 32, srd, 0u); store16_sc1(__builtin_bit_cast(u32x4, sn), lane * 32 + 16, srd, 0u);
    __builtin_amdgcn_raw_buffer_store_b64((u32x2){__float_as_uint(zp + zo[0]), __float_as_uint(zn + zo[1])}, srd, lane < 16 ? 2048 + lane * 8 : 0x7fffffff, 0, 16);
}
template <int N> DI void waitN(const u64* flags, u64 tok, int lane) {
    const u64* p = flags + (lane & (N - 1)) * FL_STRIDE;
    for (unsigned spins = 0; spins < (1u << 15); ++spins) { const u64 v = ld_agent(p); if (__all(v == tok)) break; __builtin_amdgcn_s_sleep(1); }
    asm volatile("" ::: "memory");
}
struct B2a { f32x4 v[16]; };
DI void b2a_load(B2a& b, const float* Bst, int rc, int hg, int lane) {
    const int h = 4 * hg + (rc >> 3), c = lane & 31, half = lane >> 5;
    const srd_t srd = mk_srd(Bst + (size_t)h * 32 * 576, 32 * 2304);
    const unsigned vo = (c < 18 ? (18 * (rc & 7) + c) * 16 : 0x7fffffff) + half * 16 * 2304;
#pragma unroll
    for (int i = 0; i < 16; ++i) b.v[i] = __builtin_bit_cast(f32x4, __builtin_amdgcn_raw_buffer_load_b128(srd, vo, i * 2304, 16));
}
DI void b2a_finish(const B2a& b, float* Bred, int rc, int hg, int lane) {
    const int h = 4 * hg + (rc >> 3), c = lane & 31, half = lane >> 5;
    f32x4 s = b.v[0];
#pragma unroll
    for (int i = 1; i < 16; ++i) s += b.v[i];
#pragma unroll
    for (int e = 0; e < 4; ++e) s[e] = swap_add(s[e]);
    const srd_t srdR = mk_srd(Bred + (size_t)h * 576, 2304);
    store16_sc1(__builtin_bit_cast(u32x4, s), (half == 0 && c < 18) ? (18 * (rc & 7) + c) * 16 : 0x7fffffffu, srdR, 0u);
}
template <int NRB> struct B2b { bf16x8 ph[NRB]; f32x4 sp, sn, za, zb; };
template <int NRB>
DI void b2b_load(B2b<NRB>& b, const u16* PhiQ, const float* Bred, const u64* flagsR, u64 tok, int h, int rb0, int lane) {
    PIN(lane);
    const int g = lane >> 4;
    waitN<8>(flagsR, tok, lane);
#pragma unroll
    for (int r = 0; r < NRB; ++r) b.ph[r] = __builtin_bit_cast(bf16x8, __builtin_amdgcn_raw_buffer_load_b128(mk_srd(PhiQ, 4194304), lane * 16, (h * 128 + rb0 + r) * 1024, 0));
    const srd_t srd = mk_srd(Bred + (size_t)h * 576, 2304);
    b.sp = __builtin_bit_cast(f32x4, __builtin_amdgcn_raw_buffer_load_b128(srd, lane * 32, 0, 16)); b.sn = __builtin_bit_cast(f32x4, __builtin_amdgcn_raw_buffer_load_b128(srd, lane * 32 + 16, 0, 16));
    b.za = __builtin_bit_cast(f32x4, __builtin_amdgcn_raw_buffer_load_b128(srd, 2048 + g * 32, 0, 16)); b.zb = __builtin_bit_cast(f32x4, __builtin_amdgcn_raw_buffer_load_b128(srd, 2048 + g * 32 + 16, 0, 16));
}
template <int NRB>
DI void b2b_finish(const B2b<NRB>& b, u16* Of, int h, int rb0, int lane) {
    PIN(lane);
    const int l16 = lane & 15, g = lane >> 4;
    const u32x4 kvw = {cvtpk(b.sp[0], b.sp[1]), cvtpk(b.sp[2], b.sp[3]), cvtpk(b.sn[0], b.sn[1]), cvtpk(b.sn[2], b.sn[3])};
    const u32x4 zw = {cvtpk(b.za[0], b.za[2]), cvtpk(b.zb[0], b.zb[2]), cvtpk(b.za[1], b.za[3]), cvtpk(b.zb[1], b.zb[3])};
    const srd_t srdO = mk_srd(Of, 2097152);
#pragma unroll
    for (int r = 0; r < NRB; ++r) { const int rb16 = rb0 + r;
        const f32x4 num = MFMA16(__builtin_bit_cast(bf16x8, kvw), b.ph[r], f32x4{});
        const f32x4 den = MFMA16(__builtin_bit_cast(bf16x8, zw), b.ph[r], f32x4{});
        const float dn = rbf16(rbf16(den[0]) + 1e-6f);
        const u32x2 w = {cvtpk(rbf16(num[0]) / dn, rbf16(num[1]) / dn), cvtpk(rbf16(num[2]) / dn, rbf16(num[3]) / dn)};
        __builtin_amdgcn_raw_buffer_store_b64(w, srdO, (((rb16 >> 1) * 32 + h) * 2 + (g & 1)) * 512 + ((rb16 & 1) * 16 + l16) * 16 + (g >> 1) * 8, 0, 16); }
}
#define KVPRIO 0
#define KROT 0
struct ProjCtx {
    srd_t srdC, srdX, srdW, srdWq, srdK, srdV;
    unsigned va[2], vw[2], vwq, voK, voV, lds0, l128;
    unsigned sA, sW, sWq, sXa;
    int wid, rot;
};
constexpr unsigned TOUCH_LDS = 163840 - 256;
DI void issueKV(const ProjCtx& c, int kt, int i) {
    const unsigned st = c.lds0 + (kt & 1) * 65536;
    kt = (kt + c.rot) & 7;
    if (i < 4) dma1(st + (4 * c.wid + i) * 1024, c.va[i & 1], c.srdC, c.sA + i * 8192 + kt * 128);
    else dma1(st + 32768 + (c.wid >> 2) * 16384 + (4 * (c.wid & 3) + (i - 4)) * 1024, c.vw[(i - 4) >> 1], c.srdW, c.sW + ((i - 4) >> 1) * 65536 + ((i - 4) & 1) * 128 + kt * 524288);
}
DI void issueQ(const ProjCtx& c, int kt, int i) {
    const unsigned st = c.lds0 + (kt % 3) * 49152;
    kt = (kt + c.rot) & 7;
    if (i < 4) dma1(st + (4 * c.wid + i) * 1024, c.va[i & 1], c.srdX, c.sXa + i * 8192 + kt * 128);
    else dma1(st + 32768 + (2 * c.wid + (i - 4)) * 1024, c.vwq, c.srdWq, c.sWq + (i - 4) * 128 + kt * 524288);
}
#define TOUCHMODE 3
template <int WHICH> DI void touch_g(unsigned m0v, unsigned voff, const void* base) {
    if (!(TOUCHMODE & WHICH)) voff = 0;
    const u64 b = (u64)base; const u64 sb = ((u64)rfl((unsigned)(b >> 32)) << 32) | rfl((unsigned)b);
    asm volatile("s_nop 4\n\ts_mov_b32 m0, %0\n\ts_nop 0\n\tglobal_load_lds_dword %1, %2" :: "s"(rfl(m0v)), "v"(voff), "s"(sb) : "memory");
}

struct GAddr { unsigned a[2]; unsigned w[2][2]; };
DI void gaddr_add(GAddr& g, unsigned d) { g.a[0] += d; g.a[1] += d; g.w[0][0] += d; g.w[0][1] += d; g.w[1][0] += d; g.w[1][1] += d; PIN(g.a[0]); PIN(g.a[1]); PIN(g.w[0][0]); PIN(g.w[0][1]); PIN(g.w[1][0]); PIN(g.w[1][1]); }
DI void gaddr_xor(GAddr& g, unsigned d) { g.a[0] ^= d; g.a[1] ^= d; g.w[0][0] ^= d; g.w[0][1] ^= d; g.w[1][0] ^= d; g.w[1][1] ^= d; }
DI bf16x8 wfrag(const GAddr& g, int ks, int nb) { return cat4(ldstr(g.w[nb & 1][0] + ks * 8192 + (nb >> 1) * 512), ldstr(g.w[nb & 1][1] + ks * 8192 + (nb >> 1) * 512)); }
DI bf16x8 wfragKV(const GAddr& g, int ks, int nb) { return cat4(ldstr(g.w[nb & 1][0] + ks * 8192 + (nb >> 1) * 16384), ldstr(g.w[nb & 1][1] + ks * 8192 + (nb >> 1) * 16384)); }
DI void gaddr_w(GAddr& g, unsigned base, int lane) {
    const int gq = lane >> 4, q4 = (lane & 15) >> 2, p4 = lane & 3;
#pragma unroll
    for (int o = 0; o < 2; ++o)
#pragma unroll
        for (int t = 0; t < 2; ++t) g.w[o][t] = base + 2048 * gq + 64 * (4 * t + q4) + 16 * ((2 * o + (p4 >> 1)) ^ ((2 * gq + t) & 3)) + 8 * (p4 & 1);
}
DI void gaddr_a(GAddr& g, unsigned base, int row0, int lane) {
    const int row = row0 + (lane & 15), gq = lane >> 4;
#pragma unroll
    for (int ks = 0; ks < 2; ++ks) g.a[ks] = base + row * 128 + (((4 * ks + gq) ^ ((row >> 1) & 7)) << 4);
}

#define KVAH 3
constexpr int AH = KVAH;
#define KV_MMA(mb, W) do { MFMA16I(acc[mb][0], W[0], a[mb]); MFMA16I(acc[mb][1], W[1], a[mb]); MFMA16I(acc[mb][2], a[mb], W[2]); MFMA16I(acc[mb][3], a[mb], W[3]); } while (0)
template <class Hook>
DI void kv_step0(f32x4 (&acc)[8][4], const bf16x8 (&w)[4], bf16x8 (&wn)[4], bf16x8 (&an)[AH], const GAddr& g, Hook hook) {
    bf16x8 a[8 + AH];
#pragma unroll
    for (int i = 0; i < AH; ++i) a[i] = an[i];
#pragma unroll
    for (int mb = 0; mb < 8; ++mb) {
        a[mb + AH] = mb + AH < 8 ? ldsf(g.a[0] + (mb + AH) * 2048) : ldsf(g.a[1] + (mb + AH - 8) * 2048);
        if (mb < 4) wn[mb] = wfragKV(g, 1, mb);
        SBAR();
        KV_MMA(mb, w);
        SBAR();
        hook(mb);
        SBAR();
    }
#pragma unroll
    for (int i = 0; i < AH; ++i) an[i] = a[8 + i];
}
struct NoHook { DI void operator()(int) const {} };

template <class Touch, class IssueB1>
DI void kv_part(const ProjCtx& c, GAddr g, u32x4 (&pk)[16], Touch touchk, IssueB1 issueB1, Stamper& stamp) {
    f32x4 acc[8][4];
#pragma unroll
    for (int a = 0; a < 8; ++a)
#pragma unroll
        for (int b = 0; b < 4; ++b) { acc[a][b] = f32x4{}; PIN(acc[a][b]); }
    bf16x8 w0[4], w1[4], an[AH];
    wait_bar<9>();
    stamp();
#pragma unroll
    for (int nb = 0; nb < 4; ++nb) w0[nb] = wfragKV(g, 0, nb);
#pragma unroll
    for (int i = 0; i < AH; ++i) an[i] = ldsf(g.a[0] + i * 2048);
    auto tile = [&](int kt, auto late, auto mid, auto waitB, auto early, bool next) {
        kv_step0(acc, w0, w1, an, g, late);
        bf16x8 a[8];
#pragma unroll
        for (int i = 0; i < AH; ++i) a[i] = an[i];
#pragma unroll
        for (int mb = 0; mb < 8 - AH; ++mb) {
            a[mb + AH] = ldsf(g.a[1] + (mb + AH) * 2048);
            SBAR();
            KV_MMA(mb, w1);
            SBAR();
            if (mb == 0) { mid(); SBAR(); }
        }
        waitB();
        if (next) { gaddr_xor(g, 65536u);
#pragma unroll
            for (int nb = 0; nb < 4; ++nb) w0[nb] = wfragKV(g, 0, nb);
#pragma unroll
            for (int i = 0; i < AH; ++i) an[i] = ldsf(g.a[0] + i * 2048); }
        SBAR();
#pragma unroll
        for (int mb = 8 - AH; mb < 8; ++mb) { KV_MMA(mb, w1); SBAR(); early(mb - (8 - AH)); SBAR(); }
    };
    for (int kt = 0; kt < 6; ++kt)
        tile(kt, [&](int mb) { if (kt > 0 && (mb & 1)) issueKV(c, kt + 1, 4 + (mb >> 1)); }, [&]() { touchk(kt); }, [&]() { wait_bar<1>(); },
             [&](int i) { if (i == 0) { issueKV(c, kt + 2, 0); issueKV(c, kt + 2, 1); } else issueKV(c, kt + 2, 1 + i); }, true);
    tile(6, [&](int mb) { if (mb & 1) issueKV(c, 7, 4 + (mb >> 1)); }, [&]() {}, [&]() { wait_bar<0>(); }, [&](int i) { if (i < 2) issueB1(i); }, true);
    tile(7, NoHook{}, [&]() {}, [&]() { stamp(); wait_bar<4>(); stamp(); }, [&](int i) { if (i < 2) issueB1(2 + i); }, false);
    __builtin_amdgcn_s_setprio(0);
    MFMA_DRAIN();
#pragma unroll
    for (int n = 0; n < 16; ++n) { const int m = n - 8;
        const f32x4 lo = n < 8 ? acc[n & 7][0] : acc[2 * ((m >> 1) & 3)][2 + (m & 1)], hi = n < 8 ? acc[n & 7][1] : acc[2 * ((m >> 1) & 3) + 1][2 + (m & 1)];
        pk[n] = (u32x4){cvtpk(lo[0], lo[1]), cvtpk(lo[2], lo[3]), cvtpk(hi[0], hi[1]), cvtpk(hi[2], hi[3])}; PIN(pk[n]); }
}
DI void kv_store(const ProjCtx& c, const u32x4 (&pk)[16], int n) {
    const int m = n - 8;
    if (n < 8) store16_sc1(pk[n], c.voK, c.srdK, (unsigned)((n >> 2) * 16384 + (n & 3) * 4096));
    else store16_sc1(pk[n], c.voV, c.srdV, (unsigned)((m >> 2) * 16384 + (m & 1) * 2048 + ((m >> 1) & 1) * 1024));
}

struct QFr { bf16x8 x[2]; bf16x8 w[8]; };
template <int KS> DI void q_read(QFr& f, const GAddr& g, int i) {
    if (i == 1) f.x[0] = ldsf(g.a[KS]); else if (i == 2) f.x[1] = ldsf(g.a[KS] + 2048); else { const int nb = i == 0 ? 0 : i - 2; f.w[nb] = wfrag(g, KS, nb); }
}
template <int KSN, bool NEXT, class Hook>
DI void q_kstep(f32x4 (&acc)[8][2], const QFr& cur, QFr& nxt, const GAddr& gn, Hook hook) {
#pragma unroll
    for (int nb = 0; nb < 8; ++nb) {
        MFMA16I(acc[nb][0], cur.w[nb], cur.x[0]); MFMA16I(acc[nb][1], cur.w[nb], cur.x[1]);
        SBAR();
        if (NEXT) { if (nb < 2) { q_read<KSN>(nxt, gn, 2 * nb); q_read<KSN>(nxt, gn, 2 * nb + 1); } else q_read<KSN>(nxt, gn, nb + 2); }
        hook(nb);
        SBAR();
    }
}
template <int KT, class Pub>
DI void q_tile(const ProjCtx& c, bool early, GAddr& g, f32x4 (&acc)[8][2], QFr& f0, QFr& f1, const u32x4 (&pk)[16], Pub after, Stamper& stamp) {
    q_kstep<1, true>(acc, f0, f1, g, [&](int nb) {
        if (KT >= 1 && KT + 2 < 8 && nb < 3) issueQ(c, KT + 2, 3 + nb);
        if (nb == 4) { if (early) { if (KT < 2) kv_store(c, pk, 12 + 2 * (KT < 2 ? KT : 0)); } else if (KT < 5) kv_store(c, pk, 11 + (KT < 5 ? KT : 0)); }
        if (nb == 7 && early && KT < 2) kv_store(c, pk, 13 + 2 * (KT < 2 ? KT : 0)); });
    if constexpr (KT < 7) {
        constexpr int WE = KT == 0 ? 17 : (KT == 1 ? 19 : (KT == 2 ? 8 : (KT <= 5 ? 6 : 0)));
        constexpr int WL = KT == 0 ? 17 : (KT == 1 ? 18 : (KT <= 4 ? 8 : (KT == 5 ? 7 : 0)));
        const bool w7 = KT == 5 && c.wid == 7;
        if (early) { if (w7) wait_bar<WE + 16>(); else wait_bar<WE>(); } else { if (w7) wait_bar<WL + 16>(); else wait_bar<WL>(); }
        stamp();
        after(KT);
        gaddr_add(g, (unsigned)(((KT + 1) % 3 - KT % 3) * 49152));
        q_kstep<0, true>(acc, f1, f0, g, [&](int nb) { if (KT + 3 < 8 && nb < 3) issueQ(c, KT + 3, nb); });
    } else q_kstep<0, false>(acc, f1, f0, g, NoHook{});
}

DI void proj_setup(ProjCtx& c, const u16* X, const u16* Ckv, const u16* Wq, const u16* Wuk, const u16* Wuv, u16* Kf, u16* Vf, unsigned lds0, int h, int j, int wid, int lane) {
    const int wr = wid >> 2, wc = wid & 3;
    c.wid = wid; c.lds0 = lds0; c.rot = KROT ? (2 * (h & 3) + j) & 7 : 0;
    c.srdC = mk_srd(Ckv, 2097152); c.srdX = mk_srd(X, 2097152);
    c.srdW = mk_srd((wid < 4 ? Wuk : Wuv), 4194304); c.srdWq = mk_srd(Wq, 4194304);
    c.srdK = mk_srd((const char*)Kf + (size_t)h * 524288, 524288); c.srdV = mk_srd((const char*)Vf + (size_t)h * 524288, 524288);
#pragma unroll
    for (int p = 0; p < 2; ++p) {
        c.va[p] = (lane >> 3) * 1024 + (((lane & 7) ^ (4 * p + (lane >> 4))) * 16);
        c.vw[p] = ((lane >> 2) & 7) * 8192 + (4 * (lane >> 5) + ((lane & 3) ^ ((2 * p + ((lane >> 4) & 1)) & 3))) * 16;
    }
    c.vwq = (wid & 1) ? c.vw[1] : c.vw[0];
    c.l128 = lane * 128;
    c.sA = (256 * j + 32 * wid) * 1024;
    c.sW = (16 * (wid & 3)) * 8192 + h * 256;
    c.sXa = (wid < 4 ? 128 * (15 - j) + 32 * wid : 128 * j + 32 * (wid - 4)) * 1024;
    c.sWq = (8 * wid) * 8192 + h * 256;
    c.voK = lane * 16 + (4 * j + 2 * wr) * 16384 + wc * 1024; c.voV = lane * 16 + (4 * j + 2 * wr) * 16384 + wc * 4096;
}
template <class Touch, class IssueB1>
DI void kv_run(const ProjCtx& c, int lane, u32x4 (&pk)[16], Touch touchk, IssueB1 issueB1, Stamper& stamp) {
    const int wid = c.wid, wr = wid >> 2, wc = wid & 3;
    GAddr g; gaddr_a(g, c.lds0, 128 * wr, lane); gaddr_w(g, c.lds0 + 32768 + wc * 512, lane);
    kv_part(c, g, pk, touchk, issueB1, stamp);
}
template <class Pub>
DI void q_run(const ProjCtx& c, bool early, int lane, const u32x4 (&pk)[16], bf16x8 (&q)[8], Pub after, Stamper& stamp) {
    GAddr g; gaddr_a(g, c.lds0, 32 * c.wid, lane); gaddr_w(g, c.lds0 + 32768, lane); gaddr_add(g, 0u);
    f32x4 acc[8][2];
#pragma unroll
    for (int a = 0; a < 8; ++a) { acc[a][0] = f32x4{}; acc[a][1] = f32x4{}; PIN(acc[a][0]); PIN(acc[a][1]); }
    QFr f0, f1;
#pragma unroll
    for (int i = 0; i < 10; ++i) q_read<0>(f0, g, i);
    q_tile<0>(c, early, g, acc, f0, f1, pk, after, stamp); q_tile<1>(c, early, g, acc, f0, f1, pk, after, stamp); q_tile<2>(c, early, g, acc, f0, f1, pk, after, stamp); q_tile<3>(c, early, g, acc, f0, f1, pk, after, stamp);
    q_tile<4>(c, early, g, acc, f0, f1, pk, after, stamp); q_tile<5>(c, early, g, acc, f0, f1, pk, after, stamp); q_tile<6>(c, early, g, acc, f0, f1, pk, after, stamp); q_tile<7>(c, early, g, acc, f0, f1, pk, after, stamp);
    MFMA_DRAIN();
    constexpr float QSC = 0.08838834764831845f * 1.4426950408889634f;
#pragma unroll
    for (int qb = 0; qb < 2; ++qb)
#pragma unroll
        for (int ds = 0; ds < 4; ++ds) { const f32x4 lo = acc[2 * ds][qb] * QSC, hi = acc[2 * ds + 1][qb] * QSC;
            q[qb * 4 + ds] = __builtin_bit_cast(bf16x8, (u32x4){cvtpk(lo[0], lo[1]), cvtpk(lo[2], lo[3]), cvtpk(hi[0], hi[1]), cvtpk(hi[2], hi[3])}); }
}
template <int OFF, bool SETM0> DI void dma_piece(unsigned m0v, unsigned voff, srd_t srd, unsigned soff) {
    if constexpr (SETM0) asm volatile("s_mov_b32 m0, %0\n\ts_nop 0\n\tbuffer_load_dwordx4 %1, %2, %3 offen offset:%4 sc1 lds" :: "s"(m0v), "v"(voff), "s"(srd), "s"(soff), "i"(OFF) : "memory");
    else asm volatile("buffer_load_dwordx4 %0, %1, %2 offen offset:%3 sc1 lds" :: "v"(voff), "s"(srd), "s"(soff), "i"(OFF) : "memory");
}
#define ATT_THRV 64.f
constexpr float ATT_THR = ATT_THRV;
#define KSPLIT 1
#define ATT_WIN 5
constexpr int ATT_W = ATT_WIN;

struct AttnState { f32x4 o[8][2]; f32x4 s[4][2]; f32x4 negm[2]; bf16x8 q[8]; u32x4 p[2][2]; f32x4 l[2]; f32x2 mhat; u32x4 ones; };
struct AttnCtx {
    srd_t srdK, srdV, srdC; unsigned voff, lane16, kslot0, vslot0, coffL;
    int g, nl  , thrg, dq0, relH, jump; unsigned qstash;
    unsigned soA, soB, ksoM; int rel;
    srd_t srdF; u64 tok, prev; unsigned known;
};
DI u64 ld_flag8(const AttnCtx& c) { return __builtin_bit_cast(u64, __builtin_amdgcn_raw_buffer_load_b64(c.srdF, (c.lane16 & 0x70u) << 3, 0, 16)); }

DI unsigned m_off(int i) { return i < 16 ? (unsigned)(((i & 7) * 2 + (i >> 3)) * 1024) : (unsigned)((((i - 16) & 3) * 4 + ((i - 16) >> 2)) * 1024); }
template <bool DO_PV, bool DO_QK, int KP, int VP, class Hook>
DI void m_phase(AttnState& st, unsigned kbase, unsigned vbase, bf16x8 (&pre)[ATT_W], Hook hook) {
    constexpr int N0 = DO_PV ? 0 : 16, N1 = DO_QK ? 32 : 16;
    bf16x8 fr[32];
#pragma unroll
    for (int i = 0; i < ATT_W; ++i) fr[N0 + i] = pre[i];
#pragma unroll
    for (int i = N0; i < N1; ++i) {
        const int n = i + ATT_W;
        if (n < N1) fr[n] = ldsf((n < 16 ? vbase + VP * 16384 : kbase + KP * 16384) + m_off(n));
        SBAR();
        if (i >= 16) { const int ds = (i - 16) >> 2, kb = (i - 16) & 3;
            if (ds == 0) { asm volatile("v_mfma_f32_16x16x32_bf16 %0, %2, %3, %1" : "=&v"(st.s[kb][0]), "+v"(st.negm[0]) : "v"(fr[i]), "v"(st.q[0]));
                           asm volatile("v_mfma_f32_16x16x32_bf16 %0, %2, %3, %1" : "=&v"(st.s[kb][1]), "+v"(st.negm[1]) : "v"(fr[i]), "v"(st.q[4])); }
            else { MFMA16I(st.s[kb][0], fr[i], st.q[ds]); MFMA16I(st.s[kb][1], fr[i], st.q[4 + ds]); }
        } else { const int ks = i >> 3, db = i & 7;
            MFMA16I(st.o[db][0], fr[i], st.p[ks][0]); MFMA16I(st.o[db][1], fr[i], st.p[ks][1]);
            if (db == 3) { MFMA16I(st.l[0], st.ones, st.p[ks][0]); MFMA16I(st.l[1], st.ones, st.p[ks][1]); } }
        SBAR();
        hook(i);
        SBAR();
    }
    MFMA_DRAIN();
    PIN(st.negm[0]); PIN(st.negm[1]);
}
struct NoHookI { DI void operator()(int) const {} };

DI void store_ctx(const f32x4 (&o)[8][2], float inv0, float inv1, srd_t srd, unsigned voff) {
    PIN(voff);
#pragma unroll
    for (int p = 0; p < 4; ++p)
#pragma unroll
        for (int qb = 0; qb < 2; ++qb) { const float inv = qb ? inv1 : inv0; const f32x4 lo = o[2 * p][qb] * inv, hi = o[2 * p + 1][qb] * inv;
            __builtin_amdgcn_raw_buffer_store_b128((u32x4){cvtpk(lo[0], lo[1]), cvtpk(lo[2], lo[3]), cvtpk(hi[0], hi[1]), cvtpk(hi[2], hi[3])}, srd, voff + p * 2048 + qb * 256, 0, 16); }
}
DI float row_add(float x) { return swap_add(x16_add(x)); }
DI float row_max4(float x) { return swap_max(x16_max(x)); }

template <bool LAST, int PAR>
DI void v_phase(AttnState& st, AttnCtx& c, const int t) {
    const bool sw = (t == c.nl);
    const unsigned vm0 = c.vslot0 + (PAR ^ 1) * 16384;
    const unsigned vso = c.soA, kso = c.soB;
    if (!LAST && __builtin_expect(c.known != 0xffu, 0)) {
        c.known |= (unsigned)__builtin_amdgcn_ballot_w64(c.prev == c.tok) & 0xffu;
        const unsigned need = 1u << ((kso >> 16) & 7);
        if (kso < 524288u && !(c.known & need)) {
            for (unsigned spins = 0; spins < (1u << 15); ++spins) { const u64 v = ld_flag8(c); c.known |= (unsigned)__builtin_amdgcn_ballot_w64(v == c.tok) & 0xffu; if (c.known & need) break; __builtin_amdgcn_s_sleep(1); } }
        asm volatile("" ::: "memory");
        c.prev = ld_flag8(c);
    }
    SBAR();
    if (__builtin_expect(sw, 0)) { store_ctx(st.o, __builtin_amdgcn_rcpf(st.l[0][0]), __builtin_amdgcn_rcpf(st.l[1][0]), c.srdC, ((c.lane16 >> 8) << 9) + (c.lane16 & 0xf0u) + c.coffL);
#pragma unroll
        for (int d = 0; d < 8; ++d) { st.o[d][0] = f32x4{}; st.o[d][1] = f32x4{}; PIN(st.o[d][0]); PIN(st.o[d][1]); }
        st.l[0] = f32x4{}; st.l[1] = f32x4{}; PIN(st.l[0]); PIN(st.l[1]); c.rel = c.relH; }
    if (__builtin_expect(c.rel > c.thrg, 0)) {
        const int u = 32 * c.g - c.rel;
        int dq = c.dq0; PIN(dq); const float NEG = -__builtin_inff(); const f32x4 NEG4 = {NEG, NEG, NEG, NEG};
#define HIDE(kb, qb) do { st.s[kb][qb] = NEG4; PIN(st.s[kb][qb]); } while (0)
#define DIAG(kb, qb) do { _Pragma("unroll") for (int e = 0; e < 4; ++e) if (e > dq) st.s[kb][qb][e] = NEG; PIN(st.s[kb][qb]); } while (0)
        if (u == 32) { DIAG(2, 0); DIAG(3, 1); HIDE(3, 0); }
        else {
            if (u == 0) { DIAG(0, 0); DIAG(1, 1); } else { HIDE(0, 0); HIDE(0, 1); HIDE(1, 1); }
            HIDE(1, 0); HIDE(2, 0); HIDE(2, 1); HIDE(3, 0); HIDE(3, 1);
        }
#undef HIDE
#undef DIAG
    }
    SBAR();
    if (!LAST) dma_piece<0, true>(vm0, c.voff, c.srdV, vso);
#define MX3(a, b, c) __builtin_fmaxf(__builtin_fmaxf((a), (b)), (c))
    float mx[2];
#pragma unroll
    for (int qb = 0; qb < 2; ++qb) {
        float a = MX3(st.s[0][qb][0], st.s[0][qb][1], st.s[0][qb][2]), b = MX3(st.s[0][qb][3], st.s[1][qb][0], st.s[1][qb][1]);
        a = MX3(a, st.s[1][qb][2], st.s[1][qb][3]); b = MX3(b, st.s[2][qb][0], st.s[2][qb][1]); a = MX3(a, st.s[2][qb][2], st.s[2][qb][3]);
        b = MX3(b, st.s[3][qb][0], st.s[3][qb][1]); a = MX3(a, st.s[3][qb][2], st.s[3][qb][3]); mx[qb] = __builtin_fmaxf(a, b); }
    const unsigned long long firstm = (t == 0 || sw) ? ~0ull : 0ull;
    if (__builtin_expect((__builtin_amdgcn_ballot_w64(__builtin_fmaxf(mx[0], mx[1]) > ATT_THR) | firstm) != 0ull, 0)) {
        const bool first = firstm != 0ull;
#pragma unroll
        for (int qb = 0; qb < 2; ++qb) {
            const float delta = __builtin_fmaxf(row_max4(mx[qb]), first ? -1e30f : 0.f);
            st.mhat[qb] = (first ? 0.f : st.mhat[qb]) + delta;
#pragma unroll
            for (int kb = 0; kb < 4; ++kb) st.s[kb][qb] -= delta;
            st.negm[qb] = (f32x4){-st.mhat[qb], -st.mhat[qb], -st.mhat[qb], -st.mhat[qb]};
            if (!first) { const float f = __builtin_amdgcn_exp2f(-delta); st.l[qb] *= f;
#pragma unroll
                for (int d = 0; d < 8; ++d) st.o[d][qb] *= f; } }
    }
    SBAR();
    float e[8][4];
#define EXPG(k) do { _Pragma("unroll") for (int i_ = 0; i_ < 4; ++i_) e[k][i_] = __builtin_amdgcn_exp2f(st.s[(k) >> 1][(k) & 1][i_]); } while (0)
#define FING(k) do { constexpr int kb_ = (k) >> 1, qb_ = (k) & 1; \
        unsigned w0_ = cvtpk(e[k][0], e[k][1]), w1_ = cvtpk(e[k][2], e[k][3]); PIN(w0_); PIN(w1_); \
        st.p[kb_ >> 1][qb_][2 * (kb_ & 1)] = w0_; st.p[kb_ >> 1][qb_][2 * (kb_ & 1) + 1] = w1_; } while (0)
    EXPG(0); SBAR();
    EXPG(1); FING(0); SBAR();
    if (!LAST) dma_piece<1024, false>(vm0, c.voff, c.srdV, vso);
    EXPG(2); FING(1); SBAR();
    EXPG(3); FING(2); SBAR();
    if (!LAST) dma_piece<2048, false>(vm0, c.voff, c.srdV, vso);
    EXPG(4); FING(3); SBAR();
    EXPG(5); FING(4); SBAR();
    if (!LAST) dma_piece<3072, false>(vm0, c.voff, c.srdV, vso);
    EXPG(6); FING(5); SBAR();
    if (!LAST) dma_piece<0, true>(c.kslot0 + PAR * 16384, c.voff, c.srdK, kso);
    EXPG(7); FING(6); SBAR();
    if (!LAST) dma_piece<1024, false>(c.kslot0 + PAR * 16384, c.voff, c.srdK, kso);
    FING(7);
#undef EXPG
#undef FING
    if (!LAST) {
        if (__builtin_expect(t == c.nl - 1, 0)) {
            unsigned qs = c.qstash; PIN(qs);
#pragma unroll
            for (int s = 0; s < 8; ++s) st.q[s] = ldsf(qs + s * 1024);
            st.negm[0] = f32x4{}; st.negm[1] = f32x4{}; PIN(st.negm[0]); PIN(st.negm[1]);
        }
        c.ksoM = kso;
        c.soA = c.soB; c.soB += (t + 3 == c.nl) ? c.jump : 16384; c.rel += 64;
    }
    SBAR();
}

DI void attn_setup(AttnCtx& c, const u16* Kf, const u16* Vf, u16* Cf, unsigned lds0, int h, int j, int wid, int lane) {
    PIN(lane);
    const int grp = wid >> 2; c.g = wid & 3; c.dq0 = (lane & 15) - 4 * (lane >> 4);
    const int qbH = 15 - j, qbL = j, nl = 2 * j + 2;
    c.nl = grp ? nl : 1000; c.thrg = 32 * c.g - 63; c.jump = (18 - nl) * 16384; c.relH = 64 * (17 - 2 * qbH);
    c.rel = grp ? -128 * qbL : -128 * qbH;
    c.soA = 16384; c.soB = (c.nl == 2) ? 17 * 16384 : 2 * 16384;
    c.srdK = mk_srd((const char*)Kf + (size_t)h * 524288, 524288);
    c.srdV = mk_srd((const char*)Vf + (size_t)h * 524288, 524288);
    c.srdC = mk_srd((const char*)Cf, 16777216);
    c.lane16 = lane * 16; c.voff = lane * 16 + c.g * 4096;
    c.kslot0 = lds0 + grp * 65536 + c.g * 4096; c.vslot0 = c.kslot0 + 32768;
    c.coffL = ((qbL * 4 + c.g) * 32 + h) * 8192;
    c.qstash = lds0 + 131072 + c.g * 8192 + lane * 16;
}
#define DMA4(m_, vo_, srd, so_) do { dma_piece<0, true>(m_, vo_, srd, so_); dma_piece<1024, false>(m_, vo_, srd, so_); dma_piece<2048, false>(m_, vo_, srd, so_); dma_piece<3072, false>(m_, vo_, srd, so_); } while (0)
DI void attn_prime(const AttnCtx& c, int grp) { DMA4(grp ? c.vslot0 + 16384 : c.kslot0, c.voff, c.srdK, 0u); DMA4(c.vslot0, c.voff, c.srdV, 0u); if (grp == 0) DMA4(c.kslot0 + 16384, c.voff, c.srdK, 16384u); }

template <class First, class G0Pre, class G0Tail>
DI void attn_run(AttnState& st, AttnCtx& c, unsigned lds0, int h, int j, int wid, int lane, First afterFirst, G0Pre g0pre, G0Tail g0tail, Stamper& stamp) {
    PIN(lane);
    const int grp = wid >> 2;
    const unsigned kbase = lds0 + grp * 65536 + lane * 16;
    const unsigned vbase = lds0 + grp * 65536 + 32768 + lane * 16;
#pragma unroll
    for (int d = 0; d < 8; ++d) { st.o[d][0] = f32x4{}; st.o[d][1] = f32x4{}; PIN(st.o[d][0]); PIN(st.o[d][1]); }
    st.negm[0] = f32x4{}; st.negm[1] = f32x4{}; st.l[0] = f32x4{}; st.l[1] = f32x4{}; st.mhat = (f32x2){0.f, 0.f};
    st.ones = (u32x4){0x3f803f80u, 0x3f803f80u, 0x3f803f80u, 0x3f803f80u}; PIN(st.ones); PIN(st.l[0]); PIN(st.l[1]);
    PIN(st.negm[0]); PIN(st.negm[1]);
    if (grp == 1) { DMA4(c.kslot0 + 16384, c.voff, c.srdK, 16384u); BARRIER(); }
    wait_bar<8>();
    stamp();
    bf16x8 pre[ATT_W];
#define PRE_V(P) do { _Pragma("unroll") for (int i = 0; i < ATT_W; ++i) pre[i] = ldsf(vbase + (P) * 16384 + m_off(i)); } while (0)
    const unsigned kbase0 = grp ? kbase + 49152 : kbase;
#pragma unroll
    for (int i = 0; i < ATT_W; ++i) pre[i] = ldsf(kbase0 + m_off(16 + i));
    __builtin_amdgcn_s_setprio(1);
    m_phase<false, true, 0, 0>(st, kbase0, vbase, pre, NoHookI{});
    __builtin_amdgcn_s_setprio(0);
    wait_bar<0>();
    afterFirst();
#define PSTAMP(t) do {} while (0)
#define STEP(PAR, t) do { \
        v_phase<false, PAR>(st, c, t); \
        PRE_V(PAR); \
        PSTAMP(t); \
        wait_bar_keep<(KSPLIT ? 6 : 4)>(); \
        PSTAMP(t); \
        __builtin_amdgcn_s_setprio(1); \
        m_phase<true, true, PAR ^ 1, PAR>(st, kbase, vbase, pre, [&](int i) { \
            if (!KSPLIT) { if (i == 3) dma_piece<0, true>(c.kslot0 + PAR * 16384, c.voff, c.srdK, c.ksoM); if (i == 9) dma_piece<1024, false>(c.kslot0 + PAR * 16384, c.voff, c.srdK, c.ksoM); } \
            if (i == (KSPLIT ? 5 : 15)) dma_piece<2048, KSPLIT>(c.kslot0 + PAR * 16384, c.voff, c.srdK, c.ksoM); if (i == (KSPLIT ? 17 : 21)) dma_piece<3072, false>(c.kslot0 + PAR * 16384, c.voff, c.srdK, c.ksoM); }); \
        __builtin_amdgcn_s_setprio(0); \
        PSTAMP(t); \
        wait_bar<4>(); \
        PSTAMP(t); } while (0)
    for (int t = 0; t < 16; t += 2) { stamp(); STEP(0, t); STEP(1, t + 1); }
    stamp();
    v_phase<true, 0>(st, c, 16);
    if (grp == 0) g0pre();
    PRE_V(0);
    asm volatile("s_barrier" ::: "memory");
    __builtin_amdgcn_s_setprio(1);
    m_phase<true, false, 0, 0>(st, kbase, vbase, pre, NoHookI{});
    __builtin_amdgcn_s_setprio(0);
    wait_bar<0>();
    stamp();
    PIN(lane);
    const unsigned xo = lds0 + grp * 65536 + c.g * 8192 + lane * 16;
    const unsigned xi = lds0 + (grp ^ 1) * 65536 + c.g * 8192 + lane * 16;
    const unsigned so = lds0 + grp * 65536 + 32768 + c.g * 512 + lane * 8, si = lds0 + (grp ^ 1) * 65536 + 32768 + c.g * 512 + lane * 8;
    const unsigned voff = (lane >> 4) * 512 + (lane & 15) * 16 + (((15 - j) * 4 + c.g) * 32 + h) * 8192;
#define MERGE_PUT(qb) do { _Pragma("unroll") for (int d = 0; d < 8; ++d) *(LAS f32x4*)(xo + d * 1024) = st.o[d][qb]; *(LAS f32x2*)so = (f32x2){st.mhat[qb], st.l[qb][0]}; } while (0)
#define MERGE_FIN(qb) do { const f32x2 ot = *(const LAS f32x2*)si; const float mh = st.mhat[qb], lt = st.l[qb][0]; \
        const float m = __builtin_fmaxf(ot[0], mh), f0 = __builtin_amdgcn_exp2f(ot[0] - m), f1 = __builtin_amdgcn_exp2f(mh - m); \
        const float inv = __builtin_amdgcn_rcpf(ot[1] * f0 + lt * f1), a0 = f0 * inv, a1 = f1 * inv; \
        _Pragma("unroll") for (int p = 0; p < 4; ++p) { \
            const f32x4 lo = st.o[2 * p][qb] * a1 + *(const LAS f32x4*)(xi + (2 * p) * 1024) * a0, hi = st.o[2 * p + 1][qb] * a1 + *(const LAS f32x4*)(xi + (2 * p + 1) * 1024) * a0; \
            __builtin_amdgcn_raw_buffer_store_b128((u32x4){cvtpk(lo[0], lo[1]), cvtpk(lo[2], lo[3]), cvtpk(hi[0], hi[1]), cvtpk(hi[2], hi[3])}, c.srdC, voff + p * 2048 + (qb) * 256, 0, 16); } } while (0)
    if (grp == 0) { MERGE_PUT(1); BARRIER(); g0tail(); BARRIER(); MERGE_FIN(0); asm volatile("; merge 0"); }
    else          { asm volatile("; merge 1"); MERGE_PUT(0); BARRIER(); MERGE_FIN(1); }
#undef MERGE_PUT
#undef MERGE_FIN
    stamp();
#undef PRE_V
#undef STEP
}
#define OUT_AUX 0
constexpr int OUT_LDS = 163840;
constexpr unsigned OUT_O = 131072;
struct OutCtx { srd_t srdC, srdW, srdB; unsigned lane16, vw0, vwA, sC, sW, sB, lds0; int wid, rot; };
DI void out_issueA(const OutCtx& c, int kt, int i) {
    const unsigned st = c.lds0 + (kt & 3) * 32768;
    const int kr = (kt + c.rot) & 15;
    if (i < 2) { const int p = 2 * c.wid + i;
        dma1s(st + p * 1024, c.lane16, c.srdC, c.sC + (p >> 2) * 262144 + (p & 3) * 1024 + kr * 4096); }
    else dma1(st + 16384 + (2 * c.wid + (i - 2)) * 1024, c.vwA, c.srdW, c.sW + (8 * c.wid) * 1024 + (i - 2) * 128 + kr * 65536);
}
DI void out_issueB(const OutCtx& c, int ch, int i) {
    const int rg = 2 * c.wid + (i >> 1);
    dma1(c.lds0 + ch * 32768 + rg * 2048 + (i & 1) * 1024, (i >> 1) ? (c.vw0 ^ 32u) : c.vw0, c.srdB, c.sB + (ch * 128 + 8 * rg) * 1024 + (i & 1) * 128);
}
DI u32x4 out_pack(f32x4 s0, f32x4 s1) {
    unsigned a0 = cvtpk(s0[0], s0[1]), a1 = cvtpk(s0[2], s0[3]), b0 = cvtpk(s1[0], s1[1]), b1 = cvtpk(s1[2], s1[3]);
    auto x0 = __builtin_amdgcn_permlane32_swap(a0, b0, false, false); auto x1 = __builtin_amdgcn_permlane32_swap(a1, b1, false, false);
    return (u32x4){x0[0], x1[0], x0[1], x1[1]};
}
DI f32x4 quad(const f32x16& x, int rq) { return (f32x4){x[4 * rq], x[4 * rq + 1], x[4 * rq + 2], x[4 * rq + 3]}; }

DI void out_phase(const u16* Wo, const u16* Bwo, u16* out, char* ws, u64 tok, unsigned lds0, int bid, int wid, int lane, Stamper& stamp) {
    PIN(lane);
    const int tid = wid * 64 + lane, r32 = lane & 31, hi = lane >> 5, wr = wid >> 2, wc = wid & 3;
    const int x = bid & 7, u = bid >> 3, s = x >> 1, rb = 8 * (x & 1) + (u >> 2), cb = u & 3, tile = rb * 4 + cb;
    OutCtx c; c.wid = wid; c.lds0 = lds0; c.lane16 = lane * 16;
    c.srdC = mk_srd(ws + WS_CF + (size_t)rb * 4 * 262144, 4 * 262144); c.srdW = mk_srd(Wo, 4194304); c.srdB = mk_srd(Bwo, 524288);
    c.vw0 = ((lane >> 2) & 7) * 1024 + (4 * (lane >> 5) + ((lane & 3) ^ ((lane >> 4) & 1))) * 16; c.vwA = (wid & 1) ? (c.vw0 ^ 32u) : c.vw0;
    c.rot = 4 * cb + 2 * (rb & 7);
    c.sC = s * 8 * 8192; c.sW = s * 1024 * 1024 + cb * 256; c.sB = cb * 256;
#pragma unroll
    for (int kt = 0; kt < 4; ++kt) { out_issueA(c, kt, 2); out_issueA(c, kt, 3); }
    u64* flags = (u64*)(ws + WS_FLAG);
    {
        const int l = lane & 15, hh = 8 * s + (l & 7), jj = rb < 8 ? rb : 15 - rb;
        const int src = l < 8 ? ((hh >> 2) | (jj << 3) | ((hh & 3) << 6)) : (8 * (2 * rb + (s >> 1)) + (l & 7));
        const u64* fp = flags + (FL_C + src) * FL_STRIDE;
        for (unsigned spins = 0; spins < (1u << 15); ++spins) { const u64 v = ld_agent(fp); if (__all(v == tok)) break; __builtin_amdgcn_s_sleep(1); }
        asm volatile("" ::: "memory"); }
    stamp();
#pragma unroll
    for (int kt = 0; kt < 4; ++kt) { out_issueA(c, kt, 0); out_issueA(c, kt, 1); }
    { const srd_t srdO = mk_srd(ws + WS_OF + (size_t)(rb * 4 + s) * 32768, 32768);
#pragma unroll
      for (int i = 0; i < 4; ++i) dma1s(lds0 + OUT_O + (4 * wid + i) * 1024, c.lane16, srdO, (unsigned)((4 * wid + i) * 1024)); }
    const int q4 = (lane & 15) >> 2, p4 = lane & 3, blk = (lane >> 4) & 1;
    const unsigned aaddr = lds0 + (2 * wr) * 4096 + c.lane16;
    unsigned waA[2], waddr[2];
#pragma unroll
    for (int t = 0; t < 2; ++t) { waA[t] = lds0 + 2048 * t + 512 * wc + 64 * (4 * hi + q4) + 16 * ((2 * blk + (p4 >> 1)) ^ ((2 * t + hi) & 3)) + 8 * (p4 & 1);
                                  waddr[t] = waA[t]; }
    f32x16 acc[2] = {f32x16{}, f32x16{}};
    stamp();
    struct Fr { bf16x8 a[2][2]; bf16x4 w[2][2]; };
    auto rd = [&](Fr& f, unsigned st, int half) {
        unsigned w0 = waA[0] + st + 16384 + half * 8192, w1 = waA[1] + st + 16384 + half * 8192, aa = aaddr + st; PIN(w0); PIN(w1); PIN(aa);
#pragma unroll
        for (int k = 0; k < 2; ++k) { const int ks = 2 * half + k; const unsigned wb = k ? w1 : w0;
            f.w[k][0] = ldstr(wb); f.w[k][1] = ldstr(wb + 4096);
            f.a[k][0] = ldsf(aa + ks * 1024); f.a[k][1] = ldsf(aa + 4096 + ks * 1024); } };
    Fr f0, f1;
    wait_bar<10>();
    rd(f0, 0u, 0);
    auto step = [&](int kt, auto late, auto early) {
        const unsigned st = (kt & 3) * 32768, stn = ((kt + 1) & 3) * 32768;
        rd(f1, st, 1);
        SBAR();
#pragma unroll
        for (int k = 0; k < 2; ++k) { const bf16x8 wf = cat4(f0.w[k][0], f0.w[k][1]);
            acc[0] = MFMA32(wf, f0.a[k][0], acc[0]); acc[1] = MFMA32(wf, f0.a[k][1], acc[1]); SBAR(); late(2 + k); SBAR(); }
        wait_bar<8>();
        rd(f0, stn, 0);
        SBAR();
#pragma unroll
        for (int k = 0; k < 2; ++k) { const bf16x8 wf = cat4(f1.w[k][0], f1.w[k][1]);
            acc[0] = MFMA32(wf, f1.a[k][0], acc[0]); acc[1] = MFMA32(wf, f1.a[k][1], acc[1]); SBAR(); early(k); SBAR(); }
    };
    step(0, [&](int) {}, [&](int i) { out_issueA(c, 4, i); });
    for (int kt = 1; kt < 12; ++kt) step(kt, [&](int i) { out_issueA(c, kt + 3, i); }, [&](int i) { out_issueA(c, kt + 4, i); });
    step(12, [&](int i) { out_issueA(c, 15, i); }, [&](int i) { out_issueB(c, 0, i); });
    step(13, [&](int i) { out_issueB(c, 0, i); }, [&](int i) { out_issueB(c, 1, i); });
    step(14, [&](int i) { out_issueB(c, 1, i); }, [&](int i) { out_issueB(c, 2, i); });
    step(15, [&](int i) { out_issueB(c, 2, i); }, [&](int i) { out_issueB(c, 3, i); });
    out_issueB(c, 3, 2); out_issueB(c, 3, 3);
    stamp();
    const srd_t srdX = mk_srd(ws + WS_XCH + (size_t)tile * 262144, 262144);
#pragma unroll
    for (int mb = 0; mb < 2; ++mb) { const int q = 2 * wr + mb;
        if (q != s) {
#pragma unroll
            for (int rq = 0; rq < 4; ++rq) store16_sc1(__builtin_bit_cast(u32x4, quad(acc[mb], rq)), c.lane16, srdX, (unsigned)(((q * 4 + s) * 4 + wc) * 4096 + rq * 1024)); } }
    wait_bar<0>();
    if (tid == 0) st_agent(flags + (FL_XF + tile * 4 + s) * FL_STRIDE, tok);
    stamp();
    const int row = rb * 128 + 32 * s + r32;
    const srd_t srdOut = mk_srd(out, 4194304);
    if (wr == (s >> 1)) {
        const u64* fp = flags + (FL_XF + tile * 4 + (lane & 3)) * FL_STRIDE;
        for (unsigned spins = 0; spins < (1u << 15); ++spins) { const u64 v = ld_agent(fp); if (__all(v == tok)) break; __builtin_amdgcn_s_sleep(1); }
        asm volatile("" ::: "memory");
        f32x4 p[4][4];
#pragma unroll
        for (int q = 0; q < 4; ++q)
#pragma unroll
            for (int rq = 0; rq < 4; ++rq) p[q][rq] = __builtin_bit_cast(f32x4, __builtin_amdgcn_raw_buffer_load_b128(srdX, (q == s ? 0x7ffffff0u : c.lane16) + rq * 1024, ((s * 4 + q) * 4 + wc) * 4096, 16));
        f32x4 r[4];
#pragma unroll
        for (int rq = 0; rq < 4; ++rq) r[rq] = ((p[0][rq] + p[1][rq]) + (p[2][rq] + p[3][rq])) + ((s & 1) ? quad(acc[1], rq) : quad(acc[0], rq));
        const unsigned vo = (row * 1024 + cb * 128 + 32 * wc + 8 * hi) * 2;
        __builtin_amdgcn_raw_buffer_store_b128(out_pack(r[0], r[1]), srdOut, vo, 0, OUT_AUX); __builtin_amdgcn_raw_buffer_store_b128(out_pack(r[2], r[3]), srdOut, vo + 32, 0, OUT_AUX);
    } else {
        f32x16 accB[2] = {f32x16{}, f32x16{}};
#pragma unroll
        for (int ch = 0; ch < 4; ++ch)
#pragma unroll
            for (int hf = 0; hf < 2; ++hf) {
                unsigned w0 = waddr[0] + ch * 32768 + hf * 16384, w1 = waddr[1] + ch * 32768 + hf * 16384, oa = lds0 + OUT_O + (8 * ch + 4 * hf) * 1024 + c.lane16; PIN(w0); PIN(w1); PIN(oa);
                bf16x8 a[4]; bf16x4 w[4][2];
#pragma unroll
                for (int ks = 0; ks < 4; ++ks) { w[ks][0] = ldstr(w0 + ks * 4096); w[ks][1] = ldstr(w1 + ks * 4096); a[ks] = ldsf(oa + ks * 1024); }
#pragma unroll
                for (int ks = 0; ks < 4; ++ks) accB[ks & 1] = MFMA32(cat4(w[ks][0], w[ks][1]), a[ks], accB[ks & 1]);
            }
        const f32x16 rB = accB[0] + accB[1];
        const unsigned vo = (row * 1024 + 512 + cb * 128 + 32 * wc + 8 * hi) * 2;
        __builtin_amdgcn_raw_buffer_store_b128(out_pack(quad(rB, 0), quad(rB, 1)), srdOut, vo, 0, OUT_AUX); __builtin_amdgcn_raw_buffer_store_b128(out_pack(quad(rB, 2), quad(rB, 3)), srdOut, vo + 32, 0, OUT_AUX);
    }
}
#define BXTOUCH 0
#define XTOUCH 0
#define TLBWARM 0
#define CODEWARM 1
constexpr unsigned CODE_BYTES = 44 * 1024;
constexpr int FUSED_LDS = 163840;
constexpr unsigned WPHI_LDS = 156160;
struct FusedArgs { const u16 *X, *Ckv, *Wq, *Wuk, *Wuv, *Wo, *Bx, *Wqkv, *Wphi, *Bwo; char* ws; u16* out; };

__global__ __launch_bounds__(512, 2) void fused_kernel(FusedArgs a) {
    const int tid = threadIdx.x, lane = tid & 63;
    const int wid = rfl(tid >> 6);
    const int bid = blockIdx.x, h = (bid & 7) * 4 + (bid >> 6), j = (bid >> 3) & 7;
    if (bid < CODEWARM * 8) {
        u64 pc_; asm volatile("s_getpc_b64 %0" : "=s"(pc_));
        const unsigned o_ = wid * 8192 + lane * 128;
        asm volatile("s_mov_b32 m0, %0\n\ts_nop 0\n\tglobal_load_lds_dword %1, %2" :: "s"(163840u - 256u), "v"(o_ < CODE_BYTES ? o_ : 0u), "s"(pc_ & ~(u64)127) : "memory"); }
    const unsigned lds0 = 0u;
    Stamper stamp{0u, 0, lane};
    stamp();
    u16* Kf = (u16*)(a.ws + WS_KF); u16* Vf = (u16*)(a.ws + WS_VF); u16* Cf = (u16*)(a.ws + WS_CF);
    u16* PhiQ = (u16*)(a.ws + WS_PHIQ); u16* Of = (u16*)(a.ws + WS_OF); float* Bst = (float*)(a.ws + WS_BST);
    u64* flags = (u64*)(a.ws + WS_FLAG);
    float* Bred = (float*)(a.ws + WS_BRED);
    const int hg = bid & 7, rc = bid >> 3;
    asm volatile("" :: "s"(a.X), "s"(a.Ckv), "s"(a.Wq), "s"(a.Wuk), "s"(a.Wuv), "s"(a.Wphi), "s"(a.ws));
    ProjCtx pc; proj_setup(pc, a.X, a.Ckv, a.Wq, a.Wuk, a.Wuv, Kf, Vf, lds0, h, j, wid, lane);
#pragma unroll
    for (int i = 0; i < 8; ++i) issueKV(pc, 0, i);
#pragma unroll
    for (int i = 0; i < 8; ++i) issueKV(pc, 1, i);
    const unsigned tsc = lds0 + TOUCH_LDS;
    { const unsigned vW = (lane >> 4) * 524288 + (lane & 15) * 128;
      const char* base; unsigned vo = pc.l128;
      if (wid < 4) { base = (const char*)((wid & 1) ? a.Wuv : a.Wuk) + ((wid & 2) ? 4 : 2) * 524288 + bid * 2048 - ((wid & 2) ? 0 : 0); vo = vW; }
      else if (wid == 4) base = (const char*)a.Ckv + bid * 8192;
      else { base = (const char*)a.Ckv; vo = 0; }
      touch_g<1>(tsc, vo, base); }
    if (wid < 2) dma1(lds0 + WPHI_LDS + wid * 1024, lane * 16, mk_srd(a.Wphi, 16384), (unsigned)(hg * 2048 + wid * 1024));
    const u64 epoch = *(const u64 __attribute__((address_space(4)))*)(flags + FL_EPOCH * FL_STRIDE);
    u64 tok = mix64(epoch); if (tok == epoch) tok ^= 1ull;
    tok = ((u64)rfl((unsigned)(tok >> 32)) << 32) | rfl((unsigned)tok); asm volatile("" : "+s"(tok));
    B1Ctx bc; b1_setup(bc, a.Bx, a.Wqkv, lds0, rc, hg, wid, lane);
    stamp();
    auto touchk = [&](int kt) {
        const char* base = (const char*)a.Ckv; unsigned vo = 0;
        if (kt == 0) {
            if (wid == 0) { base = (const char*)a.Bx + bid * 8192; vo = pc.l128; }
            else if (wid == 1) { base = (const char*)a.X + bid * 8192; vo = pc.l128; }
            else if (wid == 2) { base = (const char*)a.Wqkv + (bid < 192 ? bid : 0) * 8192; vo = pc.l128; }
        } else if (kt == 1) {
            if (wid < 2) { base = (const char*)a.Wq + (bid * 2 + wid) * 8192; vo = pc.l128; }
        } else if (kt == 2) {
            if (wid < 2) { base = (const char*)a.Wo + (bid * 2 + wid) * 8192; vo = pc.l128; }
            else if (wid == 2) { base = (const char*)a.Bwo + (bid & 63) * 8192; vo = pc.l128; }
        }
        touch_g<2>(tsc, vo, base); };
    u32x4 pk[16];
    kv_run(pc, lane, pk, touchk, [&](int kt) { b1_issue(bc, kt); }, stamp);
    stamp();
    B1Acc ba;
    const bool early = (j & 3) == 0;
    b1_gemm(bc, ba, lane, early, [&](int n) { if (early) kv_store(pc, pk, n); else if (n >= 1 && n <= 6) kv_store(pc, pk, n - 1); },
            [&](int n) {
#pragma unroll
                for (int i = 0; i < 6; ++i) issueQ(pc, n, i); }, stamp);
    stamp();
    u32x2 wp; { const int l16 = lane & 15, g = lane >> 4; const LAS u16* w = (const LAS u16*)(lds0 + WPHI_LDS + (wid & 3) * 512);
        wp[0] = (unsigned)w[(4 * g) * 16 + l16] | ((unsigned)w[(4 * g + 1) * 16 + l16] << 16); wp[1] = (unsigned)w[(4 * g + 2) * 16 + l16] | ((unsigned)w[(4 * g + 3) * 16 + l16] << 16); }
    b1_phi(ba, [&](int n) { SBAR(); if (n < 6) issueQ(pc, 1, n); else if (n < 12) issueQ(pc, 2, n - 6); else if (early) { if (n < 16) kv_store(pc, pk, 8 + (n < 16 ? n - 12 : 0)); } else kv_store(pc, pk, 6 + n - 12); SBAR(); }, wp, PhiQ, Bst, lds0, rc, hg, wid, lane, stamp);
    stamp();
    wait_bar<21>();
    stamp();
    AttnState st;
    AttnCtx c; attn_setup(c, Kf, Vf, Cf, lds0, h, j, wid, lane);
    B2a b2;
    auto spin = [&](const u64* fp) {
        for (unsigned spins = 0; spins < (1u << 15); ++spins) { const u64 v = ld_agent(fp); if (__all(v == tok)) break; __builtin_amdgcn_s_sleep(1); }
        asm volatile("" ::: "memory"); };
    const u64* fkv = flags + (FL_KV + 8 * h + (lane & 7)) * FL_STRIDE;
    const u64* fbs = flags + (FL_BS + 32 * hg + (lane & 31)) * FL_STRIDE;
    u64 pre = 0;
    q_run(pc, early, lane, pk, st.q, [&](int kt) {
        if (kt == 2) { if (tid == 0) st_agent(flags + (FL_BS + 32 * hg + rc) * FL_STRIDE, tok); }
        if (kt == 3) { if (early && tid == 0) st_agent(flags + (FL_KV + 8 * h + j) * FL_STRIDE, tok); }
        if (kt == 3) { if (wid == 7) pre = ld_agent(fbs); }
        if (kt == 4) {
            if (wid == 7) {
                if (!__all(pre == tok)) spin(fbs);
                asm volatile("" ::: "memory");
                b2a_load(b2, Bst, rc, hg, lane); } }
        if (kt == 5) { pre = ld_agent(fkv); }
        if (kt == 6) {
            if (!early && tid == 0) st_agent(flags + (FL_KV + 8 * h + j) * FL_STRIDE, tok);
            if (wid == 7) b2a_finish(b2, Bred, rc, hg, lane);
            c.known = (unsigned)__builtin_amdgcn_ballot_w64(pre == tok) & 0xffu;
            for (unsigned spins = 0; spins < (1u << 15) && !(c.known & 1u); ++spins) { const u64 v = ld_agent(fkv); c.known = (unsigned)__builtin_amdgcn_ballot_w64(v == tok) & 0xffu; if (!(c.known & 1u)) __builtin_amdgcn_s_sleep(1); }
            asm volatile("" ::: "memory");
            attn_prime(c, wid >> 2);
        } }, stamp);
    c.srdF = mk_srd(flags + (FL_KV + 8 * h) * FL_STRIDE, 1024); c.tok = tok; c.prev = 0;
    stamp();
    BARRIER();
    if (wid < 4) {
#pragma unroll
        for (int s = 0; s < 8; ++s) *(LAS bf16x8*)(c.qstash + s * 1024) = st.q[s]; }
    stamp();
    B2b<4> b2b;
    attn_run(st, c, lds0, h, j, wid, lane,
             [&]() { if (tid == 448) st_agent(flags + (FL_BR + 32 * hg + rc) * FL_STRIDE, tok); },
             [&]() { b2b_load<4>(b2b, PhiQ, Bred, flags + (FL_BR + 32 * hg + 8 * wid) * FL_STRIDE, tok, 4 * hg + wid, 4 * rc, lane); },
             [&]() { b2b_finish<4>(b2b, Of, 4 * hg + wid, 4 * rc, lane); }, stamp);
    wait_bar<0>();
    if (tid == 0) st_agent(flags + (FL_C + bid) * FL_STRIDE, tok);
    stamp();
    out_phase(a.Wo, a.Bwo, a.out, a.ws, tok, lds0, bid, wid, lane, stamp);
    if (bid == 0 && wid == 7) {
        bool ok = false;
        for (unsigned spins = 0; spins < (1u << 15) && !ok; ++spins) { bool all = true;
#pragma unroll
            for (int i = 0; i < 4; ++i) all &= ld_agent(flags + (FL_C + lane + 64 * i) * FL_STRIDE) == tok;
            ok = __all(all); if (!ok) __builtin_amdgcn_s_sleep(1); }
        if (lane == 0) st_agent(flags + FL_EPOCH * FL_STRIDE, tok);
    }
    stamp();
}

#define WS_END 1
#define WS_SHIFT(n) (WS_END && (n) >= ((size_t)160 << 20) ? (((n) - ((size_t)80 << 20)) & ~(size_t)0x1fffff) : (size_t)0)
extern "C" void kernel_launch(void* const* d_in, const int* in_sizes, int n_in, void* d_out, int out_size, void* d_ws, size_t ws_size, hipStream_t stream) {
    FusedArgs a;
    a.X = (const u16*)d_in[0]; a.Ckv = (const u16*)d_in[1]; a.Wq = (const u16*)d_in[2]; a.Wuk = (const u16*)d_in[3]; a.Wuv = (const u16*)d_in[4]; a.Wo = (const u16*)d_in[5];
    a.Bx = (const u16*)d_in[6]; a.Wqkv = (const u16*)d_in[7]; a.Wphi = (const u16*)d_in[8]; a.Bwo = (const u16*)d_in[9]; a.ws = (char*)d_ws + WS_SHIFT(ws_size); a.out = (u16*)d_out;
    (void)hipFuncSetAttribute((const void*)fused_kernel, hipFuncAttributeMaxDynamicSharedMemorySize, FUSED_LDS);
    fused_kernel<<<256, 512, FUSED_LDS, stream>>>(a);
}
```

```cpp
#include <hip/hip_runtime.h>
#include <stdint.h>
#include <type_traits>

typedef unsigned short u16;
typedef unsigned long long u64;
typedef __bf16 bf16x8 __attribute__((ext_vector_type(8)));
typedef __bf16 bf16x4 __attribute__((ext_vector_type(4)));
typedef __bf16 bf16x2 __attribute__((ext_vector_type(2)));
typedef short s16x4 __attribute__((ext_vector_type(4)));
typedef float f32x16 __attribute__((ext_vector_type(16)));
typedef float f32x4 __attribute__((ext_vector_type(4)));
typedef float f32x2 __attribute__((ext_vector_type(2)));
typedef unsigned u32x4 __attribute__((ext_vector_type(4)));
typedef unsigned u32x2 __attribute__((ext_vector_type(2)));
typedef __amdgpu_buffer_rsrc_t srd_t;
#define LAS __attribute__((address_space(3)))
#define GAS __attribute__((address_space(1)))
#define SBAR() __builtin_amdgcn_sched_barrier(0)
#define PIN(x) asm volatile("" : "+v"(x))
#define MFMA32(a, b, c) __builtin_amdgcn_mfma_f32_32x32x16_bf16((a), (b), (c), 0, 0, 0)
#define MFMA16(a, b, c) __builtin_amdgcn_mfma_f32_16x16x32_bf16((a), (b), (c), 0, 0, 0)
#define DI __device__ __forceinline__
#define MFMA16I(acc, a, b) asm volatile("v_mfma_f32_16x16x32_bf16 %0, %1, %2, %0" : "+v"(acc) : "v"(a), "v"(b))
#define MFMA_DRAIN() asm volatile("s_nop 7")
#pragma clang diagnostic ignored "-Wint-to-pointer-cast"

DI unsigned cvtpk(float lo, float hi) { f32x2 v = {lo, hi}; bf16x2 b = __builtin_convertvector(v, bf16x2); return __builtin_bit_cast(unsigned, b); }
DI unsigned rfl(unsigned x) { return __builtin_amdgcn_readfirstlane(x); }
DI float swap_max(float x) { auto rr = __builtin_amdgcn_permlane32_swap(__float_as_uint(x), __float_as_uint(x), false, false); return __builtin_fmaxf(__uint_as_float(rr[0]), __uint_as_float(rr[1])); }
DI float swap_add(float x) { auto rr = __builtin_amdgcn_permlane32_swap(__float_as_uint(x), __float_as_uint(x), false, false); return __uint_as_float(rr[0]) + __uint_as_float(rr[1]); }
DI srd_t mk_srd(const void* p, int bytes) { return __builtin_amdgcn_make_buffer_rsrc((void*)p, (short)0, bytes, 0x00020000); }
DI bf16x4 ldstr(unsigned a) { return __builtin_amdgcn_ds_read_tr16_b64_v4bf16((LAS bf16x4*)a); }
DI bf16x8 ldsf(unsigned a) { return *(const LAS bf16x8*)a; }
DI bf16x8 cat4(bf16x4 a, bf16x4 b) { return __builtin_shufflevector(a, b, 0, 1, 2, 3, 4, 5, 6, 7); }
DI void dma1(unsigned m0v, unsigned voff, srd_t srd, unsigned soff) {
    asm volatile("s_mov_b32 m0, %0\n\ts_nop 0\n\tbuffer_load_dwordx4 %1, %2, %3 offen lds" :: "s"(m0v), "v"(voff), "s"(srd), "s"(soff) : "memory");
}
DI void dma1s(unsigned m0v, unsigned voff, srd_t srd, unsigned soff) {
    asm volatile("s_mov_b32 m0, %0\n\ts_nop 0\n\tbuffer_load_dwordx4 %1, %2, %3 offen sc1 lds" :: "s"(m0v), "v"(voff), "s"(srd), "s"(soff) : "memory");
}
DI void store16_sc1(u32x4 w, unsigned voff, srd_t srd, unsigned soff) {
    asm volatile("buffer_store_dwordx4 %0, %1, %2, %3 offen sc1\n\ts_nop 1" :: "v"(w), "v"(voff), "s"(srd), "s"(soff) : "memory");
}
template <int N> DI void wait_vm() { asm volatile("s_waitcnt vmcnt(%0)" :: "i"(N) : "memory"); }
template <int N> DI void wait_bar() { asm volatile("s_waitcnt vmcnt(%0) lgkmcnt(0)\n\ts_barrier" :: "i"(N) : "memory"); }
template <int N> DI void wait_bar_keep() { asm volatile("s_waitcnt vmcnt(%0)\n\ts_barrier" :: "i"(N) : "memory"); }
#define BARRIER() asm volatile("s_waitcnt lgkmcnt(0)\n\ts_barrier" ::: "memory")

constexpr size_t WS_FLAG = 0;
constexpr size_t WS_KF = 1 << 20, WS_VF = WS_KF + (16 << 20), WS_CF = WS_VF + (16 << 20), WS_PHIQ = WS_CF + (16 << 20), WS_OF = WS_PHIQ + (4 << 20), WS_BST = WS_OF + (2 << 20), WS_BRED = WS_BST + (3 << 20), WS_XCH = WS_BRED + (1 << 20);
constexpr int FL_STRIDE = 16;
constexpr int FL_EPOCH = 0, FL_KV = 32, FL_BS = 32 + 256, FL_BR = 32 + 512, FL_XF = 32 + 768, FL_C = FL_XF + 256;
typedef GAS u64 gu64;
DI u64 ld_agent(const u64* p) { return __hip_atomic_load((gu64*)p, __ATOMIC_RELAXED, __HIP_MEMORY_SCOPE_AGENT); }
DI void st_agent(u64* p, u64 v) { __hip_atomic_store((gu64*)p, v, __ATOMIC_RELAXED, __HIP_MEMORY_SCOPE_AGENT); }
DI u64 mix64(u64 z) { z += 0x9E3779B97F4A7C15ull; z = (z ^ (z >> 30)) * 0xBF58476D1CE4E5B9ull; z = (z ^ (z >> 27)) * 0x94D049BB133111EBull; return z ^ (z >> 31); }
DI f32x4 mfma16_16(u32x2 a, u32x2 b, f32x4 c) { return __builtin_amdgcn_mfma_f32_16x16x16bf16_1k(__builtin_bit_cast(s16x4, a), __builtin_bit_cast(s16x4, b), c, 0, 0, 0); }
DI float rbf16(float x) { return __uint_as_float(cvtpk(x, 0.f) << 16); }
DI float x16_max(float x) { auto rr = __builtin_amdgcn_permlane16_swap(__float_as_uint(x), __float_as_uint(x), false, false); return __builtin_fmaxf(__uint_as_float(rr[0]), __uint_as_float(rr[1])); }
DI float x16_add(float x) { auto rr = __builtin_amdgcn_permlane16_swap(__float_as_uint(x), __float_as_uint(x), false, false); return __uint_as_float(rr[0]) + __uint_as_float(rr[1]); }
DI float grp_max(float x) { return swap_max(x16_max(x)); }
DI float grp_sum(float x) { return swap_add(x16_add(x)); }
constexpr float LOG2E = 1.4426950408889634f;
constexpr unsigned B1_RED = 147456;
struct B1Ctx { srd_t srdX, srdW; unsigned vx, vw[2], sx, sw; int wid; unsigned lds0; };
DI void b1_setup(B1Ctx& c, const u16* Bx, const u16* Wqkv, unsigned lds0, int rc, int hg, int wid, int lane) {
    c.srdX = mk_srd(Bx, 2097152); c.srdW = mk_srd(Wqkv, 512 * 1536 * 2); c.wid = wid; c.lds0 = lds0;
    c.vx = (lane >> 3) * 1024 + (((lane & 7) ^ ((4 * wid + (lane >> 4)) & 7)) * 16);
#pragma unroll
    for (int p = 0; p < 2; ++p) c.vw[p] = (lane >> 3) * 3072 + (((lane & 7) ^ (2 * (((lane >> 4) & 1) | (p << 1)))) * 16);
    c.sx = (64 * rc + 8 * wid) * 1024; c.sw = hg * 128;
}
DI void b1_issue(const B1Ctx& c, int kt) {
    const unsigned st = c.lds0 + (kt & 3) * 32768;
    dma1(st + c.wid * 1024, c.vx, c.srdX, c.sx + kt * 128);
#pragma unroll
    for (int i = 0; i < 3; ++i) { const int pw = 3 * c.wid + i, m = pw >> 3, pr = pw & 7;
        dma1(st + 8192 + pw * 1024, (pr & 1) ? c.vw[1] : c.vw[0], c.srdW, c.sw + (kt * 64 + 8 * pr) * 3072 + m * 1024); }
}
struct B1Acc { f32x4 aq[2], ak[2], av[2]; };
struct B1Fr { bf16x8 x[2][2]; bf16x4 w[2][3][2]; };
struct B1Ad { unsigned xa[2][2], wa[2]; };
template <int KT> DI void b1_read(B1Fr& f, const B1Ad& ad) {
    constexpr unsigned st = (KT & 1) * 32768; constexpr int sp = (KT >> 1) & 1;
#pragma unroll
    for (int ks = 0; ks < 2; ++ks) { const unsigned xa = ad.xa[sp][ks];
        f.x[ks][0] = ldsf(xa + st); f.x[ks][1] = ldsf(xa + st + 2048);
#pragma unroll
        for (int m = 0; m < 3; ++m) { f.w[ks][m][0] = ldstr(ad.wa[sp] + st + m * 8192 + ks * 4096); f.w[ks][m][1] = ldstr(ad.wa[sp] + st + m * 8192 + ks * 4096 + 512); } }
}
DI void b1_mma(B1Acc& a, const B1Fr& f) {
#pragma unroll
    for (int ks = 0; ks < 2; ++ks) {
        const bf16x8 wq = cat4(f.w[ks][0][0], f.w[ks][0][1]), wk = cat4(f.w[ks][1][0], f.w[ks][1][1]), wv = cat4(f.w[ks][2][0], f.w[ks][2][1]);
        a.aq[0] = MFMA16(wq, f.x[ks][0], a.aq[0]); a.aq[1] = MFMA16(wq, f.x[ks][1], a.aq[1]); a.ak[0] = MFMA16(wk, f.x[ks][0], a.ak[0]); a.ak[1] = MFMA16(wk, f.x[ks][1], a.ak[1]);
        a.av[0] = MFMA16(f.x[ks][0], wv, a.av[0]); a.av[1] = MFMA16(f.x[ks][1], wv, a.av[1]);
    }
}
template <class St, class IQ>
DI void b1_gemm(const B1Ctx& c, B1Acc& a, int lane, bool early, St store, IQ issueQt) {
    const int l16 = lane & 15, g = lane >> 4, q4 = l16 >> 2, p4 = lane & 3, wid = c.wid;
    const int row = 32 * (wid >> 2) + l16;
    B1Ad ad;
#pragma unroll
    for (int sp = 0; sp < 2; ++sp) {
        ad.xa[sp][0] = c.lds0 + sp * 65536 + row * 128 + (((0 + g) ^ ((row >> 1) & 7)) << 4); ad.xa[sp][1] = c.lds0 + sp * 65536 + row * 128 + (((4 + g) ^ ((row >> 1) & 7)) << 4);
        ad.wa[sp] = c.lds0 + sp * 65536 + 8192 + (8 * g + q4) * 128 + (((wid & 3) ^ (((q4 >> 1) & 1) | ((g & 1) << 1))) * 32) + p4 * 8;
        PIN(ad.xa[sp][0]); PIN(ad.xa[sp][1]); PIN(ad.wa[sp]); }
#pragma unroll
    for (int r = 0; r < 2; ++r) { a.aq[r] = f32x4{}; a.ak[r] = f32x4{}; a.av[r] = f32x4{}; }
#pragma unroll
    for (int r = 0; r < 2; ++r) { PIN(a.aq[r]); PIN(a.ak[r]); PIN(a.av[r]); }
    B1Fr f0, f1;
    b1_read<0>(f0, ad);
#define B1W(E, L) do { if (early) wait_bar<E>(); else wait_bar<L>(); } while (0)
#define B1T(KT, CUR, NXT, PRE) do { b1_read<KT + 1>(NXT, ad); SBAR(); PRE; store(KT); SBAR(); b1_mma(a, CUR); SBAR(); } while (0)
    B1W(8, 8);   B1T(0, f0, f1, b1_issue(c, 4));
    B1W(9, 8);   B1T(1, f1, f0, b1_issue(c, 5));
    B1W(10, 9);  B1T(2, f0, f1, b1_issue(c, 6));
    B1W(11, 10); B1T(3, f1, f0, b1_issue(c, 7));
    B1W(11, 11); B1T(4, f0, f1, (void)0);
    B1W(7, 7);   B1T(5, f1, f0, issueQt(0));
    B1W(9, 9);   B1T(6, f0, f1, (void)0);
    BARRIER();   store(7); SBAR(); b1_mma(a, f1);
#undef B1W
#undef B1T
}
template <int N, class Tick> DI void phi_map(const f32x4 (&p)[N], u32x4 (&out)[N], Tick tick) {
    float x[N][4], mx[N], nm[N], ep[N][4], en[N][4], sp[N], sn[N];
#pragma unroll
    for (int i = 0; i < N; ++i) {
#pragma unroll
        for (int e = 0; e < 4; ++e) x[i][e] = rbf16(p[i][e]) * LOG2E;
        mx[i] = __builtin_fmaxf(__builtin_fmaxf(x[i][0], x[i][1]), __builtin_fmaxf(x[i][2], x[i][3])); nm[i] = -__builtin_fminf(__builtin_fminf(x[i][0], x[i][1]), __builtin_fminf(x[i][2], x[i][3]));
        if (i & 1) tick(i >> 1); }
#pragma unroll
    for (int i = 0; i < N; ++i) { mx[i] = x16_max(mx[i]); nm[i] = x16_max(nm[i]); if (i & 1) tick(2 + (i >> 1)); }
#pragma unroll
    for (int i = 0; i < N; ++i) { mx[i] = swap_max(mx[i]); nm[i] = swap_max(nm[i]); if (i & 1) tick(4 + (i >> 1)); }
#pragma unroll
    for (int i = 0; i < N; ++i) {
#pragma unroll
        for (int e = 0; e < 4; ++e) { ep[i][e] = __builtin_amdgcn_exp2f(x[i][e] - mx[i]); en[i][e] = __builtin_amdgcn_exp2f(-x[i][e] - nm[i]); }
        sp[i] = (ep[i][0] + ep[i][1]) + (ep[i][2] + ep[i][3]); sn[i] = (en[i][0] + en[i][1]) + (en[i][2] + en[i][3]); tick(6 + i); }
#pragma unroll
    for (int i = 0; i < N; ++i) { sp[i] = x16_add(sp[i]); sn[i] = x16_add(sn[i]); if (i & 1) tick(10 + (i >> 1)); }
#pragma unroll
    for (int i = 0; i < N; ++i) { sp[i] = __builtin_amdgcn_rcpf(swap_add(sp[i])); sn[i] = __builtin_amdgcn_rcpf(swap_add(sn[i])); if (i & 1) tick(12 + (i >> 1)); }
#pragma unroll
    for (int i = 0; i < N; ++i) { out[i] = (u32x4){cvtpk(ep[i][0] * sp[i], ep[i][1] * sp[i]), cvtpk(ep[i][2] * sp[i], ep[i][3] * sp[i]), cvtpk(en[i][0] * sn[i], en[i][1] * sn[i]), cvtpk(en[i][2] * sn[i], en[i][3] * sn[i])};
        if (i & 1) tick(14 + (i >> 1)); }
}
template <class Tick>
DI void b1_phi(const B1Acc& a, Tick tick, u32x2 wp, u16* PhiQ, float* Bst, unsigned lds0, int rc, int hg, int wid, int lane) {
    const int h = 4 * hg + (wid & 3), row0 = 64 * rc + 32 * (wid >> 2);
    const int l16 = lane & 15, g = lane >> 4;
    f32x4 sp = f32x4{}, sn = f32x4{}; float zp = 0.f, zn = 0.f;
    f32x4 pin[4]; u32x4 pout[4];
#pragma unroll
    for (int r = 0; r < 2; ++r) {
        const u32x2 qb = {cvtpk(a.aq[r][0], a.aq[r][1]), cvtpk(a.aq[r][2], a.aq[r][3])}, kb = {cvtpk(a.ak[r][0], a.ak[r][1]), cvtpk(a.ak[r][2], a.ak[r][3])};
        pin[r] = mfma16_16(wp, qb, f32x4{}); pin[2 + r] = mfma16_16(wp, kb, f32x4{});
    }
    phi_map<4>(pin, pout, tick);
    tick(16);
    const u32x4 wq[2] = {pout[0], pout[1]}, wk[2] = {pout[2], pout[3]};
    const unsigned tb = lds0 + B1_RED + wid * 1024;
#pragma unroll
    for (int r = 0; r < 2; ++r) {
        *(LAS u32x2*)(tb + l16 * 32 + g * 8) = (u32x2){wk[r][0], wk[r][1]};
        *(LAS u32x2*)(tb + 512 + l16 * 32 + g * 8) = (u32x2){wk[r][2], wk[r][3]};
        __builtin_amdgcn_raw_buffer_store_b128(wq[r], mk_srd(PhiQ, 4194304), ((h * 128 + (row0 >> 4) + r) * 64 + lane) * 16, 0, 0);
        asm volatile("s_waitcnt lgkmcnt(0)" ::: "memory");
        const unsigned ta = tb + (4 * g + (l16 >> 2)) * 32 + (lane & 3) * 8;
        u32x2 pp = __builtin_bit_cast(u32x2, ldstr(ta)), pn = __builtin_bit_cast(u32x2, ldstr(ta + 512));
        asm volatile("s_waitcnt lgkmcnt(0)" : "+v"(pp), "+v"(pn) :: "memory");
        const u32x2 vb = {cvtpk(a.av[r][0], a.av[r][1]), cvtpk(a.av[r][2], a.av[r][3])};
        sp = mfma16_16(pp, vb, sp); sn = mfma16_16(pn, vb, sn);
        zp += (__uint_as_float(pp[0] << 16) + __uint_as_float(pp[0] & 0xffff0000u)) + (__uint_as_float(pp[1] << 16) + __uint_as_float(pp[1] & 0xffff0000u));
        zn += (__uint_as_float(pn[0] << 16) + __uint_as_float(pn[0] & 0xffff0000u)) + (__uint_as_float(pn[1] << 16) + __uint_as_float(pn[1] & 0xffff0000u));
    }
    zp = grp_sum(zp); zn = grp_sum(zn);
    BARRIER();
    LAS char* red = (LAS char*)(lds0 + B1_RED + (wid & 3) * 2176);
    if (wid >= 4) { *(LAS f32x4*)(red + lane * 32) = sp; *(LAS f32x4*)(red + lane * 32 + 16) = sn; if (lane < 16) *(LAS f32x2*)(red + 2048 + lane * 8) = (f32x2){zp, zn}; }
    BARRIER();
    sp += *(const LAS f32x4*)(red + lane * 32); sn += *(const LAS f32x4*)(red + lane * 32 + 16);
    const f32x2 zo = *(const LAS f32x2*)(red + 2048 + l16 * 8);
    const srd_t srd = mk_srd(Bst + (size_t)(h * 32 + rc) * 576, wid < 4 ? 2304 : 0);
    store16_sc1(__builtin_bit_cast(u32x4, sp), lane * 32, srd, 0u); store16_sc1(__builtin_bit_cast(u32x4, sn), lane * 32 + 16, srd, 0u);
    __builtin_amdgcn_raw_buffer_store_b64((u32x2){__float_as_uint(zp + zo[0]), __float_as_uint(zn + zo[1])}, srd, lane < 16 ? 2048 + lane * 8 : 0x7fffffff, 0, 16);
}
template <int N> DI void waitN(const u64* flags, u64 tok, int lane) {
    const u64* p = flags + (lane & (N - 1)) * FL_STRIDE;
    for (unsigned spins = 0; spins < (1u << 15); ++spins) { const u64 v = ld_agent(p); if (__all(v == tok)) break; __builtin_amdgcn_s_sleep(1); }
    asm volatile("" ::: "memory");
}
struct B2a { f32x4 v[16]; };
DI void b2a_load(B2a& b, const float* Bst, int rc, int hg, int lane) {
    const int h = 4 * hg + (rc >> 3), c = lane & 31, half = lane >> 5;
    const srd_t srd = mk_srd(Bst + (size_t)h * 32 * 576, 32 * 2304);
    const unsigned vo = (c < 18 ? (18 * (rc & 7) + c) * 16 : 0x7fffffff) + half * 16 * 2304;
#pragma unroll
    for (int i = 0; i < 16; ++i) b.v[i] = __builtin_bit_cast(f32x4, __builtin_amdgcn_raw_buffer_load_b128(srd, vo, i * 2304, 16));
}
DI void b2a_finish(const B2a& b, float* Bred, int rc, int hg, int lane) {
    const int h = 4 * hg + (rc >> 3), c = lane & 31, half = lane >> 5;
    f32x4 s = b.v[0];
#pragma unroll
    for (int i = 1; i < 16; ++i) s += b.v[i];
#pragma unroll
    for (int e = 0; e < 4; ++e) s[e] = swap_add(s[e]);
    const srd_t srdR = mk_srd(Bred + (size_t)h * 576, 2304);
    store16_sc1(__builtin_bit_cast(u32x4, s), (half == 0 && c < 18) ? (18 * (rc & 7) + c) * 16 : 0x7fffffffu, srdR, 0u);
}
template <int NRB> struct B2b { bf16x8 ph[NRB]; f32x4 sp, sn, za, zb; };
template <int NRB>
DI void b2b_load(B2b<NRB>& b, const u16* PhiQ, const float* Bred, const u64* flagsR, u64 tok, int h, int rb0, int lane) {
    PIN(lane);
    const int g = lane >> 4;
    waitN<8>(flagsR, tok, lane);
#pragma unroll
    for (int r = 0; r < NRB; ++r) b.ph[r] = __builtin_bit_cast(bf16x8, __builtin_amdgcn_raw_buffer_load_b128(mk_srd(PhiQ, 4194304), lane * 16, (h * 128 + rb0 + r) * 1024, 0));
    const srd_t srd = mk_srd(Bred + (size_t)h * 576, 2304);
    b.sp = __builtin_bit_cast(f32x4, __builtin_amdgcn_raw_buffer_load_b128(srd, lane * 32, 0, 16)); b.sn = __builtin_bit_cast(f32x4, __builtin_amdgcn_raw_buffer_load_b128(srd, lane * 32 + 16, 0, 16));
    b.za = __builtin_bit_cast(f32x4, __builtin_amdgcn_raw_buffer_load_b128(srd, 2048 + g * 32, 0, 16)); b.zb = __builtin_bit_cast(f32x4, __builtin_amdgcn_raw_buffer_load_b128(srd, 2048 + g * 32 + 16, 0, 16));
}
template <int NRB>
DI void b2b_finish(const B2b<NRB>& b, u16* Of, int h, int rb0, int lane) {
    PIN(lane);
    const int l16 = lane & 15, g = lane >> 4;
    const u32x4 kvw = {cvtpk(b.sp[0], b.sp[1]), cvtpk(b.sp[2], b.sp[3]), cvtpk(b.sn[0], b.sn[1]), cvtpk(b.sn[2], b.sn[3])};
    const u32x4 zw = {cvtpk(b.za[0], b.za[2]), cvtpk(b.zb[0], b.zb[2]), cvtpk(b.za[1], b.za[3]), cvtpk(b.zb[1], b.zb[3])};
    const srd_t srdO = mk_srd(Of, 2097152);
#pragma unroll
    for (int r = 0; r < NRB; ++r) { const int rb16 = rb0 + r;
        const f32x4 num = MFMA16(__builtin_bit_cast(bf16x8, kvw), b.ph[r], f32x4{});
        const f32x4 den = MFMA16(__builtin_bit_cast(bf16x8, zw), b.ph[r], f32x4{});
        const float dn = rbf16(rbf16(den[0]) + 1e-6f);
        const u32x2 w = {cvtpk(rbf16(num[0]) / dn, rbf16(num[1]) / dn), cvtpk(rbf16(num[2]) / dn, rbf16(num[3]) / dn)};
        __builtin_amdgcn_raw_buffer_store_b64(w, srdO, (((rb16 >> 1) * 32 + h) * 2 + (g & 1)) * 512 + ((rb16 & 1) * 16 + l16) * 16 + (g >> 1) * 8, 0, 16); }
}
struct ProjCtx {
    srd_t srdC, srdX, srdW, srdWq, srdK, srdV;
    unsigned va[2], vw[2], vwq, voK, voV, lds0, l128;
    unsigned sA, sW, sWq, sXa;
    int wid;
};
constexpr unsigned TOUCH_LDS = 163840 - 256;
DI void issueKV(const ProjCtx& c, int kt, int i) {
    const unsigned st = c.lds0 + (kt & 1) * 65536;
    if (i < 4) dma1(st + (4 * c.wid + i) * 1024, c.va[i & 1], c.srdC, c.sA + i * 8192 + kt * 128);
    else dma1(st + 32768 + (c.wid >> 2) * 16384 + (4 * (c.wid & 3) + (i - 4)) * 1024, c.vw[(i - 4) >> 1], c.srdW, c.sW + ((i - 4) >> 1) * 65536 + ((i - 4) & 1) * 128 + kt * 524288);
}
DI void issueQ(const ProjCtx& c, int kt, int i) {
    const unsigned st = c.lds0 + (kt % 3) * 49152;
    if (i < 4) dma1(st + (4 * c.wid + i) * 1024, c.va[i & 1], c.srdX, c.sXa + i * 8192 + kt * 128);
    else dma1(st + 32768 + (2 * c.wid + (i - 4)) * 1024, c.vwq, c.srdWq, c.sWq + (i - 4) * 128 + kt * 524288);
}
DI void touch_g(unsigned m0v, unsigned voff, const void* base) {
    const u64 b = (u64)base; const u64 sb = ((u64)rfl((unsigned)(b >> 32)) << 32) | rfl((unsigned)b);
    asm volatile("s_nop 4\n\ts_mov_b32 m0, %0\n\ts_nop 0\n\tglobal_load_lds_dword %1, %2" :: "s"(rfl(m0v)), "v"(voff), "s"(sb) : "memory");
}

struct GAddr { unsigned a[2]; unsigned w[2][2]; };
DI void gaddr_add(GAddr& g, unsigned d) { g.a[0] += d; g.a[1] += d; g.w[0][0] += d; g.w[0][1] += d; g.w[1][0] += d; g.w[1][1] += d; PIN(g.a[0]); PIN(g.a[1]); PIN(g.w[0][0]); PIN(g.w[0][1]); PIN(g.w[1][0]); PIN(g.w[1][1]); }
DI void gaddr_xor(GAddr& g, unsigned d) { g.a[0] ^= d; g.a[1] ^= d; g.w[0][0] ^= d; g.w[0][1] ^= d; g.w[1][0] ^= d; g.w[1][1] ^= d; }
DI bf16x8 wfrag(const GAddr& g, int ks, int nb) { return cat4(ldstr(g.w[nb & 1][0] + ks * 8192 + (nb >> 1) * 512), ldstr(g.w[nb & 1][1] + ks * 8192 + (nb >> 1) * 512)); }
DI bf16x8 wfragKV(const GAddr& g, int ks, int nb) { return cat4(ldstr(g.w[nb & 1][0] + ks * 8192 + (nb >> 1) * 16384), ldstr(g.w[nb & 1][1] + ks * 8192 + (nb >> 1) * 16384)); }
DI void gaddr_w(GAddr& g, unsigned base, int lane) {
    const int gq = lane >> 4, q4 = (lane & 15) >> 2, p4 = lane & 3;
#pragma unroll
    for (int o = 0; o < 2; ++o)
#pragma unroll
        for (int t = 0; t < 2; ++t) g.w[o][t] = base + 2048 * gq + 64 * (4 * t + q4) + 16 * ((2 * o + (p4 >> 1)) ^ ((2 * gq + t) & 3)) + 8 * (p4 & 1);
}
DI void gaddr_a(GAddr& g, unsigned base, int row0, int lane) {
    const int row = row0 + (lane & 15), gq = lane >> 4;
#pragma unroll
    for (int ks = 0; ks < 2; ++ks) g.a[ks] = base + row * 128 + (((4 * ks + gq) ^ ((row >> 1) & 7)) << 4);
}

constexpr int AH = 3;
#define KV_MMA(mb, W) do { MFMA16I(acc[mb][0], W[0], a[mb]); MFMA16I(acc[mb][1], W[1], a[mb]); MFMA16I(acc[mb][2], a[mb], W[2]); MFMA16I(acc[mb][3], a[mb], W[3]); } while (0)
template <class Hook>
DI void kv_step0(f32x4 (&acc)[8][4], const bf16x8 (&w)[4], bf16x8 (&wn)[4], bf16x8 (&an)[AH], const GAddr& g, Hook hook) {
    bf16x8 a[8 + AH];
#pragma unroll
    for (int i = 0; i < AH; ++i) a[i] = an[i];
#pragma unroll
    for (int mb = 0; mb < 8; ++mb) {
        a[mb + AH] = mb + AH < 8 ? ldsf(g.a[0] + (mb + AH) * 2048) : ldsf(g.a[1] + (mb + AH - 8) * 2048);
        if (mb < 4) wn[mb] = wfragKV(g, 1, mb);
        SBAR();
        KV_MMA(mb, w);
        SBAR();
        hook(mb);
        SBAR();
    }
#pragma unroll
    for (int i = 0; i < AH; ++i) an[i] = a[8 + i];
}
struct NoHook { DI void operator()(int) const {} };

template <class Touch, class IssueB1>
DI void kv_part(const ProjCtx& c, GAddr g, u32x4 (&pk)[16], Touch touchk, IssueB1 issueB1) {
    f32x4 acc[8][4];
#pragma unroll
    for (int a = 0; a < 8; ++a)
#pragma unroll
        for (int b = 0; b < 4; ++b) { acc[a][b] = f32x4{}; PIN(acc[a][b]); }
    bf16x8 w0[4], w1[4], an[AH];
    wait_bar<9>();
#pragma unroll
    for (int nb = 0; nb < 4; ++nb) w0[nb] = wfragKV(g, 0, nb);
#pragma unroll
    for (int i = 0; i < AH; ++i) an[i] = ldsf(g.a[0] + i * 2048);
    auto tile = [&](int kt, auto late, auto mid, auto waitB, auto early, bool next) {
        kv_step0(acc, w0, w1, an, g, late);
        bf16x8 a[8];
#pragma unroll
        for (int i = 0; i < AH; ++i) a[i] = an[i];
#pragma unroll
        for (int mb = 0; mb < 8 - AH; ++mb) {
            a[mb + AH] = ldsf(g.a[1] + (mb + AH) * 2048);
            SBAR();
            KV_MMA(mb, w1);
            SBAR();
            if (mb == 0) { mid(); SBAR(); }
        }
        waitB();
        if (next) { gaddr_xor(g, 65536u);
#pragma unroll
            for (int nb = 0; nb < 4; ++nb) w0[nb] = wfragKV(g, 0, nb);
#pragma unroll
            for (int i = 0; i < AH; ++i) an[i] = ldsf(g.a[0] + i * 2048); }
        SBAR();
#pragma unroll
        for (int mb = 8 - AH; mb < 8; ++mb) { KV_MMA(mb, w1); SBAR(); early(mb - (8 - AH)); SBAR(); }
    };
    for (int kt = 0; kt < 6; ++kt)
        tile(kt, [&](int mb) { if (kt > 0 && (mb & 1)) issueKV(c, kt + 1, 4 + (mb >> 1)); }, [&]() { touchk(kt); }, [&]() { wait_bar<1>(); },
             [&](int i) { if (i == 0) { issueKV(c, kt + 2, 0); issueKV(c, kt + 2, 1); } else issueKV(c, kt + 2, 1 + i); }, true);
    tile(6, [&](int mb) { if (mb & 1) issueKV(c, 7, 4 + (mb >> 1)); }, [&]() {}, [&]() { wait_bar<0>(); }, [&](int i) { if (i < 2) issueB1(i); }, true);
    tile(7, NoHook{}, [&]() {}, [&]() { wait_bar<4>(); }, [&](int i) { if (i < 2) issueB1(2 + i); }, false);
    __builtin_amdgcn_s_setprio(0);
    MFMA_DRAIN();
#pragma unroll
    for (int n = 0; n < 16; ++n) { const int m = n - 8;
        const f32x4 lo = n < 8 ? acc[n & 7][0] : acc[2 * ((m >> 1) & 3)][2 + (m & 1)], hi = n < 8 ? acc[n & 7][1] : acc[2 * ((m >> 1) & 3) + 1][2 + (m & 1)];
        pk[n] = (u32x4){cvtpk(lo[0], lo[1]), cvtpk(lo[2], lo[3]), cvtpk(hi[0], hi[1]), cvtpk(hi[2], hi[3])}; PIN(pk[n]); }
}
DI void kv_store(const ProjCtx& c, const u32x4 (&pk)[16], int n) {
    const int m = n - 8;
    if (n < 8) store16_sc1(pk[n], c.voK, c.srdK, (unsigned)((n >> 2) * 16384 + (n & 3) * 4096));
    else store16_sc1(pk[n], c.voV, c.srdV, (unsigned)((m >> 2) * 16384 + (m & 1) * 2048 + ((m >> 1) & 1) * 1024));
}

struct QFr { bf16x8 x[2]; bf16x8 w[8]; };
template <int KS> DI void q_read(QFr& f, const GAddr& g, int i) {
    if (i == 1) f.x[0] = ldsf(g.a[KS]); else if (i == 2) f.x[1] = ldsf(g.a[KS] + 2048); else { const int nb = i == 0 ? 0 : i - 2; f.w[nb] = wfrag(g, KS, nb); }
}
template <int KSN, bool NEXT, class Hook>
DI void q_kstep(f32x4 (&acc)[8][2], const QFr& cur, QFr& nxt, const GAddr& gn, Hook hook) {
#pragma unroll
    for (int nb = 0; nb < 8; ++nb) {
        MFMA16I(acc[nb][0], cur.w[nb], cur.x[0]); MFMA16I(acc[nb][1], cur.w[nb], cur.x[1]);
        SBAR();
        if (NEXT) { if (nb < 2) { q_read<KSN>(nxt, gn, 2 * nb); q_read<KSN>(nxt, gn, 2 * nb + 1); } else q_read<KSN>(nxt, gn, nb + 2); }
        hook(nb);
        SBAR();
    }
}
template <int KT, class Pub>
DI void q_tile(const ProjCtx& c, bool early, GAddr& g, f32x4 (&acc)[8][2], QFr& f0, QFr& f1, const u32x4 (&pk)[16], Pub after) {
    q_kstep<1, true>(acc, f0, f1, g, [&](int nb) {
        if (KT >= 1 && KT + 2 < 8 && nb < 3) issueQ(c, KT + 2, 3 + nb);
        if (nb == 4) { if (early) { if (KT < 2) kv_store(c, pk, 12 + 2 * (KT < 2 ? KT : 0)); } else if (KT < 5) kv_store(c, pk, 11 + (KT < 5 ? KT : 0)); }
        if (nb == 7 && early && KT < 2) kv_store(c, pk, 13 + 2 * (KT < 2 ? KT : 0)); });
    if constexpr (KT < 7) {
        constexpr int WE = KT == 0 ? 17 : (KT == 1 ? 19 : (KT == 2 ? 8 : (KT <= 5 ? 6 : 0)));
        constexpr int WL = KT == 0 ? 17 : (KT == 1 ? 18 : (KT <= 4 ? 8 : (KT == 5 ? 7 : 0)));
        const bool w7 = KT == 5 && c.wid == 7;
        if (early) { if (w7) wait_bar<WE + 16>(); else wait_bar<WE>(); } else { if (w7) wait_bar<WL + 16>(); else wait_bar<WL>(); }
        after(KT);
        gaddr_add(g, (unsigned)(((KT + 1) % 3 - KT % 3) * 49152));
        q_kstep<0, true>(acc, f1, f0, g, [&](int nb) { if (KT + 3 < 8 && nb < 3) issueQ(c, KT + 3, nb); });
    } else q_kstep<0, false>(acc, f1, f0, g, NoHook{});
}

DI void proj_setup(ProjCtx& c, const u16* X, const u16* Ckv, const u16* Wq, const u16* Wuk, const u16* Wuv, u16* Kf, u16* Vf, unsigned lds0, int h, int j, int wid, int lane) {
    const int wr = wid >> 2, wc = wid & 3;
    c.wid = wid; c.lds0 = lds0;
    c.srdC = mk_srd(Ckv, 2097152); c.srdX = mk_srd(X, 2097152);
    c.srdW = mk_srd((wid < 4 ? Wuk : Wuv), 4194304); c.srdWq = mk_srd(Wq, 4194304);
    c.srdK = mk_srd((const char*)Kf + (size_t)h * 524288, 524288); c.srdV = mk_srd((const char*)Vf + (size_t)h * 524288, 524288);
#pragma unroll
    for (int p = 0; p < 2; ++p) {
        c.va[p] = (lane >> 3) * 1024 + (((lane & 7) ^ (4 * p + (lane >> 4))) * 16);
        c.vw[p] = ((lane >> 2) & 7) * 8192 + (4 * (lane >> 5) + ((lane & 3) ^ ((2 * p + ((lane >> 4) & 1)) & 3))) * 16;
    }
    c.vwq = (wid & 1) ? c.vw[1] : c.vw[0];
    c.l128 = lane * 128;
    c.sA = (256 * j + 32 * wid) * 1024;
    c.sW = (16 * (wid & 3)) * 8192 + h * 256;
    c.sXa = (wid < 4 ? 128 * (15 - j) + 32 * wid : 128 * j + 32 * (wid - 4)) * 1024;
    c.sWq = (8 * wid) * 8192 + h * 256;
    c.voK = lane * 16 + (4 * j + 2 * wr) * 16384 + wc * 1024; c.voV = lane * 16 + (4 * j + 2 * wr) * 16384 + wc * 4096;
}
template <class Touch, class IssueB1>
DI void kv_run(const ProjCtx& c, int lane, u32x4 (&pk)[16], Touch touchk, IssueB1 issueB1) {
    const int wid = c.wid, wr = wid >> 2, wc = wid & 3;
    GAddr g; gaddr_a(g, c.lds0, 128 * wr, lane); gaddr_w(g, c.lds0 + 32768 + wc * 512, lane);
    kv_part(c, g, pk, touchk, issueB1);
}
template <class Pub>
DI void q_run(const ProjCtx& c, bool early, int lane, const u32x4 (&pk)[16], bf16x8 (&q)[8], Pub after) {
    GAddr g; gaddr_a(g, c.lds0, 32 * c.wid, lane); gaddr_w(g, c.lds0 + 32768, lane); gaddr_add(g, 0u);
    f32x4 acc[8][2];
#pragma unroll
    for (int a = 0; a < 8; ++a) { acc[a][0] = f32x4{}; acc[a][1] = f32x4{}; PIN(acc[a][0]); PIN(acc[a][1]); }
    QFr f0, f1;
#pragma unroll
    for (int i = 0; i < 10; ++i) q_read<0>(f0, g, i);
    q_tile<0>(c, early, g, acc, f0, f1, pk, after); q_tile<1>(c, early, g, acc, f0, f1, pk, after); q_tile<2>(c, early, g, acc, f0, f1, pk, after); q_tile<3>(c, early, g, acc, f0, f1, pk, after);
    q_tile<4>(c, early, g, acc, f0, f1, pk, after); q_tile<5>(c, early, g, acc, f0, f1, pk, after); q_tile<6>(c, early, g, acc, f0, f1, pk, after); q_tile<7>(c, early, g, acc, f0, f1, pk, after);
    MFMA_DRAIN();
    constexpr float QSC = 0.08838834764831845f * 1.4426950408889634f;
#pragma unroll
    for (int qb = 0; qb < 2; ++qb)
#pragma unroll
        for (int ds = 0; ds < 4; ++ds) { const f32x4 lo = acc[2 * ds][qb] * QSC, hi = acc[2 * ds + 1][qb] * QSC;
            q[qb * 4 + ds] = __builtin_bit_cast(bf16x8, (u32x4){cvtpk(lo[0], lo[1]), cvtpk(lo[2], lo[3]), cvtpk(hi[0], hi[1]), cvtpk(hi[2], hi[3])}); }
}
template <int OFF, bool SETM0> DI void dma_piece(unsigned m0v, unsigned voff, srd_t srd, unsigned soff) {
    if constexpr (SETM0) asm volatile("s_mov_b32 m0, %0\n\ts_nop 0\n\tbuffer_load_dwordx4 %1, %2, %3 offen offset:%4 sc1 lds" :: "s"(m0v), "v"(voff), "s"(srd), "s"(soff), "i"(OFF) : "memory");
    else asm volatile("buffer_load_dwordx4 %0, %1, %2 offen offset:%3 sc1 lds" :: "v"(voff), "s"(srd), "s"(soff), "i"(OFF) : "memory");
}
constexpr float ATT_THR = 64.f;
constexpr int ATT_W = 5;

struct AttnState { f32x4 o[8][2]; f32x4 s[4][2]; f32x4 negm[2]; bf16x8 q[8]; u32x4 p[2][2]; f32x4 l[2]; f32x2 mhat; u32x4 ones; };
struct AttnCtx {
    srd_t srdK, srdV, srdC; unsigned voff, lane16, kslot0, vslot0, coffL;
    int g, nl  , thrg, dq0, relH, jump; unsigned qstash;
    unsigned soA, soB, ksoM; int rel;
    srd_t srdF; u64 tok, prev; unsigned known;
};
DI u64 ld_flag8(const AttnCtx& c) { return __builtin_bit_cast(u64, __builtin_amdgcn_raw_buffer_load_b64(c.srdF, (c.lane16 & 0x70u) << 3, 0, 16)); }

DI unsigned m_off(int i) { return i < 16 ? (unsigned)(((i & 7) * 2 + (i >> 3)) * 1024) : (unsigned)((((i - 16) & 3) * 4 + ((i - 16) >> 2)) * 1024); }
template <bool DO_PV, bool DO_QK, int KP, int VP, class Hook>
DI void m_phase(AttnState& st, unsigned kbase, unsigned vbase, bf16x8 (&pre)[ATT_W], Hook hook) {
    constexpr int N0 = DO_PV ? 0 : 16, N1 = DO_QK ? 32 : 16;
    bf16x8 fr[32];
#pragma unroll
    for (int i = 0; i < ATT_W; ++i) fr[N0 + i] = pre[i];
#pragma unroll
    for (int i = N0; i < N1; ++i) {
        const int n = i + ATT_W;
        if (n < N1) fr[n] = ldsf((n < 16 ? vbase + VP * 16384 : kbase + KP * 16384) + m_off(n));
        SBAR();
        if (i >= 16) { const int ds = (i - 16) >> 2, kb = (i - 16) & 3;
            if (ds == 0) { asm volatile("v_mfma_f32_16x16x32_bf16 %0, %2, %3, %1" : "=&v"(st.s[kb][0]), "+v"(st.negm[0]) : "v"(fr[i]), "v"(st.q[0]));
                           asm volatile("v_mfma_f32_16x16x32_bf16 %0, %2, %3, %1" : "=&v"(st.s[kb][1]), "+v"(st.negm[1]) : "v"(fr[i]), "v"(st.q[4])); }
            else { MFMA16I(st.s[kb][0], fr[i], st.q[ds]); MFMA16I(st.s[kb][1], fr[i], st.q[4 + ds]); }
        } else { const int ks = i >> 3, db = i & 7;
            MFMA16I(st.o[db][0], fr[i], st.p[ks][0]); MFMA16I(st.o[db][1], fr[i], st.p[ks][1]);
            if (db == 3) { MFMA16I(st.l[0], st.ones, st.p[ks][0]); MFMA16I(st.l[1], st.ones, st.p[ks][1]); } }
        SBAR();
        hook(i);
        SBAR();
    }
    MFMA_DRAIN();
    PIN(st.negm[0]); PIN(st.negm[1]);
}
struct NoHookI { DI void operator()(int) const {} };

DI void store_ctx(const f32x4 (&o)[8][2], float inv0, float inv1, srd_t srd, unsigned voff) {
    PIN(voff);
#pragma unroll
    for (int p = 0; p < 4; ++p)
#pragma unroll
        for (int qb = 0; qb < 2; ++qb) { const float inv = qb ? inv1 : inv0; const f32x4 lo = o[2 * p][qb] * inv, hi = o[2 * p + 1][qb] * inv;
            __builtin_amdgcn_raw_buffer_store_b128((u32x4){cvtpk(lo[0], lo[1]), cvtpk(lo[2], lo[3]), cvtpk(hi[0], hi[1]), cvtpk(hi[2], hi[3])}, srd, voff + p * 2048 + qb * 256, 0, 16); }
}
DI float row_add(float x) { return swap_add(x16_add(x)); }
DI float row_max4(float x) { return swap_max(x16_max(x)); }

template <bool LAST, int PAR>
DI void v_phase(AttnState& st, AttnCtx& c, const int t) {
    const bool sw = (t == c.nl);
    const unsigned vm0 = c.vslot0 + (PAR ^ 1) * 16384;
    const unsigned vso = c.soA, kso = c.soB;
    if (!LAST && __builtin_expect(c.known != 0xffu, 0)) {
        c.known |= (unsigned)__builtin_amdgcn_ballot_w64(c.prev == c.tok) & 0xffu;
        const unsigned need = 1u << ((kso >> 16) & 7);
        if (kso < 524288u && !(c.known & need)) {
            for (unsigned spins = 0; spins < (1u << 15); ++spins) { const u64 v = ld_flag8(c); c.known |= (unsigned)__builtin_amdgcn_ballot_w64(v == c.tok) & 0xffu; if (c.known & need) break; __builtin_amdgcn_s_sleep(1); } }
        asm volatile("" ::: "memory");
        c.prev = ld_flag8(c);
    }
    SBAR();
    if (__builtin_expect(sw, 0)) { store_ctx(st.o, __builtin_amdgcn_rcpf(st.l[0][0]), __builtin_amdgcn_rcpf(st.l[1][0]), c.srdC, ((c.lane16 >> 8) << 9) + (c.lane16 & 0xf0u) + c.coffL);
#pragma unroll
        for (int d = 0; d < 8; ++d) { st.o[d][0] = f32x4{}; st.o[d][1] = f32x4{}; PIN(st.o[d][0]); PIN(st.o[d][1]); }
        st.l[0] = f32x4{}; st.l[1] = f32x4{}; PIN(st.l[0]); PIN(st.l[1]); c.rel = c.relH; }
    if (__builtin_expect(c.rel > c.thrg, 0)) {
        const int u = 32 * c.g - c.rel;
        int dq = c.dq0; PIN(dq); const float NEG = -__builtin_inff(); const f32x4 NEG4 = {NEG, NEG, NEG, NEG};
#define HIDE(kb, qb) do { st.s[kb][qb] = NEG4; PIN(st.s[kb][qb]); } while (0)
#define DIAG(kb, qb) do { _Pragma("unroll") for (int e = 0; e < 4; ++e) if (e > dq) st.s[kb][qb][e] = NEG; PIN(st.s[kb][qb]); } while (0)
        if (u == 32) { DIAG(2, 0); DIAG(3, 1); HIDE(3, 0); }
        else {
            if (u == 0) { DIAG(0, 0); DIAG(1, 1); } else { HIDE(0, 0); HIDE(0, 1); HIDE(1, 1); }
            HIDE(1, 0); HIDE(2, 0); HIDE(2, 1); HIDE(3, 0); HIDE(3, 1);
        }
#undef HIDE
#undef DIAG
    }
    SBAR();
    if (!LAST) dma_piece<0, true>(vm0, c.voff, c.srdV, vso);
#define MX3(a, b, c) __builtin_fmaxf(__builtin_fmaxf((a), (b)), (c))
    float mx[2];
#pragma unroll
    for (int qb = 0; qb < 2; ++qb) {
        float a = MX3(st.s[0][qb][0], st.s[0][qb][1], st.s[0][qb][2]), b = MX3(st.s[0][qb][3], st.s[1][qb][0], st.s[1][qb][1]);
        a = MX3(a, st.s[1][qb][2], st.s[1][qb][3]); b = MX3(b, st.s[2][qb][0], st.s[2][qb][1]); a = MX3(a, st.s[2][qb][2], st.s[2][qb][3]);
        b = MX3(b, st.s[3][qb][0], st.s[3][qb][1]); a = MX3(a, st.s[3][qb][2], st.s[3][qb][3]); mx[qb] = __builtin_fmaxf(a, b); }
    const unsigned long long firstm = (t == 0 || sw) ? ~0ull : 0ull;
    if (__builtin_expect((__builtin_amdgcn_ballot_w64(__builtin_fmaxf(mx[0], mx[1]) > ATT_THR) | firstm) != 0ull, 0)) {
        const bool first = firstm != 0ull;
#pragma unroll
        for (int qb = 0; qb < 2; ++qb) {
            const float delta = __builtin_fmaxf(row_max4(mx[qb]), first ? -1e30f : 0.f);
            st.mhat[qb] = (first ? 0.f : st.mhat[qb]) + delta;
#pragma unroll
            for (int kb = 0; kb < 4; ++kb) st.s[kb][qb] -= delta;
            st.negm[qb] = (f32x4){-st.mhat[qb], -st.mhat[qb], -st.mhat[qb], -st.mhat[qb]};
            if (!first) { const float f = __builtin_amdgcn_exp2f(-delta); st.l[qb] *= f;
#pragma unroll
                for (int d = 0; d < 8; ++d) st.o[d][qb] *= f; } }
    }
    SBAR();
    float e[8][4];
#define EXPG(k) do { _Pragma("unroll") for (int i_ = 0; i_ < 4; ++i_) e[k][i_] = __builtin_amdgcn_exp2f(st.s[(k) >> 1][(k) & 1][i_]); } while (0)
#define FING(k) do { constexpr int kb_ = (k) >> 1, qb_ = (k) & 1; \
        unsigned w0_ = cvtpk(e[k][0], e[k][1]), w1_ = cvtpk(e[k][2], e[k][3]); PIN(w0_); PIN(w1_); \
        st.p[kb_ >> 1][qb_][2 * (kb_ & 1)] = w0_; st.p[kb_ >> 1][qb_][2 * (kb_ & 1) + 1] = w1_; } while (0)
    EXPG(0); SBAR();
    EXPG(1); FING(0); SBAR();
    if (!LAST) dma_piece<1024, false>(vm0, c.voff, c.srdV, vso);
    EXPG(2); FING(1); SBAR();
    EXPG(3); FING(2); SBAR();
    if (!LAST) dma_piece<2048, false>(vm0, c.voff, c.srdV, vso);
    EXPG(4); FING(3); SBAR();
    EXPG(5); FING(4); SBAR();
    if (!LAST) dma_piece<3072, false>(vm0, c.voff, c.srdV, vso);
    EXPG(6); FING(5); SBAR();
    EXPG(7); FING(6); SBAR();
    FING(7);
#undef EXPG
#undef FING
    if (!LAST) {
        if (__builtin_expect(t == c.nl - 1, 0)) {
            unsigned qs = c.qstash; PIN(qs);
#pragma unroll
            for (int s = 0; s < 8; ++s) st.q[s] = ldsf(qs + s * 1024);
            st.negm[0] = f32x4{}; st.negm[1] = f32x4{}; PIN(st.negm[0]); PIN(st.negm[1]);
        }
        c.ksoM = kso;
        c.soA = c.soB; c.soB += (t + 3 == c.nl) ? c.jump : 16384; c.rel += 64;
    }
    SBAR();
}

DI void attn_setup(AttnCtx& c, const u16* Kf, const u16* Vf, u16* Cf, unsigned lds0, int h, int j, int wid, int lane) {
    PIN(lane);
    const int grp = wid >> 2; c.g = wid & 3; c.dq0 = (lane & 15) - 4 * (lane >> 4);
    const int qbH = 15 - j, qbL = j, nl = 2 * j + 2;
    c.nl = grp ? nl : 1000; c.thrg = 32 * c.g - 63; c.jump = (18 - nl) * 16384; c.relH = 64 * (17 - 2 * qbH);
    c.rel = grp ? -128 * qbL : -128 * qbH;
    c.soA = 16384; c.soB = (c.nl == 2) ? 17 * 16384 : 2 * 16384;
    c.srdK = mk_srd((const char*)Kf + (size_t)h * 524288, 524288);
    c.srdV = mk_srd((const char*)Vf + (size_t)h * 524288, 524288);
    c.srdC = mk_srd((const char*)Cf, 16777216);
    c.lane16 = lane * 16; c.voff = lane * 16 + c.g * 4096;
    c.kslot0 = lds0 + grp * 65536 + c.g * 4096; c.vslot0 = c.kslot0 + 32768;
    c.coffL = ((qbL * 4 + c.g) * 32 + h) * 8192;
    c.qstash = lds0 + 131072 + c.g * 8192 + lane * 16;
}
#define DMA4(m_, vo_, srd, so_) do { dma_piece<0, true>(m_, vo_, srd, so_); dma_piece<1024, false>(m_, vo_, srd, so_); dma_piece<2048, false>(m_, vo_, srd, so_); dma_piece<3072, false>(m_, vo_, srd, so_); } while (0)
DI void attn_prime(const AttnCtx& c, int grp) { DMA4(grp ? c.vslot0 + 16384 : c.kslot0, c.voff, c.srdK, 0u); DMA4(c.vslot0, c.voff, c.srdV, 0u); if (grp == 0) DMA4(c.kslot0 + 16384, c.voff, c.srdK, 16384u); }

template <class First, class G0Pre, class G0Tail>
DI void attn_run(AttnState& st, AttnCtx& c, unsigned lds0, int h, int j, int wid, int lane, First afterFirst, G0Pre g0pre, G0Tail g0tail) {
    PIN(lane);
    const int grp = wid >> 2;
    const unsigned kbase = lds0 + grp * 65536 + lane * 16;
    const unsigned vbase = lds0 + grp * 65536 + 32768 + lane * 16;
#pragma unroll
    for (int d = 0; d < 8; ++d) { st.o[d][0] = f32x4{}; st.o[d][1] = f32x4{}; PIN(st.o[d][0]); PIN(st.o[d][1]); }
    st.negm[0] = f32x4{}; st.negm[1] = f32x4{}; st.l[0] = f32x4{}; st.l[1] = f32x4{}; st.mhat = (f32x2){0.f, 0.f};
    st.ones = (u32x4){0x3f803f80u, 0x3f803f80u, 0x3f803f80u, 0x3f803f80u}; PIN(st.ones); PIN(st.l[0]); PIN(st.l[1]);
    PIN(st.negm[0]); PIN(st.negm[1]);
    if (grp == 1) { DMA4(c.kslot0 + 16384, c.voff, c.srdK, 16384u); BARRIER(); }
    wait_bar<8>();
    bf16x8 pre[ATT_W];
#define PRE_V(P) do { _Pragma("unroll") for (int i = 0; i < ATT_W; ++i) pre[i] = ldsf(vbase + (P) * 16384 + m_off(i)); } while (0)
    const unsigned kbase0 = grp ? kbase + 49152 : kbase;
#pragma unroll
    for (int i = 0; i < ATT_W; ++i) pre[i] = ldsf(kbase0 + m_off(16 + i));
    __builtin_amdgcn_s_setprio(1);
    m_phase<false, true, 0, 0>(st, kbase0, vbase, pre, NoHookI{});
    __builtin_amdgcn_s_setprio(0);
    wait_bar<0>();
    afterFirst();
#define STEP(PAR, t) do { \
        v_phase<false, PAR>(st, c, t); \
        PRE_V(PAR); \
        wait_bar_keep<4>(); \
        __builtin_amdgcn_s_setprio(1); \
        m_phase<true, true, PAR ^ 1, PAR>(st, kbase, vbase, pre, [&](int i) { \
            if (i == 3) dma_piece<0, true>(c.kslot0 + PAR * 16384, c.voff, c.srdK, c.ksoM); if (i == 9) dma_piece<1024, false>(c.kslot0 + PAR * 16384, c.voff, c.srdK, c.ksoM); \
            if (i == 15) dma_piece<2048, false>(c.kslot0 + PAR * 16384, c.voff, c.srdK, c.ksoM); if (i == 21) dma_piece<3072, false>(c.kslot0 + PAR * 16384, c.voff, c.srdK, c.ksoM); }); \
        __builtin_amdgcn_s_setprio(0); \
        wait_bar<4>(); } while (0)
    for (int t = 0; t < 16; t += 2) { STEP(0, t); STEP(1, t + 1); }
    v_phase<true, 0>(st, c, 16);
    if (grp == 0) g0pre();
    PRE_V(0);
    asm volatile("s_barrier" ::: "memory");
    __builtin_amdgcn_s_setprio(1);
    m_phase<true, false, 0, 0>(st, kbase, vbase, pre, NoHookI{});
    __builtin_amdgcn_s_setprio(0);
    wait_bar<0>();
    PIN(lane);
    const unsigned xo = lds0 + grp * 65536 + c.g * 8192 + lane * 16;
    const unsigned xi = lds0 + (grp ^ 1) * 65536 + c.g * 8192 + lane * 16;
    const unsigned so = lds0 + grp * 65536 + 32768 + c.g * 512 + lane * 8, si = lds0 + (grp ^ 1) * 65536 + 32768 + c.g * 512 + lane * 8;
    const unsigned voff = (lane >> 4) * 512 + (lane & 15) * 16 + (((15 - j) * 4 + c.g) * 32 + h) * 8192;
#define MERGE_PUT(qb) do { _Pragma("unroll") for (int d = 0; d < 8; ++d) *(LAS f32x4*)(xo + d * 1024) = st.o[d][qb]; *(LAS f32x2*)so = (f32x2){st.mhat[qb], st.l[qb][0]}; } while (0)
#define MERGE_FIN(qb) do { const f32x2 ot = *(const LAS f32x2*)si; const float mh = st.mhat[qb], lt = st.l[qb][0]; \
        const float m = __builtin_fmaxf(ot[0], mh), f0 = __builtin_amdgcn_exp2f(ot[0] - m), f1 = __builtin_amdgcn_exp2f(mh - m); \
        const float inv = __builtin_amdgcn_rcpf(ot[1] * f0 + lt * f1), a0 = f0 * inv, a1 = f1 * inv; \
        _Pragma("unroll") for (int p = 0; p < 4; ++p) { \
            const f32x4 lo = st.o[2 * p][qb] * a1 + *(const LAS f32x4*)(xi + (2 * p) * 1024) * a0, hi = st.o[2 * p + 1][qb] * a1 + *(const LAS f32x4*)(xi + (2 * p + 1) * 1024) * a0; \
            __builtin_amdgcn_raw_buffer_store_b128((u32x4){cvtpk(lo[0], lo[1]), cvtpk(lo[2], lo[3]), cvtpk(hi[0], hi[1]), cvtpk(hi[2], hi[3])}, c.srdC, voff + p * 2048 + (qb) * 256, 0, 16); } } while (0)
    if (grp == 0) { MERGE_PUT(1); BARRIER(); g0tail(); BARRIER(); MERGE_FIN(0); asm volatile("; merge 0"); }
    else          { asm volatile("; merge 1"); MERGE_PUT(0); BARRIER(); MERGE_FIN(1); }
#undef MERGE_PUT
#undef MERGE_FIN
#undef PRE_V
#undef STEP
}
constexpr unsigned OUT_O = 131072;
struct OutCtx { srd_t srdC, srdW, srdB; unsigned lane16, vw0, vwA, sC, sW, sB, lds0; int wid, rot; };
DI void out_issueA(const OutCtx& c, int kt, int i) {
    const unsigned st = c.lds0 + (kt & 3) * 32768;
    const int kr = (kt + c.rot) & 15;
    if (i < 2) { const int p = 2 * c.wid + i;
        dma1s(st + p * 1024, c.lane16, c.srdC, c.sC + (p >> 2) * 262144 + (p & 3) * 1024 + kr * 4096); }
    else dma1(st + 16384 + (2 * c.wid + (i - 2)) * 1024, c.vwA, c.srdW, c.sW + (8 * c.wid) * 1024 + (i - 2) * 128 + kr * 65536);
}
DI void out_issueB(const OutCtx& c, int ch, int i) {
    const int rg = 2 * c.wid + (i >> 1);
    dma1(c.lds0 + ch * 32768 + rg * 2048 + (i & 1) * 1024, (i >> 1) ? (c.vw0 ^ 32u) : c.vw0, c.srdB, c.sB + (ch * 128 + 8 * rg) * 1024 + (i & 1) * 128);
}
DI u32x4 out_pack(f32x4 s0, f32x4 s1) {
    unsigned a0 = cvtpk(s0[0], s0[1]), a1 = cvtpk(s0[2], s0[3]), b0 = cvtpk(s1[0], s1[1]), b1 = cvtpk(s1[2], s1[3]);
    auto x0 = __builtin_amdgcn_permlane32_swap(a0, b0, false, false); auto x1 = __builtin_amdgcn_permlane32_swap(a1, b1, false, false);
    return (u32x4){x0[0], x1[0], x0[1], x1[1]};
}
DI f32x4 quad(const f32x16& x, int rq) { return (f32x4){x[4 * rq], x[4 * rq + 1], x[4 * rq + 2], x[4 * rq + 3]}; }

DI void out_phase(const u16* Wo, const u16* Bwo, u16* out, char* ws, u64 tok, unsigned lds0, int bid, int wid, int lane) {
    PIN(lane);
    const int tid = wid * 64 + lane, r32 = lane & 31, hi = lane >> 5, wr = wid >> 2, wc = wid & 3;
    const int x = bid & 7, u = bid >> 3, s = x >> 1, rb = 8 * (x & 1) + (u >> 2), cb = u & 3, tile = rb * 4 + cb;
    OutCtx c; c.wid = wid; c.lds0 = lds0; c.lane16 = lane * 16;
    c.srdC = mk_srd(ws + WS_CF + (size_t)rb * 4 * 262144, 4 * 262144); c.srdW = mk_srd(Wo, 4194304); c.srdB = mk_srd(Bwo, 524288);
    c.vw0 = ((lane >> 2) & 7) * 1024 + (4 * (lane >> 5) + ((lane & 3) ^ ((lane >> 4) & 1))) * 16; c.vwA = (wid & 1) ? (c.vw0 ^ 32u) : c.vw0;
    c.rot = 4 * cb + 2 * (rb & 7);
    c.sC = s * 8 * 8192; c.sW = s * 1024 * 1024 + cb * 256; c.sB = cb * 256;
#pragma unroll
    for (int kt = 0; kt < 4; ++kt) { out_issueA(c, kt, 2); out_issueA(c, kt, 3); }
    u64* flags = (u64*)(ws + WS_FLAG);
    {
        const int l = lane & 15, hh = 8 * s + (l & 7), jj = rb < 8 ? rb : 15 - rb;
        const int src = l < 8 ? ((hh >> 2) | (jj << 3) | ((hh & 3) << 6)) : (8 * (2 * rb + (s >> 1)) + (l & 7));
        const u64* fp = flags + (FL_C + src) * FL_STRIDE;
        for (unsigned spins = 0; spins < (1u << 15); ++spins) { const u64 v = ld_agent(fp); if (__all(v == tok)) break; __builtin_amdgcn_s_sleep(1); }
        asm volatile("" ::: "memory"); }
#pragma unroll
    for (int kt = 0; kt < 4; ++kt) { out_issueA(c, kt, 0); out_issueA(c, kt, 1); }
    { const srd_t srdO = mk_srd(ws + WS_OF + (size_t)(rb * 4 + s) * 32768, 32768);
#pragma unroll
      for (int i = 0; i < 4; ++i) dma1s(lds0 + OUT_O + (4 * wid + i) * 1024, c.lane16, srdO, (unsigned)((4 * wid + i) * 1024)); }
    const int q4 = (lane & 15) >> 2, p4 = lane & 3, blk = (lane >> 4) & 1;
    const unsigned aaddr = lds0 + (2 * wr) * 4096 + c.lane16;
    unsigned waA[2], waddr[2];
#pragma unroll
    for (int t = 0; t < 2; ++t) { waA[t] = lds0 + 2048 * t + 512 * wc + 64 * (4 * hi + q4) + 16 * ((2 * blk + (p4 >> 1)) ^ ((2 * t + hi) & 3)) + 8 * (p4 & 1);
                                  waddr[t] = waA[t]; }
    f32x16 acc[2] = {f32x16{}, f32x16{}};
    struct Fr { bf16x8 a[2][2]; bf16x4 w[2][2]; };
    auto rd = [&](Fr& f, unsigned st, int half) {
        unsigned w0 = waA[0] + st + 16384 + half * 8192, w1 = waA[1] + st + 16384 + half * 8192, aa = aaddr + st; PIN(w0); PIN(w1); PIN(aa);
#pragma unroll
        for (int k = 0; k < 2; ++k) { const int ks = 2 * half + k; const unsigned wb = k ? w1 : w0;
            f.w[k][0] = ldstr(wb); f.w[k][1] = ldstr(wb + 4096);
            f.a[k][0] = ldsf(aa + ks * 1024); f.a[k][1] = ldsf(aa + 4096 + ks * 1024); } };
    Fr f0, f1;
    wait_bar<10>();
    rd(f0, 0u, 0);
    auto step = [&](int kt, auto late, auto early) {
        const unsigned st = (kt & 3) * 32768, stn = ((kt + 1) & 3) * 32768;
        rd(f1, st, 1);
        SBAR();
#pragma unroll
        for (int k = 0; k < 2; ++k) { const bf16x8 wf = cat4(f0.w[k][0], f0.w[k][1]);
            acc[0] = MFMA32(wf, f0.a[k][0], acc[0]); acc[1] = MFMA32(wf, f0.a[k][1], acc[1]); SBAR(); late(2 + k); SBAR(); }
        wait_bar<8>();
        rd(f0, stn, 0);
        SBAR();
#pragma unroll
        for (int k = 0; k < 2; ++k) { const bf16x8 wf = cat4(f1.w[k][0], f1.w[k][1]);
            acc[0] = MFMA32(wf, f1.a[k][0], acc[0]); acc[1] = MFMA32(wf, f1.a[k][1], acc[1]); SBAR(); early(k); SBAR(); }
    };
    step(0, [&](int) {}, [&](int i) { out_issueA(c, 4, i); });
    for (int kt = 1; kt < 12; ++kt) step(kt, [&](int i) { out_issueA(c, kt + 3, i); }, [&](int i) { out_issueA(c, kt + 4, i); });
    step(12, [&](int i) { out_issueA(c, 15, i); }, [&](int i) { out_issueB(c, 0, i); });
    step(13, [&](int i) { out_issueB(c, 0, i); }, [&](int i) { out_issueB(c, 1, i); });
    step(14, [&](int i) { out_issueB(c, 1, i); }, [&](int i) { out_issueB(c, 2, i); });
    step(15, [&](int i) { out_issueB(c, 2, i); }, [&](int i) { out_issueB(c, 3, i); });
    out_issueB(c, 3, 2); out_issueB(c, 3, 3);
    const srd_t srdX = mk_srd(ws + WS_XCH + (size_t)tile * 262144, 262144);
#pragma unroll
    for (int mb = 0; mb < 2; ++mb) { const int q = 2 * wr + mb;
        if (q != s) {
#pragma unroll
            for (int rq = 0; rq < 4; ++rq) store16_sc1(__builtin_bit_cast(u32x4, quad(acc[mb], rq)), c.lane16, srdX, (unsigned)(((q * 4 + s) * 4 + wc) * 4096 + rq * 1024)); } }
    wait_bar<0>();
    if (tid == 0) st_agent(flags + (FL_XF + tile * 4 + s) * FL_STRIDE, tok);
    const int row = rb * 128 + 32 * s + r32;
    const srd_t srdOut = mk_srd(out, 4194304);
    if (wr == (s >> 1)) {
        const u64* fp = flags + (FL_XF + tile * 4 + (lane & 3)) * FL_STRIDE;
        for (unsigned spins = 0; spins < (1u << 15); ++spins) { const u64 v = ld_agent(fp); if (__all(v == tok)) break; __builtin_amdgcn_s_sleep(1); }
        asm volatile("" ::: "memory");
        f32x4 p[4][4];
#pragma unroll
        for (int q = 0; q < 4; ++q)
#pragma unroll
            for (int rq = 0; rq < 4; ++rq) p[q][rq] = __builtin_bit_cast(f32x4, __builtin_amdgcn_raw_buffer_load_b128(srdX, (q == s ? 0x7ffffff0u : c.lane16) + rq * 1024, ((s * 4 + q) * 4 + wc) * 4096, 16));
        f32x4 r[4];
#pragma unroll
        for (int rq = 0; rq < 4; ++rq) r[rq] = ((p[0][rq] + p[1][rq]) + (p[2][rq] + p[3][rq])) + ((s & 1) ? quad(acc[1], rq) : quad(acc[0], rq));
        const unsigned vo = (row * 1024 + cb * 128 + 32 * wc + 8 * hi) * 2;
        __builtin_amdgcn_raw_buffer_store_b128(out_pack(r[0], r[1]), srdOut, vo, 0, 0); __builtin_amdgcn_raw_buffer_store_b128(out_pack(r[2], r[3]), srdOut, vo + 32, 0, 0);
    } else {
        f32x16 accB[2] = {f32x16{}, f32x16{}};
#pragma unroll
        for (int ch = 0; ch < 4; ++ch)
#pragma unroll
            for (int hf = 0; hf < 2; ++hf) {
                unsigned w0 = waddr[0] + ch * 32768 + hf * 16384, w1 = waddr[1] + ch * 32768 + hf * 16384, oa = lds0 + OUT_O + (8 * ch + 4 * hf) * 1024 + c.lane16; PIN(w0); PIN(w1); PIN(oa);
                bf16x8 a[4]; bf16x4 w[4][2];
#pragma unroll
                for (int ks = 0; ks < 4; ++ks) { w[ks][0] = ldstr(w0 + ks * 4096); w[ks][1] = ldstr(w1 + ks * 4096); a[ks] = ldsf(oa + ks * 1024); }
#pragma unroll
                for (int ks = 0; ks < 4; ++ks) accB[ks & 1] = MFMA32(cat4(w[ks][0], w[ks][1]), a[ks], accB[ks & 1]);
            }
        const f32x16 rB = accB[0] + accB[1];
        const unsigned vo = (row * 1024 + 512 + cb * 128 + 32 * wc + 8 * hi) * 2;
        __builtin_amdgcn_raw_buffer_store_b128(out_pack(quad(rB, 0), quad(rB, 1)), srdOut, vo, 0, 0); __builtin_amdgcn_raw_buffer_store_b128(out_pack(quad(rB, 2), quad(rB, 3)), srdOut, vo + 32, 0, 0);
    }
}
constexpr unsigned CODE_BYTES = 44 * 1024;
constexpr int FUSED_LDS = 163840;
constexpr unsigned WPHI_LDS = 156160;
struct FusedArgs { const u16 *X, *Ckv, *Wq, *Wuk, *Wuv, *Wo, *Bx, *Wqkv, *Wphi, *Bwo; char* ws; u16* out; };

__global__ __launch_bounds__(512, 2) void fused_kernel(FusedArgs a) {
    const int tid = threadIdx.x, lane = tid & 63;
    const int wid = rfl(tid >> 6);
    const int bid = blockIdx.x, h = (bid & 7) * 4 + (bid >> 6), j = (bid >> 3) & 7;
    if (bid < 8) {
        u64 pc_; asm volatile("s_getpc_b64 %0" : "=s"(pc_));
        const unsigned o_ = wid * 8192 + lane * 128;
        asm volatile("s_mov_b32 m0, %0\n\ts_nop 0\n\tglobal_load_lds_dword %1, %2" :: "s"(163840u - 256u), "v"(o_ < CODE_BYTES ? o_ : 0u), "s"(pc_ & ~(u64)127) : "memory"); }
    const unsigned lds0 = 0u;
    u16* Kf = (u16*)(a.ws + WS_KF); u16* Vf = (u16*)(a.ws + WS_VF); u16* Cf = (u16*)(a.ws + WS_CF);
    u16* PhiQ = (u16*)(a.ws + WS_PHIQ); u16* Of = (u16*)(a.ws + WS_OF); float* Bst = (float*)(a.ws + WS_BST);
    u64* flags = (u64*)(a.ws + WS_FLAG);
    float* Bred = (float*)(a.ws + WS_BRED);
    const int hg = bid & 7, rc = bid >> 3;
    asm volatile("" :: "s"(a.X), "s"(a.Ckv), "s"(a.Wq), "s"(a.Wuk), "s"(a.Wuv), "s"(a.Wphi), "s"(a.ws));
    ProjCtx pc; proj_setup(pc, a.X, a.Ckv, a.Wq, a.Wuk, a.Wuv, Kf, Vf, lds0, h, j, wid, lane);
#pragma unroll
    for (int i = 0; i < 8; ++i) issueKV(pc, 0, i);
#pragma unroll
    for (int i = 0; i < 8; ++i) issueKV(pc, 1, i);
    const unsigned tsc = lds0 + TOUCH_LDS;
    { const unsigned vW = (lane >> 4) * 524288 + (lane & 15) * 128;
      const char* base; unsigned vo = pc.l128;
      if (wid < 4) { base = (const char*)((wid & 1) ? a.Wuv : a.Wuk) + ((wid & 2) ? 4 : 2) * 524288 + bid * 2048; vo = vW; }
      else if (wid == 4) base = (const char*)a.Ckv + bid * 8192;
      else { base = (const char*)a.Ckv; vo = 0; }
      touch_g(tsc, vo, base); }
    if (wid < 2) dma1(lds0 + WPHI_LDS + wid * 1024, lane * 16, mk_srd(a.Wphi, 16384), (unsigned)(hg * 2048 + wid * 1024));
    const u64 epoch = *(const u64 __attribute__((address_space(4)))*)(flags + FL_EPOCH * FL_STRIDE);
    u64 tok = mix64(epoch); if (tok == epoch) tok ^= 1ull;
    tok = ((u64)rfl((unsigned)(tok >> 32)) << 32) | rfl((unsigned)tok); asm volatile("" : "+s"(tok));
    B1Ctx bc; b1_setup(bc, a.Bx, a.Wqkv, lds0, rc, hg, wid, lane);
    auto touchk = [&](int kt) {
        const char* base = (const char*)a.Ckv; unsigned vo = 0;
        if (kt == 0) {
            if (wid == 0) { base = (const char*)a.Bx + bid * 8192; vo = pc.l128; }
            else if (wid == 1) { base = (const char*)a.X + bid * 8192; vo = pc.l128; }
            else if (wid == 2) { base = (const char*)a.Wqkv + (bid < 192 ? bid : 0) * 8192; vo = pc.l128; }
        } else if (kt == 1) {
            if (wid < 2) { base = (const char*)a.Wq + (bid * 2 + wid) * 8192; vo = pc.l128; }
        } else if (kt == 2) {
            if (wid < 2) { base = (const char*)a.Wo + (bid * 2 + wid) * 8192; vo = pc.l128; }
            else if (wid == 2) { base = (const char*)a.Bwo + (bid & 63) * 8192; vo = pc.l128; }
        }
        touch_g(tsc, vo, base); };
    u32x4 pk[16];
    kv_run(pc, lane, pk, touchk, [&](int kt) { b1_issue(bc, kt); });
    B1Acc ba;
    const bool early = (j & 3) == 0;
    b1_gemm(bc, ba, lane, early, [&](int n) { if (early) kv_store(pc, pk, n); else if (n >= 1 && n <= 6) kv_store(pc, pk, n - 1); },
            [&](int n) {
#pragma unroll
                for (int i = 0; i < 6; ++i) issueQ(pc, n, i); });
    u32x2 wp; { const int l16 = lane & 15, g = lane >> 4; const LAS u16* w = (const LAS u16*)(lds0 + WPHI_LDS + (wid & 3) * 512);
        wp[0] = (unsigned)w[(4 * g) * 16 + l16] | ((unsigned)w[(4 * g + 1) * 16 + l16] << 16); wp[1] = (unsigned)w[(4 * g + 2) * 16 + l16] | ((unsigned)w[(4 * g + 3) * 16 + l16] << 16); }
    b1_phi(ba, [&](int n) { SBAR(); if (n < 6) issueQ(pc, 1, n); else if (n < 12) issueQ(pc, 2, n - 6); else if (early) { if (n < 16) kv_store(pc, pk, 8 + (n < 16 ? n - 12 : 0)); } else kv_store(pc, pk, 6 + n - 12); SBAR(); }, wp, PhiQ, Bst, lds0, rc, hg, wid, lane);
    wait_bar<21>();
    AttnState st;
    AttnCtx c; attn_setup(c, Kf, Vf, Cf, lds0, h, j, wid, lane);
    B2a b2;
    auto spin = [&](const u64* fp) {
        for (unsigned spins = 0; spins < (1u << 15); ++spins) { const u64 v = ld_agent(fp); if (__all(v == tok)) break; __builtin_amdgcn_s_sleep(1); }
        asm volatile("" ::: "memory"); };
    const u64* fkv = flags + (FL_KV + 8 * h + (lane & 7)) * FL_STRIDE;
    const u64* fbs = flags + (FL_BS + 32 * hg + (lane & 31)) * FL_STRIDE;
    u64 pre = 0;
    q_run(pc, early, lane, pk, st.q, [&](int kt) {
        if (kt == 2) { if (tid == 0) st_agent(flags + (FL_BS + 32 * hg + rc) * FL_STRIDE, tok); }
        if (kt == 3) { if (early && tid == 0) st_agent(flags + (FL_KV + 8 * h + j) * FL_STRIDE, tok); }
        if (kt == 3) { if (wid == 7) pre = ld_agent(fbs); }
        if (kt == 4) {
            if (wid == 7) {
                if (!__all(pre == tok)) spin(fbs);
                asm volatile("" ::: "memory");
                b2a_load(b2, Bst, rc, hg, lane); } }
        if (kt == 5) { pre = ld_agent(fkv); }
        if (kt == 6) {
            if (!early && tid == 0) st_agent(flags + (FL_KV + 8 * h + j) * FL_STRIDE, tok);
            if (wid == 7) b2a_finish(b2, Bred, rc, hg, lane);
            c.known = (unsigned)__builtin_amdgcn_ballot_w64(pre == tok) & 0xffu;
            for (unsigned spins = 0; spins < (1u << 15) && !(c.known & 1u); ++spins) { const u64 v = ld_agent(fkv); c.known = (unsigned)__builtin_amdgcn_ballot_w64(v == tok) & 0xffu; if (!(c.known & 1u)) __builtin_amdgcn_s_sleep(1); }
            asm volatile("" ::: "memory");
            attn_prime(c, wid >> 2);
        } });
    c.srdF = mk_srd(flags + (FL_KV + 8 * h) * FL_STRIDE, 1024); c.tok = tok; c.prev = 0;
    BARRIER();
    if (wid < 4) {
#pragma unroll
        for (int s = 0; s < 8; ++s) *(LAS bf16x8*)(c.qstash + s * 1024) = st.q[s]; }
    B2b<4> b2b;
    attn_run(st, c, lds0, h, j, wid, lane,
             [&]() { if (tid == 448) st_agent(flags + (FL_BR + 32 * hg + rc) * FL_STRIDE, tok); },
             [&]() { b2b_load<4>(b2b, PhiQ, Bred, flags + (FL_BR + 32 * hg + 8 * wid) * FL_STRIDE, tok, 4 * hg + wid, 4 * rc, lane); },
             [&]() { b2b_finish<4>(b2b, Of, 4 * hg + wid, 4 * rc, lane); });
    wait_bar<0>();
    if (tid == 0) st_agent(flags + (FL_C + bid) * FL_STRIDE, tok);
    out_phase(a.Wo, a.Bwo, a.out, a.ws, tok, lds0, bid, wid, lane);
    if (bid == 0 && wid == 7) {
        bool ok = false;
        for (unsigned spins = 0; spins < (1u << 15) && !ok; ++spins) { bool all = true;
#pragma unroll
            for (int i = 0; i < 4; ++i) all &= ld_agent(flags + (FL_C + lane + 64 * i) * FL_STRIDE) == tok;
            ok = __all(all); if (!ok) __builtin_amdgcn_s_sleep(1); }
        if (lane == 0) st_agent(flags + FL_EPOCH * FL_STRIDE, tok);
    }
}

#define WS_SHIFT(n) ((n) >= ((size_t)160 << 20) ? (((n) - ((size_t)80 << 20)) & ~(size_t)0x1fffff) : (size_t)0)
extern "C" void kernel_launch(void* const* d_in, const int* in_sizes, int n_in, void* d_out, int out_size, void* d_ws, size_t ws_size, hipStream_t stream) {
    FusedArgs a;
    a.X = (const u16*)d_in[0]; a.Ckv = (const u16*)d_in[1]; a.Wq = (const u16*)d_in[2]; a.Wuk = (const u16*)d_in[3]; a.Wuv = (const u16*)d_in[4]; a.Wo = (const u16*)d_in[5];
    a.Bx = (const u16*)d_in[6]; a.Wqkv = (const u16*)d_in[7]; a.Wphi = (const u16*)d_in[8]; a.Bwo = (const u16*)d_in[9]; a.ws = (char*)d_ws + WS_SHIFT(ws_size); a.out = (u16*)d_out;
    (void)hipFuncSetAttribute((const void*)fused_kernel, hipFuncAttributeMaxDynamicSharedMemorySize, FUSED_LDS);
    fused_kernel<<<256, 512, FUSED_LDS, stream>>>(a);
}
```
